```python
import torch

def _softmax(x, axis=-1):
    return torch.softmax(x, dim=axis)
def _gelu(x):
    return torch.nn.functional.gelu(x, approximate="tanh")
def _silu(x):
    return torch.nn.functional.silu(x)
def _sigmoid(x):
    return torch.sigmoid(x)
def _layernorm(x, axis=-1, eps=1e-5):
    m = x.mean(dim=axis, keepdim=True)
    v = x.var(dim=axis, keepdim=True, unbiased=False)
    return (x - m) / torch.sqrt(v + eps)
def _scatter_add(tgt, idx, src, axis=0):
    return tgt.clone().index_add_(axis, idx, src)
def _segment_sum(src, seg, n):
    out = torch.zeros((n,) + src.shape[1:], dtype=src.dtype)
    return out.index_add_(0, seg, src)
def _pad(x, pw):
    flat = tuple(v for pair in reversed(pw) for v in pair)
    return torch.nn.functional.pad(x, flat)

kernel_name = 'recurrent_scan__compressed_kv_cla__geglu_ffn__prompt'

def setup_inputs():

    _w = (lambda a: (lambda t: t.to(torch.bfloat16) if t.is_floating_point() else t)(torch.as_tensor(a)))
    def _A_setup():
        B, H, Sq, Sk, D, M = 1, 32, 2048, 2048, 128, 512
        Dc = 4 * D
        return {
            "x": _w((lambda *s: torch.randn(*s, dtype=torch.bfloat16) * 0.05)(B, Sq, M)),
            "c_kv": _w((lambda *s: torch.randn(*s, dtype=torch.bfloat16) * 0.2)(B, Sk, Dc)),
            "w_q": _w((lambda *s: torch.randn(*s, dtype=torch.bfloat16) * 3.2)(M, H, D) * (M ** -0.5)),
            "w_uk": _w((lambda *s: torch.randn(*s, dtype=torch.bfloat16) * 3.2)(Dc, H, D) * (Dc ** -0.5)),
            "w_uv": _w((lambda *s: torch.randn(*s, dtype=torch.bfloat16) * 0.2)(Dc, H, D) * (Dc ** -0.5)),
            "w_o": _w((lambda *s: torch.randn(*s, dtype=torch.bfloat16) * 0.2)(H, D, M) * ((H * D) ** -0.5)),
        }
    _A_in = _A_setup()

    def _B_setup():
        B, S, D, F = 1, 2048, 512, 1792
        return {
            "x": _w((lambda *s: torch.randn(*s, dtype=torch.bfloat16) * 0.05)(B, S, D)),
            "w_gate": _w((lambda *s: torch.randn(*s, dtype=torch.bfloat16) * 0.1)(D, F)),
            "w_up": _w((lambda *s: torch.randn(*s, dtype=torch.bfloat16) * 0.1)(D, F)),
            "w_down": _w((lambda *s: torch.randn(*s, dtype=torch.bfloat16) * 0.1)(F, D)),
        }
    _B_in = _B_setup()

    return {
        "A_x": _A_in["x"],
        "A_c_kv": _A_in["c_kv"],
        "A_w_q": _A_in["w_q"],
        "A_w_uk": _A_in["w_uk"],
        "A_w_uv": _A_in["w_uv"],
        "A_w_o": _A_in["w_o"],
        "B_x": _B_in["x"],
        "B_w_gate": _B_in["w_gate"],
        "B_w_up": _B_in["w_up"],
        "B_w_down": _B_in["w_down"],
    }

def reference(A_x, A_c_kv, A_w_q, A_w_uk, A_w_uv, A_w_o, B_x, B_w_gate, B_w_up, B_w_down):
    def _A_ref(x, c_kv, w_q, w_uk, w_uv, w_o):
        B, Sq, M = x.shape[0], x.shape[1], x.shape[2]
        Sk = c_kv.shape[1]
        H, D = w_q.shape[1], w_q.shape[2]
        q = torch.einsum("bqm,mhd->bhqd", x, w_q)
        k = torch.einsum("bkc,chd->bhkd", c_kv, w_uk)
        v = torch.einsum("bkc,chd->bhkd", c_kv, w_uv)
        scores = torch.einsum("bhqd,bhkd->bhqk", q, k) * (D ** -0.5)
        off = Sk - Sq
        qi = torch.arange(Sq)[:, None]
        kj = torch.arange(Sk)[None, :]
        cmask = kj <= (qi + off)
        scores = torch.where(cmask[None, None], scores, -1e9)
        p = _softmax(scores, axis=-1)
        ctx = torch.einsum("bhqk,bhkd->bhqd", p, v)
        return torch.einsum("bhqd,hdm->bqm", ctx, w_o)

    def _B_ref(x, w_gate, w_up, w_down):
        return (_gelu(x @ w_gate) * (x @ w_up)) @ w_down

    x = A_x
    xn = _layernorm(x, axis=-1)
    s = _A_ref(xn, A_c_kv, A_w_q, A_w_uk, A_w_uv, A_w_o)
    g = _silu(_B_ref(xn, B_w_gate, B_w_up, B_w_down))
    return x + s * g
```

```cpp
#include <hip/hip_runtime.h>
#include <hip/hip_bf16.h>
typedef unsigned short u16;
__device__ __forceinline__ float bf2f(u16 b){ return __uint_as_float(((unsigned)b)<<16); }
__device__ __forceinline__ u16 f2bf(float f){ unsigned u=__float_as_uint(f); u += 0x7FFFu + ((u>>16)&1u); return (u16)(u>>16); }

__global__ void ln_k(const u16* x, u16* xn){
  int row = blockIdx.x*4 + (threadIdx.x>>6), lane = threadIdx.x&63;
  float v[8]; float s=0;
  for(int j=0;j<8;j++){ v[j]=bf2f(x[row*512+lane*8+j]); s+=v[j]; }
  for(int o=32;o>=1;o>>=1) s+=__shfl_xor(s,o);
  float m=s*(1.f/512.f); float q=0;
  for(int j=0;j<8;j++){ float d=v[j]-m; q+=d*d; }
  for(int o=32;o>=1;o>>=1) q+=__shfl_xor(q,o);
  float r=rsqrtf(q*(1.f/512.f)+1e-5f);
  for(int j=0;j<8;j++) xn[row*512+lane*8+j]=f2bf((v[j]-m)*r);
}
__global__ void gemm_k(const u16* A, const u16* B, float* C, int M, int N, int K){
  __shared__ float As[16][65], Bs[16][65];
  int tx=threadIdx.x&15, ty=threadIdx.x>>4; int bm=blockIdx.y*64, bn=blockIdx.x*64;
  float acc[4][4]={};
  for(int k0=0;k0<K;k0+=16){
    for(int i=threadIdx.x;i<1024;i+=256){ int r=i>>4,c=i&15; As[c][r]=bf2f(A[(size_t)(bm+r)*K+k0+c]); }
    for(int i=threadIdx.x;i<1024;i+=256){ int r=i>>6,c=i&63; Bs[r][c]=bf2f(B[(size_t)(k0+r)*N+bn+c]); }
    __syncthreads();
    for(int k=0;k<16;k++){ float a[4],b[4]; for(int i=0;i<4;i++){a[i]=As[k][ty*4+i]; b[i]=Bs[k][tx*4+i];}
      for(int i=0;i<4;i++)for(int j=0;j<4;j++) acc[i][j]+=a[i]*b[j]; }
    __syncthreads();
  }
  for(int i=0;i<4;i++)for(int j=0;j<4;j++) C[(size_t)(bm+ty*4+i)*N+bn+tx*4+j]=acc[i][j];
}
__global__ void tobf_k(const float* a, u16* o, int n){ int i=blockIdx.x*256+threadIdx.x; if(i<n) o[i]=f2bf(a[i]); }
__global__ void geglu_k(const float* g, const float* u, u16* h, int n){ int i=blockIdx.x*256+threadIdx.x; if(i<n){ float x=bf2f(f2bf(g[i])); float t=tanhf(0.7978845608f*(x+0.044715f*x*x*x)); float ge=bf2f(f2bf(0.5f*x*(1.f+t))); h[i]=f2bf(ge*bf2f(f2bf(u[i]))); } }
__global__ void attn_k(const u16* q, const u16* k, const u16* v, u16* ctx){
  __shared__ float ps[4][2048]; __shared__ float qs[4][128];
  int w=threadIdx.x>>6, lane=threadIdx.x&63; int id=blockIdx.x*4+w; int h=id>>11, row=id&2047;
  for(int d=lane;d<128;d+=64) qs[w][d]=bf2f(q[(size_t)row*4096+h*128+d]);
  __syncthreads();
  float mx=-1e30f;
  for(int j=lane;j<=row;j+=64){ float s=0; const u16* kp=k+(size_t)j*4096+h*128; for(int d=0;d<128;d++) s+=qs[w][d]*bf2f(kp[d]); s*=0.08838834764f; ps[w][j]=s; mx=fmaxf(mx,s);}
  for(int o=32;o>=1;o>>=1) mx=fmaxf(mx,__shfl_xor(mx,o));
  float l=0; for(int j=lane;j<=row;j+=64){ float p=__expf(ps[w][j]-mx); ps[w][j]=p; l+=p; }
  for(int o=32;o>=1;o>>=1) l+=__shfl_xor(l,o);
  __syncthreads();
  float a0=0,a1=0; for(int j=0;j<=row;j++){ float p=ps[w][j]; const u16* vp=v+(size_t)j*4096+h*128; a0+=p*bf2f(vp[lane]); a1+=p*bf2f(vp[lane+64]); }
  ctx[(size_t)row*4096+h*128+lane]=f2bf(a0/l); ctx[(size_t)row*4096+h*128+lane+64]=f2bf(a1/l);
}
__global__ void final_k(const u16* x, const float* s, const float* f, u16* out, int n){ int i=blockIdx.x*256+threadIdx.x; if(i<n){ float ff=f[i]; float g=ff/(1.f+__expf(-ff)); out[i]=f2bf(bf2f(x[i])+s[i]*g);} }

extern "C" void kernel_launch(void* const* d_in, const int* in_sizes, int n_in, void* d_out, int out_size, void* d_ws, size_t ws_size, hipStream_t stream){
  const u16 *x=(const u16*)d_in[0], *ckv=(const u16*)d_in[1], *wq=(const u16*)d_in[2], *wuk=(const u16*)d_in[3], *wuv=(const u16*)d_in[4], *wo=(const u16*)d_in[5];
  const u16 *wg=(const u16*)d_in[7], *wu=(const u16*)d_in[8], *wd=(const u16*)d_in[9];
  char* ws=(char*)d_ws; size_t MB=1<<20;
  u16* xn=(u16*)(ws); float* t0=(float*)(ws+2*MB); float* t1=(float*)(ws+34*MB);
  u16* q=(u16*)(ws+66*MB); u16* k=(u16*)(ws+82*MB); u16* v=(u16*)(ws+98*MB); u16* ctx=(u16*)(ws+114*MB); u16* h=(u16*)(ws+130*MB);
  ln_k<<<512,256,0,stream>>>(x,xn);
  gemm_k<<<dim3(64,32),256,0,stream>>>(xn,wq,t0,2048,4096,512); tobf_k<<<32768,256,0,stream>>>(t0,q,2048*4096);
  gemm_k<<<dim3(64,32),256,0,stream>>>(ckv,wuk,t0,2048,4096,512); tobf_k<<<32768,256,0,stream>>>(t0,k,2048*4096);
  gemm_k<<<dim3(64,32),256,0,stream>>>(ckv,wuv,t0,2048,4096,512); tobf_k<<<32768,256,0,stream>>>(t0,v,2048*4096);
  attn_k<<<32*2048/4,256,0,stream>>>(q,k,v,ctx);
  gemm_k<<<dim3(28,32),256,0,stream>>>(xn,wg,t0,2048,1792,512);
  gemm_k<<<dim3(28,32),256,0,stream>>>(xn,wu,t1,2048,1792,512);
  geglu_k<<<2048*1792/256,256,0,stream>>>(t0,t1,h,2048*1792);
  gemm_k<<<dim3(8,32),256,0,stream>>>(ctx,wo,t0,2048,512,4096);
  gemm_k<<<dim3(8,32),256,0,stream>>>(h,wd,t1,2048,512,1792);
  final_k<<<4096,256,0,stream>>>(x,t0,t1,(u16*)d_out,2048*512);
}
```

```cpp
#define K2_FORCE_SAFE 0
#include <hip/hip_runtime.h>
#include <hip/hip_bf16.h>

typedef unsigned short u16;
typedef __attribute__((ext_vector_type(8))) short bf16x8;
typedef __attribute__((ext_vector_type(4))) float f32x4;
typedef __attribute__((ext_vector_type(2))) float f32x2;
typedef __attribute__((ext_vector_type(4))) unsigned u32x4;
typedef __attribute__((ext_vector_type(2))) unsigned u32x2;
typedef __attribute__((ext_vector_type(2))) __bf16 bf16x2v;
#define DI __device__ __forceinline__

DI float bf2f(u16 b) { return __uint_as_float(((unsigned)b) << 16); }
DI float bflo(unsigned w) { return __uint_as_float(w << 16); }
DI float bfhi(unsigned w) { return __uint_as_float(w & 0xffff0000u); }
DI unsigned cvtpk(float lo, float hi) { f32x2 x = {lo, hi}; bf16x2v b = __builtin_convertvector(x, bf16x2v); return __builtin_bit_cast(unsigned, b); }
#define MFMA16(a, b, c) __builtin_amdgcn_mfma_f32_16x16x32_bf16((a), (b), (c), 0, 0, 0)

constexpr int SEQ = 2048, DM = 512, NH = 32, HD = 128, FF = 1792;
constexpr float QSCALE = 0.08838834764831845f * 1.4426950408889634f;
constexpr size_t WS_XF = 0;
constexpr size_t WS_QF = 2u << 20;
constexpr size_t WS_KF = 18u << 20;
constexpr size_t WS_VF = 34u << 20;
constexpr size_t WS_HF = 50u << 20;
constexpr size_t WS_CF = 58u << 20;
constexpr size_t WS_END = 74u << 20;
constexpr size_t WS_EX = 80u << 20;
#define M_ "v_mfma_f32_16x16x32_bf16 "
#define T_ "ds_read_b64_tr_b16 "
#define B_ "buffer_load_dwordx4 "
#define C_ "v_cvt_pk_bf16_f32 "
#define E_ "v_exp_f32 "
#define D_ "ds_read_b128 "
#define U_ "v_mul_f32 "
#define W_ "s_waitcnt "
#define R_ "v_accvgpr_read_b32 "
#define S_ "buffer_store_dwordx4 "
#define A_ "s_add_u32 "
#define K_ "v_cndmask_b32 "
DI void attn_safe_qblock(const u16* __restrict__ xf, const u16* __restrict__ wq, const u16* __restrict__ kf, const u16* __restrict__ vf, u16* __restrict__ cf, int h, int tb, int l) {
  const int t = l & 15, g = l >> 4;
  bf16x8 Q[4];
#pragma unroll
  for (int sp = 0; sp < 4; ++sp) {
    f32x4 q0 = {0.f, 0.f, 0.f, 0.f}, q1 = {0.f, 0.f, 0.f, 0.f};
    for (int s = 0; s < 16; ++s) {
      const bf16x8 T = *(const bf16x8*)(xf + ((size_t)(tb * 16 + s) * 64 + l) * 8);
      bf16x8 w0, w1;
#pragma unroll
      for (int j = 0; j < 8; ++j) { const u16* p = wq + (size_t)(32 * s + 8 * g + j) * 4096 + h * 128 + 32 * sp + t; w0[j] = (short)p[0]; w1[j] = (short)p[16]; }
      q0 = MFMA16(w0, T, q0); q1 = MFMA16(w1, T, q1);
    }
    u32x4 o = {cvtpk(q0[0], q0[1]), cvtpk(q0[2], q0[3]), cvtpk(q1[0], q1[1]), cvtpk(q1[2], q1[3])};
    Q[sp] = __builtin_bit_cast(bf16x8, o);
  }
  f32x4 O[8];
#pragma unroll
  for (int d = 0; d < 8; ++d) O[d] = (f32x4){0.f, 0.f, 0.f, 0.f};
  float m = -__builtin_inff(), lsum = 0.f;
  const int nu = (tb >> 1) + 1;
  for (int u = 0; u < nu; ++u) {
    f32x4 S[2];
#pragma unroll
    for (int kb = 0; kb < 2; ++kb) {
      S[kb] = (f32x4){0.f, 0.f, 0.f, 0.f};
#pragma unroll
      for (int s = 0; s < 4; ++s) {
        const bf16x8 K = *(const bf16x8*)(kf + (((size_t)(h * 128 + 2 * u + kb) * 4 + s) * 64 + l) * 8);
        S[kb] = MFMA16(K, Q[s], S[kb]);
      }
#pragma unroll
      for (int r = 0; r < 4; ++r) if (32 * u + 16 * kb + 4 * g + r > 16 * tb + t) S[kb][r] = -__builtin_inff();
    }
    float mx = fmaxf(fmaxf(fmaxf(S[0][0], S[0][1]), fmaxf(S[0][2], S[0][3])), fmaxf(fmaxf(S[1][0], S[1][1]), fmaxf(S[1][2], S[1][3])));
    mx = fmaxf(mx, __shfl_xor(mx, 16)); mx = fmaxf(mx, __shfl_xor(mx, 32));
    const float mn = fmaxf(m, mx), f = __builtin_amdgcn_exp2f(m - mn);
    m = mn;
    float p[8]; float ps = 0.f;
#pragma unroll
    for (int j = 0; j < 8; ++j) p[j] = __builtin_amdgcn_exp2f(S[j >> 2][j & 3] - mn);
    u32x4 pw = {cvtpk(p[0], p[1]), cvtpk(p[2], p[3]), cvtpk(p[4], p[5]), cvtpk(p[6], p[7])};
#pragma unroll
    for (int j = 0; j < 4; ++j) ps += bflo(pw[j]) + bfhi(pw[j]);
    ps += __shfl_xor(ps, 16); ps += __shfl_xor(ps, 32);
    lsum = lsum * f + ps;
    const bf16x8 P = __builtin_bit_cast(bf16x8, pw);
#pragma unroll
    for (int d = 0; d < 8; ++d) {
      const bf16x8 V = *(const bf16x8*)(vf + (((size_t)(h * 64 + u) * 8 + d) * 64 + l) * 8);
      O[d] = MFMA16(V, P, O[d] * f);
    }
  }
  const float il = 1.0f / lsum;
#pragma unroll
  for (int s = 0; s < 4; ++s) {
    const f32x4 a0 = O[2 * s] * il, a1 = O[2 * s + 1] * il;
    u32x4 o = {cvtpk(a0[0], a0[1]), cvtpk(a0[2], a0[3]), cvtpk(a1[0], a1[1]), cvtpk(a1[2], a1[3])};
    u16* p = cf + (((size_t)(tb * 32 + h) * 4 + s) * 64 + l) * 8;
    asm volatile("global_store_dwordx4 %0, %1, off sc1\n s_nop 1" :: "v"(p), "v"(o) : "memory");
  }
}

struct KArgs { const u16 *x, *ckv, *wq, *wuk, *wuv, *wo, *wg, *wu, *wd; u16* out; char* ws; };
__global__ __launch_bounds__(256, 1) __attribute__((aligned(4096))) void fused(const u16* x, const u16* ckv, const u16* wq, const u16* wuk, const u16* wuv, const u16* wo,
                                                const u16* wg, const u16* wu, const u16* wd, u16* out, char* ws) {
  __shared__ __attribute__((aligned(16))) char lds[163840];
  const unsigned wid0 = __builtin_amdgcn_readfirstlane(threadIdx.x >> 6);
  const unsigned long long ka0 = (unsigned long long)__builtin_amdgcn_kernarg_segment_ptr();
  unsigned long long ka;
  const unsigned b1 = (blockIdx.x + 224) & 255, b0 = b1 ^ ((b1 & 0) << 2);
  unsigned tag, b, wid, bad;
  asm volatile("" :: "s"((unsigned)(size_t)lds));
  asm volatile(
"L_m_org:\n"
"s_load_dwordx16 s[8:23],%5,0x0\n"
"s_load_dwordx4 s[24:27],%5,0x40\n"
"s_load_dwordx2 s[28:29],%5,0x50\n"
"s_mov_b32 s30,%6\n"
"s_mov_b32 s31,%7\n"
"v_mbcnt_lo_u32_b32 v247,-1,0\n"
"v_mbcnt_hi_u32_b32 v247,-1,v247\n"
"v_lshrrev_b32 v224,3,v247\n"
"v_mul_u32_u24 v196,8192,v224\n"
"v_and_b32 v224,2,v224\n"
"v_and_b32 v225,7,v247\n"
"v_xor_b32 v224,v224,v225\n"
"v_lshl_add_u32 v196,v224,4,v196\n"
"v_xor_b32 v224,64,v196\n"
"v_add_u32 v197,64512,v224\n"
"v_add_u32 v198,129024,v196\n"
"v_add_u32 v199,193536,v224\n"
"s_bfe_u32 s85,s30,0x20003\n"
"s_lshr_b32 s89,s30,5\n"
"s_lshr_b32 s87,s85,1\n"
"s_lshl_b32 s88,s30,1\n"
A_"s88,s88,s85\n"
"s_and_b32 s88,s88,15\n"
A_"s90,s89,s30\n"
"s_and_b32 s86,s85,1\n"
"s_mov_b32 s94,s89\n"
"s_lshl_b32 s86,s86,1\n"
"s_lshl_b32 s74,s31,14\n"
"s_lshl_b32 s91,s31,18\n"
"s_lshl_b32 s85,s88,9\n"
A_"s91,s91,s85\n"
A_"s70,s94,0\n"
"s_and_b32 s70,s70,3\n"
"s_lshl_b32 s85,s70,7\n"
A_"s64,s91,s85\n"
A_"s71,s94,1\n"
"s_and_b32 s71,s71,3\n"
"s_lshl_b32 s85,s71,7\n"
A_"s65,s91,s85\n"
A_"s72,s94,2\n"
"s_and_b32 s72,s72,3\n"
"s_lshl_b32 s85,s72,7\n"
A_"s66,s91,s85\n"
A_"s73,s94,3\n"
"s_and_b32 s73,s73,3\n"
"s_lshl_b32 s85,s73,7\n"
A_"s67,s91,s85\n"
"v_lshrrev_b32 v224,4,v247\n"
"v_and_b32 v225,15,v247\n"
"v_xor_b32 v225,v225,v224\n"
"v_lshlrev_b32 v224,10,v224\n"
"v_lshl_add_u32 v208,v225,4,v224\n"
"v_xor_b32 v209,64,v208\n"
"v_add_u32 v209,3072,v209\n"
"v_xor_b32 v210,128,v208\n"
"v_add_u32 v210,6144,v210\n"
"v_xor_b32 v211,192,v208\n"
"v_add_u32 v211,9216,v211\n"
"s_lshl_b32 s91,s89,18\n"
"s_lshl_b32 s85,s31,16\n"
A_"s91,s91,s85\n"
"s_lshl_b32 s85,s86,14\n"
A_"s91,s91,s85\n"
A_"s85,s90,0\n"
"s_and_b32 s85,s85,3\n"
"s_lshl_b32 s85,s85,8\n"
A_"s36,s91,s85\n"
A_"s85,s90,1\n"
"s_and_b32 s85,s85,3\n"
"s_lshl_b32 s85,s85,8\n"
A_"s37,s91,s85\n"
A_"s85,s90,2\n"
"s_and_b32 s85,s85,3\n"
"s_lshl_b32 s85,s85,8\n"
A_"s38,s91,s85\n"
A_"s85,s90,3\n"
"s_and_b32 s85,s85,3\n"
"s_lshl_b32 s85,s85,8\n"
A_"s39,s91,s85\n"
"s_lshl_b32 s56,s31,12\n"
"s_mov_b32 s50,0x400000\n"
"s_mov_b32 s51,0x20000\n"
"s_mov_b32 s42,0x200000\n"
"s_mov_b32 s43,0x20000\n"
"s_cmp_eq_u32 s87,0\n"
W_"lgkmcnt(0)\n"
"s_cselect_b64 s[48:49],s[14:15],s[16:17]\n"
"s_mov_b64 s[40:41],s[10:11]\n"
"s_bfe_u32 s75,s36,0x20008\n"
"s_lshl_b32 s75,s75,20\n"
A_"s75,s75,s64\n"
A_"m0,s56,81920\n"
"s_nop 0\n"
B_"v196,s[48:51],s75 offen offset:0  lds\n"
B_"v197,s[48:51],s75 offen offset:1024  lds\n"
B_"v198,s[48:51],s75 offen offset:2048  lds\n"
B_"v199,s[48:51],s75 offen offset:3072  lds\n"
"s_xor_b32 s75,s36,0\n"
A_"m0,s56,98304\n"
"s_nop 0\n"
B_"v208,s[40:43],s75 offen offset:0  lds\n"
B_"v209,s[40:43],s75 offen offset:1024  lds\n"
B_"v210,s[40:43],s75 offen offset:2048  lds\n"
B_"v211,s[40:43],s75 offen offset:3072  lds\n"
"s_mov_b64 s[52:53],s[28:29]\n"
"s_mov_b32 s54,0x6000000\n"
"s_mov_b32 s55,0x20000\n"
A_"s94,s28,0x4f00000\n"
"s_addc_u32 s95,s29,0\n"
"s_load_dword s32,s[94:95],0x0\n"
"s_getpc_b64 s[44:45]\n"
"L_m_pc1:\n"
A_"s44,s44,L_m_code-L_m_pc1\n"
"s_addc_u32 s45,s45,0\n"
"s_mov_b32 s46,L_m_end-L_m_code\n"
"s_mov_b32 s47,0x20000\n"
A_"s85,s30,96\n"
"s_bfe_u32 s85,s85,0x50003\n"
"s_lshl2_add_u32 s85,s85,s31\n"
"s_lshl_b32 s85,s85,13\n"
"v_lshrrev_b32 v224,0,v247\n"
"v_lshlrev_b32 v224,7,v224\n"
"buffer_load_dword v255,v224,s[44:47],s85 offen\n"
"s_bfe_u32 s75,s36,0x20008\n"
"s_lshl_b32 s75,s75,20\n"
A_"s75,s75,s65\n"
A_"m0,s56,131072\n"
"s_nop 0\n"
B_"v196,s[48:51],s75 offen offset:0  lds\n"
B_"v197,s[48:51],s75 offen offset:1024  lds\n"
B_"v198,s[48:51],s75 offen offset:2048  lds\n"
B_"v199,s[48:51],s75 offen offset:3072  lds\n"
"v_lshlrev_b32 v244,4,v247\n"
"s_mov_b64 s[44:45],s[8:9]\n"
"s_mov_b32 s46,0x200000\n"
"s_mov_b32 s47,0x20000\n"
"s_lshr_b32 s85,s30,5\n"
"s_lshl_b32 s85,s85,4\n"
"s_sub_u32 s79,224,s85\n"
"s_bitcmp1_b32 s30,4\n"
"s_cselect_b32 s79,s79,s85\n"
"s_and_b32 s85,s30,31\n"
A_"s79,s79,s85\n"
"s_lshl2_add_u32 s84,s79,s31\n"
"s_lshl_b32 s84,s84,11\n"
"s_lshl_b32 s85,s86,12\n"
"v_add_u32 v204,s85,v244\n"
"s_lshr_b32 s79,s79,1\n"
"s_lshl_b32 s79,s79,14\n"
"s_and_b32 s85,s30,1\n"
"s_lshl_b32 s85,s85,7\n"
A_"s79,s79,s85\n"
A_"s79,s79,s56\n"
"v_lshrrev_b32 v224,3,v247\n"
"v_and_b32 v225,7,v247\n"
"v_lshlrev_b32 v222,4,v225\n"
"v_lshl_add_u32 v222,v224,8,v222\n"
"v_lshlrev_b32 v220,5,v247\n"
A_"s85,s56,0xc000\n"
"v_add_u32 v220,s85,v220\n"
"v_lshrrev_b32 v225,1,v225\n"
"v_lshlrev_b32 v221,12,v225\n"
"v_lshl_add_u32 v221,v224,5,v221\n"
"v_and_b32 v225,1,v247\n"
"v_lshl_add_u32 v221,v225,4,v221\n"
"s_lshl_b32 s85,s31,9\n"
A_"s85,s85,0xc000\n"
"v_add_u32 v221,s85,v221\n"
"v_lshrrev_b32 v224,4,v247\n"
"v_lshlrev_b32 v192,10,v224\n"
"v_and_b32 v224,1,v224\n"
"v_lshl_add_u32 v192,v224,6,v192\n"
"v_bfe_u32 v224,v247,2,2\n"
"v_lshl_add_u32 v192,v224,7,v192\n"
"v_bfe_u32 v224,v247,3,1\n"
"v_lshl_add_u32 v192,v224,5,v192\n"
"v_and_b32 v224,3,v247\n"
"v_lshl_add_u32 v192,v224,3,v192\n"
"v_xor_b32 v193,32,v192\n"
"v_xor_b32 v194,64,v192\n"
"v_xor_b32 v195,96,v192\n"
"v_xor_b32 v192,0x10000,v192\n"
"v_xor_b32 v193,0x10000,v193\n"
"v_xor_b32 v194,0x10000,v194\n"
"v_xor_b32 v195,0x10000,v195\n"
"v_lshrrev_b32 v224,3,v247\n"
"v_mul_u32_u24 v200,3584,v224\n"
"v_and_b32 v224,2,v224\n"
"v_and_b32 v225,7,v247\n"
"v_xor_b32 v224,v224,v225\n"
"v_lshl_add_u32 v200,v224,4,v200\n"
"v_xor_b32 v224,64,v200\n"
"v_add_u32 v201,27648,v224\n"
"v_add_u32 v202,55296,v200\n"
"v_add_u32 v203,82944,v224\n"
"v_and_b32 v224,15,v247\n"
"v_lshrrev_b32 v225,4,v247\n"
"v_xor_b32 v225,v225,v224\n"
"v_lshlrev_b32 v224,8,v224\n"
"v_lshl_add_u32 v224,v225,4,v224\n"
A_"s85,s56,0x10000\n"
"v_add_u32 v212,s85,v224\n"
"v_xor_b32 v213,64,v212\n"
"v_xor_b32 v214,128,v212\n"
"v_xor_b32 v215,192,v212\n"
"s_mov_b32 s77,0x3e0293ee\n"
"s_and_b32 s33,s30,7\n"
"s_lshl_b32 s33,s33,2\n"
"s_bfe_u32 s34,s30,0x20003\n"
A_"s33,s33,s34\n"
"s_lshr_b32 s34,s30,5\n"
"s_lshl_b32 s85,s34,3\n"
A_"s96,s85,s31\n"
A_"s97,s96,4\n"
"s_sub_u32 s85,120,s85\n"
A_"s98,s85,s31\n"
A_"s99,s98,4\n"
"s_lshl_b32 s96,s96,14\n"
"s_lshl_b32 s97,s97,14\n"
"s_lshl_b32 s98,s98,14\n"
"s_lshl_b32 s99,s99,14\n"
A_"s80,s30,0\n"
"s_and_b32 s80,s80,3\n"
"s_lshl_b32 s80,s80,12\n"
A_"s81,s30,1\n"
"s_and_b32 s81,s81,3\n"
"s_lshl_b32 s81,s81,12\n"
A_"s82,s30,2\n"
"s_and_b32 s82,s82,3\n"
"s_lshl_b32 s82,s82,12\n"
A_"s83,s30,3\n"
"s_and_b32 s83,s83,3\n"
"s_lshl_b32 s83,s83,12\n"
A_"s85,s31,s30\n"
"s_and_b32 s85,s85,3\n"
"s_lshl_b32 s69,s85,20\n"
"s_lshl_b32 s91,s33,8\n"
A_"s69,s69,s91\n"
"s_mul_i32 s68,s31,114688\n"
"s_lshr_b32 s91,s33,3\n"
"s_and_b32 s85,s33,7\n"
"s_sub_u32 s85,s85,s34\n"
"s_cmp_gt_i32 s85,0\n"
"s_subb_u32 s92,s33,s91\n"
"s_cmp_eq_u32 s85,0\n"
"s_cselect_b32 s92,0xfe000,s92\n"
W_"lgkmcnt(0)\n"
"s_lshl_b32 s95,s92,7\n"
A_"s68,s68,s95\n"
"s_lshl_b32 s91,s87,4\n"
A_"s91,s91,s88\n"
"s_lshl_b32 s86,s91,3\n"
"s_lshl_b32 s91,s91,20\n"
"s_lshl_b32 s85,s89,16\n"
A_"s91,s91,s85\n"
A_"s91,s91,s74\n"
A_"s91,s91,0x1200000\n"
A_"s86,s86,s89\n"
"s_lshl_b32 s86,s86,7\n"
A_"s86,s86,0x4f20000\n"
"s_lshl2_add_u32 s85,s34,s31\n"
"s_mul_i32 s88,s85,57344\n"
A_"s88,s88,s95\n"
"s_lshl_b32 s85,s85,9\n"
A_"s90,s85,s33\n"
"s_lshl_b32 s90,s90,8\n"
"s_cmp_eq_u32 s31,0\n"
"s_cselect_b32 s86,s86,0x7f000000\n"
"s_lshl_b32 s78,s30,7\n"
A_"s78,s78,0x4f10010\n"
"s_cmp_eq_u32 s31,0\n"
"s_cselect_b32 s78,s78,0x7f000000\n"
W_"lgkmcnt(0)\n"
"s_mul_i32 s32,s32,1664525\n"
A_"s32,s32,1013904223\n"
"v_mov_b32 v254,s32\n"
"L_m_code:\n"
"s_cmp_eq_u32 s87,1\n"
"s_cbranch_scc1 L_m_V\n"
"v_xor_b32 v205,4096,v204\n"
"v_xor_b32 v206,8192,v204\n"
"v_xor_b32 v207,12288,v204\n"
"s_lshr_b32 s85,s70,1\n"
"s_lshl_b32 s85,s85,19\n"
"s_and_b32 s70,s70,1\n"
"s_lshl_b32 s70,s70,11\n"
A_"s70,s70,s85\n"
A_"s70,s70,s91\n"
"s_lshr_b32 s85,s71,1\n"
"s_lshl_b32 s85,s85,19\n"
"s_and_b32 s71,s71,1\n"
"s_lshl_b32 s71,s71,11\n"
A_"s71,s71,s85\n"
A_"s71,s71,s91\n"
"s_lshr_b32 s85,s72,1\n"
"s_lshl_b32 s85,s85,19\n"
"s_and_b32 s72,s72,1\n"
"s_lshl_b32 s72,s72,11\n"
A_"s72,s72,s85\n"
A_"s72,s72,s91\n"
"s_lshr_b32 s85,s73,1\n"
"s_lshl_b32 s85,s85,19\n"
"s_and_b32 s73,s73,1\n"
"s_lshl_b32 s73,s73,11\n"
A_"s73,s73,s85\n"
A_"s73,s73,s91\n"
"s_xor_b32 s75,s36,16384\n"
A_"m0,s56,114688\n"
"s_nop 0\n"
B_"v208,s[40:43],s75 offen offset:0  lds\n"
B_"v209,s[40:43],s75 offen offset:1024  lds\n"
B_"v210,s[40:43],s75 offen offset:2048  lds\n"
B_"v211,s[40:43],s75 offen offset:3072  lds\n"
W_"vmcnt(13)\n"
"s_barrier\n"
T_"v[0:1],v192 offset:16384\n"
T_"v[2:3],v192 offset:16896\n"
T_"v[4:5],v192 offset:20480\n"
T_"v[6:7],v192 offset:20992\n"
T_"v[8:9],v192 offset:24576\n"
T_"v[10:11],v192 offset:25088\n"
T_"v[12:13],v192 offset:28672\n"
T_"v[14:15],v192 offset:29184\n"
T_"v[16:17],v193 offset:16384\n"
T_"v[18:19],v193 offset:16896\n"
T_"v[20:21],v193 offset:20480\n"
T_"v[22:23],v193 offset:20992\n"
T_"v[24:25],v193 offset:24576\n"
T_"v[26:27],v193 offset:25088\n"
T_"v[28:29],v193 offset:28672\n"
T_"v[30:31],v193 offset:29184\n"
T_"v[32:33],v194 offset:16384\n"
T_"v[34:35],v194 offset:16896\n"
T_"v[36:37],v194 offset:20480\n"
T_"v[38:39],v194 offset:20992\n"
T_"v[40:41],v194 offset:24576\n"
T_"v[42:43],v194 offset:25088\n"
T_"v[44:45],v194 offset:28672\n"
T_"v[46:47],v194 offset:29184\n"
T_"v[48:49],v195 offset:16384\n"
T_"v[50:51],v195 offset:16896\n"
T_"v[52:53],v195 offset:20480\n"
T_"v[54:55],v195 offset:20992\n"
T_"v[56:57],v195 offset:24576\n"
T_"v[58:59],v195 offset:25088\n"
T_"v[60:61],v195 offset:28672\n"
T_"v[62:63],v195 offset:29184\n"
"s_xor_b32 s75,s36,32768\n"
A_"m0,s56,147456\n"
"s_nop 0\n"
B_"v208,s[40:43],s75 offen offset:0  lds\n"
B_"v209,s[40:43],s75 offen offset:1024  lds\n"
B_"v210,s[40:43],s75 offen offset:2048  lds\n"
B_"v211,s[40:43],s75 offen offset:3072  lds\n"
"s_xor_b32 s75,s36,49152\n"
A_"m0,s56,0\n"
"s_nop 0\n"
B_"v208,s[40:43],s75 offen offset:0  lds\n"
B_"v209,s[40:43],s75 offen offset:1024  lds\n"
B_"v210,s[40:43],s75 offen offset:2048  lds\n"
B_"v211,s[40:43],s75 offen offset:3072  lds\n"
B_"v[240:243],v244,s[44:47],s84 offen offset:0\n"
B_"v[248:251],v244,s[44:47],s84 offen offset:1024\n"
W_"vmcnt(19)\n"
D_"a[192:195],v212 offset:32768\n"
D_"a[196:199],v213 offset:32768\n"
D_"a[200:203],v214 offset:32768\n"
D_"a[204:207],v215 offset:32768\n"
W_"lgkmcnt(0)\n"
M_"v[128:131],v[0:3],a[192:195],0\n"
M_"v[128:131],v[4:7],a[196:199],v[128:131]\n"
M_"v[128:131],v[8:11],a[200:203],v[128:131]\n"
M_"v[128:131],v[12:15],a[204:207],v[128:131]\n"
M_"v[144:147],v[16:19],a[192:195],0\n"
W_"vmcnt(10)\n"
D_"a[208:211],v212 offset:49152\n"
M_"v[144:147],v[20:23],a[196:199],v[144:147]\n"
D_"a[212:215],v213 offset:49152\n"
D_"a[216:219],v214 offset:49152\n"
M_"v[144:147],v[24:27],a[200:203],v[144:147]\n"
D_"a[220:223],v215 offset:49152\n"
M_"v[144:147],v[28:31],a[204:207],v[144:147]\n"
M_"v[160:163],v[32:35],a[192:195],0\n"
"s_bfe_u32 s75,s37,0x20008\n"
M_"v[160:163],v[36:39],a[196:199],v[160:163]\n"
"s_lshl_b32 s75,s75,20\n"
M_"v[160:163],v[40:43],a[200:203],v[160:163]\n"
A_"s75,s75,s64\n"
M_"v[160:163],v[44:47],a[204:207],v[160:163]\n"
A_"m0,s56,16384\n"
M_"v[176:179],v[48:51],a[192:195],0\n"
M_"v[176:179],v[52:55],a[196:199],v[176:179]\n"
B_"v196,s[48:51],s75 offen offset:0  lds\n"
M_"v[176:179],v[56:59],a[200:203],v[176:179]\n"
M_"v[176:179],v[60:63],a[204:207],v[176:179]\n"
W_"lgkmcnt(0)\n"
M_"v[132:135],v[0:3],a[208:211],0\n"
M_"v[132:135],v[4:7],a[212:215],v[132:135]\n"
B_"v197,s[48:51],s75 offen offset:1024  lds\n"
M_"v[132:135],v[8:11],a[216:219],v[132:135]\n"
M_"v[132:135],v[12:15],a[220:223],v[132:135]\n"
M_"v[148:151],v[16:19],a[208:211],0\n"
W_"vmcnt(8)\n"
"v_xor_b32 v212,0x30000,v212\n"
"v_xor_b32 v213,0x30000,v213\n"
M_"v[148:151],v[20:23],a[212:215],v[148:151]\n"
B_"v198,s[48:51],s75 offen offset:2048  lds\n"
M_"v[148:151],v[24:27],a[216:219],v[148:151]\n"
"v_xor_b32 v214,0x30000,v214\n"
"v_xor_b32 v215,0x30000,v215\n"
M_"v[148:151],v[28:31],a[220:223],v[148:151]\n"
D_"a[224:227],v212 offset:16384\n"
D_"a[228:231],v213 offset:16384\n"
M_"v[164:167],v[32:35],a[208:211],0\n"
D_"a[232:235],v214 offset:16384\n"
D_"a[236:239],v215 offset:16384\n"
M_"v[164:167],v[36:39],a[212:215],v[164:167]\n"
W_"vmcnt(5)\n"
"v_xor_b32 v212,0x20000,v212\n"
"v_xor_b32 v213,0x20000,v213\n"
M_"v[164:167],v[40:43],a[216:219],v[164:167]\n"
B_"v199,s[48:51],s75 offen offset:3072  lds\n"
M_"v[164:167],v[44:47],a[220:223],v[164:167]\n"
"s_xor_b32 s75,s37,0\n"
"v_xor_b32 v214,0x20000,v214\n"
"v_xor_b32 v215,0x20000,v215\n"
D_"a[240:243],v212 offset:0\n"
D_"a[244:247],v213 offset:0\n"
D_"a[248:251],v214 offset:0\n"
D_"a[252:255],v215 offset:0\n"
M_"v[180:183],v[48:51],a[208:211],0\n"
A_"m0,s56,32768\n"
M_"v[180:183],v[52:55],a[212:215],v[180:183]\n"
M_"v[180:183],v[56:59],a[216:219],v[180:183]\n"
B_"v208,s[40:43],s75 offen offset:0  lds\n"
M_"v[180:183],v[60:63],a[220:223],v[180:183]\n"
W_"lgkmcnt(4)\n"
"s_nop 3\n"
M_"v[136:139],v[0:3],a[224:227],0\n"
M_"v[136:139],v[4:7],a[228:231],v[136:139]\n"
M_"v[136:139],v[8:11],a[232:235],v[136:139]\n"
B_"v209,s[40:43],s75 offen offset:1024  lds\n"
M_"v[136:139],v[12:15],a[236:239],v[136:139]\n"
W_"lgkmcnt(0)\n"
"s_nop 3\n"
M_"v[140:143],v[0:3],a[240:243],0\n"
W_" lgkmcnt(0)\n"
"s_barrier\n"
M_"v[140:143],v[4:7],a[244:247],v[140:143]\n"
M_"v[140:143],v[8:11],a[248:251],v[140:143]\n"
B_"v210,s[40:43],s75 offen offset:2048  lds\n"
M_"v[140:143],v[12:15],a[252:255],v[140:143]\n"
"s_nop 3\n"
M_"v[152:155],v[16:19],a[224:227],0\n"
"v_xor_b32 v192,0x30000,v192\n"
M_"v[152:155],v[20:23],a[228:231],v[152:155]\n"
"v_xor_b32 v193,0x30000,v193\n"
M_"v[152:155],v[24:27],a[232:235],v[152:155]\n"
B_"v211,s[40:43],s75 offen offset:3072  lds\n"
M_"v[152:155],v[28:31],a[236:239],v[152:155]\n"
"s_xor_b32 s75,s37,16384\n"
"v_xor_b32 v194,0x30000,v194\n"
"v_xor_b32 v195,0x30000,v195\n"
"s_nop 3\n"
M_"v[156:159],v[16:19],a[240:243],0\n"
A_"m0,s56,49152\n"
T_"v[0:1],v192 offset:0\n"
W_"vmcnt(8)\n"
M_"v[156:159],v[20:23],a[244:247],v[156:159]\n"
T_"v[2:3],v192 offset:512\n"
"v_lshlrev_b32 v224,16,v240\n"
M_"v[156:159],v[24:27],a[248:251],v[156:159]\n"
B_"v208,s[40:43],s75 offen offset:0  lds\n"
M_"v[156:159],v[28:31],a[252:255],v[156:159]\n"
T_"v[4:5],v192 offset:4096\n"
T_"v[6:7],v192 offset:4608\n"
"s_nop 3\n"
M_"v[168:171],v[32:35],a[224:227],0\n"
T_"v[8:9],v192 offset:8192\n"
"v_lshlrev_b32 v232,16,v248\n"
M_"v[168:171],v[36:39],a[228:231],v[168:171]\n"
T_"v[10:11],v192 offset:8704\n"
"v_and_b32 v225,0xffff0000,v240\n"
M_"v[168:171],v[40:43],a[232:235],v[168:171]\n"
B_"v209,s[40:43],s75 offen offset:1024  lds\n"
M_"v[168:171],v[44:47],a[236:239],v[168:171]\n"
T_"v[12:13],v192 offset:12288\n"
T_"v[14:15],v192 offset:12800\n"
"s_nop 3\n"
M_"v[172:175],v[32:35],a[240:243],0\n"
T_"v[16:17],v193 offset:0\n"
T_"v[18:19],v193 offset:512\n"
M_"v[172:175],v[36:39],a[244:247],v[172:175]\n"
T_"v[20:21],v193 offset:4096\n"
T_"v[22:23],v193 offset:4608\n"
M_"v[172:175],v[40:43],a[248:251],v[172:175]\n"
B_"v210,s[40:43],s75 offen offset:2048  lds\n"
M_"v[172:175],v[44:47],a[252:255],v[172:175]\n"
T_"v[24:25],v193 offset:8192\n"
"v_and_b32 v233,0xffff0000,v248\n"
"s_nop 3\n"
M_"v[184:187],v[48:51],a[224:227],0\n"
T_"v[26:27],v193 offset:8704\n"
"v_lshlrev_b32 v226,16,v241\n"
M_"v[184:187],v[52:55],a[228:231],v[184:187]\n"
"v_lshlrev_b32 v234,16,v249\n"
"v_and_b32 v227,0xffff0000,v241\n"
M_"v[184:187],v[56:59],a[232:235],v[184:187]\n"
B_"v211,s[40:43],s75 offen offset:3072  lds\n"
M_"v[184:187],v[60:63],a[236:239],v[184:187]\n"
"s_bfe_u32 s75,s37,0x20008\n"
T_"v[28:29],v193 offset:12288\n"
"v_and_b32 v235,0xffff0000,v249\n"
"s_nop 3\n"
M_"v[188:191],v[48:51],a[240:243],0\n"
"s_lshl_b32 s75,s75,20\n"
T_"v[30:31],v193 offset:12800\n"
T_"v[32:33],v194 offset:0\n"
M_"v[188:191],v[52:55],a[244:247],v[188:191]\n"
A_"s75,s75,s65\n"
T_"v[34:35],v194 offset:512\n"
T_"v[36:37],v194 offset:4096\n"
M_"v[188:191],v[56:59],a[248:251],v[188:191]\n"
A_"m0,s56,65536\n"
T_"v[38:39],v194 offset:4608\n"
"v_lshlrev_b32 v228,16,v242\n"
M_"v[188:191],v[60:63],a[252:255],v[188:191]\n"
W_"lgkmcnt(12)\n"
T_"v[40:41],v194 offset:8192\n"
"v_lshlrev_b32 v236,16,v250\n"
"s_nop 3\n"
M_"v[64:67],v[0:3],a[192:195],0\n"
B_"v196,s[48:51],s75 offen offset:0  lds\n"
M_"v[64:67],v[4:7],a[196:199],v[64:67]\n"
T_"v[42:43],v194 offset:8704\n"
"v_and_b32 v229,0xffff0000,v242\n"
M_"v[64:67],v[8:11],a[200:203],v[64:67]\n"
T_"v[44:45],v194 offset:12288\n"
"v_and_b32 v237,0xffff0000,v250\n"
M_"v[64:67],v[12:15],a[204:207],v[64:67]\n"
"v_lshlrev_b32 v230,16,v243\n"
"v_lshlrev_b32 v238,16,v251\n"
"s_nop 3\n"
M_"v[68:71],v[0:3],a[208:211],0\n"
B_"v197,s[48:51],s75 offen offset:1024  lds\n"
M_"v[68:71],v[4:7],a[212:215],v[68:71]\n"
T_"v[46:47],v194 offset:12800\n"
T_"v[48:49],v195 offset:0\n"
M_"v[68:71],v[8:11],a[216:219],v[68:71]\n"
T_"v[50:51],v195 offset:512\n"
T_"v[52:53],v195 offset:4096\n"
M_"v[68:71],v[12:15],a[220:223],v[68:71]\n"
T_"v[54:55],v195 offset:4608\n"
T_"v[56:57],v195 offset:8192\n"
"s_nop 3\n"
M_"v[72:75],v[0:3],a[224:227],0\n"
B_"v198,s[48:51],s75 offen offset:2048  lds\n"
M_"v[72:75],v[4:7],a[228:231],v[72:75]\n"
T_"v[58:59],v195 offset:8704\n"
T_"v[60:61],v195 offset:12288\n"
M_"v[72:75],v[8:11],a[232:235],v[72:75]\n"
T_"v[62:63],v195 offset:12800\n"
"v_and_b32 v231,0xffff0000,v243\n"
M_"v[72:75],v[12:15],a[236:239],v[72:75]\n"
"v_and_b32 v239,0xffff0000,v251\n"
"v_add_f32 v240,v224,v225\n"
"s_nop 3\n"
M_"v[76:79],v[0:3],a[240:243],0\n"
B_"v199,s[48:51],s75 offen offset:3072  lds\n"
M_"v[76:79],v[4:7],a[244:247],v[76:79]\n"
"s_xor_b32 s75,s37,32768\n"
"v_add_f32 v248,v232,v233\n"
"v_add_f32 v240,v240,v226\n"
M_"v[76:79],v[8:11],a[248:251],v[76:79]\n"
A_"m0,s56,81920\n"
"v_add_f32 v248,v248,v234\n"
"v_add_f32 v240,v240,v227\n"
M_"v[76:79],v[12:15],a[252:255],v[76:79]\n"
W_"lgkmcnt(15)\n"
"v_add_f32 v248,v248,v235\n"
"v_add_f32 v240,v240,v228\n"
"s_nop 3\n"
M_"v[80:83],v[16:19],a[192:195],0\n"
B_"v208,s[40:43],s75 offen offset:0  lds\n"
M_"v[80:83],v[20:23],a[196:199],v[80:83]\n"
"v_add_f32 v248,v248,v236\n"
M_"v[80:83],v[24:27],a[200:203],v[80:83]\n"
M_"v[80:83],v[28:31],a[204:207],v[80:83]\n"
"v_add_f32 v240,v240,v229\n"
"s_nop 3\n"
M_"v[84:87],v[16:19],a[208:211],0\n"
W_"vmcnt(13) lgkmcnt(0)\n"
"s_barrier\n"
"v_add_f32 v248,v248,v237\n"
M_"v[84:87],v[20:23],a[212:215],v[84:87]\n"
B_"v209,s[40:43],s75 offen offset:1024  lds\n"
M_"v[84:87],v[24:27],a[216:219],v[84:87]\n"
"v_xor_b32 v192,0x20000,v192\n"
"v_xor_b32 v193,0x20000,v193\n"
M_"v[84:87],v[28:31],a[220:223],v[84:87]\n"
"v_xor_b32 v194,0x20000,v194\n"
"v_add_f32 v240,v240,v230\n"
"s_nop 3\n"
M_"v[88:91],v[16:19],a[224:227],0\n"
"v_xor_b32 v195,0x20000,v195\n"
"v_add_f32 v248,v248,v238\n"
M_"v[88:91],v[20:23],a[228:231],v[88:91]\n"
B_"v210,s[40:43],s75 offen offset:2048  lds\n"
M_"v[88:91],v[24:27],a[232:235],v[88:91]\n"
T_"v[0:1],v192 offset:16384\n"
T_"v[2:3],v192 offset:16896\n"
M_"v[88:91],v[28:31],a[236:239],v[88:91]\n"
"v_add_f32 v240,v240,v231\n"
"v_add_f32 v248,v248,v239\n"
"s_nop 3\n"
M_"v[92:95],v[16:19],a[240:243],0\n"
T_"v[4:5],v192 offset:20480\n"
"v_add_f32_dpp v240,v240,v240 row_ror:8 row_mask:0xf bank_mask:0xf\n"
M_"v[92:95],v[20:23],a[244:247],v[92:95]\n"
B_"v211,s[40:43],s75 offen offset:3072  lds\n"
M_"v[92:95],v[24:27],a[248:251],v[92:95]\n"
"s_xor_b32 s75,s37,49152\n"
T_"v[6:7],v192 offset:20992\n"
T_"v[8:9],v192 offset:24576\n"
M_"v[92:95],v[28:31],a[252:255],v[92:95]\n"
T_"v[10:11],v192 offset:25088\n"
"v_add_f32_dpp v248,v248,v248 row_ror:8 row_mask:0xf bank_mask:0xf\n"
"s_nop 3\n"
M_"v[96:99],v[32:35],a[192:195],0\n"
A_"m0,s56,98304\n"
T_"v[12:13],v192 offset:28672\n"
"v_add_f32_dpp v240,v240,v240 row_ror:4 row_mask:0xf bank_mask:0xf\n"
M_"v[96:99],v[36:39],a[196:199],v[96:99]\n"
B_"v208,s[40:43],s75 offen offset:0  lds\n"
M_"v[96:99],v[40:43],a[200:203],v[96:99]\n"
T_"v[14:15],v192 offset:29184\n"
T_"v[16:17],v193 offset:16384\n"
M_"v[96:99],v[44:47],a[204:207],v[96:99]\n"
T_"v[18:19],v193 offset:16896\n"
T_"v[20:21],v193 offset:20480\n"
"s_nop 3\n"
M_"v[100:103],v[32:35],a[208:211],0\n"
"v_add_f32_dpp v248,v248,v248 row_ror:4 row_mask:0xf bank_mask:0xf\n"
"v_add_f32_dpp v240,v240,v240 row_ror:2 row_mask:0xf bank_mask:0xf\n"
M_"v[100:103],v[36:39],a[212:215],v[100:103]\n"
B_"v209,s[40:43],s75 offen offset:1024  lds\n"
M_"v[100:103],v[40:43],a[216:219],v[100:103]\n"
T_"v[22:23],v193 offset:20992\n"
"v_add_f32_dpp v248,v248,v248 row_ror:2 row_mask:0xf bank_mask:0xf\n"
M_"v[100:103],v[44:47],a[220:223],v[100:103]\n"
T_"v[24:25],v193 offset:24576\n"
"v_add_f32_dpp v240,v240,v240 row_ror:1 row_mask:0xf bank_mask:0xf\n"
"s_nop 3\n"
M_"v[104:107],v[32:35],a[224:227],0\n"
T_"v[26:27],v193 offset:25088\n"
"v_add_f32_dpp v248,v248,v248 row_ror:1 row_mask:0xf bank_mask:0xf\n"
M_"v[104:107],v[36:39],a[228:231],v[104:107]\n"
B_"v210,s[40:43],s75 offen offset:2048  lds\n"
M_"v[104:107],v[40:43],a[232:235],v[104:107]\n"
T_"v[28:29],v193 offset:28672\n"
"v_mov_b32 v241,v240\n"
M_"v[104:107],v[44:47],a[236:239],v[104:107]\n"
"v_mov_b32 v249,v248\n"
"s_nop 3\n"
M_"v[108:111],v[32:35],a[240:243],0\n"
T_"v[30:31],v193 offset:29184\n"
"v_permlane16_swap_b32 v240,v241\n"
M_"v[108:111],v[36:39],a[244:247],v[108:111]\n"
B_"v211,s[40:43],s75 offen offset:3072  lds\n"
M_"v[108:111],v[40:43],a[248:251],v[108:111]\n"
"s_bfe_u32 s75,s38,0x20008\n"
"v_permlane16_swap_b32 v248,v249\n"
"s_lshl_b32 s75,s75,20\n"
M_"v[108:111],v[44:47],a[252:255],v[108:111]\n"
"v_add_f32 v240,v240,v241\n"
A_"s75,s75,s64\n"
"s_nop 3\n"
M_"v[112:115],v[48:51],a[192:195],0\n"
A_"m0,s56,114688\n"
T_"v[32:33],v194 offset:16384\n"
"v_add_f32 v248,v248,v249\n"
M_"v[112:115],v[52:55],a[196:199],v[112:115]\n"
B_"v196,s[48:51],s75 offen offset:0  lds\n"
M_"v[112:115],v[56:59],a[200:203],v[112:115]\n"
T_"v[34:35],v194 offset:16896\n"
"v_mov_b32 v241,v240\n"
M_"v[112:115],v[60:63],a[204:207],v[112:115]\n"
T_"v[36:37],v194 offset:20480\n"
"v_mov_b32 v249,v248\n"
"s_nop 3\n"
M_"v[116:119],v[48:51],a[208:211],0\n"
W_"vmcnt(17)\n"
D_"a[64:67],v212 offset:32768\n"
M_"v[116:119],v[52:55],a[212:215],v[116:119]\n"
B_"v197,s[48:51],s75 offen offset:1024  lds\n"
M_"v[116:119],v[56:59],a[216:219],v[116:119]\n"
D_"a[68:71],v213 offset:32768\n"
D_"a[72:75],v214 offset:32768\n"
M_"v[116:119],v[60:63],a[220:223],v[116:119]\n"
D_"a[76:79],v215 offset:32768\n"
T_"v[38:39],v194 offset:20992\n"
"s_nop 3\n"
M_"v[120:123],v[48:51],a[224:227],0\n"
W_"vmcnt(14)\n"
D_"a[80:83],v212 offset:49152\n"
D_"a[84:87],v213 offset:49152\n"
M_"v[120:123],v[52:55],a[228:231],v[120:123]\n"
B_"v198,s[48:51],s75 offen offset:2048  lds\n"
M_"v[120:123],v[56:59],a[232:235],v[120:123]\n"
D_"a[88:91],v214 offset:49152\n"
D_"a[92:95],v215 offset:49152\n"
M_"v[120:123],v[60:63],a[236:239],v[120:123]\n"
T_"v[40:41],v194 offset:24576\n"
T_"v[42:43],v194 offset:25088\n"
"s_nop 3\n"
M_"v[124:127],v[48:51],a[240:243],0\n"
T_"v[44:45],v194 offset:28672\n"
T_"v[46:47],v194 offset:29184\n"
M_"v[124:127],v[52:55],a[244:247],v[124:127]\n"
B_"v199,s[48:51],s75 offen offset:3072  lds\n"
M_"v[124:127],v[56:59],a[248:251],v[124:127]\n"
"s_xor_b32 s75,s38,0\n"
"v_permlane32_swap_b32 v240,v241\n"
"v_permlane32_swap_b32 v248,v249\n"
M_"v[124:127],v[60:63],a[252:255],v[124:127]\n"
W_"lgkmcnt(15)\n"
"v_add_f32 v240,v240,v241\n"
W_"lgkmcnt(9)\n"
"s_nop 3\n"
M_"v[128:131],v[0:3],a[64:67],v[128:131]\n"
A_"m0,s56,131072\n"
T_"v[48:49],v195 offset:16384\n"
"v_add_f32 v248,v248,v249\n"
M_"v[128:131],v[4:7],a[68:71],v[128:131]\n"
B_"v208,s[40:43],s75 offen offset:0  lds\n"
M_"v[128:131],v[8:11],a[72:75],v[128:131]\n"
T_"v[50:51],v195 offset:16896\n"
U_"v240,0xbb000000,v240\n"
M_"v[128:131],v[12:15],a[76:79],v[128:131]\n"
W_"lgkmcnt(6)\n"
T_"v[52:53],v195 offset:20480\n"
U_"v248,0xbb000000,v248\n"
"s_nop 3\n"
M_"v[132:135],v[0:3],a[80:83],v[132:135]\n"
T_"v[54:55],v195 offset:20992\n"
"v_add_f32 v224,v240,v224\n"
M_"v[132:135],v[4:7],a[84:87],v[132:135]\n"
B_"v209,s[40:43],s75 offen offset:1024  lds\n"
M_"v[132:135],v[8:11],a[88:91],v[132:135]\n"
T_"v[56:57],v195 offset:24576\n"
"v_add_f32 v232,v248,v232\n"
M_"v[132:135],v[12:15],a[92:95],v[132:135]\n"
"v_add_f32 v225,v240,v225\n"
"v_add_f32 v233,v248,v233\n"
"s_nop 3\n"
M_"v[144:147],v[16:19],a[64:67],v[144:147]\n"
T_"v[58:59],v195 offset:25088\n"
"v_add_f32 v226,v240,v226\n"
M_"v[144:147],v[20:23],a[68:71],v[144:147]\n"
B_"v210,s[40:43],s75 offen offset:2048  lds\n"
M_"v[144:147],v[24:27],a[72:75],v[144:147]\n"
T_"v[60:61],v195 offset:28672\n"
"v_add_f32 v234,v248,v234\n"
M_"v[144:147],v[28:31],a[76:79],v[144:147]\n"
T_"v[62:63],v195 offset:29184\n"
"v_add_f32 v227,v240,v227\n"
"s_nop 3\n"
M_"v[148:151],v[16:19],a[80:83],v[148:151]\n"
"v_add_f32 v235,v248,v235\n"
"v_add_f32 v228,v240,v228\n"
M_"v[148:151],v[20:23],a[84:87],v[148:151]\n"
B_"v211,s[40:43],s75 offen offset:3072  lds\n"
M_"v[148:151],v[24:27],a[88:91],v[148:151]\n"
"s_xor_b32 s75,s38,16384\n"
"v_add_f32 v236,v248,v236\n"
"v_add_f32 v229,v240,v229\n"
M_"v[148:151],v[28:31],a[92:95],v[148:151]\n"
W_"lgkmcnt(8)\n"
"v_add_f32 v237,v248,v237\n"
A_"m0,s56,147456\n"
"s_nop 3\n"
M_"v[160:163],v[32:35],a[64:67],v[160:163]\n"
M_"v[160:163],v[36:39],a[68:71],v[160:163]\n"
B_"v208,s[40:43],s75 offen offset:0  lds\n"
M_"v[160:163],v[40:43],a[72:75],v[160:163]\n"
"v_add_f32 v230,v240,v230\n"
M_"v[160:163],v[44:47],a[76:79],v[160:163]\n"
"v_add_f32 v238,v248,v238\n"
"s_nop 3\n"
M_"v[164:167],v[32:35],a[80:83],v[164:167]\n"
W_"vmcnt(13)\n"
"v_xor_b32 v212,0x10000,v212\n"
"v_xor_b32 v213,0x10000,v213\n"
M_"v[164:167],v[36:39],a[84:87],v[164:167]\n"
B_"v209,s[40:43],s75 offen offset:1024  lds\n"
M_"v[164:167],v[40:43],a[88:91],v[164:167]\n"
"v_xor_b32 v214,0x10000,v214\n"
"v_xor_b32 v215,0x10000,v215\n"
M_"v[164:167],v[44:47],a[92:95],v[164:167]\n"
W_"lgkmcnt(0)\n"
D_"a[96:99],v212 offset:16384\n"
D_"a[100:103],v213 offset:16384\n"
"s_nop 3\n"
M_"v[176:179],v[48:51],a[64:67],v[176:179]\n"
D_"a[104:107],v214 offset:16384\n"
D_"a[108:111],v215 offset:16384\n"
M_"v[176:179],v[52:55],a[68:71],v[176:179]\n"
W_"vmcnt(10)\n"
D_"a[112:115],v212 offset:32768\n"
D_"a[116:119],v213 offset:32768\n"
M_"v[176:179],v[56:59],a[72:75],v[176:179]\n"
B_"v210,s[40:43],s75 offen offset:2048  lds\n"
M_"v[176:179],v[60:63],a[76:79],v[176:179]\n"
D_"a[120:123],v214 offset:32768\n"
D_"a[124:127],v215 offset:32768\n"
"s_nop 3\n"
M_"v[180:183],v[48:51],a[80:83],v[180:183]\n"
"v_add_f32 v231,v240,v231\n"
"v_add_f32 v239,v248,v239\n"
M_"v[180:183],v[52:55],a[84:87],v[180:183]\n"
U_"v240,v224,v224\n"
U_"v248,v232,v232\n"
M_"v[180:183],v[56:59],a[88:91],v[180:183]\n"
B_"v211,s[40:43],s75 offen offset:3072  lds\n"
M_"v[180:183],v[60:63],a[92:95],v[180:183]\n"
W_"lgkmcnt(4)\n"
"v_fmac_f32 v240,v225,v225\n"
"v_fmac_f32 v248,v233,v233\n"
"s_nop 3\n"
M_"v[136:139],v[0:3],a[96:99],v[136:139]\n"
"s_bfe_u32 s75,s38,0x20008\n"
"v_fmac_f32 v240,v226,v226\n"
"v_fmac_f32 v248,v234,v234\n"
M_"v[136:139],v[4:7],a[100:103],v[136:139]\n"
"s_lshl_b32 s75,s75,20\n"
"v_fmac_f32 v240,v227,v227\n"
A_"s75,s75,s65\n"
M_"v[136:139],v[8:11],a[104:107],v[136:139]\n"
A_"m0,s56,0\n"
"v_fmac_f32 v248,v235,v235\n"
M_"v[136:139],v[12:15],a[108:111],v[136:139]\n"
W_"lgkmcnt(0)\n"
"s_nop 3\n"
M_"v[140:143],v[0:3],a[112:115],v[140:143]\n"
W_" lgkmcnt(0)\n"
"s_barrier\n"
"v_fmac_f32 v240,v228,v228\n"
M_"v[140:143],v[4:7],a[116:119],v[140:143]\n"
B_"v196,s[48:51],s75 offen offset:0  lds\n"
M_"v[140:143],v[8:11],a[120:123],v[140:143]\n"
"v_fmac_f32 v248,v236,v236\n"
M_"v[140:143],v[12:15],a[124:127],v[140:143]\n"
"v_fmac_f32 v240,v229,v229\n"
"s_nop 3\n"
M_"v[152:155],v[16:19],a[96:99],v[152:155]\n"
"v_xor_b32 v192,0x10000,v192\n"
"v_fmac_f32 v248,v237,v237\n"
M_"v[152:155],v[20:23],a[100:103],v[152:155]\n"
B_"v197,s[48:51],s75 offen offset:1024  lds\n"
M_"v[152:155],v[24:27],a[104:107],v[152:155]\n"
"v_xor_b32 v193,0x10000,v193\n"
"v_xor_b32 v194,0x10000,v194\n"
M_"v[152:155],v[28:31],a[108:111],v[152:155]\n"
"v_xor_b32 v195,0x10000,v195\n"
"v_fmac_f32 v240,v230,v230\n"
"s_nop 3\n"
M_"v[156:159],v[16:19],a[112:115],v[156:159]\n"
T_"v[0:1],v192 offset:0\n"
"v_fmac_f32 v248,v238,v238\n"
M_"v[156:159],v[20:23],a[116:119],v[156:159]\n"
B_"v198,s[48:51],s75 offen offset:2048  lds\n"
M_"v[156:159],v[24:27],a[120:123],v[156:159]\n"
T_"v[2:3],v192 offset:512\n"
T_"v[4:5],v192 offset:4096\n"
M_"v[156:159],v[28:31],a[124:127],v[156:159]\n"
T_"v[6:7],v192 offset:4608\n"
"v_fmac_f32 v240,v231,v231\n"
"s_nop 3\n"
M_"v[168:171],v[32:35],a[96:99],v[168:171]\n"
T_"v[8:9],v192 offset:8192\n"
"v_fmac_f32 v248,v239,v239\n"
M_"v[168:171],v[36:39],a[100:103],v[168:171]\n"
B_"v199,s[48:51],s75 offen offset:3072  lds\n"
M_"v[168:171],v[40:43],a[104:107],v[168:171]\n"
"s_xor_b32 s75,s38,32768\n"
T_"v[10:11],v192 offset:8704\n"
T_"v[12:13],v192 offset:12288\n"
M_"v[168:171],v[44:47],a[108:111],v[168:171]\n"
A_"m0,s56,16384\n"
T_"v[14:15],v192 offset:12800\n"
T_"v[16:17],v193 offset:0\n"
"s_nop 3\n"
M_"v[172:175],v[32:35],a[112:115],v[172:175]\n"
T_"v[18:19],v193 offset:512\n"
T_"v[20:21],v193 offset:4096\n"
M_"v[172:175],v[36:39],a[116:119],v[172:175]\n"
B_"v208,s[40:43],s75 offen offset:0  lds\n"
M_"v[172:175],v[40:43],a[120:123],v[172:175]\n"
T_"v[22:23],v193 offset:4608\n"
"v_add_f32_dpp v240,v240,v240 row_ror:8 row_mask:0xf bank_mask:0xf\n"
M_"v[172:175],v[44:47],a[124:127],v[172:175]\n"
T_"v[24:25],v193 offset:8192\n"
"v_add_f32_dpp v248,v248,v248 row_ror:8 row_mask:0xf bank_mask:0xf\n"
"s_nop 3\n"
M_"v[184:187],v[48:51],a[96:99],v[184:187]\n"
T_"v[26:27],v193 offset:8704\n"
"v_add_f32_dpp v240,v240,v240 row_ror:4 row_mask:0xf bank_mask:0xf\n"
M_"v[184:187],v[52:55],a[100:103],v[184:187]\n"
B_"v209,s[40:43],s75 offen offset:1024  lds\n"
M_"v[184:187],v[56:59],a[104:107],v[184:187]\n"
T_"v[28:29],v193 offset:12288\n"
"v_add_f32_dpp v248,v248,v248 row_ror:4 row_mask:0xf bank_mask:0xf\n"
M_"v[184:187],v[60:63],a[108:111],v[184:187]\n"
"v_add_f32_dpp v240,v240,v240 row_ror:2 row_mask:0xf bank_mask:0xf\n"
"v_add_f32_dpp v248,v248,v248 row_ror:2 row_mask:0xf bank_mask:0xf\n"
"s_nop 3\n"
M_"v[188:191],v[48:51],a[112:115],v[188:191]\n"
T_"v[30:31],v193 offset:12800\n"
T_"v[32:33],v194 offset:0\n"
M_"v[188:191],v[52:55],a[116:119],v[188:191]\n"
B_"v210,s[40:43],s75 offen offset:2048  lds\n"
M_"v[188:191],v[56:59],a[120:123],v[188:191]\n"
T_"v[34:35],v194 offset:512\n"
T_"v[36:37],v194 offset:4096\n"
M_"v[188:191],v[60:63],a[124:127],v[188:191]\n"
W_"lgkmcnt(11)\n"
T_"v[38:39],v194 offset:4608\n"
T_"v[40:41],v194 offset:8192\n"
"s_nop 3\n"
M_"v[64:67],v[0:3],a[64:67],v[64:67]\n"
T_"v[42:43],v194 offset:8704\n"
"v_add_f32_dpp v240,v240,v240 row_ror:1 row_mask:0xf bank_mask:0xf\n"
M_"v[64:67],v[4:7],a[68:71],v[64:67]\n"
B_"v211,s[40:43],s75 offen offset:3072  lds\n"
M_"v[64:67],v[8:11],a[72:75],v[64:67]\n"
"s_xor_b32 s75,s38,49152\n"
T_"v[44:45],v194 offset:12288\n"
"v_add_f32_dpp v248,v248,v248 row_ror:1 row_mask:0xf bank_mask:0xf\n"
M_"v[64:67],v[12:15],a[76:79],v[64:67]\n"
A_"m0,s56,32768\n"
"v_mov_b32 v241,v240\n"
"v_mov_b32 v249,v248\n"
"s_nop 3\n"
M_"v[68:71],v[0:3],a[80:83],v[68:71]\n"
T_"v[46:47],v194 offset:12800\n"
T_"v[48:49],v195 offset:0\n"
M_"v[68:71],v[4:7],a[84:87],v[68:71]\n"
B_"v208,s[40:43],s75 offen offset:0  lds\n"
M_"v[68:71],v[8:11],a[88:91],v[68:71]\n"
T_"v[50:51],v195 offset:512\n"
T_"v[52:53],v195 offset:4096\n"
M_"v[68:71],v[12:15],a[92:95],v[68:71]\n"
T_"v[54:55],v195 offset:4608\n"
T_"v[56:57],v195 offset:8192\n"
"s_nop 3\n"
M_"v[72:75],v[0:3],a[96:99],v[72:75]\n"
T_"v[58:59],v195 offset:8704\n"
T_"v[60:61],v195 offset:12288\n"
M_"v[72:75],v[4:7],a[100:103],v[72:75]\n"
B_"v209,s[40:43],s75 offen offset:1024  lds\n"
M_"v[72:75],v[8:11],a[104:107],v[72:75]\n"
T_"v[62:63],v195 offset:12800\n"
"v_permlane16_swap_b32 v240,v241\n"
M_"v[72:75],v[12:15],a[108:111],v[72:75]\n"
"v_permlane16_swap_b32 v248,v249\n"
"v_add_f32 v240,v240,v241\n"
"s_nop 3\n"
M_"v[76:79],v[0:3],a[112:115],v[76:79]\n"
"v_add_f32 v248,v248,v249\n"
"v_mov_b32 v241,v240\n"
M_"v[76:79],v[4:7],a[116:119],v[76:79]\n"
B_"v210,s[40:43],s75 offen offset:2048  lds\n"
M_"v[76:79],v[8:11],a[120:123],v[76:79]\n"
"v_mov_b32 v249,v248\n"
"v_permlane32_swap_b32 v240,v241\n"
M_"v[76:79],v[12:15],a[124:127],v[76:79]\n"
W_"lgkmcnt(15)\n"
"v_permlane32_swap_b32 v248,v249\n"
"v_add_f32 v240,v240,v241\n"
"s_nop 3\n"
M_"v[80:83],v[16:19],a[64:67],v[80:83]\n"
"v_add_f32 v248,v248,v249\n"
U_"v240,0x3b000000,v240\n"
M_"v[80:83],v[20:23],a[68:71],v[80:83]\n"
B_"v211,s[40:43],s75 offen offset:3072  lds\n"
M_"v[80:83],v[24:27],a[72:75],v[80:83]\n"
U_"v248,0x3b000000,v248\n"
M_"v[80:83],v[28:31],a[76:79],v[80:83]\n"
"v_add_f32 v240,0x3727c5ac,v240\n"
"s_nop 3\n"
M_"v[84:87],v[16:19],a[80:83],v[84:87]\n"
W_"vmcnt(20) lgkmcnt(0)\n"
"s_barrier\n"
"v_add_f32 v248,0x3727c5ac,v248\n"
M_"v[84:87],v[20:23],a[84:87],v[84:87]\n"
"s_bfe_u32 s75,s39,0x20008\n"
T_"v[0:1],v192 offset:49152\n"
M_"v[84:87],v[24:27],a[88:91],v[84:87]\n"
"s_lshl_b32 s75,s75,20\n"
"v_rsq_f32 v240,v240\n"
M_"v[84:87],v[28:31],a[92:95],v[84:87]\n"
A_"s75,s75,s64\n"
T_"v[2:3],v192 offset:49664\n"
"s_nop 3\n"
M_"v[88:91],v[16:19],a[96:99],v[88:91]\n"
A_"m0,s56,65536\n"
T_"v[4:5],v192 offset:53248\n"
M_"v[88:91],v[20:23],a[100:103],v[88:91]\n"
"v_rsq_f32 v248,v248\n"
M_"v[88:91],v[24:27],a[104:107],v[88:91]\n"
B_"v196,s[48:51],s75 offen offset:0  lds\n"
M_"v[88:91],v[28:31],a[108:111],v[88:91]\n"
T_"v[6:7],v192 offset:53760\n"
"s_nop 3\n"
M_"v[92:95],v[16:19],a[112:115],v[92:95]\n"
T_"v[8:9],v192 offset:57344\n"
U_"v224,v240,v224\n"
M_"v[92:95],v[20:23],a[116:119],v[92:95]\n"
T_"v[10:11],v192 offset:57856\n"
M_"v[92:95],v[24:27],a[120:123],v[92:95]\n"
B_"v197,s[48:51],s75 offen offset:1024  lds\n"
M_"v[92:95],v[28:31],a[124:127],v[92:95]\n"
T_"v[12:13],v192 offset:61440\n"
U_"v232,v248,v232\n"
"s_nop 3\n"
M_"v[96:99],v[32:35],a[64:67],v[96:99]\n"
U_"v225,v240,v225\n"
M_"v[96:99],v[36:39],a[68:71],v[96:99]\n"
T_"v[14:15],v192 offset:61952\n"
T_"v[16:17],v193 offset:49152\n"
M_"v[96:99],v[40:43],a[72:75],v[96:99]\n"
B_"v198,s[48:51],s75 offen offset:2048  lds\n"
M_"v[96:99],v[44:47],a[76:79],v[96:99]\n"
T_"v[18:19],v193 offset:49664\n"
T_"v[20:21],v193 offset:53248\n"
"s_nop 3\n"
M_"v[100:103],v[32:35],a[80:83],v[100:103]\n"
U_"v233,v248,v233\n"
U_"v226,v240,v226\n"
M_"v[100:103],v[36:39],a[84:87],v[100:103]\n"
T_"v[22:23],v193 offset:53760\n"
U_"v234,v248,v234\n"
M_"v[100:103],v[40:43],a[88:91],v[100:103]\n"
B_"v199,s[48:51],s75 offen offset:3072  lds\n"
M_"v[100:103],v[44:47],a[92:95],v[100:103]\n"
T_"v[24:25],v193 offset:57344\n"
U_"v227,v240,v227\n"
"s_nop 3\n"
M_"v[104:107],v[32:35],a[96:99],v[104:107]\n"
T_"v[26:27],v193 offset:57856\n"
U_"v235,v248,v235\n"
M_"v[104:107],v[36:39],a[100:103],v[104:107]\n"
U_"v228,v240,v228\n"
M_"v[104:107],v[40:43],a[104:107],v[104:107]\n"
T_"v[28:29],v193 offset:61440\n"
U_"v236,v248,v236\n"
M_"v[104:107],v[44:47],a[108:111],v[104:107]\n"
"s_nop 3\n"
M_"v[108:111],v[32:35],a[112:115],v[108:111]\n"
T_"v[30:31],v193 offset:61952\n"
U_"v229,v240,v229\n"
M_"v[108:111],v[36:39],a[116:119],v[108:111]\n"
U_"v237,v248,v237\n"
M_"v[108:111],v[40:43],a[120:123],v[108:111]\n"
M_"v[108:111],v[44:47],a[124:127],v[108:111]\n"
U_"v230,v240,v230\n"
"s_nop 3\n"
M_"v[112:115],v[48:51],a[64:67],v[112:115]\n"
T_"v[32:33],v194 offset:49152\n"
U_"v238,v248,v238\n"
M_"v[112:115],v[52:55],a[68:71],v[112:115]\n"
T_"v[34:35],v194 offset:49664\n"
M_"v[112:115],v[56:59],a[72:75],v[112:115]\n"
U_"v231,v240,v231\n"
M_"v[112:115],v[60:63],a[76:79],v[112:115]\n"
T_"v[36:37],v194 offset:53248\n"
U_"v239,v248,v239\n"
"s_nop 3\n"
M_"v[116:119],v[48:51],a[80:83],v[116:119]\n"
W_"vmcnt(20)\n"
"v_xor_b32 v212,0x30000,v212\n"
"v_xor_b32 v213,0x30000,v213\n"
M_"v[116:119],v[52:55],a[84:87],v[116:119]\n"
"v_xor_b32 v214,0x30000,v214\n"
"v_xor_b32 v215,0x30000,v215\n"
M_"v[116:119],v[56:59],a[88:91],v[116:119]\n"
D_"a[0:3],v212 offset:0\n"
D_"a[4:7],v213 offset:0\n"
M_"v[116:119],v[60:63],a[92:95],v[116:119]\n"
D_"a[8:11],v214 offset:0\n"
D_"a[12:15],v215 offset:0\n"
"s_nop 3\n"
M_"v[120:123],v[48:51],a[96:99],v[120:123]\n"
W_"vmcnt(16)\n"
T_"v[38:39],v194 offset:53760\n"
D_"a[16:19],v212 offset:16384\n"
M_"v[120:123],v[52:55],a[100:103],v[120:123]\n"
D_"a[20:23],v213 offset:16384\n"
D_"a[24:27],v214 offset:16384\n"
M_"v[120:123],v[56:59],a[104:107],v[120:123]\n"
D_"a[28:31],v215 offset:16384\n"
T_"v[40:41],v194 offset:57344\n"
M_"v[120:123],v[60:63],a[108:111],v[120:123]\n"
T_"v[42:43],v194 offset:57856\n"
T_"v[44:45],v194 offset:61440\n"
"s_nop 3\n"
M_"v[124:127],v[48:51],a[112:115],v[124:127]\n"
"s_xor_b32 s75,s39,0\n"
T_"v[46:47],v194 offset:61952\n"
C_"v240,v224,v225\n"
M_"v[124:127],v[52:55],a[116:119],v[124:127]\n"
A_"m0,s56,81920\n"
C_"v248,v232,v233\n"
C_"v241,v226,v227\n"
M_"v[124:127],v[56:59],a[120:123],v[124:127]\n"
C_"v249,v234,v235\n"
C_"v242,v228,v229\n"
M_"v[124:127],v[60:63],a[124:127],v[124:127]\n"
W_"lgkmcnt(15)\n"
C_"v250,v236,v237\n"
W_"lgkmcnt(9)\n"
"s_nop 3\n"
M_"v[128:131],v[0:3],a[0:3],v[128:131]\n"
B_"v208,s[40:43],s75 offen offset:0  lds\n"
M_"v[128:131],v[4:7],a[4:7],v[128:131]\n"
T_"v[48:49],v195 offset:49152\n"
T_"v[50:51],v195 offset:49664\n"
M_"v[128:131],v[8:11],a[8:11],v[128:131]\n"
C_"v243,v230,v231\n"
C_"v251,v238,v239\n"
M_"v[128:131],v[12:15],a[12:15],v[128:131]\n"
W_"lgkmcnt(6)\n"
T_"v[52:53],v195 offset:53248\n"
"s_nop 3\n"
M_"v[132:135],v[0:3],a[16:19],v[132:135]\n"
B_"v209,s[40:43],s75 offen offset:1024  lds\n"
M_"v[132:135],v[4:7],a[20:23],v[132:135]\n"
T_"v[54:55],v195 offset:53760\n"
M_"v[132:135],v[8:11],a[24:27],v[132:135]\n"
T_"v[56:57],v195 offset:57344\n"
M_"v[132:135],v[12:15],a[28:31],v[132:135]\n"
"s_nop 3\n"
M_"v[144:147],v[16:19],a[0:3],v[144:147]\n"
B_"v210,s[40:43],s75 offen offset:2048  lds\n"
M_"v[144:147],v[20:23],a[4:7],v[144:147]\n"
T_"v[58:59],v195 offset:57856\n"
T_"v[60:61],v195 offset:61440\n"
M_"v[144:147],v[24:27],a[8:11],v[144:147]\n"
M_"v[144:147],v[28:31],a[12:15],v[144:147]\n"
T_"v[62:63],v195 offset:61952\n"
"s_nop 3\n"
M_"v[148:151],v[16:19],a[16:19],v[148:151]\n"
B_"v211,s[40:43],s75 offen offset:3072  lds\n"
M_"v[148:151],v[20:23],a[20:23],v[148:151]\n"
"s_xor_b32 s75,s39,16384\n"
A_"m0,s56,98304\n"
M_"v[148:151],v[24:27],a[24:27],v[148:151]\n"
M_"v[148:151],v[28:31],a[28:31],v[148:151]\n"
W_"lgkmcnt(8)\n"
"s_nop 3\n"
M_"v[160:163],v[32:35],a[0:3],v[160:163]\n"
B_"v208,s[40:43],s75 offen offset:0  lds\n"
M_"v[160:163],v[36:39],a[4:7],v[160:163]\n"
M_"v[160:163],v[40:43],a[8:11],v[160:163]\n"
M_"v[160:163],v[44:47],a[12:15],v[160:163]\n"
"s_nop 3\n"
M_"v[164:167],v[32:35],a[16:19],v[164:167]\n"
W_"vmcnt(13)\n"
"v_xor_b32 v212,0x20000,v212\n"
"v_xor_b32 v213,0x20000,v213\n"
M_"v[164:167],v[36:39],a[20:23],v[164:167]\n"
B_"v209,s[40:43],s75 offen offset:1024  lds\n"
M_"v[164:167],v[40:43],a[24:27],v[164:167]\n"
"v_xor_b32 v214,0x20000,v214\n"
"v_xor_b32 v215,0x20000,v215\n"
M_"v[164:167],v[44:47],a[28:31],v[164:167]\n"
W_"lgkmcnt(0)\n"
D_"a[32:35],v212 offset:16384\n"
D_"a[36:39],v213 offset:16384\n"
"s_nop 3\n"
M_"v[176:179],v[48:51],a[0:3],v[176:179]\n"
D_"a[40:43],v214 offset:16384\n"
D_"a[44:47],v215 offset:16384\n"
M_"v[176:179],v[52:55],a[4:7],v[176:179]\n"
W_"vmcnt(10)\n"
D_"a[48:51],v212 offset:32768\n"
D_"a[52:55],v213 offset:32768\n"
M_"v[176:179],v[56:59],a[8:11],v[176:179]\n"
B_"v210,s[40:43],s75 offen offset:2048  lds\n"
M_"v[176:179],v[60:63],a[12:15],v[176:179]\n"
D_"a[56:59],v214 offset:32768\n"
D_"a[60:63],v215 offset:32768\n"
"ds_write_b128 v220,v[240:243] offset:0\n"
"ds_write_b128 v220,v[248:251] offset:16\n"
"s_nop 3\n"
M_"v[180:183],v[48:51],a[16:19],v[180:183]\n"
M_"v[180:183],v[52:55],a[20:23],v[180:183]\n"
M_"v[180:183],v[56:59],a[24:27],v[180:183]\n"
B_"v211,s[40:43],s75 offen offset:3072  lds\n"
M_"v[180:183],v[60:63],a[28:31],v[180:183]\n"
W_"lgkmcnt(6)\n"
"s_nop 3\n"
M_"v[136:139],v[0:3],a[32:35],v[136:139]\n"
M_"v[136:139],v[4:7],a[36:39],v[136:139]\n"
M_"v[136:139],v[8:11],a[40:43],v[136:139]\n"
M_"v[136:139],v[12:15],a[44:47],v[136:139]\n"
W_"lgkmcnt(2)\n"
"s_nop 3\n"
M_"v[140:143],v[0:3],a[48:51],v[140:143]\n"
W_" lgkmcnt(0)\n"
"s_barrier\n"
M_"v[140:143],v[4:7],a[52:55],v[140:143]\n"
"s_bfe_u32 s75,s39,0x20008\n"
D_"v[240:243],v221 offset:0\n"
D_"v[248:251],v221 offset:256\n"
M_"v[140:143],v[8:11],a[56:59],v[140:143]\n"
"s_lshl_b32 s75,s75,20\n"
M_"v[140:143],v[12:15],a[60:63],v[140:143]\n"
A_"s75,s75,s65\n"
"s_nop 3\n"
M_"v[152:155],v[16:19],a[32:35],v[152:155]\n"
A_"m0,s56,114688\n"
"v_xor_b32 v192,0x10000,v192\n"
M_"v[152:155],v[20:23],a[36:39],v[152:155]\n"
"v_xor_b32 v193,0x10000,v193\n"
M_"v[152:155],v[24:27],a[40:43],v[152:155]\n"
B_"v196,s[48:51],s75 offen offset:0  lds\n"
M_"v[152:155],v[28:31],a[44:47],v[152:155]\n"
"v_xor_b32 v194,0x10000,v194\n"
"v_xor_b32 v195,0x10000,v195\n"
M_"v[156:159],v[16:19],a[48:51],v[156:159]\n"
T_"v[0:1],v192 offset:0\n"
M_"v[156:159],v[20:23],a[52:55],v[156:159]\n"
T_"v[2:3],v192 offset:512\n"
M_"v[156:159],v[24:27],a[56:59],v[156:159]\n"
B_"v197,s[48:51],s75 offen offset:1024  lds\n"
M_"v[156:159],v[28:31],a[60:63],v[156:159]\n"
T_"v[4:5],v192 offset:4096\n"
T_"v[6:7],v192 offset:4608\n"
M_"v[168:171],v[32:35],a[32:35],v[168:171]\n"
T_"v[8:9],v192 offset:8192\n"
M_"v[168:171],v[36:39],a[36:39],v[168:171]\n"
W_"lgkmcnt(5)\n"
T_"v[10:11],v192 offset:8704\n"
M_"v[168:171],v[40:43],a[40:43],v[168:171]\n"
S_"v[240:243],v222,s[52:55],s79 offen offset:0 sc1\n"
M_"v[168:171],v[44:47],a[44:47],v[168:171]\n"
T_"v[12:13],v192 offset:12288\n"
T_"v[14:15],v192 offset:12800\n"
M_"v[172:175],v[32:35],a[48:51],v[172:175]\n"
T_"v[16:17],v193 offset:0\n"
T_"v[18:19],v193 offset:512\n"
M_"v[172:175],v[36:39],a[52:55],v[172:175]\n"
S_"v[248:251],v222,s[52:55],s79 offen offset:2048 sc1\n"
M_"v[172:175],v[40:43],a[56:59],v[172:175]\n"
T_"v[20:21],v193 offset:4096\n"
T_"v[22:23],v193 offset:4608\n"
M_"v[172:175],v[44:47],a[60:63],v[172:175]\n"
T_"v[24:25],v193 offset:8192\n"
M_"v[184:187],v[48:51],a[32:35],v[184:187]\n"
T_"v[26:27],v193 offset:8704\n"
M_"v[184:187],v[52:55],a[36:39],v[184:187]\n"
B_"v198,s[48:51],s75 offen offset:2048  lds\n"
M_"v[184:187],v[56:59],a[40:43],v[184:187]\n"
T_"v[28:29],v193 offset:12288\n"
M_"v[184:187],v[60:63],a[44:47],v[184:187]\n"
B_"v199,s[48:51],s75 offen offset:3072  lds\n"
M_"v[188:191],v[48:51],a[48:51],v[188:191]\n"
"s_xor_b32 s75,s39,32768\n"
T_"v[30:31],v193 offset:12800\n"
T_"v[32:33],v194 offset:0\n"
M_"v[188:191],v[52:55],a[52:55],v[188:191]\n"
A_"m0,s56,131072\n"
T_"v[34:35],v194 offset:512\n"
T_"v[36:37],v194 offset:4096\n"
M_"v[188:191],v[56:59],a[56:59],v[188:191]\n"
T_"v[38:39],v194 offset:4608\n"
M_"v[188:191],v[60:63],a[60:63],v[188:191]\n"
W_"lgkmcnt(12)\n"
T_"v[40:41],v194 offset:8192\n"
M_"v[64:67],v[0:3],a[0:3],v[64:67]\n"
B_"v208,s[40:43],s75 offen offset:0  lds\n"
M_"v[64:67],v[4:7],a[4:7],v[64:67]\n"
T_"v[42:43],v194 offset:8704\n"
M_"v[64:67],v[8:11],a[8:11],v[64:67]\n"
T_"v[44:45],v194 offset:12288\n"
M_"v[64:67],v[12:15],a[12:15],v[64:67]\n"
M_"v[68:71],v[0:3],a[16:19],v[68:71]\n"
B_"v209,s[40:43],s75 offen offset:1024  lds\n"
M_"v[68:71],v[4:7],a[20:23],v[68:71]\n"
T_"v[46:47],v194 offset:12800\n"
T_"v[48:49],v195 offset:0\n"
M_"v[68:71],v[8:11],a[24:27],v[68:71]\n"
T_"v[50:51],v195 offset:512\n"
T_"v[52:53],v195 offset:4096\n"
M_"v[68:71],v[12:15],a[28:31],v[68:71]\n"
T_"v[54:55],v195 offset:4608\n"
T_"v[56:57],v195 offset:8192\n"
M_"v[72:75],v[0:3],a[32:35],v[72:75]\n"
B_"v210,s[40:43],s75 offen offset:2048  lds\n"
M_"v[72:75],v[4:7],a[36:39],v[72:75]\n"
T_"v[58:59],v195 offset:8704\n"
T_"v[60:61],v195 offset:12288\n"
M_"v[72:75],v[8:11],a[40:43],v[72:75]\n"
T_"v[62:63],v195 offset:12800\n"
M_"v[72:75],v[12:15],a[44:47],v[72:75]\n"
B_"v211,s[40:43],s75 offen offset:3072  lds\n"
M_"v[76:79],v[0:3],a[48:51],v[76:79]\n"
"s_xor_b32 s75,s39,49152\n"
A_"m0,s56,147456\n"
M_"v[76:79],v[4:7],a[52:55],v[76:79]\n"
M_"v[76:79],v[8:11],a[56:59],v[76:79]\n"
M_"v[76:79],v[12:15],a[60:63],v[76:79]\n"
W_"lgkmcnt(15)\n"
M_"v[80:83],v[16:19],a[0:3],v[80:83]\n"
B_"v208,s[40:43],s75 offen offset:0  lds\n"
M_"v[80:83],v[20:23],a[4:7],v[80:83]\n"
M_"v[80:83],v[24:27],a[8:11],v[80:83]\n"
M_"v[80:83],v[28:31],a[12:15],v[80:83]\n"
M_"v[84:87],v[16:19],a[16:19],v[84:87]\n"
W_"vmcnt(19) lgkmcnt(0)\n"
"s_barrier\n"
M_"v[84:87],v[20:23],a[20:23],v[84:87]\n"
B_"v209,s[40:43],s75 offen offset:1024  lds\n"
M_"v[84:87],v[24:27],a[24:27],v[84:87]\n"
"v_xor_b32 v192,0x10000,v192\n"
"v_xor_b32 v193,0x10000,v193\n"
M_"v[84:87],v[28:31],a[28:31],v[84:87]\n"
"v_xor_b32 v194,0x10000,v194\n"
M_"v[88:91],v[16:19],a[32:35],v[88:91]\n"
B_"v210,s[40:43],s75 offen offset:2048  lds\n"
M_"v[88:91],v[20:23],a[36:39],v[88:91]\n"
"v_xor_b32 v195,0x10000,v195\n"
T_"v[0:1],v192 offset:0\n"
M_"v[88:91],v[24:27],a[40:43],v[88:91]\n"
T_"v[2:3],v192 offset:512\n"
M_"v[88:91],v[28:31],a[44:47],v[88:91]\n"
B_"v211,s[40:43],s75 offen offset:3072  lds\n"
M_"v[92:95],v[16:19],a[48:51],v[92:95]\n"
"s_bfe_u32 s75,s36,0x20008\n"
T_"v[4:5],v192 offset:4096\n"
"s_lshl_b32 s75,s75,20\n"
M_"v[92:95],v[20:23],a[52:55],v[92:95]\n"
A_"s75,s75,s66\n"
T_"v[6:7],v192 offset:4608\n"
A_"m0,s56,0\n"
M_"v[92:95],v[24:27],a[56:59],v[92:95]\n"
T_"v[8:9],v192 offset:8192\n"
M_"v[92:95],v[28:31],a[60:63],v[92:95]\n"
T_"v[10:11],v192 offset:8704\n"
M_"v[96:99],v[32:35],a[0:3],v[96:99]\n"
B_"v196,s[48:51],s75 offen offset:0  lds\n"
M_"v[96:99],v[36:39],a[4:7],v[96:99]\n"
T_"v[12:13],v192 offset:12288\n"
T_"v[14:15],v192 offset:12800\n"
M_"v[96:99],v[40:43],a[8:11],v[96:99]\n"
T_"v[16:17],v193 offset:0\n"
T_"v[18:19],v193 offset:512\n"
M_"v[96:99],v[44:47],a[12:15],v[96:99]\n"
T_"v[20:21],v193 offset:4096\n"
M_"v[100:103],v[32:35],a[16:19],v[100:103]\n"
B_"v197,s[48:51],s75 offen offset:1024  lds\n"
M_"v[100:103],v[36:39],a[20:23],v[100:103]\n"
T_"v[22:23],v193 offset:4608\n"
M_"v[100:103],v[40:43],a[24:27],v[100:103]\n"
M_"v[100:103],v[44:47],a[28:31],v[100:103]\n"
T_"v[24:25],v193 offset:8192\n"
M_"v[104:107],v[32:35],a[32:35],v[104:107]\n"
B_"v198,s[48:51],s75 offen offset:2048  lds\n"
M_"v[104:107],v[36:39],a[36:39],v[104:107]\n"
T_"v[26:27],v193 offset:8704\n"
M_"v[104:107],v[40:43],a[40:43],v[104:107]\n"
T_"v[28:29],v193 offset:12288\n"
M_"v[104:107],v[44:47],a[44:47],v[104:107]\n"
B_"v199,s[48:51],s75 offen offset:3072  lds\n"
M_"v[108:111],v[32:35],a[48:51],v[108:111]\n"
"s_bfe_u32 s75,s37,0x20008\n"
T_"v[30:31],v193 offset:12800\n"
"s_lshl_b32 s75,s75,20\n"
M_"v[108:111],v[36:39],a[52:55],v[108:111]\n"
A_"s75,s75,s66\n"
A_"m0,s56,16384\n"
M_"v[108:111],v[40:43],a[56:59],v[108:111]\n"
M_"v[108:111],v[44:47],a[60:63],v[108:111]\n"
M_"v[112:115],v[48:51],a[0:3],v[112:115]\n"
B_"v196,s[48:51],s75 offen offset:0  lds\n"
M_"v[112:115],v[52:55],a[4:7],v[112:115]\n"
T_"v[32:33],v194 offset:0\n"
T_"v[34:35],v194 offset:512\n"
M_"v[112:115],v[56:59],a[8:11],v[112:115]\n"
M_"v[112:115],v[60:63],a[12:15],v[112:115]\n"
T_"v[36:37],v194 offset:4096\n"
M_"v[116:119],v[48:51],a[16:19],v[116:119]\n"
B_"v197,s[48:51],s75 offen offset:1024  lds\n"
M_"v[116:119],v[52:55],a[20:23],v[116:119]\n"
W_"vmcnt(24)\n"
"v_xor_b32 v212,0x10000,v212\n"
"v_xor_b32 v213,0x10000,v213\n"
M_"v[116:119],v[56:59],a[24:27],v[116:119]\n"
"v_xor_b32 v214,0x10000,v214\n"
"v_xor_b32 v215,0x10000,v215\n"
M_"v[116:119],v[60:63],a[28:31],v[116:119]\n"
D_"a[128:131],v212 offset:16384\n"
D_"a[132:135],v213 offset:16384\n"
M_"v[120:123],v[48:51],a[32:35],v[120:123]\n"
B_"v198,s[48:51],s75 offen offset:2048  lds\n"
M_"v[120:123],v[52:55],a[36:39],v[120:123]\n"
D_"a[136:139],v214 offset:16384\n"
D_"a[140:143],v215 offset:16384\n"
M_"v[120:123],v[56:59],a[40:43],v[120:123]\n"
"s_getpc_b64 s[44:45]\n"
"L_crK:\n"
"s_sub_u32 s44,s44,L_crK-L_m_org\n"
"s_subb_u32 s45,s45,0\n"
M_"v[120:123],v[60:63],a[44:47],v[120:123]\n"
B_"v199,s[48:51],s75 offen offset:3072  lds\n"
T_"v[38:39],v194 offset:4608\n"
W_"vmcnt(22)\n"
D_"a[144:147],v212 offset:32768\n"
D_"a[148:151],v213 offset:32768\n"
D_"a[152:155],v214 offset:32768\n"
D_"a[156:159],v215 offset:32768\n"
M_"v[124:127],v[48:51],a[48:51],v[124:127]\n"
"s_bfe_u32 s75,s38,0x20008\n"
T_"v[40:41],v194 offset:8192\n"
T_"v[42:43],v194 offset:8704\n"
M_"v[124:127],v[52:55],a[52:55],v[124:127]\n"
"s_lshl_b32 s75,s75,20\n"
T_"v[44:45],v194 offset:12288\n"
T_"v[46:47],v194 offset:12800\n"
M_"v[124:127],v[56:59],a[56:59],v[124:127]\n"
A_"s75,s75,s66\n"
A_"m0,s56,32768\n"
"s_mov_b32 s46,L_m_end-L_m_org\n"
M_"v[124:127],v[60:63],a[60:63],v[124:127]\n"
W_"lgkmcnt(15)\n"
"s_lshr_b32 s57,s30,3\n"
W_"lgkmcnt(9)\n"
M_"v[128:131],v[0:3],a[128:131],v[128:131]\n"
B_"v196,s[48:51],s75 offen offset:0  lds\n"
M_"v[128:131],v[4:7],a[132:135],v[128:131]\n"
"s_lshl2_add_u32 s57,s57,s31\n"
T_"v[48:49],v195 offset:0\n"
T_"v[50:51],v195 offset:512\n"
M_"v[128:131],v[8:11],a[136:139],v[128:131]\n"
M_"v[128:131],v[12:15],a[140:143],v[128:131]\n"
W_"lgkmcnt(6)\n"
T_"v[52:53],v195 offset:4096\n"
"s_lshl_b32 s57,s57,11\n"
M_"v[132:135],v[0:3],a[144:147],v[132:135]\n"
B_"v197,s[48:51],s75 offen offset:1024  lds\n"
M_"v[132:135],v[4:7],a[148:151],v[132:135]\n"
T_"v[54:55],v195 offset:4608\n"
"v_lshlrev_b32 v253,1,v244\n"
M_"v[132:135],v[8:11],a[152:155],v[132:135]\n"
T_"v[56:57],v195 offset:8192\n"
M_"v[132:135],v[12:15],a[156:159],v[132:135]\n"
"v_and_b32 v253,0x780,v253\n"
M_"v[144:147],v[16:19],a[128:131],v[144:147]\n"
B_"v198,s[48:51],s75 offen offset:2048  lds\n"
M_"v[144:147],v[20:23],a[132:135],v[144:147]\n"
"v_add_u32 v243,s57,v253\n"
T_"v[58:59],v195 offset:8704\n"
M_"v[144:147],v[24:27],a[136:139],v[144:147]\n"
T_"v[60:61],v195 offset:12288\n"
M_"v[144:147],v[28:31],a[140:143],v[144:147]\n"
B_"v199,s[48:51],s75 offen offset:3072  lds\n"
M_"v[148:151],v[16:19],a[144:147],v[148:151]\n"
"s_bfe_u32 s75,s39,0x20008\n"
T_"v[62:63],v195 offset:12800\n"
"s_lshl_b32 s75,s75,20\n"
M_"v[148:151],v[20:23],a[148:151],v[148:151]\n"
A_"s75,s75,s66\n"
A_"m0,s56,49152\n"
M_"v[148:151],v[24:27],a[152:155],v[148:151]\n"
U_"v128,s77,v128\n"
M_"v[148:151],v[28:31],a[156:159],v[148:151]\n"
W_"lgkmcnt(8)\n"
U_"v129,s77,v129\n"
M_"v[160:163],v[32:35],a[128:131],v[160:163]\n"
B_"v196,s[48:51],s75 offen offset:0  lds\n"
M_"v[160:163],v[36:39],a[132:135],v[160:163]\n"
"v_lshrrev_b32 v228,6,v244\n"
U_"v130,s77,v130\n"
M_"v[160:163],v[40:43],a[136:139],v[160:163]\n"
U_"v131,s77,v131\n"
M_"v[160:163],v[44:47],a[140:143],v[160:163]\n"
M_"v[164:167],v[32:35],a[144:147],v[164:167]\n"
B_"v197,s[48:51],s75 offen offset:1024  lds\n"
M_"v[164:167],v[36:39],a[148:151],v[164:167]\n"
W_"vmcnt(18)\n"
"v_xor_b32 v212,0x30000,v212\n"
"v_xor_b32 v213,0x30000,v213\n"
M_"v[164:167],v[40:43],a[152:155],v[164:167]\n"
"v_xor_b32 v214,0x30000,v214\n"
"v_xor_b32 v215,0x30000,v215\n"
M_"v[164:167],v[44:47],a[156:159],v[164:167]\n"
W_"lgkmcnt(0)\n"
D_"a[160:163],v212 offset:0\n"
D_"a[164:167],v213 offset:0\n"
M_"v[176:179],v[48:51],a[128:131],v[176:179]\n"
B_"v198,s[48:51],s75 offen offset:2048  lds\n"
M_"v[176:179],v[52:55],a[132:135],v[176:179]\n"
D_"a[168:171],v214 offset:0\n"
D_"a[172:175],v215 offset:0\n"
M_"v[176:179],v[56:59],a[136:139],v[176:179]\n"
W_"vmcnt(15)\n"
D_"a[176:179],v212 offset:16384\n"
D_"a[180:183],v213 offset:16384\n"
M_"v[176:179],v[60:63],a[140:143],v[176:179]\n"
B_"v199,s[48:51],s75 offen offset:3072  lds\n"
D_"a[184:187],v214 offset:16384\n"
D_"a[188:191],v215 offset:16384\n"
M_"v[180:183],v[48:51],a[144:147],v[180:183]\n"
U_"v144,s77,v144\n"
U_"v145,s77,v145\n"
M_"v[180:183],v[52:55],a[148:151],v[180:183]\n"
U_"v146,s77,v146\n"
U_"v147,s77,v147\n"
M_"v[180:183],v[56:59],a[152:155],v[180:183]\n"
C_"v128,v128,v129\n"
C_"v129,v130,v131\n"
M_"v[180:183],v[60:63],a[156:159],v[180:183]\n"
W_"lgkmcnt(4)\n"
C_"v130,v144,v145\n"
C_"v131,v146,v147\n"
M_"v[136:139],v[0:3],a[160:163],v[136:139]\n"
S_"v[128:131],v204,s[52:55],s70 offen offset:0 sc1\n"
M_"v[136:139],v[4:7],a[164:167],v[136:139]\n"
M_"v[136:139],v[8:11],a[168:171],v[136:139]\n"
M_"v[136:139],v[12:15],a[172:175],v[136:139]\n"
W_"lgkmcnt(0)\n"
M_"v[140:143],v[0:3],a[176:179],v[140:143]\n"
W_" lgkmcnt(0)\n"
"s_barrier\n"
M_"v[140:143],v[4:7],a[180:183],v[140:143]\n"
"s_bfe_u32 s75,s36,0x20008\n"
M_"v[140:143],v[8:11],a[184:187],v[140:143]\n"
"s_lshl_b32 s75,s75,20\n"
U_"v132,s77,v132\n"
M_"v[140:143],v[12:15],a[188:191],v[140:143]\n"
A_"s75,s75,s67\n"
U_"v133,s77,v133\n"
M_"v[152:155],v[16:19],a[160:163],v[152:155]\n"
A_"m0,s56,65536\n"
T_"v[0:1],v192 offset:49152\n"
M_"v[152:155],v[20:23],a[164:167],v[152:155]\n"
T_"v[2:3],v192 offset:49664\n"
U_"v134,s77,v134\n"
M_"v[152:155],v[24:27],a[168:171],v[152:155]\n"
B_"v196,s[48:51],s75 offen offset:0  lds\n"
M_"v[152:155],v[28:31],a[172:175],v[152:155]\n"
T_"v[4:5],v192 offset:53248\n"
U_"v135,s77,v135\n"
M_"v[156:159],v[16:19],a[176:179],v[156:159]\n"
T_"v[6:7],v192 offset:53760\n"
U_"v148,s77,v148\n"
M_"v[156:159],v[20:23],a[180:183],v[156:159]\n"
U_"v149,s77,v149\n"
M_"v[156:159],v[24:27],a[184:187],v[156:159]\n"
B_"v197,s[48:51],s75 offen offset:1024  lds\n"
M_"v[156:159],v[28:31],a[188:191],v[156:159]\n"
T_"v[8:9],v192 offset:57344\n"
U_"v150,s77,v150\n"
M_"v[168:171],v[32:35],a[160:163],v[168:171]\n"
T_"v[10:11],v192 offset:57856\n"
U_"v151,s77,v151\n"
M_"v[168:171],v[36:39],a[164:167],v[168:171]\n"
T_"v[12:13],v192 offset:61440\n"
M_"v[168:171],v[40:43],a[168:171],v[168:171]\n"
B_"v198,s[48:51],s75 offen offset:2048  lds\n"
M_"v[168:171],v[44:47],a[172:175],v[168:171]\n"
T_"v[14:15],v192 offset:61952\n"
T_"v[16:17],v193 offset:49152\n"
M_"v[172:175],v[32:35],a[176:179],v[172:175]\n"
T_"v[18:19],v193 offset:49664\n"
T_"v[20:21],v193 offset:53248\n"
M_"v[172:175],v[36:39],a[180:183],v[172:175]\n"
C_"v132,v132,v133\n"
C_"v133,v134,v135\n"
M_"v[172:175],v[40:43],a[184:187],v[172:175]\n"
B_"v199,s[48:51],s75 offen offset:3072  lds\n"
M_"v[172:175],v[44:47],a[188:191],v[172:175]\n"
"s_bfe_u32 s75,s37,0x20008\n"
T_"v[22:23],v193 offset:53760\n"
C_"v134,v148,v149\n"
M_"v[184:187],v[48:51],a[160:163],v[184:187]\n"
"s_lshl_b32 s75,s75,20\n"
T_"v[24:25],v193 offset:57344\n"
C_"v135,v150,v151\n"
M_"v[184:187],v[52:55],a[164:167],v[184:187]\n"
A_"s75,s75,s67\n"
T_"v[26:27],v193 offset:57856\n"
A_"m0,s56,81920\n"
M_"v[184:187],v[56:59],a[168:171],v[184:187]\n"
S_"v[132:135],v205,s[52:55],s70 offen offset:0 sc1\n"
M_"v[184:187],v[60:63],a[172:175],v[184:187]\n"
T_"v[28:29],v193 offset:61440\n"
M_"v[188:191],v[48:51],a[176:179],v[188:191]\n"
T_"v[30:31],v193 offset:61952\n"
T_"v[32:33],v194 offset:49152\n"
M_"v[188:191],v[52:55],a[180:183],v[188:191]\n"
T_"v[34:35],v194 offset:49664\n"
T_"v[36:37],v194 offset:53248\n"
M_"v[188:191],v[56:59],a[184:187],v[188:191]\n"
B_"v196,s[48:51],s75 offen offset:0  lds\n"
M_"v[188:191],v[60:63],a[188:191],v[188:191]\n"
W_"lgkmcnt(11)\n"
T_"v[38:39],v194 offset:53760\n"
T_"v[40:41],v194 offset:57344\n"
M_"v[64:67],v[0:3],a[128:131],v[64:67]\n"
U_"v136,s77,v136\n"
T_"v[42:43],v194 offset:57856\n"
M_"v[64:67],v[4:7],a[132:135],v[64:67]\n"
U_"v137,s77,v137\n"
U_"v138,s77,v138\n"
M_"v[64:67],v[8:11],a[136:139],v[64:67]\n"
B_"v197,s[48:51],s75 offen offset:1024  lds\n"
M_"v[64:67],v[12:15],a[140:143],v[64:67]\n"
T_"v[44:45],v194 offset:61440\n"
U_"v139,s77,v139\n"
M_"v[68:71],v[0:3],a[144:147],v[68:71]\n"
T_"v[46:47],v194 offset:61952\n"
T_"v[48:49],v195 offset:49152\n"
M_"v[68:71],v[4:7],a[148:151],v[68:71]\n"
T_"v[50:51],v195 offset:49664\n"
T_"v[52:53],v195 offset:53248\n"
M_"v[68:71],v[8:11],a[152:155],v[68:71]\n"
B_"v198,s[48:51],s75 offen offset:2048  lds\n"
M_"v[68:71],v[12:15],a[156:159],v[68:71]\n"
T_"v[54:55],v195 offset:53760\n"
T_"v[56:57],v195 offset:57344\n"
M_"v[72:75],v[0:3],a[160:163],v[72:75]\n"
T_"v[58:59],v195 offset:57856\n"
T_"v[60:61],v195 offset:61440\n"
M_"v[72:75],v[4:7],a[164:167],v[72:75]\n"
T_"v[62:63],v195 offset:61952\n"
U_"v152,s77,v152\n"
M_"v[72:75],v[8:11],a[168:171],v[72:75]\n"
B_"v199,s[48:51],s75 offen offset:3072  lds\n"
M_"v[72:75],v[12:15],a[172:175],v[72:75]\n"
"s_bfe_u32 s75,s38,0x20008\n"
U_"v153,s77,v153\n"
U_"v154,s77,v154\n"
M_"v[76:79],v[0:3],a[176:179],v[76:79]\n"
"s_lshl_b32 s75,s75,20\n"
U_"v155,s77,v155\n"
C_"v136,v136,v137\n"
M_"v[76:79],v[4:7],a[180:183],v[76:79]\n"
"s_memtime s[60:61]\n"
"s_memrealtime s[62:63]\n"
C_"v137,v138,v139\n"
M_"v[76:79],v[8:11],a[184:187],v[76:79]\n"
A_"s75,s75,s67\n"
C_"v138,v152,v153\n"
C_"v139,v154,v155\n"
M_"v[76:79],v[12:15],a[188:191],v[76:79]\n"
W_"lgkmcnt(15)\n"
A_"m0,s56,98304\n"
M_"v[80:83],v[16:19],a[128:131],v[80:83]\n"
B_"v196,s[48:51],s75 offen offset:0  lds\n"
M_"v[80:83],v[20:23],a[132:135],v[80:83]\n"
M_"v[80:83],v[24:27],a[136:139],v[80:83]\n"
M_"v[80:83],v[28:31],a[140:143],v[80:83]\n"
M_"v[84:87],v[16:19],a[144:147],v[84:87]\n"
W_"vmcnt(11) lgkmcnt(0)\n"
"s_barrier\n"
M_"v[84:87],v[20:23],a[148:151],v[84:87]\n"
B_"v197,s[48:51],s75 offen offset:1024  lds\n"
M_"v[84:87],v[24:27],a[152:155],v[84:87]\n"
"v_xor_b32 v192,0x10000,v192\n"
"v_xor_b32 v193,0x10000,v193\n"
M_"v[84:87],v[28:31],a[156:159],v[84:87]\n"
"v_xor_b32 v194,0x10000,v194\n"
M_"v[88:91],v[16:19],a[160:163],v[88:91]\n"
S_"v[136:139],v206,s[52:55],s70 offen offset:0 sc1\n"
M_"v[88:91],v[20:23],a[164:167],v[88:91]\n"
B_"v198,s[48:51],s75 offen offset:2048  lds\n"
M_"v[88:91],v[24:27],a[168:171],v[88:91]\n"
U_"v140,s77,v140\n"
"v_xor_b32 v195,0x10000,v195\n"
M_"v[88:91],v[28:31],a[172:175],v[88:91]\n"
B_"v199,s[48:51],s75 offen offset:3072  lds\n"
"buffer_store_dword v254,off,s[52:55],s78 sc1\n"
M_"v[92:95],v[16:19],a[176:179],v[92:95]\n"
"s_bfe_u32 s75,s39,0x20008\n"
U_"v141,s77,v141\n"
T_"v[0:1],v192 offset:0\n"
M_"v[92:95],v[20:23],a[180:183],v[92:95]\n"
"s_lshl_b32 s75,s75,20\n"
T_"v[2:3],v192 offset:512\n"
U_"v142,s77,v142\n"
M_"v[92:95],v[24:27],a[184:187],v[92:95]\n"
A_"s75,s75,s67\n"
U_"v143,s77,v143\n"
T_"v[4:5],v192 offset:4096\n"
M_"v[92:95],v[28:31],a[188:191],v[92:95]\n"
T_"v[6:7],v192 offset:4608\n"
U_"v156,s77,v156\n"
M_"v[96:99],v[32:35],a[128:131],v[96:99]\n"
A_"m0,s56,114688\n"
T_"v[8:9],v192 offset:8192\n"
U_"v157,s77,v157\n"
M_"v[96:99],v[36:39],a[132:135],v[96:99]\n"
B_"v196,s[48:51],s75 offen offset:0  lds\n"
M_"v[96:99],v[40:43],a[136:139],v[96:99]\n"
T_"v[10:11],v192 offset:8704\n"
T_"v[12:13],v192 offset:12288\n"
M_"v[96:99],v[44:47],a[140:143],v[96:99]\n"
U_"v158,s77,v158\n"
T_"v[14:15],v192 offset:12800\n"
M_"v[100:103],v[32:35],a[144:147],v[100:103]\n"
T_"v[16:17],v192 offset:16384\n"
T_"v[18:19],v192 offset:16896\n"
M_"v[100:103],v[36:39],a[148:151],v[100:103]\n"
B_"v197,s[48:51],s75 offen offset:1024  lds\n"
M_"v[100:103],v[40:43],a[152:155],v[100:103]\n"
U_"v159,s77,v159\n"
T_"v[20:21],v192 offset:20480\n"
M_"v[100:103],v[44:47],a[156:159],v[100:103]\n"
C_"v140,v140,v141\n"
C_"v141,v142,v143\n"
M_"v[104:107],v[32:35],a[160:163],v[104:107]\n"
T_"v[22:23],v192 offset:20992\n"
C_"v142,v156,v157\n"
M_"v[104:107],v[36:39],a[164:167],v[104:107]\n"
B_"v198,s[48:51],s75 offen offset:2048  lds\n"
M_"v[104:107],v[40:43],a[168:171],v[104:107]\n"
T_"v[24:25],v192 offset:24576\n"
C_"v143,v158,v159\n"
M_"v[104:107],v[44:47],a[172:175],v[104:107]\n"
B_"v199,s[48:51],s75 offen offset:3072  lds\n"
M_"v[108:111],v[32:35],a[176:179],v[108:111]\n"
T_"v[26:27],v192 offset:25088\n"
T_"v[28:29],v192 offset:28672\n"
M_"v[108:111],v[36:39],a[180:183],v[108:111]\n"
T_"v[30:31],v192 offset:29184\n"
M_"v[108:111],v[40:43],a[184:187],v[108:111]\n"
M_"v[108:111],v[44:47],a[188:191],v[108:111]\n"
M_"v[112:115],v[48:51],a[128:131],v[112:115]\n"
S_"v[140:143],v207,s[52:55],s70 offen offset:0 sc1\n"
M_"v[112:115],v[52:55],a[132:135],v[112:115]\n"
U_"v160,s77,v160\n"
T_"v[32:33],v192 offset:32768\n"
M_"v[112:115],v[56:59],a[136:139],v[112:115]\n"
U_"v161,s77,v161\n"
T_"v[34:35],v192 offset:33280\n"
M_"v[112:115],v[60:63],a[140:143],v[112:115]\n"
U_"v162,s77,v162\n"
T_"v[36:37],v192 offset:36864\n"
M_"v[116:119],v[48:51],a[144:147],v[116:119]\n"
U_"v163,s77,v163\n"
U_"v176,s77,v176\n"
M_"v[116:119],v[52:55],a[148:151],v[116:119]\n"
T_"v[38:39],v192 offset:37376\n"
U_"v177,s77,v177\n"
M_"v[116:119],v[56:59],a[152:155],v[116:119]\n"
M_"v[116:119],v[60:63],a[156:159],v[116:119]\n"
T_"v[40:41],v192 offset:40960\n"
U_"v178,s77,v178\n"
M_"v[120:123],v[48:51],a[160:163],v[120:123]\n"
T_"v[42:43],v192 offset:41472\n"
U_"v179,s77,v179\n"
M_"v[120:123],v[52:55],a[164:167],v[120:123]\n"
M_"v[120:123],v[56:59],a[168:171],v[120:123]\n"
T_"v[44:45],v192 offset:45056\n"
C_"v160,v160,v161\n"
M_"v[120:123],v[60:63],a[172:175],v[120:123]\n"
C_"v161,v162,v163\n"
M_"v[124:127],v[48:51],a[176:179],v[124:127]\n"
T_"v[46:47],v192 offset:45568\n"
M_"v[124:127],v[52:55],a[180:183],v[124:127]\n"
C_"v162,v176,v177\n"
M_"v[124:127],v[56:59],a[184:187],v[124:127]\n"
C_"v163,v178,v179\n"
M_"v[124:127],v[60:63],a[188:191],v[124:127]\n"
W_"lgkmcnt(15)\n"
M_"v[128:131],v[0:3],a[192:195],0\n"
S_"v[160:163],v204,s[52:55],s70 offen offset:1024 sc1\n"
M_"v[128:131],v[4:7],a[196:199],v[128:131]\n"
T_"v[48:49],v192 offset:49152\n"
T_"v[50:51],v192 offset:49664\n"
M_"v[128:131],v[8:11],a[200:203],v[128:131]\n"
M_"v[128:131],v[12:15],a[204:207],v[128:131]\n"
T_"v[52:53],v192 offset:53248\n"
M_"v[132:135],v[0:3],a[208:211],0\n"
M_"v[132:135],v[4:7],a[212:215],v[132:135]\n"
T_"v[54:55],v192 offset:53760\n"
M_"v[132:135],v[8:11],a[216:219],v[132:135]\n"
U_"v164,s77,v164\n"
M_"v[132:135],v[12:15],a[220:223],v[132:135]\n"
T_"v[56:57],v192 offset:57344\n"
U_"v165,s77,v165\n"
M_"v[136:139],v[0:3],a[224:227],0\n"
"s_mov_b64 s[40:41],s[20:21]\n"
T_"v[58:59],v192 offset:57856\n"
M_"v[136:139],v[4:7],a[228:231],v[136:139]\n"
U_"v166,s77,v166\n"
M_"v[136:139],v[8:11],a[232:235],v[136:139]\n"
"s_mov_b32 s42,0x1c0000\n"
T_"v[60:61],v192 offset:61440\n"
U_"v167,s77,v167\n"
M_"v[136:139],v[12:15],a[236:239],v[136:139]\n"
M_"v[140:143],v[0:3],a[240:243],0\n"
"v_mul_u32_u24 v229,3584,v228\n"
T_"v[62:63],v192 offset:61952\n"
M_"v[140:143],v[4:7],a[244:247],v[140:143]\n"
U_"v180,s77,v180\n"
U_"v181,s77,v181\n"
M_"v[140:143],v[8:11],a[248:251],v[140:143]\n"
"buffer_load_dword v229,v229,s[40:43],s88 offen\n"
M_"v[140:143],v[12:15],a[252:255],v[140:143]\n"
W_"lgkmcnt(15)\n"
U_"v182,s77,v182\n"
M_"v[128:131],v[16:19],a[64:67],v[128:131]\n"
T_"v[0:1],v193 offset:0\n"
U_"v183,s77,v183\n"
M_"v[128:131],v[20:23],a[68:71],v[128:131]\n"
T_"v[2:3],v193 offset:512\n"
M_"v[128:131],v[24:27],a[72:75],v[128:131]\n"
C_"v164,v164,v165\n"
M_"v[128:131],v[28:31],a[76:79],v[128:131]\n"
T_"v[4:5],v193 offset:4096\n"
C_"v165,v166,v167\n"
M_"v[132:135],v[16:19],a[80:83],v[132:135]\n"
M_"v[132:135],v[20:23],a[84:87],v[132:135]\n"
T_"v[6:7],v193 offset:4608\n"
C_"v166,v180,v181\n"
M_"v[132:135],v[24:27],a[88:91],v[132:135]\n"
C_"v167,v182,v183\n"
M_"v[132:135],v[28:31],a[92:95],v[132:135]\n"
T_"v[8:9],v193 offset:8192\n"
M_"v[136:139],v[16:19],a[96:99],v[136:139]\n"
S_"v[164:167],v205,s[52:55],s70 offen offset:1024 sc1\n"
M_"v[136:139],v[20:23],a[100:103],v[136:139]\n"
T_"v[10:11],v193 offset:8704\n"
M_"v[136:139],v[24:27],a[104:107],v[136:139]\n"
T_"v[12:13],v193 offset:12288\n"
M_"v[136:139],v[28:31],a[108:111],v[136:139]\n"
M_"v[140:143],v[16:19],a[112:115],v[140:143]\n"
T_"v[14:15],v193 offset:12800\n"
M_"v[140:143],v[20:23],a[116:119],v[140:143]\n"
M_"v[140:143],v[24:27],a[120:123],v[140:143]\n"
U_"v168,s77,v168\n"
M_"v[140:143],v[28:31],a[124:127],v[140:143]\n"
W_"lgkmcnt(15)\n"
U_"v169,s77,v169\n"
M_"v[128:131],v[32:35],a[0:3],v[128:131]\n"
T_"v[16:17],v193 offset:16384\n"
M_"v[128:131],v[36:39],a[4:7],v[128:131]\n"
T_"v[18:19],v193 offset:16896\n"
U_"v170,s77,v170\n"
M_"v[128:131],v[40:43],a[8:11],v[128:131]\n"
U_"v171,s77,v171\n"
M_"v[128:131],v[44:47],a[12:15],v[128:131]\n"
T_"v[20:21],v193 offset:20480\n"
M_"v[132:135],v[32:35],a[16:19],v[132:135]\n"
U_"v184,s77,v184\n"
M_"v[132:135],v[36:39],a[20:23],v[132:135]\n"
T_"v[22:23],v193 offset:20992\n"
U_"v185,s77,v185\n"
M_"v[132:135],v[40:43],a[24:27],v[132:135]\n"
M_"v[132:135],v[44:47],a[28:31],v[132:135]\n"
T_"v[24:25],v193 offset:24576\n"
U_"v186,s77,v186\n"
M_"v[136:139],v[32:35],a[32:35],v[136:139]\n"
T_"v[26:27],v193 offset:25088\n"
U_"v187,s77,v187\n"
M_"v[136:139],v[36:39],a[36:39],v[136:139]\n"
M_"v[136:139],v[40:43],a[40:43],v[136:139]\n"
T_"v[28:29],v193 offset:28672\n"
C_"v168,v168,v169\n"
M_"v[136:139],v[44:47],a[44:47],v[136:139]\n"
C_"v169,v170,v171\n"
M_"v[140:143],v[32:35],a[48:51],v[140:143]\n"
"s_mov_b32 s94,0x7f000000\n"
T_"v[30:31],v193 offset:29184\n"
M_"v[140:143],v[36:39],a[52:55],v[140:143]\n"
C_"v170,v184,v185\n"
M_"v[140:143],v[40:43],a[56:59],v[140:143]\n"
C_"v171,v186,v187\n"
M_"v[140:143],v[44:47],a[60:63],v[140:143]\n"
W_"lgkmcnt(15)\n"
M_"v[128:131],v[48:51],a[128:131],v[128:131]\n"
A_"s78,s78,4\n"
T_"v[32:33],v193 offset:32768\n"
M_"v[128:131],v[52:55],a[132:135],v[128:131]\n"
S_"v[168:171],v206,s[52:55],s70 offen offset:1024 sc1\n"
M_"v[128:131],v[56:59],a[136:139],v[128:131]\n"
T_"v[34:35],v193 offset:33280\n"
M_"v[128:131],v[60:63],a[140:143],v[128:131]\n"
T_"v[36:37],v193 offset:36864\n"
M_"v[132:135],v[48:51],a[144:147],v[132:135]\n"
M_"v[132:135],v[52:55],a[148:151],v[132:135]\n"
T_"v[38:39],v193 offset:37376\n"
M_"v[132:135],v[56:59],a[152:155],v[132:135]\n"
U_"v172,s77,v172\n"
M_"v[132:135],v[60:63],a[156:159],v[132:135]\n"
T_"v[40:41],v193 offset:40960\n"
U_"v173,s77,v173\n"
M_"v[136:139],v[48:51],a[160:163],v[136:139]\n"
T_"v[42:43],v193 offset:41472\n"
M_"v[136:139],v[52:55],a[164:167],v[136:139]\n"
U_"v174,s77,v174\n"
M_"v[136:139],v[56:59],a[168:171],v[136:139]\n"
"s_mov_b64 s[40:41],s[22:23]\n"
T_"v[44:45],v193 offset:45056\n"
U_"v175,s77,v175\n"
M_"v[136:139],v[60:63],a[172:175],v[136:139]\n"
M_"v[140:143],v[48:51],a[176:179],v[140:143]\n"
"s_mov_b32 s42,0x1c0000\n"
T_"v[46:47],v193 offset:45568\n"
U_"v188,s77,v188\n"
M_"v[140:143],v[52:55],a[180:183],v[140:143]\n"
U_"v189,s77,v189\n"
M_"v[140:143],v[56:59],a[184:187],v[140:143]\n"
"v_mul_u32_u24 v230,3584,v228\n"
M_"v[140:143],v[60:63],a[188:191],v[140:143]\n"
W_"lgkmcnt(15)\n"
U_"v190,s77,v190\n"
M_"v[144:147],v[0:3],a[192:195],0\n"
"buffer_load_dword v230,v230,s[40:43],s88 offen\n"
M_"v[144:147],v[4:7],a[196:199],v[144:147]\n"
T_"v[48:49],v193 offset:49152\n"
T_"v[50:51],v193 offset:49664\n"
M_"v[144:147],v[8:11],a[200:203],v[144:147]\n"
U_"v191,s77,v191\n"
C_"v172,v172,v173\n"
M_"v[144:147],v[12:15],a[204:207],v[144:147]\n"
T_"v[52:53],v193 offset:53248\n"
C_"v173,v174,v175\n"
M_"v[148:151],v[0:3],a[208:211],0\n"
M_"v[148:151],v[4:7],a[212:215],v[148:151]\n"
T_"v[54:55],v193 offset:53760\n"
C_"v174,v188,v189\n"
M_"v[148:151],v[8:11],a[216:219],v[148:151]\n"
C_"v175,v190,v191\n"
M_"v[148:151],v[12:15],a[220:223],v[148:151]\n"
T_"v[56:57],v193 offset:57344\n"
M_"v[152:155],v[0:3],a[224:227],0\n"
S_"v[172:175],v207,s[52:55],s70 offen offset:1024 sc1\n"
M_"v[152:155],v[4:7],a[228:231],v[152:155]\n"
T_"v[58:59],v193 offset:57856\n"
M_"v[152:155],v[8:11],a[232:235],v[152:155]\n"
T_"v[60:61],v193 offset:61440\n"
M_"v[152:155],v[12:15],a[236:239],v[152:155]\n"
M_"v[156:159],v[0:3],a[240:243],0\n"
T_"v[62:63],v193 offset:61952\n"
M_"v[156:159],v[4:7],a[244:247],v[156:159]\n"
M_"v[156:159],v[8:11],a[248:251],v[156:159]\n"
U_"v64,s77,v64\n"
M_"v[156:159],v[12:15],a[252:255],v[156:159]\n"
W_"lgkmcnt(15)\n"
U_"v65,s77,v65\n"
M_"v[144:147],v[16:19],a[64:67],v[144:147]\n"
T_"v[0:1],v194 offset:0\n"
M_"v[144:147],v[20:23],a[68:71],v[144:147]\n"
T_"v[2:3],v194 offset:512\n"
U_"v66,s77,v66\n"
M_"v[144:147],v[24:27],a[72:75],v[144:147]\n"
U_"v67,s77,v67\n"
M_"v[144:147],v[28:31],a[76:79],v[144:147]\n"
T_"v[4:5],v194 offset:4096\n"
M_"v[148:151],v[16:19],a[80:83],v[148:151]\n"
U_"v80,s77,v80\n"
M_"v[148:151],v[20:23],a[84:87],v[148:151]\n"
T_"v[6:7],v194 offset:4608\n"
U_"v81,s77,v81\n"
M_"v[148:151],v[24:27],a[88:91],v[148:151]\n"
M_"v[148:151],v[28:31],a[92:95],v[148:151]\n"
T_"v[8:9],v194 offset:8192\n"
U_"v82,s77,v82\n"
M_"v[152:155],v[16:19],a[96:99],v[152:155]\n"
T_"v[10:11],v194 offset:8704\n"
U_"v83,s77,v83\n"
M_"v[152:155],v[20:23],a[100:103],v[152:155]\n"
M_"v[152:155],v[24:27],a[104:107],v[152:155]\n"
T_"v[12:13],v194 offset:12288\n"
C_"v64,v64,v65\n"
M_"v[152:155],v[28:31],a[108:111],v[152:155]\n"
C_"v65,v66,v67\n"
M_"v[156:159],v[16:19],a[112:115],v[156:159]\n"
T_"v[14:15],v194 offset:12800\n"
M_"v[156:159],v[20:23],a[116:119],v[156:159]\n"
C_"v66,v80,v81\n"
M_"v[156:159],v[24:27],a[120:123],v[156:159]\n"
C_"v67,v82,v83\n"
M_"v[156:159],v[28:31],a[124:127],v[156:159]\n"
W_"lgkmcnt(15)\n"
M_"v[144:147],v[32:35],a[0:3],v[144:147]\n"
S_"v[64:67],v204,s[52:55],s71 offen offset:0 sc1\n"
M_"v[144:147],v[36:39],a[4:7],v[144:147]\n"
T_"v[16:17],v194 offset:16384\n"
T_"v[18:19],v194 offset:16896\n"
M_"v[144:147],v[40:43],a[8:11],v[144:147]\n"
M_"v[144:147],v[44:47],a[12:15],v[144:147]\n"
T_"v[20:21],v194 offset:20480\n"
M_"v[148:151],v[32:35],a[16:19],v[148:151]\n"
M_"v[148:151],v[36:39],a[20:23],v[148:151]\n"
T_"v[22:23],v194 offset:20992\n"
M_"v[148:151],v[40:43],a[24:27],v[148:151]\n"
U_"v68,s77,v68\n"
M_"v[148:151],v[44:47],a[28:31],v[148:151]\n"
T_"v[24:25],v194 offset:24576\n"
U_"v69,s77,v69\n"
M_"v[152:155],v[32:35],a[32:35],v[152:155]\n"
T_"v[26:27],v194 offset:25088\n"
M_"v[152:155],v[36:39],a[36:39],v[152:155]\n"
U_"v70,s77,v70\n"
M_"v[152:155],v[40:43],a[40:43],v[152:155]\n"
T_"v[28:29],v194 offset:28672\n"
U_"v71,s77,v71\n"
M_"v[152:155],v[44:47],a[44:47],v[152:155]\n"
M_"v[156:159],v[32:35],a[48:51],v[156:159]\n"
"s_mov_b64 s[40:41],s[12:13]\n"
T_"v[30:31],v194 offset:29184\n"
U_"v84,s77,v84\n"
M_"v[156:159],v[36:39],a[52:55],v[156:159]\n"
U_"v85,s77,v85\n"
M_"v[156:159],v[40:43],a[56:59],v[156:159]\n"
"s_mov_b32 s42,0x400000\n"
M_"v[156:159],v[44:47],a[60:63],v[156:159]\n"
W_"lgkmcnt(15)\n"
U_"v86,s77,v86\n"
M_"v[144:147],v[48:51],a[128:131],v[144:147]\n"
"v_mul_u32_u24 v231,8192,v228\n"
T_"v[32:33],v194 offset:32768\n"
M_"v[144:147],v[52:55],a[132:135],v[144:147]\n"
T_"v[34:35],v194 offset:33280\n"
U_"v87,s77,v87\n"
M_"v[144:147],v[56:59],a[136:139],v[144:147]\n"
"buffer_load_dword v231,v231,s[40:43],s90 offen\n"
M_"v[144:147],v[60:63],a[140:143],v[144:147]\n"
T_"v[36:37],v194 offset:36864\n"
C_"v68,v68,v69\n"
M_"v[148:151],v[48:51],a[144:147],v[148:151]\n"
C_"v69,v70,v71\n"
M_"v[148:151],v[52:55],a[148:151],v[148:151]\n"
T_"v[38:39],v194 offset:37376\n"
C_"v70,v84,v85\n"
M_"v[148:151],v[56:59],a[152:155],v[148:151]\n"
C_"v71,v86,v87\n"
M_"v[148:151],v[60:63],a[156:159],v[148:151]\n"
T_"v[40:41],v194 offset:40960\n"
M_"v[152:155],v[48:51],a[160:163],v[152:155]\n"
S_"v[68:71],v205,s[52:55],s71 offen offset:0 sc1\n"
M_"v[152:155],v[52:55],a[164:167],v[152:155]\n"
T_"v[42:43],v194 offset:41472\n"
M_"v[152:155],v[56:59],a[168:171],v[152:155]\n"
T_"v[44:45],v194 offset:45056\n"
M_"v[152:155],v[60:63],a[172:175],v[152:155]\n"
M_"v[156:159],v[48:51],a[176:179],v[156:159]\n"
T_"v[46:47],v194 offset:45568\n"
M_"v[156:159],v[52:55],a[180:183],v[156:159]\n"
M_"v[156:159],v[56:59],a[184:187],v[156:159]\n"
U_"v72,s77,v72\n"
M_"v[156:159],v[60:63],a[188:191],v[156:159]\n"
W_"lgkmcnt(15)\n"
U_"v73,s77,v73\n"
M_"v[160:163],v[0:3],a[192:195],0\n"
T_"v[48:49],v194 offset:49152\n"
M_"v[160:163],v[4:7],a[196:199],v[160:163]\n"
T_"v[50:51],v194 offset:49664\n"
U_"v74,s77,v74\n"
M_"v[160:163],v[8:11],a[200:203],v[160:163]\n"
U_"v75,s77,v75\n"
M_"v[160:163],v[12:15],a[204:207],v[160:163]\n"
T_"v[52:53],v194 offset:53248\n"
M_"v[164:167],v[0:3],a[208:211],0\n"
U_"v88,s77,v88\n"
M_"v[164:167],v[4:7],a[212:215],v[164:167]\n"
T_"v[54:55],v194 offset:53760\n"
U_"v89,s77,v89\n"
M_"v[164:167],v[8:11],a[216:219],v[164:167]\n"
M_"v[164:167],v[12:15],a[220:223],v[164:167]\n"
T_"v[56:57],v194 offset:57344\n"
U_"v90,s77,v90\n"
M_"v[168:171],v[0:3],a[224:227],0\n"
T_"v[58:59],v194 offset:57856\n"
U_"v91,s77,v91\n"
M_"v[168:171],v[4:7],a[228:231],v[168:171]\n"
M_"v[168:171],v[8:11],a[232:235],v[168:171]\n"
"v_bfe_u32 v241,v244,4,5\n"
T_"v[60:61],v194 offset:61440\n"
M_"v[168:171],v[12:15],a[236:239],v[168:171]\n"
"v_lshlrev_b32 v241,7,v241\n"
C_"v72,v72,v73\n"
M_"v[172:175],v[0:3],a[240:243],0\n"
"s_lshl_b32 s79,s34,12\n"
T_"v[62:63],v194 offset:61952\n"
C_"v73,v74,v75\n"
M_"v[172:175],v[4:7],a[244:247],v[172:175]\n"
C_"v74,v88,v89\n"
M_"v[172:175],v[8:11],a[248:251],v[172:175]\n"
A_"s79,s79,0x4f10010\n"
C_"v75,v90,v91\n"
M_"v[172:175],v[12:15],a[252:255],v[172:175]\n"
W_"lgkmcnt(15)\n"
M_"v[160:163],v[16:19],a[64:67],v[160:163]\n"
"buffer_load_dword v240,v241,s[52:55],s79 offen sc1\n"
M_"v[160:163],v[20:23],a[68:71],v[160:163]\n"
S_"v[72:75],v206,s[52:55],s71 offen offset:0 sc1\n"
M_"v[160:163],v[24:27],a[72:75],v[160:163]\n"
T_"v[0:1],v195 offset:0\n"
T_"v[2:3],v195 offset:512\n"
M_"v[160:163],v[28:31],a[76:79],v[160:163]\n"
T_"v[4:5],v195 offset:4096\n"
M_"v[164:167],v[16:19],a[80:83],v[164:167]\n"
M_"v[164:167],v[20:23],a[84:87],v[164:167]\n"
T_"v[6:7],v195 offset:4608\n"
M_"v[164:167],v[24:27],a[88:91],v[164:167]\n"
U_"v76,s77,v76\n"
M_"v[164:167],v[28:31],a[92:95],v[164:167]\n"
T_"v[8:9],v195 offset:8192\n"
U_"v77,s77,v77\n"
M_"v[168:171],v[16:19],a[96:99],v[168:171]\n"
T_"v[10:11],v195 offset:8704\n"
M_"v[168:171],v[20:23],a[100:103],v[168:171]\n"
U_"v78,s77,v78\n"
M_"v[168:171],v[24:27],a[104:107],v[168:171]\n"
T_"v[12:13],v195 offset:12288\n"
U_"v79,s77,v79\n"
M_"v[168:171],v[28:31],a[108:111],v[168:171]\n"
M_"v[172:175],v[16:19],a[112:115],v[172:175]\n"
T_"v[14:15],v195 offset:12800\n"
U_"v92,s77,v92\n"
M_"v[172:175],v[20:23],a[116:119],v[172:175]\n"
U_"v93,s77,v93\n"
M_"v[172:175],v[24:27],a[120:123],v[172:175]\n"
M_"v[172:175],v[28:31],a[124:127],v[172:175]\n"
W_"lgkmcnt(15)\n"
U_"v94,s77,v94\n"
M_"v[160:163],v[32:35],a[0:3],v[160:163]\n"
T_"v[16:17],v195 offset:16384\n"
U_"v95,s77,v95\n"
M_"v[160:163],v[36:39],a[4:7],v[160:163]\n"
T_"v[18:19],v195 offset:16896\n"
M_"v[160:163],v[40:43],a[8:11],v[160:163]\n"
C_"v76,v76,v77\n"
M_"v[160:163],v[44:47],a[12:15],v[160:163]\n"
T_"v[20:21],v195 offset:20480\n"
C_"v77,v78,v79\n"
M_"v[164:167],v[32:35],a[16:19],v[164:167]\n"
M_"v[164:167],v[36:39],a[20:23],v[164:167]\n"
T_"v[22:23],v195 offset:20992\n"
C_"v78,v92,v93\n"
M_"v[164:167],v[40:43],a[24:27],v[164:167]\n"
C_"v79,v94,v95\n"
M_"v[164:167],v[44:47],a[28:31],v[164:167]\n"
T_"v[24:25],v195 offset:24576\n"
M_"v[168:171],v[32:35],a[32:35],v[168:171]\n"
S_"v[76:79],v207,s[52:55],s71 offen offset:0 sc1\n"
M_"v[168:171],v[36:39],a[36:39],v[168:171]\n"
T_"v[26:27],v195 offset:25088\n"
M_"v[168:171],v[40:43],a[40:43],v[168:171]\n"
T_"v[28:29],v195 offset:28672\n"
M_"v[168:171],v[44:47],a[44:47],v[168:171]\n"
M_"v[172:175],v[32:35],a[48:51],v[172:175]\n"
T_"v[30:31],v195 offset:29184\n"
M_"v[172:175],v[36:39],a[52:55],v[172:175]\n"
M_"v[172:175],v[40:43],a[56:59],v[172:175]\n"
U_"v96,s77,v96\n"
M_"v[172:175],v[44:47],a[60:63],v[172:175]\n"
W_"lgkmcnt(15)\n"
U_"v97,s77,v97\n"
M_"v[160:163],v[48:51],a[128:131],v[160:163]\n"
T_"v[32:33],v195 offset:32768\n"
M_"v[160:163],v[52:55],a[132:135],v[160:163]\n"
T_"v[34:35],v195 offset:33280\n"
U_"v98,s77,v98\n"
M_"v[160:163],v[56:59],a[136:139],v[160:163]\n"
U_"v99,s77,v99\n"
M_"v[160:163],v[60:63],a[140:143],v[160:163]\n"
T_"v[36:37],v195 offset:36864\n"
M_"v[164:167],v[48:51],a[144:147],v[164:167]\n"
U_"v112,s77,v112\n"
M_"v[164:167],v[52:55],a[148:151],v[164:167]\n"
T_"v[38:39],v195 offset:37376\n"
U_"v113,s77,v113\n"
M_"v[164:167],v[56:59],a[152:155],v[164:167]\n"
M_"v[164:167],v[60:63],a[156:159],v[164:167]\n"
T_"v[40:41],v195 offset:40960\n"
U_"v114,s77,v114\n"
M_"v[168:171],v[48:51],a[160:163],v[168:171]\n"
T_"v[42:43],v195 offset:41472\n"
U_"v115,s77,v115\n"
M_"v[168:171],v[52:55],a[164:167],v[168:171]\n"
M_"v[168:171],v[56:59],a[168:171],v[168:171]\n"
T_"v[44:45],v195 offset:45056\n"
C_"v96,v96,v97\n"
M_"v[168:171],v[60:63],a[172:175],v[168:171]\n"
C_"v97,v98,v99\n"
M_"v[172:175],v[48:51],a[176:179],v[172:175]\n"
T_"v[46:47],v195 offset:45568\n"
M_"v[172:175],v[52:55],a[180:183],v[172:175]\n"
C_"v98,v112,v113\n"
M_"v[172:175],v[56:59],a[184:187],v[172:175]\n"
C_"v99,v114,v115\n"
M_"v[172:175],v[60:63],a[188:191],v[172:175]\n"
W_"lgkmcnt(15)\n"
M_"v[176:179],v[0:3],a[192:195],0\n"
S_"v[96:99],v204,s[52:55],s71 offen offset:1024 sc1\n"
M_"v[176:179],v[4:7],a[196:199],v[176:179]\n"
T_"v[48:49],v195 offset:49152\n"
T_"v[50:51],v195 offset:49664\n"
M_"v[176:179],v[8:11],a[200:203],v[176:179]\n"
T_"v[52:53],v195 offset:53248\n"
M_"v[176:179],v[12:15],a[204:207],v[176:179]\n"
M_"v[180:183],v[0:3],a[208:211],0\n"
T_"v[54:55],v195 offset:53760\n"
M_"v[180:183],v[4:7],a[212:215],v[180:183]\n"
T_"v[56:57],v195 offset:57344\n"
M_"v[180:183],v[8:11],a[216:219],v[180:183]\n"
T_"v[58:59],v195 offset:57856\n"
U_"v100,s77,v100\n"
M_"v[180:183],v[12:15],a[220:223],v[180:183]\n"
U_"v101,s77,v101\n"
M_"v[184:187],v[0:3],a[224:227],0\n"
T_"v[60:61],v195 offset:61440\n"
M_"v[184:187],v[4:7],a[228:231],v[184:187]\n"
T_"v[62:63],v195 offset:61952\n"
U_"v102,s77,v102\n"
M_"v[184:187],v[8:11],a[232:235],v[184:187]\n"
U_"v103,s77,v103\n"
M_"v[184:187],v[12:15],a[236:239],v[184:187]\n"
M_"v[188:191],v[0:3],a[240:243],0\n"
U_"v116,s77,v116\n"
M_"v[188:191],v[4:7],a[244:247],v[188:191]\n"
U_"v117,s77,v117\n"
M_"v[188:191],v[8:11],a[248:251],v[188:191]\n"
M_"v[188:191],v[12:15],a[252:255],v[188:191]\n"
W_"lgkmcnt(15)\n"
U_"v118,s77,v118\n"
M_"v[176:179],v[16:19],a[64:67],v[176:179]\n"
U_"v119,s77,v119\n"
M_"v[176:179],v[20:23],a[68:71],v[176:179]\n"
M_"v[176:179],v[24:27],a[72:75],v[176:179]\n"
C_"v100,v100,v101\n"
M_"v[176:179],v[28:31],a[76:79],v[176:179]\n"
C_"v101,v102,v103\n"
M_"v[180:183],v[16:19],a[80:83],v[180:183]\n"
W_"vmcnt(14) lgkmcnt(0)\n"
"s_barrier\n"
M_"v[180:183],v[20:23],a[84:87],v[180:183]\n"
"v_xor_b32 v192,0x10000,v192\n"
C_"v102,v116,v117\n"
M_"v[180:183],v[24:27],a[88:91],v[180:183]\n"
"s_mov_b64 s[48:49],s[20:21]\n"
"v_xor_b32 v193,0x10000,v193\n"
C_"v103,v118,v119\n"
M_"v[180:183],v[28:31],a[92:95],v[180:183]\n"
"s_mov_b32 s50,0x1c0000\n"
"v_xor_b32 v194,0x10000,v194\n"
M_"v[184:187],v[16:19],a[96:99],v[184:187]\n"
S_"v[100:103],v205,s[52:55],s71 offen offset:1024 sc1\n"
M_"v[184:187],v[20:23],a[100:103],v[184:187]\n"
"s_mul_i32 s75,s80,112\n"
"v_xor_b32 v195,0x10000,v195\n"
T_"v[0:1],v192 offset:0\n"
M_"v[184:187],v[24:27],a[104:107],v[184:187]\n"
A_"s75,s75,s68\n"
T_"v[2:3],v192 offset:512\n"
M_"v[184:187],v[28:31],a[108:111],v[184:187]\n"
A_"m0,s56,0\n"
M_"v[188:191],v[16:19],a[112:115],v[188:191]\n"
B_"v200,s[48:51],s75 offen offset:0  lds\n"
M_"v[188:191],v[20:23],a[116:119],v[188:191]\n"
T_"v[4:5],v192 offset:4096\n"
T_"v[6:7],v192 offset:4608\n"
M_"v[188:191],v[24:27],a[120:123],v[188:191]\n"
T_"v[8:9],v192 offset:8192\n"
U_"v104,s77,v104\n"
M_"v[188:191],v[28:31],a[124:127],v[188:191]\n"
T_"v[10:11],v192 offset:8704\n"
U_"v105,s77,v105\n"
M_"v[176:179],v[32:35],a[0:3],v[176:179]\n"
B_"v201,s[48:51],s75 offen offset:1024  lds\n"
M_"v[176:179],v[36:39],a[4:7],v[176:179]\n"
T_"v[12:13],v192 offset:12288\n"
T_"v[14:15],v192 offset:12800\n"
M_"v[176:179],v[40:43],a[8:11],v[176:179]\n"
T_"v[16:17],v193 offset:0\n"
T_"v[18:19],v193 offset:512\n"
M_"v[176:179],v[44:47],a[12:15],v[176:179]\n"
U_"v106,s77,v106\n"
T_"v[20:21],v193 offset:4096\n"
M_"v[180:183],v[32:35],a[16:19],v[180:183]\n"
B_"v202,s[48:51],s75 offen offset:2048  lds\n"
M_"v[180:183],v[36:39],a[20:23],v[180:183]\n"
U_"v107,s77,v107\n"
T_"v[22:23],v193 offset:4608\n"
M_"v[180:183],v[40:43],a[24:27],v[180:183]\n"
U_"v120,s77,v120\n"
U_"v121,s77,v121\n"
M_"v[180:183],v[44:47],a[28:31],v[180:183]\n"
T_"v[24:25],v193 offset:8192\n"
U_"v122,s77,v122\n"
M_"v[184:187],v[32:35],a[32:35],v[184:187]\n"
B_"v203,s[48:51],s75 offen offset:3072  lds\n"
M_"v[184:187],v[36:39],a[36:39],v[184:187]\n"
"s_mul_i32 s75,s81,112\n"
T_"v[26:27],v193 offset:8704\n"
U_"v123,s77,v123\n"
M_"v[184:187],v[40:43],a[40:43],v[184:187]\n"
A_"s75,s75,s68\n"
T_"v[28:29],v193 offset:12288\n"
C_"v104,v104,v105\n"
M_"v[184:187],v[44:47],a[44:47],v[184:187]\n"
A_"m0,s56,16384\n"
C_"v105,v106,v107\n"
M_"v[188:191],v[32:35],a[48:51],v[188:191]\n"
B_"v200,s[48:51],s75 offen offset:0  lds\n"
M_"v[188:191],v[36:39],a[52:55],v[188:191]\n"
T_"v[30:31],v193 offset:12800\n"
C_"v106,v120,v121\n"
M_"v[188:191],v[40:43],a[56:59],v[188:191]\n"
C_"v107,v122,v123\n"
M_"v[188:191],v[44:47],a[60:63],v[188:191]\n"
M_"v[176:179],v[48:51],a[128:131],v[176:179]\n"
B_"v201,s[48:51],s75 offen offset:1024  lds\n"
M_"v[176:179],v[52:55],a[132:135],v[176:179]\n"
T_"v[32:33],v194 offset:0\n"
T_"v[34:35],v194 offset:512\n"
M_"v[176:179],v[56:59],a[136:139],v[176:179]\n"
M_"v[176:179],v[60:63],a[140:143],v[176:179]\n"
T_"v[36:37],v194 offset:4096\n"
M_"v[180:183],v[48:51],a[144:147],v[180:183]\n"
B_"v202,s[48:51],s75 offen offset:2048  lds\n"
M_"v[180:183],v[52:55],a[148:151],v[180:183]\n"
T_"v[38:39],v194 offset:4608\n"
M_"v[180:183],v[56:59],a[152:155],v[180:183]\n"
M_"v[180:183],v[60:63],a[156:159],v[180:183]\n"
T_"v[40:41],v194 offset:8192\n"
M_"v[184:187],v[48:51],a[160:163],v[184:187]\n"
S_"v[104:107],v206,s[52:55],s71 offen offset:1024 sc1\n"
B_"v203,s[48:51],s75 offen offset:3072  lds\n"
M_"v[184:187],v[52:55],a[164:167],v[184:187]\n"
"s_mul_i32 s75,s82,112\n"
U_"v108,s77,v108\n"
T_"v[42:43],v194 offset:8704\n"
M_"v[184:187],v[56:59],a[168:171],v[184:187]\n"
A_"s75,s75,s68\n"
U_"v109,s77,v109\n"
T_"v[44:45],v194 offset:12288\n"
M_"v[184:187],v[60:63],a[172:175],v[184:187]\n"
A_"m0,s56,32768\n"
U_"v110,s77,v110\n"
U_"v111,s77,v111\n"
M_"v[188:191],v[48:51],a[176:179],v[188:191]\n"
B_"v200,s[48:51],s75 offen offset:0  lds\n"
M_"v[188:191],v[52:55],a[180:183],v[188:191]\n"
T_"v[46:47],v194 offset:12800\n"
U_"v124,s77,v124\n"
M_"v[188:191],v[56:59],a[184:187],v[188:191]\n"
U_"v125,s77,v125\n"
M_"v[188:191],v[60:63],a[188:191],v[188:191]\n"
W_"lgkmcnt(15)\n"
U_"v126,s77,v126\n"
M_"v[64:67],v[0:3],a[192:195],0\n"
B_"v201,s[48:51],s75 offen offset:1024  lds\n"
M_"v[64:67],v[4:7],a[196:199],v[64:67]\n"
T_"v[48:49],v195 offset:0\n"
T_"v[50:51],v195 offset:512\n"
M_"v[64:67],v[8:11],a[200:203],v[64:67]\n"
U_"v127,s77,v127\n"
C_"v108,v108,v109\n"
M_"v[64:67],v[12:15],a[204:207],v[64:67]\n"
T_"v[52:53],v195 offset:4096\n"
C_"v109,v110,v111\n"
M_"v[68:71],v[0:3],a[208:211],0\n"
B_"v202,s[48:51],s75 offen offset:2048  lds\n"
M_"v[68:71],v[4:7],a[212:215],v[68:71]\n"
T_"v[54:55],v195 offset:4608\n"
C_"v110,v124,v125\n"
M_"v[68:71],v[8:11],a[216:219],v[68:71]\n"
T_"v[56:57],v195 offset:8192\n"
C_"v111,v126,v127\n"
M_"v[68:71],v[12:15],a[220:223],v[68:71]\n"
W_"lgkmcnt(13)\n"
B_"v203,s[48:51],s75 offen offset:3072  lds\n"
M_"v[80:83],v[16:19],a[192:195],0\n"
"s_mul_i32 s75,s83,112\n"
T_"v[58:59],v195 offset:8704\n"
A_"s75,s75,s68\n"
M_"v[80:83],v[20:23],a[196:199],v[80:83]\n"
A_"m0,s56,49152\n"
T_"v[60:61],v195 offset:12288\n"
M_"v[80:83],v[24:27],a[200:203],v[80:83]\n"
M_"v[80:83],v[28:31],a[204:207],v[80:83]\n"
B_"v200,s[48:51],s75 offen offset:0  lds\n"
M_"v[84:87],v[16:19],a[208:211],0\n"
W_"vmcnt(18)\n"
T_"v[62:63],v195 offset:12800\n"
M_"v[84:87],v[20:23],a[212:215],v[84:87]\n"
"v_cmp_ne_u32 vcc,s32,v240\n"
M_"v[84:87],v[24:27],a[216:219],v[84:87]\n"
"s_cmp_lg_u64 vcc,0\n"
"s_cbranch_scc1 L_slow_PLK\n"
"L_back_PLK:\n"
M_"v[84:87],v[28:31],a[220:223],v[84:87]\n"
W_"lgkmcnt(8)\n"
M_"v[96:99],v[32:35],a[192:195],0\n"
S_"v[108:111],v207,s[52:55],s71 offen offset:1024 sc1\n"
B_"v201,s[48:51],s75 offen offset:1024  lds\n"
M_"v[96:99],v[36:39],a[196:199],v[96:99]\n"
M_"v[96:99],v[40:43],a[200:203],v[96:99]\n"
B_"v202,s[48:51],s75 offen offset:2048  lds\n"
B_"v203,s[48:51],s75 offen offset:3072  lds\n"
M_"v[96:99],v[44:47],a[204:207],v[96:99]\n"
M_"v[100:103],v[32:35],a[208:211],0\n"
U_"v128,s77,v128\n"
M_"v[100:103],v[36:39],a[212:215],v[100:103]\n"
U_"v129,s77,v129\n"
M_"v[100:103],v[40:43],a[216:219],v[100:103]\n"
M_"v[100:103],v[44:47],a[220:223],v[100:103]\n"
W_"lgkmcnt(0)\n"
U_"v130,s77,v130\n"
M_"v[112:115],v[48:51],a[192:195],0\n"
M_"v[112:115],v[52:55],a[196:199],v[112:115]\n"
U_"v131,s77,v131\n"
M_"v[112:115],v[56:59],a[200:203],v[112:115]\n"
M_"v[112:115],v[60:63],a[204:207],v[112:115]\n"
U_"v144,s77,v144\n"
M_"v[116:119],v[48:51],a[208:211],0\n"
A_"s10,s96,s80\n"
M_"v[116:119],v[52:55],a[212:215],v[116:119]\n"
B_"a[192:195],v244,s[52:55],s10 offen offset:0 sc1\n"
M_"v[116:119],v[56:59],a[216:219],v[116:119]\n"
U_"v145,s77,v145\n"
M_"v[116:119],v[60:63],a[220:223],v[116:119]\n"
U_"v146,s77,v146\n"
M_"v[72:75],v[0:3],a[224:227],0\n"
M_"v[72:75],v[4:7],a[228:231],v[72:75]\n"
B_"a[196:199],v244,s[52:55],s10 offen offset:1024 sc1\n"
M_"v[72:75],v[8:11],a[232:235],v[72:75]\n"
U_"v147,s77,v147\n"
M_"v[72:75],v[12:15],a[236:239],v[72:75]\n"
C_"v128,v128,v129\n"
M_"v[76:79],v[0:3],a[240:243],0\n"
M_"v[76:79],v[4:7],a[244:247],v[76:79]\n"
B_"a[200:203],v244,s[52:55],s10 offen offset:2048 sc1\n"
M_"v[76:79],v[8:11],a[248:251],v[76:79]\n"
C_"v129,v130,v131\n"
M_"v[76:79],v[12:15],a[252:255],v[76:79]\n"
C_"v130,v144,v145\n"
M_"v[88:91],v[16:19],a[224:227],0\n"
T_"v[0:1],v192 offset:16384\n"
M_"v[88:91],v[20:23],a[228:231],v[88:91]\n"
B_"a[204:207],v244,s[52:55],s10 offen offset:3072 sc1\n"
M_"v[88:91],v[24:27],a[232:235],v[88:91]\n"
T_"v[2:3],v192 offset:16896\n"
C_"v131,v146,v147\n"
M_"v[88:91],v[28:31],a[236:239],v[88:91]\n"
T_"v[4:5],v192 offset:20480\n"
M_"v[92:95],v[16:19],a[240:243],0\n"
T_"v[6:7],v192 offset:20992\n"
M_"v[92:95],v[20:23],a[244:247],v[92:95]\n"
S_"v[128:131],v204,s[52:55],s72 offen offset:0 sc1\n"
M_"v[92:95],v[24:27],a[248:251],v[92:95]\n"
T_"v[8:9],v192 offset:24576\n"
M_"v[92:95],v[28:31],a[252:255],v[92:95]\n"
M_"v[104:107],v[32:35],a[224:227],0\n"
T_"v[10:11],v192 offset:25088\n"
M_"v[104:107],v[36:39],a[228:231],v[104:107]\n"
T_"v[12:13],v192 offset:28672\n"
M_"v[104:107],v[40:43],a[232:235],v[104:107]\n"
U_"v132,s77,v132\n"
M_"v[104:107],v[44:47],a[236:239],v[104:107]\n"
T_"v[14:15],v192 offset:29184\n"
T_"v[16:17],v193 offset:16384\n"
M_"v[108:111],v[32:35],a[240:243],0\n"
T_"v[18:19],v193 offset:16896\n"
T_"v[20:21],v193 offset:20480\n"
M_"v[108:111],v[36:39],a[244:247],v[108:111]\n"
A_"s10,s97,s80\n"
T_"v[22:23],v193 offset:20992\n"
U_"v133,s77,v133\n"
M_"v[108:111],v[40:43],a[248:251],v[108:111]\n"
B_"a[208:211],v244,s[52:55],s10 offen offset:0 sc1\n"
M_"v[108:111],v[44:47],a[252:255],v[108:111]\n"
T_"v[24:25],v193 offset:24576\n"
U_"v134,s77,v134\n"
M_"v[120:123],v[48:51],a[224:227],0\n"
T_"v[26:27],v193 offset:25088\n"
U_"v135,s77,v135\n"
M_"v[120:123],v[52:55],a[228:231],v[120:123]\n"
T_"v[28:29],v193 offset:28672\n"
U_"v148,s77,v148\n"
M_"v[120:123],v[56:59],a[232:235],v[120:123]\n"
B_"a[212:215],v244,s[52:55],s10 offen offset:1024 sc1\n"
M_"v[120:123],v[60:63],a[236:239],v[120:123]\n"
T_"v[30:31],v193 offset:29184\n"
T_"v[32:33],v194 offset:16384\n"
M_"v[124:127],v[48:51],a[240:243],0\n"
T_"v[34:35],v194 offset:16896\n"
T_"v[36:37],v194 offset:20480\n"
M_"v[124:127],v[52:55],a[244:247],v[124:127]\n"
T_"v[38:39],v194 offset:20992\n"
U_"v149,s77,v149\n"
M_"v[124:127],v[56:59],a[248:251],v[124:127]\n"
B_"a[216:219],v244,s[52:55],s10 offen offset:2048 sc1\n"
M_"v[124:127],v[60:63],a[252:255],v[124:127]\n"
W_"lgkmcnt(12)\n"
T_"v[40:41],v194 offset:24576\n"
U_"v150,s77,v150\n"
M_"v[64:67],v[0:3],a[64:67],v[64:67]\n"
T_"v[42:43],v194 offset:25088\n"
U_"v151,s77,v151\n"
M_"v[64:67],v[4:7],a[68:71],v[64:67]\n"
T_"v[44:45],v194 offset:28672\n"
C_"v132,v132,v133\n"
M_"v[64:67],v[8:11],a[72:75],v[64:67]\n"
B_"a[220:223],v244,s[52:55],s10 offen offset:3072 sc1\n"
M_"v[64:67],v[12:15],a[76:79],v[64:67]\n"
T_"v[46:47],v194 offset:29184\n"
T_"v[48:49],v195 offset:16384\n"
M_"v[68:71],v[0:3],a[80:83],v[68:71]\n"
T_"v[50:51],v195 offset:16896\n"
T_"v[52:53],v195 offset:20480\n"
M_"v[68:71],v[4:7],a[84:87],v[68:71]\n"
T_"v[54:55],v195 offset:20992\n"
C_"v133,v134,v135\n"
M_"v[68:71],v[8:11],a[88:91],v[68:71]\n"
T_"v[56:57],v195 offset:24576\n"
C_"v134,v148,v149\n"
M_"v[68:71],v[12:15],a[92:95],v[68:71]\n"
W_"lgkmcnt(13)\n"
C_"v135,v150,v151\n"
M_"v[80:83],v[16:19],a[64:67],v[80:83]\n"
T_"v[58:59],v195 offset:25088\n"
M_"v[80:83],v[20:23],a[68:71],v[80:83]\n"
S_"v[132:135],v205,s[52:55],s72 offen offset:0 sc1\n"
M_"v[80:83],v[24:27],a[72:75],v[80:83]\n"
T_"v[60:61],v195 offset:28672\n"
M_"v[80:83],v[28:31],a[76:79],v[80:83]\n"
T_"v[62:63],v195 offset:29184\n"
M_"v[84:87],v[16:19],a[80:83],v[84:87]\n"
M_"v[84:87],v[20:23],a[84:87],v[84:87]\n"
M_"v[84:87],v[24:27],a[88:91],v[84:87]\n"
A_"s10,s98,s80\n"
M_"v[84:87],v[28:31],a[92:95],v[84:87]\n"
W_"lgkmcnt(8)\n"
M_"v[96:99],v[32:35],a[64:67],v[96:99]\n"
B_"a[224:227],v244,s[52:55],s10 offen offset:0 sc1\n"
M_"v[96:99],v[36:39],a[68:71],v[96:99]\n"
U_"v136,s77,v136\n"
U_"v137,s77,v137\n"
M_"v[96:99],v[40:43],a[72:75],v[96:99]\n"
M_"v[96:99],v[44:47],a[76:79],v[96:99]\n"
U_"v138,s77,v138\n"
M_"v[100:103],v[32:35],a[80:83],v[100:103]\n"
B_"a[228:231],v244,s[52:55],s10 offen offset:1024 sc1\n"
M_"v[100:103],v[36:39],a[84:87],v[100:103]\n"
U_"v139,s77,v139\n"
M_"v[100:103],v[40:43],a[88:91],v[100:103]\n"
M_"v[100:103],v[44:47],a[92:95],v[100:103]\n"
W_"lgkmcnt(0)\n"
U_"v152,s77,v152\n"
M_"v[112:115],v[48:51],a[64:67],v[112:115]\n"
B_"a[232:235],v244,s[52:55],s10 offen offset:2048 sc1\n"
M_"v[112:115],v[52:55],a[68:71],v[112:115]\n"
U_"v153,s77,v153\n"
M_"v[112:115],v[56:59],a[72:75],v[112:115]\n"
M_"v[112:115],v[60:63],a[76:79],v[112:115]\n"
U_"v154,s77,v154\n"
M_"v[116:119],v[48:51],a[80:83],v[116:119]\n"
B_"a[236:239],v244,s[52:55],s10 offen offset:3072 sc1\n"
M_"v[116:119],v[52:55],a[84:87],v[116:119]\n"
U_"v155,s77,v155\n"
M_"v[116:119],v[56:59],a[88:91],v[116:119]\n"
M_"v[116:119],v[60:63],a[92:95],v[116:119]\n"
C_"v136,v136,v137\n"
M_"v[72:75],v[0:3],a[96:99],v[72:75]\n"
M_"v[72:75],v[4:7],a[100:103],v[72:75]\n"
C_"v137,v138,v139\n"
M_"v[72:75],v[8:11],a[104:107],v[72:75]\n"
M_"v[72:75],v[12:15],a[108:111],v[72:75]\n"
C_"v138,v152,v153\n"
M_"v[76:79],v[0:3],a[112:115],v[76:79]\n"
M_"v[76:79],v[4:7],a[116:119],v[76:79]\n"
C_"v139,v154,v155\n"
M_"v[76:79],v[8:11],a[120:123],v[76:79]\n"
M_"v[76:79],v[12:15],a[124:127],v[76:79]\n"
S_"v[136:139],v206,s[52:55],s72 offen offset:0 sc1\n"
M_"v[88:91],v[16:19],a[96:99],v[88:91]\n"
A_"s10,s99,s80\n"
T_"v[0:1],v192 offset:32768\n"
M_"v[88:91],v[20:23],a[100:103],v[88:91]\n"
T_"v[2:3],v192 offset:33280\n"
M_"v[88:91],v[24:27],a[104:107],v[88:91]\n"
M_"v[88:91],v[28:31],a[108:111],v[88:91]\n"
B_"a[240:243],v244,s[52:55],s10 offen offset:0 sc1\n"
M_"v[92:95],v[16:19],a[112:115],v[92:95]\n"
T_"v[4:5],v192 offset:36864\n"
T_"v[6:7],v192 offset:37376\n"
M_"v[92:95],v[20:23],a[116:119],v[92:95]\n"
M_"v[92:95],v[24:27],a[120:123],v[92:95]\n"
T_"v[8:9],v192 offset:40960\n"
U_"v140,s77,v140\n"
M_"v[92:95],v[28:31],a[124:127],v[92:95]\n"
B_"a[244:247],v244,s[52:55],s10 offen offset:1024 sc1\n"
M_"v[104:107],v[32:35],a[96:99],v[104:107]\n"
T_"v[10:11],v192 offset:41472\n"
U_"v141,s77,v141\n"
M_"v[104:107],v[36:39],a[100:103],v[104:107]\n"
T_"v[12:13],v192 offset:45056\n"
U_"v142,s77,v142\n"
M_"v[104:107],v[40:43],a[104:107],v[104:107]\n"
M_"v[104:107],v[44:47],a[108:111],v[104:107]\n"
B_"a[248:251],v244,s[52:55],s10 offen offset:2048 sc1\n"
M_"v[108:111],v[32:35],a[112:115],v[108:111]\n"
T_"v[14:15],v192 offset:45568\n"
T_"v[16:17],v193 offset:32768\n"
M_"v[108:111],v[36:39],a[116:119],v[108:111]\n"
T_"v[18:19],v193 offset:33280\n"
T_"v[20:21],v193 offset:36864\n"
M_"v[108:111],v[40:43],a[120:123],v[108:111]\n"
T_"v[22:23],v193 offset:37376\n"
U_"v143,s77,v143\n"
M_"v[108:111],v[44:47],a[124:127],v[108:111]\n"
B_"a[252:255],v244,s[52:55],s10 offen offset:3072 sc1\n"
M_"v[120:123],v[48:51],a[96:99],v[120:123]\n"
T_"v[24:25],v193 offset:40960\n"
U_"v156,s77,v156\n"
M_"v[120:123],v[52:55],a[100:103],v[120:123]\n"
T_"v[26:27],v193 offset:41472\n"
T_"v[28:29],v193 offset:45056\n"
M_"v[120:123],v[56:59],a[104:107],v[120:123]\n"
U_"v157,s77,v157\n"
U_"v158,s77,v158\n"
M_"v[120:123],v[60:63],a[108:111],v[120:123]\n"
T_"v[30:31],v193 offset:45568\n"
T_"v[32:33],v194 offset:32768\n"
M_"v[124:127],v[48:51],a[112:115],v[124:127]\n"
T_"v[34:35],v194 offset:33280\n"
T_"v[36:37],v194 offset:36864\n"
M_"v[124:127],v[52:55],a[116:119],v[124:127]\n"
T_"v[38:39],v194 offset:37376\n"
U_"v159,s77,v159\n"
M_"v[124:127],v[56:59],a[120:123],v[124:127]\n"
T_"v[40:41],v194 offset:40960\n"
C_"v140,v140,v141\n"
M_"v[124:127],v[60:63],a[124:127],v[124:127]\n"
W_"lgkmcnt(13)\n"
C_"v141,v142,v143\n"
M_"v[64:67],v[0:3],a[0:3],v[64:67]\n"
A_"s10,s96,s81\n"
T_"v[42:43],v194 offset:41472\n"
M_"v[64:67],v[4:7],a[4:7],v[64:67]\n"
B_"a[64:67],v244,s[52:55],s10 offen offset:0 sc1\n"
M_"v[64:67],v[8:11],a[8:11],v[64:67]\n"
T_"v[44:45],v194 offset:45056\n"
C_"v142,v156,v157\n"
M_"v[64:67],v[12:15],a[12:15],v[64:67]\n"
T_"v[46:47],v194 offset:45568\n"
T_"v[48:49],v195 offset:32768\n"
M_"v[68:71],v[0:3],a[16:19],v[68:71]\n"
T_"v[50:51],v195 offset:33280\n"
T_"v[52:53],v195 offset:36864\n"
M_"v[68:71],v[4:7],a[20:23],v[68:71]\n"
B_"a[68:71],v244,s[52:55],s10 offen offset:1024 sc1\n"
M_"v[68:71],v[8:11],a[24:27],v[68:71]\n"
T_"v[54:55],v195 offset:37376\n"
C_"v143,v158,v159\n"
M_"v[68:71],v[12:15],a[28:31],v[68:71]\n"
W_"lgkmcnt(12)\n"
T_"v[56:57],v195 offset:40960\n"
M_"v[80:83],v[16:19],a[0:3],v[80:83]\n"
T_"v[58:59],v195 offset:41472\n"
M_"v[80:83],v[20:23],a[4:7],v[80:83]\n"
S_"v[140:143],v207,s[52:55],s72 offen offset:0 sc1\n"
M_"v[80:83],v[24:27],a[8:11],v[80:83]\n"
T_"v[60:61],v195 offset:45056\n"
M_"v[80:83],v[28:31],a[12:15],v[80:83]\n"
T_"v[62:63],v195 offset:45568\n"
M_"v[84:87],v[16:19],a[16:19],v[84:87]\n"
U_"v160,s77,v160\n"
M_"v[84:87],v[20:23],a[20:23],v[84:87]\n"
B_"a[72:75],v244,s[52:55],s10 offen offset:2048 sc1\n"
M_"v[84:87],v[24:27],a[24:27],v[84:87]\n"
U_"v161,s77,v161\n"
M_"v[84:87],v[28:31],a[28:31],v[84:87]\n"
W_"lgkmcnt(8)\n"
U_"v162,s77,v162\n"
M_"v[96:99],v[32:35],a[0:3],v[96:99]\n"
M_"v[96:99],v[36:39],a[4:7],v[96:99]\n"
B_"a[76:79],v244,s[52:55],s10 offen offset:3072 sc1\n"
M_"v[96:99],v[40:43],a[8:11],v[96:99]\n"
U_"v163,s77,v163\n"
M_"v[96:99],v[44:47],a[12:15],v[96:99]\n"
U_"v176,s77,v176\n"
M_"v[100:103],v[32:35],a[16:19],v[100:103]\n"
M_"v[100:103],v[36:39],a[20:23],v[100:103]\n"
U_"v177,s77,v177\n"
M_"v[100:103],v[40:43],a[24:27],v[100:103]\n"
M_"v[100:103],v[44:47],a[28:31],v[100:103]\n"
W_"lgkmcnt(0)\n"
U_"v178,s77,v178\n"
M_"v[112:115],v[48:51],a[0:3],v[112:115]\n"
M_"v[112:115],v[52:55],a[4:7],v[112:115]\n"
A_"s10,s97,s81\n"
U_"v179,s77,v179\n"
M_"v[112:115],v[56:59],a[8:11],v[112:115]\n"
B_"a[80:83],v244,s[52:55],s10 offen offset:0 sc1\n"
M_"v[112:115],v[60:63],a[12:15],v[112:115]\n"
C_"v160,v160,v161\n"
M_"v[116:119],v[48:51],a[16:19],v[116:119]\n"
M_"v[116:119],v[52:55],a[20:23],v[116:119]\n"
C_"v161,v162,v163\n"
M_"v[116:119],v[56:59],a[24:27],v[116:119]\n"
B_"a[84:87],v244,s[52:55],s10 offen offset:1024 sc1\n"
M_"v[116:119],v[60:63],a[28:31],v[116:119]\n"
C_"v162,v176,v177\n"
M_"v[72:75],v[0:3],a[32:35],v[72:75]\n"
M_"v[72:75],v[4:7],a[36:39],v[72:75]\n"
C_"v163,v178,v179\n"
M_"v[72:75],v[8:11],a[40:43],v[72:75]\n"
B_"a[88:91],v244,s[52:55],s10 offen offset:2048 sc1\n"
M_"v[72:75],v[12:15],a[44:47],v[72:75]\n"
M_"v[76:79],v[0:3],a[48:51],v[76:79]\n"
M_"v[76:79],v[4:7],a[52:55],v[76:79]\n"
M_"v[76:79],v[8:11],a[56:59],v[76:79]\n"
S_"v[160:163],v204,s[52:55],s72 offen offset:1024 sc1\n"
M_"v[76:79],v[12:15],a[60:63],v[76:79]\n"
M_"v[88:91],v[16:19],a[32:35],v[88:91]\n"
T_"v[0:1],v192 offset:49152\n"
M_"v[88:91],v[20:23],a[36:39],v[88:91]\n"
T_"v[2:3],v192 offset:49664\n"
M_"v[88:91],v[24:27],a[40:43],v[88:91]\n"
B_"a[92:95],v244,s[52:55],s10 offen offset:3072 sc1\n"
M_"v[88:91],v[28:31],a[44:47],v[88:91]\n"
T_"v[4:5],v192 offset:53248\n"
U_"v164,s77,v164\n"
M_"v[92:95],v[16:19],a[48:51],v[92:95]\n"
T_"v[6:7],v192 offset:53760\n"
U_"v165,s77,v165\n"
M_"v[92:95],v[20:23],a[52:55],v[92:95]\n"
U_"v166,s77,v166\n"
M_"v[92:95],v[24:27],a[56:59],v[92:95]\n"
T_"v[8:9],v192 offset:57344\n"
M_"v[92:95],v[28:31],a[60:63],v[92:95]\n"
U_"v167,s77,v167\n"
M_"v[104:107],v[32:35],a[32:35],v[104:107]\n"
T_"v[10:11],v192 offset:57856\n"
M_"v[104:107],v[36:39],a[36:39],v[104:107]\n"
T_"v[12:13],v192 offset:61440\n"
U_"v180,s77,v180\n"
M_"v[104:107],v[40:43],a[40:43],v[104:107]\n"
A_"s10,s98,s81\n"
M_"v[104:107],v[44:47],a[44:47],v[104:107]\n"
B_"a[96:99],v244,s[52:55],s10 offen offset:0 sc1\n"
M_"v[108:111],v[32:35],a[48:51],v[108:111]\n"
T_"v[14:15],v192 offset:61952\n"
T_"v[16:17],v193 offset:49152\n"
M_"v[108:111],v[36:39],a[52:55],v[108:111]\n"
T_"v[18:19],v193 offset:49664\n"
T_"v[20:21],v193 offset:53248\n"
M_"v[108:111],v[40:43],a[56:59],v[108:111]\n"
T_"v[22:23],v193 offset:53760\n"
U_"v181,s77,v181\n"
M_"v[108:111],v[44:47],a[60:63],v[108:111]\n"
B_"a[100:103],v244,s[52:55],s10 offen offset:1024 sc1\n"
M_"v[120:123],v[48:51],a[32:35],v[120:123]\n"
T_"v[24:25],v193 offset:57344\n"
U_"v182,s77,v182\n"
M_"v[120:123],v[52:55],a[36:39],v[120:123]\n"
T_"v[26:27],v193 offset:57856\n"
T_"v[28:29],v193 offset:61440\n"
M_"v[120:123],v[56:59],a[40:43],v[120:123]\n"
U_"v183,s77,v183\n"
C_"v164,v164,v165\n"
M_"v[120:123],v[60:63],a[44:47],v[120:123]\n"
B_"a[104:107],v244,s[52:55],s10 offen offset:2048 sc1\n"
M_"v[124:127],v[48:51],a[48:51],v[124:127]\n"
T_"v[30:31],v193 offset:61952\n"
T_"v[32:33],v194 offset:49152\n"
M_"v[124:127],v[52:55],a[52:55],v[124:127]\n"
T_"v[34:35],v194 offset:49664\n"
T_"v[36:37],v194 offset:53248\n"
M_"v[124:127],v[56:59],a[56:59],v[124:127]\n"
T_"v[38:39],v194 offset:53760\n"
C_"v165,v166,v167\n"
M_"v[124:127],v[60:63],a[60:63],v[124:127]\n"
W_"lgkmcnt(12)\n"
T_"v[40:41],v194 offset:57344\n"
C_"v166,v180,v181\n"
M_"v[64:67],v[0:3],a[128:131],v[64:67]\n"
B_"a[108:111],v244,s[52:55],s10 offen offset:3072 sc1\n"
M_"v[64:67],v[4:7],a[132:135],v[64:67]\n"
T_"v[42:43],v194 offset:57856\n"
T_"v[44:45],v194 offset:61440\n"
M_"v[64:67],v[8:11],a[136:139],v[64:67]\n"
C_"v167,v182,v183\n"
M_"v[64:67],v[12:15],a[140:143],v[64:67]\n"
T_"v[46:47],v194 offset:61952\n"
T_"v[48:49],v195 offset:49152\n"
M_"v[68:71],v[0:3],a[144:147],v[68:71]\n"
S_"v[164:167],v205,s[52:55],s72 offen offset:1024 sc1\n"
M_"v[68:71],v[4:7],a[148:151],v[68:71]\n"
T_"v[50:51],v195 offset:49664\n"
T_"v[52:53],v195 offset:53248\n"
M_"v[68:71],v[8:11],a[152:155],v[68:71]\n"
T_"v[54:55],v195 offset:53760\n"
T_"v[56:57],v195 offset:57344\n"
M_"v[68:71],v[12:15],a[156:159],v[68:71]\n"
W_"lgkmcnt(13)\n"
M_"v[80:83],v[16:19],a[128:131],v[80:83]\n"
T_"v[58:59],v195 offset:57856\n"
U_"v168,s77,v168\n"
M_"v[80:83],v[20:23],a[132:135],v[80:83]\n"
T_"v[60:61],v195 offset:61440\n"
U_"v169,s77,v169\n"
M_"v[80:83],v[24:27],a[136:139],v[80:83]\n"
M_"v[80:83],v[28:31],a[140:143],v[80:83]\n"
A_"s10,s99,s81\n"
T_"v[62:63],v195 offset:61952\n"
U_"v170,s77,v170\n"
M_"v[84:87],v[16:19],a[144:147],v[84:87]\n"
B_"a[112:115],v244,s[52:55],s10 offen offset:0 sc1\n"
M_"v[84:87],v[20:23],a[148:151],v[84:87]\n"
U_"v171,s77,v171\n"
M_"v[84:87],v[24:27],a[152:155],v[84:87]\n"
M_"v[84:87],v[28:31],a[156:159],v[84:87]\n"
W_"lgkmcnt(8)\n"
U_"v184,s77,v184\n"
M_"v[96:99],v[32:35],a[128:131],v[96:99]\n"
B_"a[116:119],v244,s[52:55],s10 offen offset:1024 sc1\n"
M_"v[96:99],v[36:39],a[132:135],v[96:99]\n"
U_"v185,s77,v185\n"
M_"v[96:99],v[40:43],a[136:139],v[96:99]\n"
M_"v[96:99],v[44:47],a[140:143],v[96:99]\n"
U_"v186,s77,v186\n"
M_"v[100:103],v[32:35],a[144:147],v[100:103]\n"
B_"a[120:123],v244,s[52:55],s10 offen offset:2048 sc1\n"
M_"v[100:103],v[36:39],a[148:151],v[100:103]\n"
U_"v187,s77,v187\n"
M_"v[100:103],v[40:43],a[152:155],v[100:103]\n"
M_"v[100:103],v[44:47],a[156:159],v[100:103]\n"
W_"lgkmcnt(0)\n"
C_"v168,v168,v169\n"
M_"v[112:115],v[48:51],a[128:131],v[112:115]\n"
B_"a[124:127],v244,s[52:55],s10 offen offset:3072 sc1\n"
M_"v[112:115],v[52:55],a[132:135],v[112:115]\n"
C_"v169,v170,v171\n"
M_"v[112:115],v[56:59],a[136:139],v[112:115]\n"
M_"v[112:115],v[60:63],a[140:143],v[112:115]\n"
C_"v170,v184,v185\n"
M_"v[116:119],v[48:51],a[144:147],v[116:119]\n"
M_"v[116:119],v[52:55],a[148:151],v[116:119]\n"
C_"v171,v186,v187\n"
M_"v[116:119],v[56:59],a[152:155],v[116:119]\n"
M_"v[116:119],v[60:63],a[156:159],v[116:119]\n"
S_"v[168:171],v206,s[52:55],s72 offen offset:1024 sc1\n"
M_"v[72:75],v[0:3],a[160:163],v[72:75]\n"
M_"v[72:75],v[4:7],a[164:167],v[72:75]\n"
M_"v[72:75],v[8:11],a[168:171],v[72:75]\n"
M_"v[72:75],v[12:15],a[172:175],v[72:75]\n"
M_"v[76:79],v[0:3],a[176:179],v[76:79]\n"
W_"vmcnt(39) lgkmcnt(0)\n"
"s_barrier\n"
A_"s10,s96,s82\n"
M_"v[76:79],v[4:7],a[180:183],v[76:79]\n"
B_"a[0:3],v244,s[52:55],s10 offen offset:0 sc1\n"
M_"v[76:79],v[8:11],a[184:187],v[76:79]\n"
"s_mov_b64 s[48:49],s[22:23]\n"
U_"v172,s77,v172\n"
M_"v[76:79],v[12:15],a[188:191],v[76:79]\n"
U_"v173,s77,v173\n"
M_"v[88:91],v[16:19],a[160:163],v[88:91]\n"
"s_mov_b32 s50,0x1c0000\n"
"v_xor_b32 v192,0x10000,v192\n"
M_"v[88:91],v[20:23],a[164:167],v[88:91]\n"
B_"a[4:7],v244,s[52:55],s10 offen offset:1024 sc1\n"
M_"v[88:91],v[24:27],a[168:171],v[88:91]\n"
"s_mul_i32 s75,s80,112\n"
"v_xor_b32 v193,0x10000,v193\n"
"v_xor_b32 v194,0x10000,v194\n"
M_"v[88:91],v[28:31],a[172:175],v[88:91]\n"
U_"v174,s77,v174\n"
"v_xor_b32 v195,0x10000,v195\n"
M_"v[92:95],v[16:19],a[176:179],v[92:95]\n"
T_"v[0:1],v192 offset:0\n"
U_"v175,s77,v175\n"
M_"v[92:95],v[20:23],a[180:183],v[92:95]\n"
A_"s75,s75,s68\n"
T_"v[2:3],v192 offset:512\n"
U_"v188,s77,v188\n"
M_"v[92:95],v[24:27],a[184:187],v[92:95]\n"
B_"a[8:11],v244,s[52:55],s10 offen offset:2048 sc1\n"
M_"v[92:95],v[28:31],a[188:191],v[92:95]\n"
A_"m0,s56,65536\n"
T_"v[4:5],v192 offset:4096\n"
T_"v[6:7],v192 offset:4608\n"
M_"v[104:107],v[32:35],a[160:163],v[104:107]\n"
T_"v[8:9],v192 offset:8192\n"
U_"v189,s77,v189\n"
M_"v[104:107],v[36:39],a[164:167],v[104:107]\n"
T_"v[10:11],v192 offset:8704\n"
U_"v190,s77,v190\n"
M_"v[104:107],v[40:43],a[168:171],v[104:107]\n"
B_"v200,s[48:51],s75 offen offset:0  lds\n"
M_"v[104:107],v[44:47],a[172:175],v[104:107]\n"
T_"v[12:13],v192 offset:12288\n"
T_"v[14:15],v192 offset:12800\n"
M_"v[108:111],v[32:35],a[176:179],v[108:111]\n"
T_"v[16:17],v193 offset:0\n"
T_"v[18:19],v193 offset:512\n"
M_"v[108:111],v[36:39],a[180:183],v[108:111]\n"
T_"v[20:21],v193 offset:4096\n"
T_"v[22:23],v193 offset:4608\n"
M_"v[108:111],v[40:43],a[184:187],v[108:111]\n"
B_"a[12:15],v244,s[52:55],s10 offen offset:3072 sc1\n"
M_"v[108:111],v[44:47],a[188:191],v[108:111]\n"
U_"v191,s77,v191\n"
T_"v[24:25],v193 offset:8192\n"
M_"v[120:123],v[48:51],a[160:163],v[120:123]\n"
C_"v172,v172,v173\n"
T_"v[26:27],v193 offset:8704\n"
M_"v[120:123],v[52:55],a[164:167],v[120:123]\n"
T_"v[28:29],v193 offset:12288\n"
C_"v173,v174,v175\n"
M_"v[120:123],v[56:59],a[168:171],v[120:123]\n"
B_"v201,s[48:51],s75 offen offset:1024  lds\n"
M_"v[120:123],v[60:63],a[172:175],v[120:123]\n"
T_"v[30:31],v193 offset:12800\n"
T_"v[32:33],v194 offset:0\n"
M_"v[124:127],v[48:51],a[176:179],v[124:127]\n"
T_"v[34:35],v194 offset:512\n"
C_"v174,v188,v189\n"
M_"v[124:127],v[52:55],a[180:183],v[124:127]\n"
A_"s10,s97,s82\n"
T_"v[36:37],v194 offset:4096\n"
T_"v[38:39],v194 offset:4608\n"
M_"v[124:127],v[56:59],a[184:187],v[124:127]\n"
B_"v202,s[48:51],s75 offen offset:2048  lds\n"
M_"v[124:127],v[60:63],a[188:191],v[124:127]\n"
W_"lgkmcnt(12)\n"
C_"v175,v190,v191\n"
W_"vmcnt(42)\n"
M_"v[128:131],v[0:3],a[192:195],0\n"
T_"v[40:41],v194 offset:8192\n"
T_"v[42:43],v194 offset:8704\n"
M_"v[128:131],v[4:7],a[196:199],v[128:131]\n"
T_"v[44:45],v194 offset:12288\n"
M_"v[128:131],v[8:11],a[200:203],v[128:131]\n"
S_"v[172:175],v207,s[52:55],s72 offen offset:1024 sc1\n"
M_"v[128:131],v[12:15],a[204:207],v[128:131]\n"
W_"vmcnt(38)\n"
T_"v[46:47],v194 offset:12800\n"
T_"v[48:49],v195 offset:0\n"
M_"v[132:135],v[0:3],a[208:211],0\n"
T_"v[50:51],v195 offset:512\n"
T_"v[52:53],v195 offset:4096\n"
M_"v[132:135],v[4:7],a[212:215],v[132:135]\n"
T_"v[54:55],v195 offset:4608\n"
M_"v[132:135],v[8:11],a[216:219],v[132:135]\n"
B_"v203,s[48:51],s75 offen offset:3072  lds\n"
M_"v[132:135],v[12:15],a[220:223],v[132:135]\n"
W_"lgkmcnt(12)\n"
T_"v[56:57],v195 offset:8192\n"
"s_mul_i32 s75,s81,112\n"
M_"v[144:147],v[16:19],a[192:195],0\n"
B_"a[16:19],v244,s[52:55],s10 offen offset:0 sc1\n"
M_"v[144:147],v[20:23],a[196:199],v[144:147]\n"
A_"s75,s75,s68\n"
T_"v[58:59],v195 offset:8704\n"
T_"v[60:61],v195 offset:12288\n"
M_"v[144:147],v[24:27],a[200:203],v[144:147]\n"
A_"m0,s56,81920\n"
M_"v[144:147],v[28:31],a[204:207],v[144:147]\n"
T_"v[62:63],v195 offset:12800\n"
M_"v[148:151],v[16:19],a[208:211],0\n"
B_"a[20:23],v244,s[52:55],s10 offen offset:1024 sc1\n"
M_"v[148:151],v[20:23],a[212:215],v[148:151]\n"
U_"v64,s77,v64\n"
U_"v65,s77,v65\n"
M_"v[148:151],v[24:27],a[216:219],v[148:151]\n"
U_"v66,s77,v66\n"
M_"v[148:151],v[28:31],a[220:223],v[148:151]\n"
W_"lgkmcnt(8)\n"
B_"a[24:27],v244,s[52:55],s10 offen offset:2048 sc1\n"
M_"v[160:163],v[32:35],a[192:195],0\n"
U_"v67,s77,v67\n"
U_"v80,s77,v80\n"
M_"v[160:163],v[36:39],a[196:199],v[160:163]\n"
U_"v81,s77,v81\n"
M_"v[160:163],v[40:43],a[200:203],v[160:163]\n"
B_"v200,s[48:51],s75 offen offset:0  lds\n"
M_"v[160:163],v[44:47],a[204:207],v[160:163]\n"
B_"a[28:31],v244,s[52:55],s10 offen offset:3072 sc1\n"
M_"v[164:167],v[32:35],a[208:211],0\n"
U_"v82,s77,v82\n"
U_"v83,s77,v83\n"
M_"v[164:167],v[36:39],a[212:215],v[164:167]\n"
C_"v64,v64,v65\n"
C_"v65,v66,v67\n"
M_"v[164:167],v[40:43],a[216:219],v[164:167]\n"
A_"s10,s98,s82\n"
C_"v66,v80,v81\n"
M_"v[164:167],v[44:47],a[220:223],v[164:167]\n"
W_"lgkmcnt(0)\n"
B_"v201,s[48:51],s75 offen offset:1024  lds\n"
M_"v[176:179],v[48:51],a[192:195],0\n"
C_"v67,v82,v83\n"
M_"v[176:179],v[52:55],a[196:199],v[176:179]\n"
M_"v[176:179],v[56:59],a[200:203],v[176:179]\n"
M_"v[176:179],v[60:63],a[204:207],v[176:179]\n"
B_"v202,s[48:51],s75 offen offset:2048  lds\n"
M_"v[180:183],v[48:51],a[208:211],0\n"
M_"v[180:183],v[52:55],a[212:215],v[180:183]\n"
M_"v[180:183],v[56:59],a[216:219],v[180:183]\n"
"v_mov_b32 v208,0xc0135761\n"
M_"v[180:183],v[60:63],a[220:223],v[180:183]\n"
W_"vmcnt(41)\n"
M_"v[136:139],v[0:3],a[224:227],0\n"
S_"v[64:67],v204,s[52:55],s73 offen offset:0 sc1\n"
B_"v203,s[48:51],s75 offen offset:3072  lds\n"
M_"v[136:139],v[4:7],a[228:231],v[136:139]\n"
B_"a[32:35],v244,s[52:55],s10 offen offset:0 sc1\n"
M_"v[136:139],v[8:11],a[232:235],v[136:139]\n"
"s_mul_i32 s75,s82,112\n"
U_"v68,s77,v68\n"
U_"v69,s77,v69\n"
M_"v[136:139],v[12:15],a[236:239],v[136:139]\n"
W_"vmcnt(39)\n"
U_"v70,s77,v70\n"
U_"v71,s77,v71\n"
M_"v[140:143],v[0:3],a[240:243],0\n"
A_"s75,s75,s68\n"
U_"v84,s77,v84\n"
U_"v85,s77,v85\n"
M_"v[140:143],v[4:7],a[244:247],v[140:143]\n"
B_"a[36:39],v244,s[52:55],s10 offen offset:1024 sc1\n"
M_"v[140:143],v[8:11],a[248:251],v[140:143]\n"
"s_mov_b32 s76,0xbdd2d3e8\n"
U_"v86,s77,v86\n"
U_"v87,s77,v87\n"
M_"v[140:143],v[12:15],a[252:255],v[140:143]\n"
A_"m0,s56,98304\n"
C_"v68,v68,v69\n"
C_"v69,v70,v71\n"
M_"v[152:155],v[16:19],a[224:227],0\n"
B_"a[40:43],v244,s[52:55],s10 offen offset:2048 sc1\n"
M_"v[152:155],v[20:23],a[228:231],v[152:155]\n"
"s_lshl_b32 s85,s92,11\n"
T_"v[0:1],v192 offset:16384\n"
T_"v[2:3],v192 offset:16896\n"
M_"v[152:155],v[24:27],a[232:235],v[152:155]\n"
A_"s85,s85,0x3200000\n"
C_"v70,v84,v85\n"
C_"v71,v86,v87\n"
M_"v[152:155],v[28:31],a[236:239],v[152:155]\n"
T_"v[4:5],v192 offset:20480\n"
M_"v[156:159],v[16:19],a[240:243],0\n"
B_"a[44:47],v244,s[52:55],s10 offen offset:3072 sc1\n"
B_"v200,s[48:51],s75 offen offset:0  lds\n"
M_"v[156:159],v[20:23],a[244:247],v[156:159]\n"
"s_lshr_b32 s36,s96,11\n"
T_"v[6:7],v192 offset:20992\n"
M_"v[156:159],v[24:27],a[248:251],v[156:159]\n"
T_"v[8:9],v192 offset:24576\n"
M_"v[156:159],v[28:31],a[252:255],v[156:159]\n"
"s_mul_i32 s36,s36,7168\n"
A_"s10,s99,s82\n"
M_"v[168:171],v[32:35],a[224:227],0\n"
B_"v201,s[48:51],s75 offen offset:1024  lds\n"
M_"v[168:171],v[36:39],a[228:231],v[168:171]\n"
T_"v[10:11],v192 offset:25088\n"
T_"v[12:13],v192 offset:28672\n"
M_"v[168:171],v[40:43],a[232:235],v[168:171]\n"
S_"v[68:71],v205,s[52:55],s73 offen offset:0 sc1\n"
M_"v[168:171],v[44:47],a[236:239],v[168:171]\n"
A_"s36,s36,s85\n"
T_"v[14:15],v192 offset:29184\n"
T_"v[16:17],v193 offset:16384\n"
M_"v[172:175],v[32:35],a[240:243],0\n"
T_"v[18:19],v193 offset:16896\n"
U_"v96,s77,v96\n"
M_"v[172:175],v[36:39],a[244:247],v[172:175]\n"
U_"v97,s77,v97\n"
T_"v[20:21],v193 offset:20480\n"
M_"v[172:175],v[40:43],a[248:251],v[172:175]\n"
B_"v202,s[48:51],s75 offen offset:2048  lds\n"
M_"v[172:175],v[44:47],a[252:255],v[172:175]\n"
"s_lshr_b32 s37,s97,11\n"
U_"v98,s77,v98\n"
T_"v[22:23],v193 offset:20992\n"
M_"v[184:187],v[48:51],a[224:227],0\n"
U_"v99,s77,v99\n"
U_"v112,s77,v112\n"
M_"v[184:187],v[52:55],a[228:231],v[184:187]\n"
T_"v[24:25],v193 offset:24576\n"
U_"v113,s77,v113\n"
M_"v[184:187],v[56:59],a[232:235],v[184:187]\n"
B_"a[48:51],v244,s[52:55],s10 offen offset:0 sc1\n"
B_"v203,s[48:51],s75 offen offset:3072  lds\n"
M_"v[184:187],v[60:63],a[236:239],v[184:187]\n"
"s_mul_i32 s75,s83,112\n"
T_"v[26:27],v193 offset:25088\n"
T_"v[28:29],v193 offset:28672\n"
M_"v[188:191],v[48:51],a[240:243],0\n"
"s_mul_i32 s37,s37,7168\n"
T_"v[30:31],v193 offset:29184\n"
U_"v114,s77,v114\n"
M_"v[188:191],v[52:55],a[244:247],v[188:191]\n"
A_"s75,s75,s68\n"
T_"v[32:33],v194 offset:16384\n"
U_"v115,s77,v115\n"
M_"v[188:191],v[56:59],a[248:251],v[188:191]\n"
B_"a[52:55],v244,s[52:55],s10 offen offset:1024 sc1\n"
M_"v[188:191],v[60:63],a[252:255],v[188:191]\n"
W_"lgkmcnt(9)\n"
T_"v[34:35],v194 offset:16896\n"
W_"vmcnt(44)\n"
M_"v[128:131],v[0:3],a[64:67],v[128:131]\n"
A_"s37,s37,s85\n"
C_"v96,v96,v97\n"
C_"v97,v98,v99\n"
M_"v[128:131],v[4:7],a[68:71],v[128:131]\n"
B_"a[56:59],v244,s[52:55],s10 offen offset:2048 sc1\n"
T_"v[36:37],v194 offset:20480\n"
M_"v[128:131],v[8:11],a[72:75],v[128:131]\n"
A_"m0,s56,114688\n"
C_"v98,v112,v113\n"
T_"v[38:39],v194 offset:20992\n"
M_"v[128:131],v[12:15],a[76:79],v[128:131]\n"
W_"vmcnt(40)\n"
C_"v99,v114,v115\n"
T_"v[40:41],v194 offset:24576\n"
M_"v[132:135],v[0:3],a[80:83],v[132:135]\n"
S_"v[96:99],v204,s[52:55],s73 offen offset:1024 sc1\n"
M_"v[132:135],v[4:7],a[84:87],v[132:135]\n"
B_"a[60:63],v244,s[52:55],s10 offen offset:3072 sc1\n"
T_"v[42:43],v194 offset:25088\n"
T_"v[44:45],v194 offset:28672\n"
T_"v[46:47],v194 offset:29184\n"
M_"v[132:135],v[8:11],a[88:91],v[132:135]\n"
B_"v200,s[48:51],s75 offen offset:0  lds\n"
T_"v[48:49],v195 offset:16384\n"
M_"v[132:135],v[12:15],a[92:95],v[132:135]\n"
W_"lgkmcnt(9)\n"
"s_lshr_b32 s38,s98,11\n"
T_"v[50:51],v195 offset:16896\n"
M_"v[144:147],v[16:19],a[64:67],v[144:147]\n"
"s_mul_i32 s38,s38,7168\n"
T_"v[52:53],v195 offset:20480\n"
U_"v100,s77,v100\n"
M_"v[144:147],v[20:23],a[68:71],v[144:147]\n"
A_"s38,s38,s85\n"
T_"v[54:55],v195 offset:20992\n"
U_"v101,s77,v101\n"
M_"v[144:147],v[24:27],a[72:75],v[144:147]\n"
B_"v201,s[48:51],s75 offen offset:1024  lds\n"
M_"v[144:147],v[28:31],a[76:79],v[144:147]\n"
"s_lshr_b32 s39,s99,11\n"
U_"v102,s77,v102\n"
T_"v[56:57],v195 offset:24576\n"
M_"v[148:151],v[16:19],a[80:83],v[148:151]\n"
A_"s10,s96,s83\n"
U_"v103,s77,v103\n"
T_"v[58:59],v195 offset:25088\n"
M_"v[148:151],v[20:23],a[84:87],v[148:151]\n"
T_"v[60:61],v195 offset:28672\n"
T_"v[62:63],v195 offset:29184\n"
M_"v[148:151],v[24:27],a[88:91],v[148:151]\n"
U_"v116,s77,v116\n"
U_"v117,s77,v117\n"
M_"v[148:151],v[28:31],a[92:95],v[148:151]\n"
W_"lgkmcnt(8)\n"
U_"v118,s77,v118\n"
U_"v119,s77,v119\n"
M_"v[160:163],v[32:35],a[64:67],v[160:163]\n"
B_"v202,s[48:51],s75 offen offset:2048  lds\n"
M_"v[160:163],v[36:39],a[68:71],v[160:163]\n"
"s_mul_i32 s39,s39,7168\n"
C_"v100,v100,v101\n"
C_"v101,v102,v103\n"
M_"v[160:163],v[40:43],a[72:75],v[160:163]\n"
A_"s39,s39,s85\n"
C_"v102,v116,v117\n"
C_"v103,v118,v119\n"
M_"v[160:163],v[44:47],a[76:79],v[160:163]\n"
B_"a[128:131],v244,s[52:55],s10 offen offset:0 sc1\n"
M_"v[164:167],v[32:35],a[80:83],v[164:167]\n"
B_"v203,s[48:51],s75 offen offset:3072  lds\n"
M_"v[164:167],v[36:39],a[84:87],v[164:167]\n"
M_"v[164:167],v[40:43],a[88:91],v[164:167]\n"
S_"v[100:103],v205,s[52:55],s73 offen offset:1024 sc1\n"
M_"v[164:167],v[44:47],a[92:95],v[164:167]\n"
W_"lgkmcnt(0)\n"
B_"a[132:135],v244,s[52:55],s10 offen offset:1024 sc1\n"
M_"v[176:179],v[48:51],a[64:67],v[176:179]\n"
U_"v72,s77,v72\n"
U_"v73,s77,v73\n"
M_"v[176:179],v[52:55],a[68:71],v[176:179]\n"
U_"v74,s77,v74\n"
U_"v75,s77,v75\n"
M_"v[176:179],v[56:59],a[72:75],v[176:179]\n"
B_"a[136:139],v244,s[52:55],s10 offen offset:2048 sc1\n"
M_"v[176:179],v[60:63],a[76:79],v[176:179]\n"
U_"v88,s77,v88\n"
U_"v89,s77,v89\n"
M_"v[180:183],v[48:51],a[80:83],v[180:183]\n"
"s_bfe_u32 s85,s30,0x20001\n"
U_"v90,s77,v90\n"
U_"v91,s77,v91\n"
M_"v[180:183],v[52:55],a[84:87],v[180:183]\n"
"s_mul_i32 s91,s85,14\n"
C_"v72,v72,v73\n"
C_"v73,v74,v75\n"
M_"v[180:183],v[56:59],a[88:91],v[180:183]\n"
B_"a[140:143],v244,s[52:55],s10 offen offset:3072 sc1\n"
M_"v[180:183],v[60:63],a[92:95],v[180:183]\n"
W_"vmcnt(47)\n"
C_"v74,v88,v89\n"
C_"v75,v90,v91\n"
M_"v[136:139],v[0:3],a[96:99],v[136:139]\n"
A_"s68,s91,s31\n"
"s_lshl_b32 s68,s68,15\n"
M_"v[136:139],v[4:7],a[100:103],v[136:139]\n"
A_"s10,s97,s83\n"
M_"v[136:139],v[8:11],a[104:107],v[136:139]\n"
S_"v[72:75],v206,s[52:55],s73 offen offset:0 sc1\n"
M_"v[136:139],v[12:15],a[108:111],v[136:139]\n"
W_"vmcnt(43)\n"
"s_lshl_b32 s91,s91,10\n"
M_"v[140:143],v[0:3],a[112:115],v[140:143]\n"
A_"s91,s91,0x3200000\n"
M_"v[140:143],v[4:7],a[116:119],v[140:143]\n"
"s_and_b32 s95,s30,1\n"
M_"v[140:143],v[8:11],a[120:123],v[140:143]\n"
B_"a[144:147],v244,s[52:55],s10 offen offset:0 sc1\n"
M_"v[140:143],v[12:15],a[124:127],v[140:143]\n"
"s_mul_i32 s14,s95,229376\n"
U_"v76,s77,v76\n"
U_"v77,s77,v77\n"
M_"v[152:155],v[16:19],a[96:99],v[152:155]\n"
A_"s91,s91,s14\n"
T_"v[0:1],v192 offset:32768\n"
U_"v78,s77,v78\n"
M_"v[152:155],v[20:23],a[100:103],v[152:155]\n"
T_"v[2:3],v192 offset:33280\n"
U_"v79,s77,v79\n"
M_"v[152:155],v[24:27],a[104:107],v[152:155]\n"
B_"a[148:151],v244,s[52:55],s10 offen offset:1024 sc1\n"
M_"v[152:155],v[28:31],a[108:111],v[152:155]\n"
"s_lshr_b32 s14,s96,11\n"
T_"v[4:5],v192 offset:36864\n"
U_"v92,s77,v92\n"
M_"v[156:159],v[16:19],a[112:115],v[156:159]\n"
"s_mul_i32 s14,s14,7168\n"
T_"v[6:7],v192 offset:37376\n"
U_"v93,s77,v93\n"
M_"v[156:159],v[20:23],a[116:119],v[156:159]\n"
A_"s14,s14,s91\n"
U_"v94,s77,v94\n"
U_"v95,s77,v95\n"
M_"v[156:159],v[24:27],a[120:123],v[156:159]\n"
B_"a[152:155],v244,s[52:55],s10 offen offset:2048 sc1\n"
M_"v[156:159],v[28:31],a[124:127],v[156:159]\n"
"s_lshr_b32 s15,s98,11\n"
T_"v[8:9],v192 offset:40960\n"
C_"v76,v76,v77\n"
M_"v[168:171],v[32:35],a[96:99],v[168:171]\n"
"s_mul_i32 s15,s15,7168\n"
T_"v[10:11],v192 offset:41472\n"
C_"v77,v78,v79\n"
M_"v[168:171],v[36:39],a[100:103],v[168:171]\n"
T_"v[12:13],v192 offset:45056\n"
C_"v78,v92,v93\n"
M_"v[168:171],v[40:43],a[104:107],v[168:171]\n"
B_"a[156:159],v244,s[52:55],s10 offen offset:3072 sc1\n"
M_"v[168:171],v[44:47],a[108:111],v[168:171]\n"
A_"s15,s15,s91\n"
T_"v[14:15],v192 offset:45568\n"
T_"v[16:17],v193 offset:32768\n"
M_"v[172:175],v[32:35],a[112:115],v[172:175]\n"
"s_and_b32 s91,s33,3\n"
T_"v[18:19],v193 offset:33280\n"
C_"v79,v94,v95\n"
M_"v[172:175],v[36:39],a[116:119],v[172:175]\n"
"s_lshl_b32 s85,s85,5\n"
T_"v[20:21],v193 offset:36864\n"
T_"v[22:23],v193 offset:37376\n"
M_"v[172:175],v[40:43],a[120:123],v[172:175]\n"
S_"v[76:79],v207,s[52:55],s73 offen offset:0 sc1\n"
M_"v[172:175],v[44:47],a[124:127],v[172:175]\n"
A_"s85,s85,s95\n"
T_"v[24:25],v193 offset:40960\n"
M_"v[184:187],v[48:51],a[96:99],v[184:187]\n"
"s_lshl4_add_u32 s85,s85,s91\n"
T_"v[26:27],v193 offset:41472\n"
U_"v104,s77,v104\n"
M_"v[184:187],v[52:55],a[100:103],v[184:187]\n"
T_"v[28:29],v193 offset:45056\n"
U_"v105,s77,v105\n"
M_"v[184:187],v[56:59],a[104:107],v[184:187]\n"
"s_lshl_b32 s85,s85,12\n"
U_"v106,s77,v106\n"
A_"s10,s98,s83\n"
M_"v[184:187],v[60:63],a[108:111],v[184:187]\n"
B_"a[160:163],v244,s[52:55],s10 offen offset:0 sc1\n"
M_"v[188:191],v[48:51],a[112:115],v[188:191]\n"
A_"s87,s85,0x200000\n"
T_"v[30:31],v193 offset:45568\n"
T_"v[32:33],v194 offset:32768\n"
M_"v[188:191],v[52:55],a[116:119],v[188:191]\n"
"s_lshl_b32 s91,s91,8\n"
T_"v[34:35],v194 offset:33280\n"
T_"v[36:37],v194 offset:36864\n"
M_"v[188:191],v[56:59],a[120:123],v[188:191]\n"
T_"v[38:39],v194 offset:37376\n"
U_"v107,s77,v107\n"
M_"v[188:191],v[60:63],a[124:127],v[188:191]\n"
W_"lgkmcnt(12)\n"
U_"v120,s77,v120\n"
W_"vmcnt(43)\n"
M_"v[128:131],v[0:3],a[0:3],v[128:131]\n"
A_"s68,s68,s91\n"
T_"v[40:41],v194 offset:40960\n"
U_"v121,s77,v121\n"
M_"v[128:131],v[4:7],a[4:7],v[128:131]\n"
"s_lshr_b32 s91,s31,1\n"
T_"v[42:43],v194 offset:41472\n"
T_"v[44:45],v194 offset:45056\n"
M_"v[128:131],v[8:11],a[8:11],v[128:131]\n"
B_"a[164:167],v244,s[52:55],s10 offen offset:1024 sc1\n"
M_"v[128:131],v[12:15],a[12:15],v[128:131]\n"
W_"vmcnt(35)\n"
T_"v[46:47],v194 offset:45568\n"
U_"v122,s77,v122\n"
M_"v[132:135],v[0:3],a[16:19],v[132:135]\n"
"s_mul_i32 s91,s91,65408\n"
T_"v[48:49],v195 offset:32768\n"
U_"v123,s77,v123\n"
M_"v[132:135],v[4:7],a[20:23],v[132:135]\n"
"s_sub_u32 s16,s68,s91\n"
T_"v[50:51],v195 offset:33280\n"
C_"v104,v104,v105\n"
M_"v[132:135],v[8:11],a[24:27],v[132:135]\n"
B_"a[168:171],v244,s[52:55],s10 offen offset:2048 sc1\n"
M_"v[132:135],v[12:15],a[28:31],v[132:135]\n"
W_"lgkmcnt(10)\n"
C_"v105,v106,v107\n"
T_"v[52:53],v195 offset:36864\n"
M_"v[144:147],v[16:19],a[0:3],v[144:147]\n"
"s_mul_i32 s91,s95,458752\n"
C_"v106,v120,v121\n"
T_"v[54:55],v195 offset:37376\n"
M_"v[144:147],v[20:23],a[4:7],v[144:147]\n"
"s_sub_u32 s17,229376,s91\n"
C_"v107,v122,v123\n"
T_"v[56:57],v195 offset:40960\n"
M_"v[144:147],v[24:27],a[8:11],v[144:147]\n"
S_"v[104:107],v206,s[52:55],s73 offen offset:1024 sc1\n"
M_"v[144:147],v[28:31],a[12:15],v[144:147]\n"
T_"v[58:59],v195 offset:41472\n"
T_"v[60:61],v195 offset:45056\n"
M_"v[148:151],v[16:19],a[16:19],v[148:151]\n"
T_"v[62:63],v195 offset:45568\n"
"v_bfe_u32 v201,v244,7,2\n"
M_"v[148:151],v[20:23],a[20:23],v[148:151]\n"
"v_lshrrev_b32 v202,9,v244\n"
U_"v108,s77,v108\n"
M_"v[148:151],v[24:27],a[24:27],v[148:151]\n"
B_"a[172:175],v244,s[52:55],s10 offen offset:3072 sc1\n"
M_"v[148:151],v[28:31],a[28:31],v[148:151]\n"
W_"lgkmcnt(8)\n"
"v_lshl_add_u32 v200,v202,4,v201\n"
U_"v109,s77,v109\n"
M_"v[160:163],v[32:35],a[0:3],v[160:163]\n"
"v_lshlrev_b32 v200,10,v200\n"
U_"v110,s77,v110\n"
M_"v[160:163],v[36:39],a[4:7],v[160:163]\n"
U_"v111,s77,v111\n"
"v_and_b32 v201,2,v201\n"
M_"v[160:163],v[40:43],a[8:11],v[160:163]\n"
U_"v124,s77,v124\n"
"v_bfe_u32 v202,v244,4,3\n"
M_"v[160:163],v[44:47],a[12:15],v[160:163]\n"
U_"v125,s77,v125\n"
U_"v126,s77,v126\n"
M_"v[164:167],v[32:35],a[16:19],v[164:167]\n"
"v_xor_b32 v201,v201,v202\n"
U_"v127,s77,v127\n"
M_"v[164:167],v[36:39],a[20:23],v[164:167]\n"
C_"v108,v108,v109\n"
"v_lshl_add_u32 v200,v201,4,v200\n"
M_"v[164:167],v[40:43],a[24:27],v[164:167]\n"
C_"v109,v110,v111\n"
"v_xor_b32 v203,64,v200\n"
M_"v[164:167],v[44:47],a[28:31],v[164:167]\n"
W_"lgkmcnt(0)\n"
C_"v110,v124,v125\n"
"v_add_u32 v201,3072,v203\n"
M_"v[176:179],v[48:51],a[0:3],v[176:179]\n"
"v_add_u32 v202,6144,v200\n"
C_"v111,v126,v127\n"
M_"v[176:179],v[52:55],a[4:7],v[176:179]\n"
S_"v[108:111],v207,s[52:55],s73 offen offset:1024 sc1\n"
M_"v[176:179],v[56:59],a[8:11],v[176:179]\n"
"v_add_u32 v203,9216,v203\n"
M_"v[176:179],v[60:63],a[12:15],v[176:179]\n"
M_"v[180:183],v[48:51],a[16:19],v[180:183]\n"
M_"v[180:183],v[52:55],a[20:23],v[180:183]\n"
M_"v[180:183],v[56:59],a[24:27],v[180:183]\n"
M_"v[180:183],v[60:63],a[28:31],v[180:183]\n"
W_"vmcnt(31)\n"
W_"lgkmcnt(0)\n"
"L_m_join:\n"
M_"v[136:139],v[0:3],a[32:35],v[136:139]\n"
A_"s10,s99,s83\n"
M_"v[136:139],v[4:7],a[36:39],v[136:139]\n"
B_"a[176:179],v244,s[52:55],s10 offen offset:0 sc1\n"
M_"v[136:139],v[8:11],a[40:43],v[136:139]\n"
M_"v[136:139],v[12:15],a[44:47],v[136:139]\n"
W_"vmcnt(22)\n"
M_"v[140:143],v[0:3],a[48:51],v[140:143]\n"
M_"v[140:143],v[4:7],a[52:55],v[140:143]\n"
B_"a[180:183],v244,s[52:55],s10 offen offset:1024 sc1\n"
M_"v[140:143],v[8:11],a[56:59],v[140:143]\n"
M_"v[140:143],v[12:15],a[60:63],v[140:143]\n"
M_"v[152:155],v[16:19],a[32:35],v[152:155]\n"
T_"v[0:1],v192 offset:49152\n"
M_"v[152:155],v[20:23],a[36:39],v[152:155]\n"
B_"a[184:187],v244,s[52:55],s10 offen offset:2048 sc1\n"
M_"v[152:155],v[24:27],a[40:43],v[152:155]\n"
T_"v[2:3],v192 offset:49664\n"
M_"v[152:155],v[28:31],a[44:47],v[152:155]\n"
T_"v[4:5],v192 offset:53248\n"
M_"v[156:159],v[16:19],a[48:51],v[156:159]\n"
T_"v[6:7],v192 offset:53760\n"
M_"v[156:159],v[20:23],a[52:55],v[156:159]\n"
B_"a[188:191],v244,s[52:55],s10 offen offset:3072 sc1\n"
M_"v[156:159],v[24:27],a[56:59],v[156:159]\n"
T_"v[8:9],v192 offset:57344\n"
M_"v[156:159],v[28:31],a[60:63],v[156:159]\n"
M_"v[168:171],v[32:35],a[32:35],v[168:171]\n"
T_"v[10:11],v192 offset:57856\n"
M_"v[168:171],v[36:39],a[36:39],v[168:171]\n"
T_"v[12:13],v192 offset:61440\n"
M_"v[168:171],v[40:43],a[40:43],v[168:171]\n"
M_"v[168:171],v[44:47],a[44:47],v[168:171]\n"
T_"v[14:15],v192 offset:61952\n"
T_"v[16:17],v193 offset:49152\n"
M_"v[172:175],v[32:35],a[48:51],v[172:175]\n"
T_"v[18:19],v193 offset:49664\n"
T_"v[20:21],v193 offset:53248\n"
M_"v[172:175],v[36:39],a[52:55],v[172:175]\n"
T_"v[22:23],v193 offset:53760\n"
M_"v[172:175],v[40:43],a[56:59],v[172:175]\n"
T_"v[24:25],v193 offset:57344\n"
M_"v[172:175],v[44:47],a[60:63],v[172:175]\n"
M_"v[184:187],v[48:51],a[32:35],v[184:187]\n"
T_"v[26:27],v193 offset:57856\n"
M_"v[184:187],v[52:55],a[36:39],v[184:187]\n"
T_"v[28:29],v193 offset:61440\n"
M_"v[184:187],v[56:59],a[40:43],v[184:187]\n"
M_"v[184:187],v[60:63],a[44:47],v[184:187]\n"
T_"v[30:31],v193 offset:61952\n"
T_"v[32:33],v194 offset:49152\n"
M_"v[188:191],v[48:51],a[48:51],v[188:191]\n"
T_"v[34:35],v194 offset:49664\n"
T_"v[36:37],v194 offset:53248\n"
M_"v[188:191],v[52:55],a[52:55],v[188:191]\n"
T_"v[38:39],v194 offset:53760\n"
M_"v[188:191],v[56:59],a[56:59],v[188:191]\n"
T_"v[40:41],v194 offset:57344\n"
M_"v[188:191],v[60:63],a[60:63],v[188:191]\n"
W_"lgkmcnt(13)\n"
W_"vmcnt(16)\n"
M_"v[128:131],v[0:3],a[128:131],v[128:131]\n"
T_"v[42:43],v194 offset:57856\n"
M_"v[128:131],v[4:7],a[132:135],v[128:131]\n"
T_"v[44:45],v194 offset:61440\n"
M_"v[128:131],v[8:11],a[136:139],v[128:131]\n"
M_"v[128:131],v[12:15],a[140:143],v[128:131]\n"
W_"vmcnt(11)\n"
T_"v[46:47],v194 offset:61952\n"
T_"v[48:49],v195 offset:49152\n"
M_"v[132:135],v[0:3],a[144:147],v[132:135]\n"
T_"v[50:51],v195 offset:49664\n"
T_"v[52:53],v195 offset:53248\n"
M_"v[132:135],v[4:7],a[148:151],v[132:135]\n"
T_"v[54:55],v195 offset:53760\n"
M_"v[132:135],v[8:11],a[152:155],v[132:135]\n"
T_"v[56:57],v195 offset:57344\n"
U_"v247,v128,v128\n"
M_"v[132:135],v[12:15],a[156:159],v[132:135]\n"
W_"lgkmcnt(13)\n"
"v_fma_f32 v247,v247,s76,v208\n"
U_"v247,v247,v128\n"
M_"v[144:147],v[16:19],a[128:131],v[144:147]\n"
E_"v247,v247\n"
M_"v[144:147],v[20:23],a[132:135],v[144:147]\n"
T_"v[58:59],v195 offset:57856\n"
T_"v[60:61],v195 offset:61440\n"
U_"v248,v129,v129\n"
M_"v[144:147],v[24:27],a[136:139],v[144:147]\n"
"v_fma_f32 v248,v248,s76,v208\n"
"v_add_f32 v247,1.0,v247\n"
M_"v[144:147],v[28:31],a[140:143],v[144:147]\n"
"v_rcp_f32 v247,v247\n"
M_"v[148:151],v[16:19],a[144:147],v[148:151]\n"
T_"v[62:63],v195 offset:61952\n"
U_"v248,v248,v129\n"
M_"v[148:151],v[20:23],a[148:151],v[148:151]\n"
E_"v248,v248\n"
M_"v[148:151],v[24:27],a[152:155],v[148:151]\n"
U_"v249,v130,v130\n"
"v_fma_f32 v249,v249,s76,v208\n"
M_"v[148:151],v[28:31],a[156:159],v[148:151]\n"
W_"lgkmcnt(8)\n"
"v_add_f32 v248,1.0,v248\n"
U_"v128,v128,v247\n"
M_"v[160:163],v[32:35],a[128:131],v[160:163]\n"
"v_rcp_f32 v248,v248\n"
M_"v[160:163],v[36:39],a[132:135],v[160:163]\n"
U_"v249,v249,v130\n"
M_"v[160:163],v[40:43],a[136:139],v[160:163]\n"
E_"v249,v249\n"
M_"v[160:163],v[44:47],a[140:143],v[160:163]\n"
U_"v250,v131,v131\n"
"v_fma_f32 v250,v250,s76,v208\n"
"v_add_f32 v249,1.0,v249\n"
M_"v[164:167],v[32:35],a[144:147],v[164:167]\n"
U_"v129,v129,v248\n"
M_"v[164:167],v[36:39],a[148:151],v[164:167]\n"
"v_rcp_f32 v249,v249\n"
M_"v[164:167],v[40:43],a[152:155],v[164:167]\n"
U_"v250,v250,v131\n"
E_"v250,v250\n"
M_"v[164:167],v[44:47],a[156:159],v[164:167]\n"
W_"lgkmcnt(0)\n"
U_"v247,v132,v132\n"
"v_fma_f32 v247,v247,s76,v208\n"
M_"v[176:179],v[48:51],a[128:131],v[176:179]\n"
"v_add_f32 v250,1.0,v250\n"
U_"v130,v130,v249\n"
M_"v[176:179],v[52:55],a[132:135],v[176:179]\n"
"v_rcp_f32 v250,v250\n"
U_"v247,v247,v132\n"
M_"v[176:179],v[56:59],a[136:139],v[176:179]\n"
E_"v247,v247\n"
M_"v[176:179],v[60:63],a[140:143],v[176:179]\n"
U_"v248,v133,v133\n"
"v_fma_f32 v248,v248,s76,v208\n"
M_"v[180:183],v[48:51],a[144:147],v[180:183]\n"
"s_memtime s[20:21]\n"
"s_memrealtime s[22:23]\n"
"v_add_f32 v247,1.0,v247\n"
U_"v131,v131,v250\n"
M_"v[180:183],v[52:55],a[148:151],v[180:183]\n"
"v_rcp_f32 v247,v247\n"
M_"v[180:183],v[56:59],a[152:155],v[180:183]\n"
U_"v248,v248,v133\n"
M_"v[180:183],v[60:63],a[156:159],v[180:183]\n"
W_"vmcnt(5)\n"
E_"v248,v248\n"
U_"v249,v134,v134\n"
M_"v[136:139],v[0:3],a[160:163],v[136:139]\n"
"v_fma_f32 v249,v249,s76,v208\n"
"v_add_f32 v248,1.0,v248\n"
M_"v[136:139],v[4:7],a[164:167],v[136:139]\n"
U_"v132,v132,v247\n"
M_"v[136:139],v[8:11],a[168:171],v[136:139]\n"
"v_rcp_f32 v248,v248\n"
U_"v249,v249,v134\n"
M_"v[136:139],v[12:15],a[172:175],v[136:139]\n"
W_"vmcnt(0)\n"
E_"v249,v249\n"
M_"v[140:143],v[0:3],a[176:179],v[140:143]\n"
W_" lgkmcnt(0)\n"
"s_barrier\n"
M_"v[140:143],v[4:7],a[180:183],v[140:143]\n"
"buffer_store_dword v254,off,s[52:55],s86 sc1\n"
M_"v[140:143],v[8:11],a[184:187],v[140:143]\n"
"s_mov_b64 s[48:49],s[12:13]\n"
U_"v250,v135,v135\n"
"v_fma_f32 v250,v250,s76,v208\n"
"v_add_f32 v249,1.0,v249\n"
M_"v[140:143],v[12:15],a[188:191],v[140:143]\n"
"s_sub_u32 s60,s20,s60\n"
U_"v133,v133,v248\n"
"s_sub_u32 s62,s22,s62\n"
M_"v[152:155],v[16:19],a[160:163],v[152:155]\n"
"v_xor_b32 v192,0x10000,v192\n"
"v_cvt_f32_u32 v206,s60\n"
M_"v[152:155],v[20:23],a[164:167],v[152:155]\n"
"s_mov_b32 s50,0x400000\n"
"v_xor_b32 v193,0x10000,v193\n"
"v_rcp_f32 v249,v249\n"
M_"v[152:155],v[24:27],a[168:171],v[152:155]\n"
"v_xor_b32 v194,0x10000,v194\n"
"v_cvt_f32_u32 v207,s62\n"
M_"v[152:155],v[28:31],a[172:175],v[152:155]\n"
"v_xor_b32 v195,0x10000,v195\n"
U_"v250,v250,v135\n"
M_"v[156:159],v[16:19],a[176:179],v[156:159]\n"
A_"s75,s69,0\n"
T_"v[0:1],v192 offset:0\n"
"v_rcp_f32 v206,v206\n"
M_"v[156:159],v[20:23],a[180:183],v[156:159]\n"
E_"v250,v250\n"
M_"v[156:159],v[24:27],a[184:187],v[156:159]\n"
T_"v[2:3],v192 offset:512\n"
T_"v[4:5],v192 offset:4096\n"
M_"v[156:159],v[28:31],a[188:191],v[156:159]\n"
A_"m0,s74,0\n"
T_"v[6:7],v192 offset:4608\n"
U_"v207,0x4788b800,v207\n"
U_"v247,v136,v136\n"
M_"v[168:171],v[32:35],a[160:163],v[168:171]\n"
T_"v[8:9],v192 offset:8192\n"
"v_fma_f32 v247,v247,s76,v208\n"
M_"v[168:171],v[36:39],a[164:167],v[168:171]\n"
T_"v[10:11],v192 offset:8704\n"
U_"v207,v206,v207\n"
M_"v[168:171],v[40:43],a[168:171],v[168:171]\n"
B_"v196,s[48:51],s75 offen offset:0  lds\n"
T_"v[12:13],v192 offset:12288\n"
M_"v[168:171],v[44:47],a[172:175],v[168:171]\n"
T_"v[14:15],v192 offset:12800\n"
T_"v[16:17],v192 offset:16384\n"
M_"v[172:175],v[32:35],a[176:179],v[172:175]\n"
T_"v[18:19],v192 offset:16896\n"
T_"v[20:21],v192 offset:20480\n"
M_"v[172:175],v[36:39],a[180:183],v[172:175]\n"
T_"v[22:23],v192 offset:20992\n"
"v_cvt_u32_f32 v207,v207\n"
"v_add_u32 v207,s22,v207\n"
M_"v[172:175],v[40:43],a[184:187],v[172:175]\n"
"v_xor_b32 v206,s32,v207\n"
"v_add_f32 v250,1.0,v250\n"
M_"v[172:175],v[44:47],a[188:191],v[172:175]\n"
T_"v[24:25],v192 offset:24576\n"
U_"v134,v134,v249\n"
M_"v[184:187],v[48:51],a[160:163],v[184:187]\n"
B_"v197,s[48:51],s75 offen offset:1024  lds\n"
T_"v[26:27],v192 offset:25088\n"
M_"v[184:187],v[52:55],a[164:167],v[184:187]\n"
"buffer_store_dwordx2 v[206:207],off,s[52:55],s78 offset:12 sc1\n"
M_"v[184:187],v[56:59],a[168:171],v[184:187]\n"
"v_rcp_f32 v250,v250\n"
M_"v[184:187],v[60:63],a[172:175],v[184:187]\n"
T_"v[28:29],v192 offset:28672\n"
U_"v247,v247,v136\n"
M_"v[188:191],v[48:51],a[176:179],v[188:191]\n"
T_"v[30:31],v192 offset:29184\n"
T_"v[32:33],v192 offset:32768\n"
T_"v[34:35],v192 offset:33280\n"
M_"v[188:191],v[52:55],a[180:183],v[188:191]\n"
T_"v[36:37],v192 offset:36864\n"
T_"v[38:39],v192 offset:37376\n"
M_"v[188:191],v[56:59],a[184:187],v[188:191]\n"
B_"v198,s[48:51],s75 offen offset:2048  lds\n"
M_"v[188:191],v[60:63],a[188:191],v[188:191]\n"
W_"lgkmcnt(12)\n"
T_"v[40:41],v192 offset:40960\n"
E_"v247,v247\n"
M_"v[64:67],v[0:3],a[192:195],0\n"
T_"v[42:43],v192 offset:41472\n"
U_"v248,v137,v137\n"
M_"v[64:67],v[4:7],a[196:199],v[64:67]\n"
"v_fma_f32 v248,v248,s76,v208\n"
"v_add_f32 v247,1.0,v247\n"
M_"v[64:67],v[8:11],a[200:203],v[64:67]\n"
T_"v[44:45],v192 offset:45056\n"
U_"v135,v135,v250\n"
M_"v[64:67],v[12:15],a[204:207],v[64:67]\n"
"v_rcp_f32 v247,v247\n"
M_"v[68:71],v[0:3],a[208:211],0\n"
B_"v199,s[48:51],s75 offen offset:3072  lds\n"
M_"v[68:71],v[4:7],a[212:215],v[68:71]\n"
T_"v[46:47],v192 offset:45568\n"
T_"v[48:49],v192 offset:49152\n"
T_"v[50:51],v192 offset:49664\n"
M_"v[68:71],v[8:11],a[216:219],v[68:71]\n"
T_"v[52:53],v192 offset:53248\n"
T_"v[54:55],v192 offset:53760\n"
M_"v[68:71],v[12:15],a[220:223],v[68:71]\n"
T_"v[56:57],v192 offset:57344\n"
U_"v248,v248,v137\n"
M_"v[72:75],v[0:3],a[224:227],0\n"
T_"v[58:59],v192 offset:57856\n"
E_"v248,v248\n"
M_"v[72:75],v[4:7],a[228:231],v[72:75]\n"
U_"v249,v138,v138\n"
"v_fma_f32 v249,v249,s76,v208\n"
M_"v[72:75],v[8:11],a[232:235],v[72:75]\n"
"s_mov_b64 s[40:41],s[12:13]\n"
T_"v[60:61],v192 offset:61440\n"
"v_add_f32 v248,1.0,v248\n"
M_"v[72:75],v[12:15],a[236:239],v[72:75]\n"
A_"s75,s69,262144\n"
U_"v136,v136,v247\n"
"v_rcp_f32 v248,v248\n"
M_"v[76:79],v[0:3],a[240:243],0\n"
"s_mov_b32 s42,0x400000\n"
T_"v[62:63],v192 offset:61952\n"
U_"v249,v249,v138\n"
M_"v[76:79],v[4:7],a[244:247],v[76:79]\n"
A_"s90,s90,128\n"
E_"v249,v249\n"
M_"v[76:79],v[8:11],a[248:251],v[76:79]\n"
A_"m0,s74,4096\n"
U_"v250,v139,v139\n"
"v_fma_f32 v250,v250,s76,v208\n"
M_"v[76:79],v[12:15],a[252:255],v[76:79]\n"
W_"lgkmcnt(15)\n"
"v_mul_u32_u24 v232,8192,v228\n"
"v_add_f32 v249,1.0,v249\n"
U_"v137,v137,v248\n"
M_"v[64:67],v[16:19],a[64:67],v[64:67]\n"
"buffer_load_dword v232,v232,s[40:43],s90 offen\n"
M_"v[64:67],v[20:23],a[68:71],v[64:67]\n"
B_"v196,s[48:51],s75 offen offset:0  lds\n"
M_"v[64:67],v[24:27],a[72:75],v[64:67]\n"
T_"v[0:1],v193 offset:0\n"
"v_rcp_f32 v249,v249\n"
M_"v[64:67],v[28:31],a[76:79],v[64:67]\n"
T_"v[2:3],v193 offset:512\n"
T_"v[4:5],v193 offset:4096\n"
M_"v[68:71],v[16:19],a[80:83],v[68:71]\n"
U_"v250,v250,v139\n"
M_"v[68:71],v[20:23],a[84:87],v[68:71]\n"
T_"v[6:7],v193 offset:4608\n"
E_"v250,v250\n"
M_"v[68:71],v[24:27],a[88:91],v[68:71]\n"
U_"v247,v140,v140\n"
"v_fma_f32 v247,v247,s76,v208\n"
M_"v[68:71],v[28:31],a[92:95],v[68:71]\n"
B_"v197,s[48:51],s75 offen offset:1024  lds\n"
M_"v[72:75],v[16:19],a[96:99],v[72:75]\n"
T_"v[8:9],v193 offset:8192\n"
T_"v[10:11],v193 offset:8704\n"
"v_add_f32 v250,1.0,v250\n"
M_"v[72:75],v[20:23],a[100:103],v[72:75]\n"
U_"v138,v138,v249\n"
M_"v[72:75],v[24:27],a[104:107],v[72:75]\n"
"v_rcp_f32 v250,v250\n"
M_"v[72:75],v[28:31],a[108:111],v[72:75]\n"
T_"v[12:13],v193 offset:12288\n"
U_"v247,v247,v140\n"
M_"v[76:79],v[16:19],a[112:115],v[76:79]\n"
E_"v247,v247\n"
M_"v[76:79],v[20:23],a[116:119],v[76:79]\n"
B_"v198,s[48:51],s75 offen offset:2048  lds\n"
M_"v[76:79],v[24:27],a[120:123],v[76:79]\n"
T_"v[14:15],v193 offset:12800\n"
U_"v248,v141,v141\n"
"v_fma_f32 v248,v248,s76,v208\n"
M_"v[76:79],v[28:31],a[124:127],v[76:79]\n"
W_"lgkmcnt(15)\n"
"v_add_f32 v247,1.0,v247\n"
U_"v139,v139,v250\n"
M_"v[64:67],v[32:35],a[0:3],v[64:67]\n"
"v_rcp_f32 v247,v247\n"
M_"v[64:67],v[36:39],a[4:7],v[64:67]\n"
T_"v[16:17],v193 offset:16384\n"
T_"v[18:19],v193 offset:16896\n"
M_"v[64:67],v[40:43],a[8:11],v[64:67]\n"
U_"v248,v248,v141\n"
E_"v248,v248\n"
M_"v[64:67],v[44:47],a[12:15],v[64:67]\n"
B_"v199,s[48:51],s75 offen offset:3072  lds\n"
M_"v[68:71],v[32:35],a[16:19],v[68:71]\n"
T_"v[20:21],v193 offset:20480\n"
U_"v249,v142,v142\n"
M_"v[68:71],v[36:39],a[20:23],v[68:71]\n"
T_"v[22:23],v193 offset:20992\n"
"v_fma_f32 v249,v249,s76,v208\n"
"v_add_f32 v248,1.0,v248\n"
M_"v[68:71],v[40:43],a[24:27],v[68:71]\n"
U_"v140,v140,v247\n"
M_"v[68:71],v[44:47],a[28:31],v[68:71]\n"
"v_rcp_f32 v248,v248\n"
M_"v[72:75],v[32:35],a[32:35],v[72:75]\n"
T_"v[24:25],v193 offset:24576\n"
T_"v[26:27],v193 offset:25088\n"
U_"v249,v249,v142\n"
M_"v[72:75],v[36:39],a[36:39],v[72:75]\n"
E_"v249,v249\n"
M_"v[72:75],v[40:43],a[40:43],v[72:75]\n"
A_"s75,s69,524288\n"
T_"v[28:29],v193 offset:28672\n"
U_"v250,v143,v143\n"
M_"v[72:75],v[44:47],a[44:47],v[72:75]\n"
"v_fma_f32 v250,v250,s76,v208\n"
"v_add_f32 v249,1.0,v249\n"
U_"v141,v141,v248\n"
M_"v[76:79],v[32:35],a[48:51],v[76:79]\n"
"v_rcp_f32 v249,v249\n"
M_"v[76:79],v[36:39],a[52:55],v[76:79]\n"
A_"m0,s74,8192\n"
T_"v[30:31],v193 offset:29184\n"
U_"v250,v250,v143\n"
M_"v[76:79],v[40:43],a[56:59],v[76:79]\n"
E_"v250,v250\n"
U_"v247,v144,v144\n"
M_"v[76:79],v[44:47],a[60:63],v[76:79]\n"
W_"lgkmcnt(15)\n"
"v_fma_f32 v247,v247,s76,v208\n"
"v_add_f32 v250,1.0,v250\n"
M_"v[64:67],v[48:51],a[128:131],v[64:67]\n"
B_"v196,s[48:51],s75 offen offset:0  lds\n"
M_"v[64:67],v[52:55],a[132:135],v[64:67]\n"
T_"v[32:33],v193 offset:32768\n"
T_"v[34:35],v193 offset:33280\n"
U_"v142,v142,v249\n"
M_"v[64:67],v[56:59],a[136:139],v[64:67]\n"
"v_rcp_f32 v250,v250\n"
M_"v[64:67],v[60:63],a[140:143],v[64:67]\n"
T_"v[36:37],v193 offset:36864\n"
U_"v247,v247,v144\n"
M_"v[68:71],v[48:51],a[144:147],v[68:71]\n"
E_"v247,v247\n"
U_"v248,v145,v145\n"
M_"v[68:71],v[52:55],a[148:151],v[68:71]\n"
T_"v[38:39],v193 offset:37376\n"
"v_fma_f32 v248,v248,s76,v208\n"
M_"v[68:71],v[56:59],a[152:155],v[68:71]\n"
B_"v197,s[48:51],s75 offen offset:1024  lds\n"
M_"v[68:71],v[60:63],a[156:159],v[68:71]\n"
T_"v[40:41],v193 offset:40960\n"
"v_add_f32 v247,1.0,v247\n"
M_"v[72:75],v[48:51],a[160:163],v[72:75]\n"
T_"v[42:43],v193 offset:41472\n"
U_"v143,v143,v250\n"
U_"v128,v128,v64\n"
M_"v[72:75],v[52:55],a[164:167],v[72:75]\n"
"v_rcp_f32 v247,v247\n"
M_"v[72:75],v[56:59],a[168:171],v[72:75]\n"
T_"v[44:45],v193 offset:45056\n"
U_"v248,v248,v145\n"
M_"v[72:75],v[60:63],a[172:175],v[72:75]\n"
U_"v129,v129,v65\n"
E_"v248,v248\n"
M_"v[76:79],v[48:51],a[176:179],v[76:79]\n"
B_"v198,s[48:51],s75 offen offset:2048  lds\n"
M_"v[76:79],v[52:55],a[180:183],v[76:79]\n"
"s_mov_b64 s[40:41],s[24:25]\n"
T_"v[46:47],v193 offset:45568\n"
U_"v249,v146,v146\n"
M_"v[76:79],v[56:59],a[184:187],v[76:79]\n"
"s_mov_b32 s42,0x1c0000\n"
"v_fma_f32 v249,v249,s76,v208\n"
"v_add_f32 v248,1.0,v248\n"
U_"v144,v144,v247\n"
M_"v[76:79],v[60:63],a[188:191],v[76:79]\n"
W_"lgkmcnt(15)\n"
"v_rcp_f32 v248,v248\n"
M_"v[80:83],v[0:3],a[192:195],0\n"
"s_bfe_u32 s88,s30,0x20001\n"
T_"v[48:49],v193 offset:49152\n"
U_"v249,v249,v146\n"
M_"v[80:83],v[4:7],a[196:199],v[80:83]\n"
"s_lshl3_add_u32 s88,s88,s34\n"
T_"v[50:51],v193 offset:49664\n"
E_"v249,v249\n"
M_"v[80:83],v[8:11],a[200:203],v[80:83]\n"
"s_lshl2_add_u32 s88,s88,s31\n"
U_"v250,v147,v147\n"
"v_fma_f32 v250,v250,s76,v208\n"
M_"v[80:83],v[12:15],a[204:207],v[80:83]\n"
"s_mul_i32 s88,s88,14336\n"
T_"v[52:53],v193 offset:53248\n"
"v_add_f32 v249,1.0,v249\n"
M_"v[84:87],v[0:3],a[208:211],0\n"
B_"v199,s[48:51],s75 offen offset:3072  lds\n"
U_"v145,v145,v248\n"
M_"v[84:87],v[4:7],a[212:215],v[84:87]\n"
"s_and_b32 s90,s33,3\n"
"s_lshl_b32 s90,s90,8\n"
A_"s88,s88,s90\n"
M_"v[84:87],v[8:11],a[216:219],v[84:87]\n"
"v_and_b32 v255,0x3c0,v244\n"
"v_add_u32 v255,v255,v244\n"
"v_and_b32 v255,0x790,v255\n"
"v_mul_u32_u24 v255,8,v255\n"
"buffer_load_dword v255,v255,s[40:43],s88 offen\n"
M_"v[84:87],v[12:15],a[220:223],v[84:87]\n"
T_"v[54:55],v193 offset:53760\n"
T_"v[56:57],v193 offset:57344\n"
U_"v130,v130,v66\n"
M_"v[88:91],v[0:3],a[224:227],0\n"
T_"v[58:59],v193 offset:57856\n"
U_"v131,v131,v67\n"
M_"v[88:91],v[4:7],a[228:231],v[88:91]\n"
A_"s75,s69,786432\n"
"v_rcp_f32 v249,v249\n"
M_"v[88:91],v[8:11],a[232:235],v[88:91]\n"
T_"v[60:61],v193 offset:61440\n"
U_"v250,v250,v147\n"
M_"v[88:91],v[12:15],a[236:239],v[88:91]\n"
C_"v128,v128,v129\n"
E_"v250,v250\n"
M_"v[92:95],v[0:3],a[240:243],0\n"
A_"m0,s74,12288\n"
T_"v[62:63],v193 offset:61952\n"
U_"v247,v148,v148\n"
M_"v[92:95],v[4:7],a[244:247],v[92:95]\n"
"v_fma_f32 v247,v247,s76,v208\n"
"v_add_f32 v250,1.0,v250\n"
M_"v[92:95],v[8:11],a[248:251],v[92:95]\n"
U_"v146,v146,v249\n"
"v_rcp_f32 v250,v250\n"
M_"v[92:95],v[12:15],a[252:255],v[92:95]\n"
W_"lgkmcnt(15)\n"
U_"v247,v247,v148\n"
C_"v129,v130,v131\n"
M_"v[80:83],v[16:19],a[64:67],v[80:83]\n"
B_"v196,s[48:51],s75 offen offset:0  lds\n"
M_"v[80:83],v[20:23],a[68:71],v[80:83]\n"
T_"v[0:1],v194 offset:0\n"
T_"v[2:3],v194 offset:512\n"
U_"v132,v132,v68\n"
M_"v[80:83],v[24:27],a[72:75],v[80:83]\n"
E_"v247,v247\n"
M_"v[80:83],v[28:31],a[76:79],v[80:83]\n"
T_"v[4:5],v194 offset:4096\n"
U_"v248,v149,v149\n"
M_"v[84:87],v[16:19],a[80:83],v[84:87]\n"
"v_fma_f32 v248,v248,s76,v208\n"
"v_add_f32 v247,1.0,v247\n"
U_"v147,v147,v250\n"
M_"v[84:87],v[20:23],a[84:87],v[84:87]\n"
B_"v197,s[48:51],s75 offen offset:1024  lds\n"
M_"v[84:87],v[24:27],a[88:91],v[84:87]\n"
T_"v[6:7],v194 offset:4608\n"
U_"v133,v133,v69\n"
M_"v[84:87],v[28:31],a[92:95],v[84:87]\n"
T_"v[8:9],v194 offset:8192\n"
"v_rcp_f32 v247,v247\n"
M_"v[88:91],v[16:19],a[96:99],v[88:91]\n"
T_"v[10:11],v194 offset:8704\n"
U_"v248,v248,v149\n"
M_"v[88:91],v[20:23],a[100:103],v[88:91]\n"
E_"v248,v248\n"
M_"v[88:91],v[24:27],a[104:107],v[88:91]\n"
T_"v[12:13],v194 offset:12288\n"
U_"v249,v150,v150\n"
"v_fma_f32 v249,v249,s76,v208\n"
M_"v[88:91],v[28:31],a[108:111],v[88:91]\n"
B_"v198,s[48:51],s75 offen offset:2048  lds\n"
M_"v[92:95],v[16:19],a[112:115],v[92:95]\n"
T_"v[14:15],v194 offset:12800\n"
"v_add_f32 v248,1.0,v248\n"
M_"v[92:95],v[20:23],a[116:119],v[92:95]\n"
U_"v148,v148,v247\n"
U_"v134,v134,v70\n"
M_"v[92:95],v[24:27],a[120:123],v[92:95]\n"
U_"v135,v135,v71\n"
"v_rcp_f32 v248,v248\n"
M_"v[92:95],v[28:31],a[124:127],v[92:95]\n"
W_"lgkmcnt(15)\n"
U_"v249,v249,v150\n"
C_"v132,v132,v133\n"
M_"v[80:83],v[32:35],a[0:3],v[80:83]\n"
T_"v[16:17],v194 offset:16384\n"
C_"v133,v134,v135\n"
M_"v[80:83],v[36:39],a[4:7],v[80:83]\n"
B_"v199,s[48:51],s75 offen offset:3072  lds\n"
T_"v[18:19],v194 offset:16896\n"
M_"v[80:83],v[40:43],a[8:11],v[80:83]\n"
E_"v249,v249\n"
M_"v[80:83],v[44:47],a[12:15],v[80:83]\n"
T_"v[20:21],v194 offset:20480\n"
U_"v250,v151,v151\n"
M_"v[84:87],v[32:35],a[16:19],v[84:87]\n"
"s_mov_b64 s[40:41],s[18:19]\n"
"v_fma_f32 v250,v250,s76,v208\n"
"v_add_f32 v249,1.0,v249\n"
U_"v149,v149,v248\n"
M_"v[84:87],v[36:39],a[20:23],v[84:87]\n"
"s_mov_b32 s42,0x400000\n"
T_"v[22:23],v194 offset:20992\n"
U_"v136,v136,v72\n"
M_"v[84:87],v[40:43],a[24:27],v[84:87]\n"
"s_lshl2_add_u32 s88,s30,s31\n"
"v_rcp_f32 v249,v249\n"
M_"v[84:87],v[44:47],a[28:31],v[84:87]\n"
"s_lshl_b32 s88,s88,12\n"
T_"v[24:25],v194 offset:24576\n"
U_"v250,v250,v151\n"
U_"v137,v137,v73\n"
M_"v[88:91],v[32:35],a[32:35],v[88:91]\n"
T_"v[26:27],v194 offset:25088\n"
U_"v138,v138,v74\n"
M_"v[88:91],v[36:39],a[36:39],v[88:91]\n"
"v_lshrrev_b32 v251,6,v244\n"
U_"v139,v139,v75\n"
M_"v[88:91],v[40:43],a[40:43],v[88:91]\n"
"v_mul_u32_u24 v251,128,v251\n"
T_"v[28:29],v194 offset:28672\n"
C_"v136,v136,v137\n"
M_"v[88:91],v[44:47],a[44:47],v[88:91]\n"
"buffer_load_dword v251,v251,s[40:43],s88 offen\n"
M_"v[92:95],v[32:35],a[48:51],v[92:95]\n"
T_"v[30:31],v194 offset:29184\n"
C_"v137,v138,v139\n"
M_"v[92:95],v[36:39],a[52:55],v[92:95]\n"
U_"v140,v140,v76\n"
E_"v250,v250\n"
M_"v[92:95],v[40:43],a[56:59],v[92:95]\n"
U_"v247,v152,v152\n"
"v_fma_f32 v247,v247,s76,v208\n"
M_"v[92:95],v[44:47],a[60:63],v[92:95]\n"
W_"lgkmcnt(15)\n"
"v_add_f32 v250,1.0,v250\n"
U_"v150,v150,v249\n"
M_"v[80:83],v[48:51],a[128:131],v[80:83]\n"
T_"v[32:33],v194 offset:32768\n"
"v_rcp_f32 v250,v250\n"
M_"v[80:83],v[52:55],a[132:135],v[80:83]\n"
T_"v[34:35],v194 offset:33280\n"
U_"v247,v247,v152\n"
M_"v[80:83],v[56:59],a[136:139],v[80:83]\n"
E_"v247,v247\n"
M_"v[80:83],v[60:63],a[140:143],v[80:83]\n"
T_"v[36:37],v194 offset:36864\n"
U_"v248,v153,v153\n"
M_"v[84:87],v[48:51],a[144:147],v[84:87]\n"
"v_fma_f32 v248,v248,s76,v208\n"
"v_add_f32 v247,1.0,v247\n"
U_"v151,v151,v250\n"
M_"v[84:87],v[52:55],a[148:151],v[84:87]\n"
T_"v[38:39],v194 offset:37376\n"
U_"v141,v141,v77\n"
M_"v[84:87],v[56:59],a[152:155],v[84:87]\n"
"v_rcp_f32 v247,v247\n"
M_"v[84:87],v[60:63],a[156:159],v[84:87]\n"
T_"v[40:41],v194 offset:40960\n"
U_"v248,v248,v153\n"
U_"v142,v142,v78\n"
M_"v[88:91],v[48:51],a[160:163],v[88:91]\n"
"buffer_load_dword v234,v243,s[44:47],0 offen\n"
M_"v[88:91],v[52:55],a[164:167],v[88:91]\n"
T_"v[42:43],v194 offset:41472\n"
U_"v143,v143,v79\n"
M_"v[88:91],v[56:59],a[168:171],v[88:91]\n"
T_"v[44:45],v194 offset:45056\n"
E_"v248,v248\n"
M_"v[88:91],v[60:63],a[172:175],v[88:91]\n"
U_"v249,v154,v154\n"
"v_fma_f32 v249,v249,s76,v208\n"
M_"v[92:95],v[48:51],a[176:179],v[92:95]\n"
T_"v[46:47],v194 offset:45568\n"
"v_add_f32 v248,1.0,v248\n"
M_"v[92:95],v[52:55],a[180:183],v[92:95]\n"
U_"v152,v152,v247\n"
"v_rcp_f32 v248,v248\n"
M_"v[92:95],v[56:59],a[184:187],v[92:95]\n"
U_"v249,v249,v154\n"
C_"v140,v140,v141\n"
M_"v[92:95],v[60:63],a[188:191],v[92:95]\n"
W_"lgkmcnt(15)\n"
E_"v249,v249\n"
M_"v[96:99],v[0:3],a[192:195],0\n"
T_"v[48:49],v194 offset:49152\n"
U_"v250,v155,v155\n"
"v_fma_f32 v250,v250,s76,v208\n"
M_"v[96:99],v[4:7],a[196:199],v[96:99]\n"
T_"v[50:51],v194 offset:49664\n"
"v_add_f32 v249,1.0,v249\n"
M_"v[96:99],v[8:11],a[200:203],v[96:99]\n"
U_"v153,v153,v248\n"
C_"v141,v142,v143\n"
M_"v[96:99],v[12:15],a[204:207],v[96:99]\n"
T_"v[52:53],v194 offset:53248\n"
"v_rcp_f32 v249,v249\n"
M_"v[100:103],v[0:3],a[208:211],0\n"
U_"v250,v250,v155\n"
U_"v144,v144,v80\n"
M_"v[100:103],v[4:7],a[212:215],v[100:103]\n"
T_"v[54:55],v194 offset:53760\n"
U_"v145,v145,v81\n"
M_"v[100:103],v[8:11],a[216:219],v[100:103]\n"
E_"v250,v250\n"
M_"v[100:103],v[12:15],a[220:223],v[100:103]\n"
T_"v[56:57],v194 offset:57344\n"
U_"v247,v156,v156\n"
"v_fma_f32 v247,v247,s76,v208\n"
M_"v[104:107],v[0:3],a[224:227],0\n"
T_"v[58:59],v194 offset:57856\n"
"v_add_f32 v250,1.0,v250\n"
M_"v[104:107],v[4:7],a[228:231],v[104:107]\n"
U_"v154,v154,v249\n"
U_"v146,v146,v82\n"
M_"v[104:107],v[8:11],a[232:235],v[104:107]\n"
T_"v[60:61],v194 offset:61440\n"
"v_rcp_f32 v250,v250\n"
M_"v[104:107],v[12:15],a[236:239],v[104:107]\n"
U_"v247,v247,v156\n"
U_"v147,v147,v83\n"
M_"v[108:111],v[0:3],a[240:243],0\n"
T_"v[62:63],v194 offset:61952\n"
C_"v130,v144,v145\n"
M_"v[108:111],v[4:7],a[244:247],v[108:111]\n"
C_"v131,v146,v147\n"
E_"v247,v247\n"
M_"v[108:111],v[8:11],a[248:251],v[108:111]\n"
S_"v[128:131],v244,s[52:55],s36 offen offset:0 sc1\n"
M_"v[108:111],v[12:15],a[252:255],v[108:111]\n"
W_"lgkmcnt(15)\n"
U_"v248,v157,v157\n"
"v_fma_f32 v248,v248,s76,v208\n"
M_"v[96:99],v[16:19],a[64:67],v[96:99]\n"
T_"v[0:1],v195 offset:0\n"
"v_add_f32 v247,1.0,v247\n"
U_"v155,v155,v250\n"
M_"v[96:99],v[20:23],a[68:71],v[96:99]\n"
T_"v[2:3],v195 offset:512\n"
U_"v148,v148,v84\n"
M_"v[96:99],v[24:27],a[72:75],v[96:99]\n"
"v_rcp_f32 v247,v247\n"
M_"v[96:99],v[28:31],a[76:79],v[96:99]\n"
T_"v[4:5],v195 offset:4096\n"
U_"v248,v248,v157\n"
U_"v149,v149,v85\n"
M_"v[100:103],v[16:19],a[80:83],v[100:103]\n"
E_"v248,v248\n"
M_"v[100:103],v[20:23],a[84:87],v[100:103]\n"
T_"v[6:7],v195 offset:4608\n"
U_"v249,v158,v158\n"
M_"v[100:103],v[24:27],a[88:91],v[100:103]\n"
"v_fma_f32 v249,v249,s76,v208\n"
"v_add_f32 v248,1.0,v248\n"
U_"v156,v156,v247\n"
M_"v[100:103],v[28:31],a[92:95],v[100:103]\n"
T_"v[8:9],v195 offset:8192\n"
U_"v150,v150,v86\n"
M_"v[104:107],v[16:19],a[96:99],v[104:107]\n"
T_"v[10:11],v195 offset:8704\n"
U_"v151,v151,v87\n"
M_"v[104:107],v[20:23],a[100:103],v[104:107]\n"
"v_rcp_f32 v248,v248\n"
M_"v[104:107],v[24:27],a[104:107],v[104:107]\n"
T_"v[12:13],v195 offset:12288\n"
U_"v249,v249,v158\n"
C_"v134,v148,v149\n"
M_"v[104:107],v[28:31],a[108:111],v[104:107]\n"
E_"v249,v249\n"
M_"v[108:111],v[16:19],a[112:115],v[108:111]\n"
T_"v[14:15],v195 offset:12800\n"
U_"v250,v159,v159\n"
M_"v[108:111],v[20:23],a[116:119],v[108:111]\n"
"v_fma_f32 v250,v250,s76,v208\n"
"v_add_f32 v249,1.0,v249\n"
U_"v157,v157,v248\n"
M_"v[108:111],v[24:27],a[120:123],v[108:111]\n"
"v_rcp_f32 v249,v249\n"
M_"v[108:111],v[28:31],a[124:127],v[108:111]\n"
W_"lgkmcnt(15)\n"
U_"v250,v250,v159\n"
C_"v135,v150,v151\n"
M_"v[96:99],v[32:35],a[0:3],v[96:99]\n"
S_"v[132:135],v244,s[52:55],s37 offen offset:0 sc1\n"
T_"v[16:17],v195 offset:16384\n"
M_"v[96:99],v[36:39],a[4:7],v[96:99]\n"
T_"v[18:19],v195 offset:16896\n"
U_"v152,v152,v88\n"
M_"v[96:99],v[40:43],a[8:11],v[96:99]\n"
E_"v250,v250\n"
M_"v[96:99],v[44:47],a[12:15],v[96:99]\n"
T_"v[20:21],v195 offset:20480\n"
U_"v247,v160,v160\n"
"v_fma_f32 v247,v247,s76,v208\n"
M_"v[100:103],v[32:35],a[16:19],v[100:103]\n"
"v_add_f32 v250,1.0,v250\n"
U_"v158,v158,v249\n"
M_"v[100:103],v[36:39],a[20:23],v[100:103]\n"
T_"v[22:23],v195 offset:20992\n"
U_"v153,v153,v89\n"
M_"v[100:103],v[40:43],a[24:27],v[100:103]\n"
U_"v154,v154,v90\n"
"v_rcp_f32 v250,v250\n"
M_"v[100:103],v[44:47],a[28:31],v[100:103]\n"
T_"v[24:25],v195 offset:24576\n"
U_"v247,v247,v160\n"
M_"v[104:107],v[32:35],a[32:35],v[104:107]\n"
T_"v[26:27],v195 offset:25088\n"
U_"v155,v155,v91\n"
M_"v[104:107],v[36:39],a[36:39],v[104:107]\n"
C_"v138,v152,v153\n"
E_"v247,v247\n"
M_"v[104:107],v[40:43],a[40:43],v[104:107]\n"
T_"v[28:29],v195 offset:28672\n"
U_"v248,v161,v161\n"
M_"v[104:107],v[44:47],a[44:47],v[104:107]\n"
"v_fma_f32 v248,v248,s76,v208\n"
"v_add_f32 v247,1.0,v247\n"
M_"v[108:111],v[32:35],a[48:51],v[108:111]\n"
T_"v[30:31],v195 offset:29184\n"
U_"v159,v159,v250\n"
M_"v[108:111],v[36:39],a[52:55],v[108:111]\n"
C_"v139,v154,v155\n"
"v_rcp_f32 v247,v247\n"
M_"v[108:111],v[40:43],a[56:59],v[108:111]\n"
S_"v[136:139],v244,s[52:55],s38 offen offset:0 sc1\n"
M_"v[108:111],v[44:47],a[60:63],v[108:111]\n"
W_"lgkmcnt(15)\n"
U_"v248,v248,v161\n"
U_"v156,v156,v92\n"
M_"v[96:99],v[48:51],a[128:131],v[96:99]\n"
T_"v[32:33],v195 offset:32768\n"
E_"v248,v248\n"
M_"v[96:99],v[52:55],a[132:135],v[96:99]\n"
T_"v[34:35],v195 offset:33280\n"
U_"v249,v162,v162\n"
M_"v[96:99],v[56:59],a[136:139],v[96:99]\n"
"v_fma_f32 v249,v249,s76,v208\n"
"v_add_f32 v248,1.0,v248\n"
M_"v[96:99],v[60:63],a[140:143],v[96:99]\n"
T_"v[36:37],v195 offset:36864\n"
U_"v160,v160,v247\n"
U_"v157,v157,v93\n"
M_"v[100:103],v[48:51],a[144:147],v[100:103]\n"
"v_rcp_f32 v248,v248\n"
M_"v[100:103],v[52:55],a[148:151],v[100:103]\n"
T_"v[38:39],v195 offset:37376\n"
U_"v249,v249,v162\n"
M_"v[100:103],v[56:59],a[152:155],v[100:103]\n"
U_"v158,v158,v94\n"
E_"v249,v249\n"
M_"v[100:103],v[60:63],a[156:159],v[100:103]\n"
T_"v[40:41],v195 offset:40960\n"
U_"v250,v163,v163\n"
M_"v[104:107],v[48:51],a[160:163],v[104:107]\n"
T_"v[42:43],v195 offset:41472\n"
"v_fma_f32 v250,v250,s76,v208\n"
M_"v[104:107],v[52:55],a[164:167],v[104:107]\n"
"v_add_f32 v249,1.0,v249\n"
U_"v161,v161,v248\n"
U_"v159,v159,v95\n"
M_"v[104:107],v[56:59],a[168:171],v[104:107]\n"
T_"v[44:45],v195 offset:45056\n"
C_"v142,v156,v157\n"
M_"v[104:107],v[60:63],a[172:175],v[104:107]\n"
"v_rcp_f32 v249,v249\n"
M_"v[108:111],v[48:51],a[176:179],v[108:111]\n"
T_"v[46:47],v195 offset:45568\n"
U_"v250,v250,v163\n"
C_"v143,v158,v159\n"
M_"v[108:111],v[52:55],a[180:183],v[108:111]\n"
S_"v[140:143],v244,s[52:55],s39 offen offset:0 sc1\n"
M_"v[108:111],v[56:59],a[184:187],v[108:111]\n"
E_"v250,v250\n"
M_"v[108:111],v[60:63],a[188:191],v[108:111]\n"
W_"lgkmcnt(15)\n"
U_"v247,v164,v164\n"
"v_fma_f32 v247,v247,s76,v208\n"
"v_add_f32 v250,1.0,v250\n"
M_"v[112:115],v[0:3],a[192:195],0\n"
T_"v[48:49],v195 offset:49152\n"
U_"v162,v162,v249\n"
M_"v[112:115],v[4:7],a[196:199],v[112:115]\n"
"v_rcp_f32 v250,v250\n"
M_"v[112:115],v[8:11],a[200:203],v[112:115]\n"
T_"v[50:51],v195 offset:49664\n"
T_"v[52:53],v195 offset:53248\n"
M_"v[112:115],v[12:15],a[204:207],v[112:115]\n"
U_"v247,v247,v164\n"
E_"v247,v247\n"
M_"v[116:119],v[0:3],a[208:211],0\n"
T_"v[54:55],v195 offset:53760\n"
U_"v248,v165,v165\n"
M_"v[116:119],v[4:7],a[212:215],v[116:119]\n"
T_"v[56:57],v195 offset:57344\n"
"v_fma_f32 v248,v248,s76,v208\n"
M_"v[116:119],v[8:11],a[216:219],v[116:119]\n"
T_"v[58:59],v195 offset:57856\n"
"v_add_f32 v247,1.0,v247\n"
U_"v163,v163,v250\n"
M_"v[116:119],v[12:15],a[220:223],v[116:119]\n"
"v_rcp_f32 v247,v247\n"
M_"v[120:123],v[0:3],a[224:227],0\n"
T_"v[60:61],v195 offset:61440\n"
U_"v248,v248,v165\n"
M_"v[120:123],v[4:7],a[228:231],v[120:123]\n"
T_"v[62:63],v195 offset:61952\n"
E_"v248,v248\n"
M_"v[120:123],v[8:11],a[232:235],v[120:123]\n"
U_"v249,v166,v166\n"
"v_fma_f32 v249,v249,s76,v208\n"
M_"v[120:123],v[12:15],a[236:239],v[120:123]\n"
"v_add_f32 v248,1.0,v248\n"
U_"v164,v164,v247\n"
M_"v[124:127],v[0:3],a[240:243],0\n"
U_"v160,v160,v96\n"
"v_rcp_f32 v248,v248\n"
M_"v[124:127],v[4:7],a[244:247],v[124:127]\n"
U_"v249,v249,v166\n"
U_"v161,v161,v97\n"
M_"v[124:127],v[8:11],a[248:251],v[124:127]\n"
E_"v249,v249\n"
M_"v[124:127],v[12:15],a[252:255],v[124:127]\n"
W_"lgkmcnt(15)\n"
U_"v250,v167,v167\n"
"v_fma_f32 v250,v250,s76,v208\n"
"v_add_f32 v249,1.0,v249\n"
M_"v[112:115],v[16:19],a[64:67],v[112:115]\n"
U_"v165,v165,v248\n"
U_"v162,v162,v98\n"
M_"v[112:115],v[20:23],a[68:71],v[112:115]\n"
"v_rcp_f32 v249,v249\n"
M_"v[112:115],v[24:27],a[72:75],v[112:115]\n"
"v_bfe_u32 v204,v244,4,3\n"
U_"v250,v250,v167\n"
U_"v163,v163,v99\n"
M_"v[112:115],v[28:31],a[76:79],v[112:115]\n"
E_"v250,v250\n"
M_"v[116:119],v[16:19],a[80:83],v[116:119]\n"
"L_ps0:\n"
"s_mov_b64 exec,0\n"
"s_cmp_eq_u32 s31,0\n"
"s_cbranch_scc1 L_pt0_0\n"
"s_cmp_eq_u32 s31,1\n"
"s_cbranch_scc1 L_pt0_1\n"
"s_cmp_eq_u32 s31,2\n"
"s_cbranch_scc1 L_pt0_2\n"
"s_branch L_pt0_3\n"
"L_pr0:\n"
"s_mov_b64 exec,-1\n"
W_"vmcnt(6) lgkmcnt(0)\n"
"s_barrier\n"
U_"v247,v168,v168\n"
M_"v[116:119],v[20:23],a[84:87],v[116:119]\n"
"v_and_b32 v205,128,v244\n"
"v_xor_b32 v192,0x10000,v192\n"
M_"v[116:119],v[24:27],a[88:91],v[116:119]\n"
A_"s75,s69,128\n"
"v_xor_b32 v193,0x10000,v193\n"
"v_fma_f32 v247,v247,s76,v208\n"
"v_add_f32 v250,1.0,v250\n"
M_"v[116:119],v[28:31],a[92:95],v[116:119]\n"
"v_xor_b32 v194,0x10000,v194\n"
U_"v166,v166,v249\n"
M_"v[120:123],v[16:19],a[96:99],v[120:123]\n"
A_"m0,s74,65536\n"
"v_xor_b32 v195,0x10000,v195\n"
C_"v160,v160,v161\n"
M_"v[120:123],v[20:23],a[100:103],v[120:123]\n"
"v_add_lshl_u32 v205,v205,v204,7\n"
T_"v[0:1],v192 offset:0\n"
C_"v161,v162,v163\n"
M_"v[120:123],v[24:27],a[104:107],v[120:123]\n"
"v_rcp_f32 v250,v250\n"
M_"v[120:123],v[28:31],a[108:111],v[120:123]\n"
B_"v196,s[48:51],s75 offen offset:0  lds\n"
M_"v[124:127],v[16:19],a[112:115],v[124:127]\n"
T_"v[2:3],v192 offset:512\n"
T_"v[4:5],v192 offset:4096\n"
U_"v247,v247,v168\n"
M_"v[124:127],v[20:23],a[116:119],v[124:127]\n"
"s_lshr_b32 s79,s33,1\n"
E_"v247,v247\n"
M_"v[124:127],v[24:27],a[120:123],v[124:127]\n"
T_"v[6:7],v192 offset:4608\n"
T_"v[8:9],v192 offset:8192\n"
M_"v[124:127],v[28:31],a[124:127],v[124:127]\n"
T_"v[10:11],v192 offset:8704\n"
U_"v248,v169,v169\n"
"v_fma_f32 v248,v248,s76,v208\n"
M_"v[112:115],v[32:35],a[0:3],v[112:115]\n"
T_"v[12:13],v192 offset:12288\n"
"v_add_f32 v247,1.0,v247\n"
M_"v[112:115],v[36:39],a[4:7],v[112:115]\n"
B_"v197,s[48:51],s75 offen offset:1024  lds\n"
M_"v[112:115],v[40:43],a[8:11],v[112:115]\n"
"s_lshl_b32 s79,s79,10\n"
T_"v[14:15],v192 offset:12800\n"
T_"v[16:17],v192 offset:16384\n"
T_"v[18:19],v192 offset:16896\n"
M_"v[112:115],v[44:47],a[12:15],v[112:115]\n"
T_"v[20:21],v192 offset:20480\n"
U_"v167,v167,v250\n"
M_"v[116:119],v[32:35],a[16:19],v[116:119]\n"
A_"s79,s79,0x4f20000\n"
"v_rcp_f32 v247,v247\n"
M_"v[116:119],v[36:39],a[20:23],v[116:119]\n"
T_"v[22:23],v192 offset:20992\n"
U_"v248,v248,v169\n"
U_"v164,v164,v100\n"
M_"v[116:119],v[40:43],a[24:27],v[116:119]\n"
B_"v198,s[48:51],s75 offen offset:2048  lds\n"
M_"v[116:119],v[44:47],a[28:31],v[116:119]\n"
T_"v[24:25],v192 offset:24576\n"
U_"v165,v165,v101\n"
M_"v[120:123],v[32:35],a[32:35],v[120:123]\n"
"buffer_load_dword v204,v205,s[52:55],s79 offen sc1\n"
M_"v[120:123],v[36:39],a[36:39],v[120:123]\n"
T_"v[26:27],v192 offset:25088\n"
E_"v248,v248\n"
M_"v[120:123],v[40:43],a[40:43],v[120:123]\n"
T_"v[28:29],v192 offset:28672\n"
U_"v249,v170,v170\n"
M_"v[120:123],v[44:47],a[44:47],v[120:123]\n"
"v_fma_f32 v249,v249,s76,v208\n"
"v_add_f32 v248,1.0,v248\n"
M_"v[124:127],v[32:35],a[48:51],v[124:127]\n"
B_"v199,s[48:51],s75 offen offset:3072  lds\n"
T_"v[30:31],v192 offset:29184\n"
M_"v[124:127],v[36:39],a[52:55],v[124:127]\n"
U_"v168,v168,v247\n"
U_"v166,v166,v102\n"
M_"v[124:127],v[40:43],a[56:59],v[124:127]\n"
"v_rcp_f32 v248,v248\n"
M_"v[124:127],v[44:47],a[60:63],v[124:127]\n"
U_"v249,v249,v170\n"
E_"v249,v249\n"
M_"v[112:115],v[48:51],a[128:131],v[112:115]\n"
T_"v[32:33],v192 offset:32768\n"
U_"v250,v171,v171\n"
M_"v[112:115],v[52:55],a[132:135],v[112:115]\n"
A_"s75,s69,262272\n"
T_"v[34:35],v192 offset:33280\n"
"v_fma_f32 v250,v250,s76,v208\n"
M_"v[112:115],v[56:59],a[136:139],v[112:115]\n"
"v_add_f32 v249,1.0,v249\n"
U_"v169,v169,v248\n"
U_"v167,v167,v103\n"
M_"v[112:115],v[60:63],a[140:143],v[112:115]\n"
T_"v[36:37],v192 offset:36864\n"
C_"v164,v164,v165\n"
M_"v[116:119],v[48:51],a[144:147],v[116:119]\n"
A_"m0,s74,69632\n"
"v_rcp_f32 v249,v249\n"
M_"v[116:119],v[52:55],a[148:151],v[116:119]\n"
T_"v[38:39],v192 offset:37376\n"
U_"v250,v250,v171\n"
C_"v165,v166,v167\n"
M_"v[116:119],v[56:59],a[152:155],v[116:119]\n"
E_"v250,v250\n"
M_"v[116:119],v[60:63],a[156:159],v[116:119]\n"
B_"v196,s[48:51],s75 offen offset:0  lds\n"
M_"v[120:123],v[48:51],a[160:163],v[120:123]\n"
T_"v[40:41],v192 offset:40960\n"
T_"v[42:43],v192 offset:41472\n"
U_"v247,v172,v172\n"
M_"v[120:123],v[52:55],a[164:167],v[120:123]\n"
"v_fma_f32 v247,v247,s76,v208\n"
"v_add_f32 v250,1.0,v250\n"
M_"v[120:123],v[56:59],a[168:171],v[120:123]\n"
T_"v[44:45],v192 offset:45056\n"
U_"v170,v170,v249\n"
M_"v[120:123],v[60:63],a[172:175],v[120:123]\n"
"v_rcp_f32 v250,v250\n"
U_"v247,v247,v172\n"
M_"v[124:127],v[48:51],a[176:179],v[124:127]\n"
B_"v197,s[48:51],s75 offen offset:1024  lds\n"
M_"v[124:127],v[52:55],a[180:183],v[124:127]\n"
E_"v247,v247\n"
M_"v[124:127],v[56:59],a[184:187],v[124:127]\n"
T_"v[46:47],v192 offset:45568\n"
U_"v248,v173,v173\n"
M_"v[124:127],v[60:63],a[188:191],v[124:127]\n"
W_"lgkmcnt(15)\n"
"v_fma_f32 v248,v248,s76,v208\n"
"v_add_f32 v247,1.0,v247\n"
U_"v171,v171,v250\n"
M_"v[64:67],v[0:3],a[192:195],0\n"
T_"v[48:49],v192 offset:49152\n"
U_"v168,v168,v104\n"
M_"v[64:67],v[4:7],a[196:199],v[64:67]\n"
T_"v[50:51],v192 offset:49664\n"
U_"v169,v169,v105\n"
M_"v[64:67],v[8:11],a[200:203],v[64:67]\n"
B_"v198,s[48:51],s75 offen offset:2048  lds\n"
U_"v170,v170,v106\n"
M_"v[64:67],v[12:15],a[204:207],v[64:67]\n"
T_"v[52:53],v192 offset:53248\n"
U_"v171,v171,v107\n"
M_"v[68:71],v[0:3],a[208:211],0\n"
"v_rcp_f32 v247,v247\n"
M_"v[68:71],v[4:7],a[212:215],v[68:71]\n"
T_"v[54:55],v192 offset:53760\n"
U_"v248,v248,v173\n"
C_"v168,v168,v169\n"
M_"v[68:71],v[8:11],a[216:219],v[68:71]\n"
E_"v248,v248\n"
M_"v[68:71],v[12:15],a[220:223],v[68:71]\n"
B_"v199,s[48:51],s75 offen offset:3072  lds\n"
M_"v[72:75],v[0:3],a[224:227],0\n"
T_"v[56:57],v192 offset:57344\n"
T_"v[58:59],v192 offset:57856\n"
U_"v249,v174,v174\n"
M_"v[72:75],v[4:7],a[228:231],v[72:75]\n"
"v_fma_f32 v249,v249,s76,v208\n"
"v_add_f32 v248,1.0,v248\n"
M_"v[72:75],v[8:11],a[232:235],v[72:75]\n"
T_"v[60:61],v192 offset:61440\n"
U_"v172,v172,v247\n"
M_"v[72:75],v[12:15],a[236:239],v[72:75]\n"
C_"v169,v170,v171\n"
"v_rcp_f32 v248,v248\n"
M_"v[76:79],v[0:3],a[240:243],0\n"
T_"v[62:63],v192 offset:61952\n"
U_"v249,v249,v174\n"
M_"v[76:79],v[4:7],a[244:247],v[76:79]\n"
A_"s75,s69,524416\n"
E_"v249,v249\n"
M_"v[76:79],v[8:11],a[248:251],v[76:79]\n"
U_"v250,v175,v175\n"
"v_fma_f32 v250,v250,s76,v208\n"
"v_add_f32 v249,1.0,v249\n"
M_"v[76:79],v[12:15],a[252:255],v[76:79]\n"
W_"lgkmcnt(15)\n"
U_"v173,v173,v248\n"
A_"m0,s74,73728\n"
M_"v[64:67],v[16:19],a[64:67],v[64:67]\n"
"v_rcp_f32 v249,v249\n"
M_"v[64:67],v[20:23],a[68:71],v[64:67]\n"
T_"v[0:1],v193 offset:0\n"
T_"v[2:3],v193 offset:512\n"
M_"v[64:67],v[24:27],a[72:75],v[64:67]\n"
B_"v196,s[48:51],s75 offen offset:0  lds\n"
U_"v250,v250,v175\n"
M_"v[64:67],v[28:31],a[76:79],v[64:67]\n"
E_"v250,v250\n"
M_"v[68:71],v[16:19],a[80:83],v[68:71]\n"
T_"v[4:5],v193 offset:4096\n"
U_"v247,v176,v176\n"
M_"v[68:71],v[20:23],a[84:87],v[68:71]\n"
T_"v[6:7],v193 offset:4608\n"
"v_fma_f32 v247,v247,s76,v208\n"
"v_add_f32 v250,1.0,v250\n"
M_"v[68:71],v[24:27],a[88:91],v[68:71]\n"
U_"v174,v174,v249\n"
M_"v[68:71],v[28:31],a[92:95],v[68:71]\n"
"v_rcp_f32 v250,v250\n"
M_"v[72:75],v[16:19],a[96:99],v[72:75]\n"
B_"v197,s[48:51],s75 offen offset:1024  lds\n"
T_"v[8:9],v193 offset:8192\n"
M_"v[72:75],v[20:23],a[100:103],v[72:75]\n"
T_"v[10:11],v193 offset:8704\n"
U_"v247,v247,v176\n"
M_"v[72:75],v[24:27],a[104:107],v[72:75]\n"
E_"v247,v247\n"
M_"v[72:75],v[28:31],a[108:111],v[72:75]\n"
T_"v[12:13],v193 offset:12288\n"
U_"v248,v177,v177\n"
"v_fma_f32 v248,v248,s76,v208\n"
M_"v[76:79],v[16:19],a[112:115],v[76:79]\n"
T_"v[14:15],v193 offset:12800\n"
"v_add_f32 v247,1.0,v247\n"
M_"v[76:79],v[20:23],a[116:119],v[76:79]\n"
B_"v198,s[48:51],s75 offen offset:2048  lds\n"
M_"v[76:79],v[24:27],a[120:123],v[76:79]\n"
U_"v175,v175,v250\n"
"v_rcp_f32 v247,v247\n"
M_"v[76:79],v[28:31],a[124:127],v[76:79]\n"
W_"lgkmcnt(15)\n"
U_"v248,v248,v177\n"
U_"v172,v172,v108\n"
M_"v[64:67],v[32:35],a[0:3],v[64:67]\n"
T_"v[16:17],v193 offset:16384\n"
U_"v173,v173,v109\n"
M_"v[64:67],v[36:39],a[4:7],v[64:67]\n"
T_"v[18:19],v193 offset:16896\n"
E_"v248,v248\n"
M_"v[64:67],v[40:43],a[8:11],v[64:67]\n"
U_"v249,v178,v178\n"
"v_fma_f32 v249,v249,s76,v208\n"
M_"v[64:67],v[44:47],a[12:15],v[64:67]\n"
B_"v199,s[48:51],s75 offen offset:3072  lds\n"
M_"v[68:71],v[32:35],a[16:19],v[68:71]\n"
T_"v[20:21],v193 offset:20480\n"
"v_add_f32 v248,1.0,v248\n"
M_"v[68:71],v[36:39],a[20:23],v[68:71]\n"
T_"v[22:23],v193 offset:20992\n"
U_"v176,v176,v247\n"
U_"v174,v174,v110\n"
M_"v[68:71],v[40:43],a[24:27],v[68:71]\n"
"v_rcp_f32 v248,v248\n"
M_"v[68:71],v[44:47],a[28:31],v[68:71]\n"
T_"v[24:25],v193 offset:24576\n"
U_"v249,v249,v178\n"
M_"v[72:75],v[32:35],a[32:35],v[72:75]\n"
A_"s75,s69,786560\n"
T_"v[26:27],v193 offset:25088\n"
E_"v249,v249\n"
M_"v[72:75],v[36:39],a[36:39],v[72:75]\n"
U_"v250,v179,v179\n"
"v_fma_f32 v250,v250,s76,v208\n"
M_"v[72:75],v[40:43],a[40:43],v[72:75]\n"
"s_mov_b64 s[40:41],s[18:19]\n"
T_"v[28:29],v193 offset:28672\n"
"v_add_f32 v249,1.0,v249\n"
M_"v[72:75],v[44:47],a[44:47],v[72:75]\n"
A_"m0,s74,77824\n"
U_"v177,v177,v248\n"
"v_rcp_f32 v249,v249\n"
M_"v[76:79],v[32:35],a[48:51],v[76:79]\n"
"s_mov_b32 s42,0x400000\n"
T_"v[30:31],v193 offset:29184\n"
U_"v250,v250,v179\n"
M_"v[76:79],v[36:39],a[52:55],v[76:79]\n"
A_"s88,s88,2048\n"
E_"v250,v250\n"
M_"v[76:79],v[40:43],a[56:59],v[76:79]\n"
B_"v196,s[48:51],s75 offen offset:0  lds\n"
"v_lshrrev_b32 v246,6,v244\n"
M_"v[76:79],v[44:47],a[60:63],v[76:79]\n"
W_"lgkmcnt(15)\n"
"v_mul_u32_u24 v246,128,v246\n"
U_"v247,v180,v180\n"
M_"v[64:67],v[48:51],a[128:131],v[64:67]\n"
"s_lshl3_add_u32 s11,s31,s34\n"
T_"v[32:33],v193 offset:32768\n"
"v_fma_f32 v247,v247,s76,v208\n"
M_"v[64:67],v[52:55],a[132:135],v[64:67]\n"
"buffer_load_dword v246,v246,s[40:43],s88 offen\n"
T_"v[34:35],v193 offset:33280\n"
M_"v[64:67],v[56:59],a[136:139],v[64:67]\n"
A_"s85,s11,64\n"
"v_add_f32 v250,1.0,v250\n"
U_"v178,v178,v249\n"
M_"v[64:67],v[60:63],a[140:143],v[64:67]\n"
W_"vmcnt(11)\n"
"v_cmp_ne_u32 s[58:59],s32,v204\n"
T_"v[36:37],v193 offset:36864\n"
M_"v[68:71],v[48:51],a[144:147],v[68:71]\n"
"s_cmp_lg_u64 s[58:59],0\n"
"s_cselect_b32 s11,s85,s11\n"
"v_rcp_f32 v250,v250\n"
M_"v[68:71],v[52:55],a[148:151],v[68:71]\n"
B_"v197,s[48:51],s75 offen offset:1024  lds\n"
M_"v[68:71],v[56:59],a[152:155],v[68:71]\n"
T_"v[38:39],v193 offset:37376\n"
U_"v247,v247,v180\n"
M_"v[68:71],v[60:63],a[156:159],v[68:71]\n"
"s_cmp_lg_u32 s11,4\n"
"s_cbranch_scc1 L_m_ni0\n"
"buffer_inv sc1\n"
"L_m_ni0:\n"
M_"v[72:75],v[48:51],a[160:163],v[72:75]\n"
T_"v[40:41],v193 offset:40960\n"
T_"v[42:43],v193 offset:41472\n"
M_"v[72:75],v[52:55],a[164:167],v[72:75]\n"
B_"v198,s[48:51],s75 offen offset:2048  lds\n"
M_"v[72:75],v[56:59],a[168:171],v[72:75]\n"
E_"v247,v247\n"
M_"v[72:75],v[60:63],a[172:175],v[72:75]\n"
T_"v[44:45],v193 offset:45056\n"
U_"v248,v181,v181\n"
"v_fma_f32 v248,v248,s76,v208\n"
M_"v[76:79],v[48:51],a[176:179],v[76:79]\n"
"v_add_f32 v247,1.0,v247\n"
T_"v[46:47],v193 offset:45568\n"
M_"v[76:79],v[52:55],a[180:183],v[76:79]\n"
U_"v179,v179,v250\n"
U_"v175,v175,v111\n"
M_"v[76:79],v[56:59],a[184:187],v[76:79]\n"
B_"v199,s[48:51],s75 offen offset:3072  lds\n"
U_"v176,v176,v112\n"
M_"v[76:79],v[60:63],a[188:191],v[76:79]\n"
W_"lgkmcnt(15)\n"
"v_rcp_f32 v247,v247\n"
M_"v[80:83],v[0:3],a[192:195],0\n"
U_"v248,v248,v181\n"
T_"v[48:49],v193 offset:49152\n"
M_"v[80:83],v[4:7],a[196:199],v[80:83]\n"
T_"v[50:51],v193 offset:49664\n"
E_"v248,v248\n"
M_"v[80:83],v[8:11],a[200:203],v[80:83]\n"
U_"v249,v182,v182\n"
"v_fma_f32 v249,v249,s76,v208\n"
M_"v[80:83],v[12:15],a[204:207],v[80:83]\n"
"v_add_f32 v248,1.0,v248\n"
U_"v180,v180,v247\n"
M_"v[84:87],v[0:3],a[208:211],0\n"
T_"v[52:53],v193 offset:53248\n"
"v_rcp_f32 v248,v248\n"
M_"v[84:87],v[4:7],a[212:215],v[84:87]\n"
U_"v249,v249,v182\n"
T_"v[54:55],v193 offset:53760\n"
M_"v[84:87],v[8:11],a[216:219],v[84:87]\n"
E_"v249,v249\n"
M_"v[84:87],v[12:15],a[220:223],v[84:87]\n"
U_"v250,v183,v183\n"
"v_fma_f32 v250,v250,s76,v208\n"
T_"v[56:57],v193 offset:57344\n"
M_"v[88:91],v[0:3],a[224:227],0\n"
"v_add_f32 v249,1.0,v249\n"
T_"v[58:59],v193 offset:57856\n"
M_"v[88:91],v[4:7],a[228:231],v[88:91]\n"
U_"v181,v181,v248\n"
U_"v177,v177,v113\n"
M_"v[88:91],v[8:11],a[232:235],v[88:91]\n"
T_"v[60:61],v193 offset:61440\n"
"v_rcp_f32 v249,v249\n"
M_"v[88:91],v[12:15],a[236:239],v[88:91]\n"
U_"v250,v250,v183\n"
U_"v178,v178,v114\n"
M_"v[92:95],v[0:3],a[240:243],0\n"
E_"v250,v250\n"
M_"v[92:95],v[4:7],a[244:247],v[92:95]\n"
T_"v[62:63],v193 offset:61952\n"
U_"v247,v184,v184\n"
M_"v[92:95],v[8:11],a[248:251],v[92:95]\n"
"v_fma_f32 v247,v247,s76,v208\n"
"v_add_f32 v250,1.0,v250\n"
U_"v182,v182,v249\n"
M_"v[92:95],v[12:15],a[252:255],v[92:95]\n"
W_"lgkmcnt(15)\n"
"v_rcp_f32 v250,v250\n"
M_"v[80:83],v[16:19],a[64:67],v[80:83]\n"
T_"v[0:1],v194 offset:0\n"
U_"v247,v247,v184\n"
M_"v[80:83],v[20:23],a[68:71],v[80:83]\n"
T_"v[2:3],v194 offset:512\n"
E_"v247,v247\n"
M_"v[80:83],v[24:27],a[72:75],v[80:83]\n"
"s_mov_b64 s[40:41],s[8:9]\n"
U_"v248,v185,v185\n"
"v_fma_f32 v248,v248,s76,v208\n"
M_"v[80:83],v[28:31],a[76:79],v[80:83]\n"
"s_mov_b32 s42,0x200000\n"
T_"v[4:5],v194 offset:4096\n"
"v_add_f32 v247,1.0,v247\n"
M_"v[84:87],v[16:19],a[80:83],v[84:87]\n"
"s_lshl2_add_u32 s88,s30,s31\n"
"v_bfe_u32 v245,v244,4,1\n"
U_"v183,v183,v250\n"
U_"v179,v179,v115\n"
M_"v[84:87],v[20:23],a[84:87],v[84:87]\n"
"s_lshl_b32 s88,s88,11\n"
T_"v[6:7],v194 offset:4608\n"
C_"v162,v176,v177\n"
M_"v[84:87],v[24:27],a[88:91],v[84:87]\n"
"v_rcp_f32 v247,v247\n"
M_"v[84:87],v[28:31],a[92:95],v[84:87]\n"
"v_xor_b32 v245,s30,v245\n"
"v_lshrrev_b32 v251,6,v244\n"
U_"v248,v248,v185\n"
M_"v[88:91],v[16:19],a[96:99],v[88:91]\n"
"v_mul_u32_u24 v251,128,v251\n"
T_"v[8:9],v194 offset:8192\n"
M_"v[88:91],v[20:23],a[100:103],v[88:91]\n"
"buffer_load_dword v251,v251,s[40:43],s88 offen\n"
M_"v[88:91],v[24:27],a[104:107],v[88:91]\n"
T_"v[10:11],v194 offset:8704\n"
E_"v248,v248\n"
M_"v[88:91],v[28:31],a[108:111],v[88:91]\n"
U_"v249,v186,v186\n"
"v_fma_f32 v249,v249,s76,v208\n"
M_"v[92:95],v[16:19],a[112:115],v[92:95]\n"
"v_lshlrev_b32 v245,7,v245\n"
"v_add_f32 v248,1.0,v248\n"
M_"v[92:95],v[20:23],a[116:119],v[92:95]\n"
T_"v[12:13],v194 offset:12288\n"
U_"v184,v184,v247\n"
"v_add_u32 v245,0x4f10020,v245\n"
M_"v[92:95],v[24:27],a[120:123],v[92:95]\n"
"buffer_load_dwordx2 v[206:207],v245,s[52:55],0 offen sc1\n"
M_"v[92:95],v[28:31],a[124:127],v[92:95]\n"
W_"lgkmcnt(15)\n"
"v_rcp_f32 v248,v248\n"
M_"v[80:83],v[32:35],a[0:3],v[80:83]\n"
T_"v[14:15],v194 offset:12800\n"
U_"v249,v249,v186\n"
M_"v[80:83],v[36:39],a[4:7],v[80:83]\n"
T_"v[16:17],v194 offset:16384\n"
E_"v249,v249\n"
M_"v[80:83],v[40:43],a[8:11],v[80:83]\n"
U_"v250,v187,v187\n"
"v_fma_f32 v250,v250,s76,v208\n"
M_"v[80:83],v[44:47],a[12:15],v[80:83]\n"
"v_add_f32 v249,1.0,v249\n"
U_"v185,v185,v248\n"
M_"v[84:87],v[32:35],a[16:19],v[84:87]\n"
"s_cmp_lg_u32 s11,5\n"
"s_cbranch_scc1 L_m_ni1\n"
"buffer_inv sc1\n"
"L_m_ni1:\n"
M_"v[84:87],v[36:39],a[20:23],v[84:87]\n"
T_"v[18:19],v194 offset:16896\n"
T_"v[20:21],v194 offset:20480\n"
M_"v[84:87],v[40:43],a[24:27],v[84:87]\n"
"v_rcp_f32 v249,v249\n"
M_"v[84:87],v[44:47],a[28:31],v[84:87]\n"
U_"v250,v250,v187\n"
E_"v250,v250\n"
M_"v[88:91],v[32:35],a[32:35],v[88:91]\n"
U_"v247,v188,v188\n"
"v_fma_f32 v247,v247,s76,v208\n"
"L_pt0_0:\n"
"s_cbranch_execz L_pr0\n"
M_"v[88:91],v[36:39],a[36:39],v[88:91]\n"
T_"v[22:23],v194 offset:20992\n"
"v_add_f32 v250,1.0,v250\n"
M_"v[88:91],v[40:43],a[40:43],v[88:91]\n"
U_"v186,v186,v249\n"
T_"v[24:25],v194 offset:24576\n"
T_"v[26:27],v194 offset:25088\n"
M_"v[88:91],v[44:47],a[44:47],v[88:91]\n"
"v_rcp_f32 v250,v250\n"
M_"v[92:95],v[32:35],a[48:51],v[92:95]\n"
U_"v247,v247,v188\n"
T_"v[28:29],v194 offset:28672\n"
M_"v[92:95],v[36:39],a[52:55],v[92:95]\n"
T_"v[30:31],v194 offset:29184\n"
E_"v247,v247\n"
M_"v[92:95],v[40:43],a[56:59],v[92:95]\n"
U_"v248,v189,v189\n"
"v_fma_f32 v248,v248,s76,v208\n"
M_"v[92:95],v[44:47],a[60:63],v[92:95]\n"
W_"lgkmcnt(15)\n"
"v_add_f32 v247,1.0,v247\n"
U_"v187,v187,v250\n"
M_"v[80:83],v[48:51],a[128:131],v[80:83]\n"
T_"v[32:33],v194 offset:32768\n"
"v_rcp_f32 v247,v247\n"
M_"v[80:83],v[52:55],a[132:135],v[80:83]\n"
U_"v248,v248,v189\n"
T_"v[34:35],v194 offset:33280\n"
M_"v[80:83],v[56:59],a[136:139],v[80:83]\n"
E_"v248,v248\n"
M_"v[80:83],v[60:63],a[140:143],v[80:83]\n"
U_"v249,v190,v190\n"
"v_fma_f32 v249,v249,s76,v208\n"
"v_add_f32 v248,1.0,v248\n"
M_"v[84:87],v[48:51],a[144:147],v[84:87]\n"
U_"v188,v188,v247\n"
T_"v[36:37],v194 offset:36864\n"
M_"v[84:87],v[52:55],a[148:151],v[84:87]\n"
"v_rcp_f32 v248,v248\n"
M_"v[84:87],v[56:59],a[152:155],v[84:87]\n"
U_"v249,v249,v190\n"
T_"v[38:39],v194 offset:37376\n"
M_"v[84:87],v[60:63],a[156:159],v[84:87]\n"
T_"v[40:41],v194 offset:40960\n"
E_"v249,v249\n"
M_"v[88:91],v[48:51],a[160:163],v[88:91]\n"
U_"v250,v191,v191\n"
"v_fma_f32 v250,v250,s76,v208\n"
M_"v[88:91],v[52:55],a[164:167],v[88:91]\n"
"v_add_f32 v249,1.0,v249\n"
T_"v[42:43],v194 offset:41472\n"
M_"v[88:91],v[56:59],a[168:171],v[88:91]\n"
U_"v189,v189,v248\n"
"v_rcp_f32 v249,v249\n"
M_"v[88:91],v[60:63],a[172:175],v[88:91]\n"
T_"v[44:45],v194 offset:45056\n"
U_"v250,v250,v191\n"
M_"v[92:95],v[48:51],a[176:179],v[92:95]\n"
E_"v250,v250\n"
M_"v[92:95],v[52:55],a[180:183],v[92:95]\n"
C_"v163,v178,v179\n"
T_"v[46:47],v194 offset:45568\n"
"v_add_f32 v250,1.0,v250\n"
M_"v[92:95],v[56:59],a[184:187],v[92:95]\n"
S_"v[160:163],v244,s[52:55],s36 offen offset:1024 sc1\n"
M_"v[92:95],v[60:63],a[188:191],v[92:95]\n"
W_"lgkmcnt(15)\n"
U_"v180,v180,v116\n"
U_"v190,v190,v249\n"
M_"v[96:99],v[0:3],a[192:195],0\n"
U_"v181,v181,v117\n"
U_"v182,v182,v118\n"
U_"v183,v183,v119\n"
M_"v[96:99],v[4:7],a[196:199],v[96:99]\n"
"v_rcp_f32 v250,v250\n"
M_"v[96:99],v[8:11],a[200:203],v[96:99]\n"
"s_lshl_b32 s91,s33,19\n"
C_"v166,v180,v181\n"
U_"v191,v191,v250\n"
M_"v[96:99],v[12:15],a[204:207],v[96:99]\n"
C_"v167,v182,v183\n"
T_"v[48:49],v194 offset:49152\n"
U_"v184,v184,v120\n"
M_"v[100:103],v[0:3],a[208:211],0\n"
S_"v[164:167],v244,s[52:55],s37 offen offset:1024 sc1\n"
M_"v[100:103],v[4:7],a[212:215],v[100:103]\n"
A_"s42,s91,0x1200000\n"
T_"v[50:51],v194 offset:49664\n"
U_"v185,v185,v121\n"
M_"v[100:103],v[8:11],a[216:219],v[100:103]\n"
A_"s40,s28,s42\n"
"s_addc_u32 s41,s29,0\n"
U_"v186,v186,v122\n"
C_"v172,v172,v173\n"
M_"v[100:103],v[12:15],a[220:223],v[100:103]\n"
C_"v173,v174,v175\n"
U_"v187,v187,v123\n"
M_"v[104:107],v[0:3],a[224:227],0\n"
C_"v170,v184,v185\n"
T_"v[52:53],v194 offset:53248\n"
M_"v[104:107],v[4:7],a[228:231],v[104:107]\n"
C_"v171,v186,v187\n"
U_"v188,v188,v124\n"
M_"v[104:107],v[8:11],a[232:235],v[104:107]\n"
S_"v[168:171],v244,s[52:55],s38 offen offset:1024 sc1\n"
T_"v[54:55],v194 offset:53760\n"
M_"v[104:107],v[12:15],a[236:239],v[104:107]\n"
"s_mov_b32 s42,0x80000\n"
U_"v189,v189,v125\n"
U_"v190,v190,v126\n"
M_"v[108:111],v[0:3],a[240:243],0\n"
U_"v191,v191,v127\n"
C_"v174,v188,v189\n"
M_"v[108:111],v[4:7],a[244:247],v[108:111]\n"
"s_mov_b32 s43,0x20000\n"
T_"v[56:57],v194 offset:57344\n"
C_"v175,v190,v191\n"
T_"v[58:59],v194 offset:57856\n"
M_"v[108:111],v[8:11],a[248:251],v[108:111]\n"
S_"v[172:175],v244,s[52:55],s39 offen offset:1024 sc1\n"
M_"v[108:111],v[12:15],a[252:255],v[108:111]\n"
W_"lgkmcnt(14)\n"
T_"v[60:61],v194 offset:61440\n"
T_"v[62:63],v194 offset:61952\n"
M_"v[96:99],v[16:19],a[64:67],v[96:99]\n"
C_"v224,v64,v65\n"
C_"v225,v66,v67\n"
T_"v[0:1],v195 offset:0\n"
M_"v[96:99],v[20:23],a[68:71],v[96:99]\n"
T_"v[2:3],v195 offset:512\n"
C_"v226,v80,v81\n"
M_"v[96:99],v[24:27],a[72:75],v[96:99]\n"
C_"v227,v82,v83\n"
C_"v228,v68,v69\n"
M_"v[96:99],v[28:31],a[76:79],v[96:99]\n"
T_"v[4:5],v195 offset:4096\n"
C_"v229,v70,v71\n"
M_"v[100:103],v[16:19],a[80:83],v[100:103]\n"
C_"v230,v84,v85\n"
C_"v231,v86,v87\n"
M_"v[100:103],v[20:23],a[84:87],v[100:103]\n"
T_"v[6:7],v195 offset:4608\n"
C_"v232,v72,v73\n"
M_"v[100:103],v[24:27],a[88:91],v[100:103]\n"
"s_cmp_lg_u32 s11,6\n"
"s_cbranch_scc1 L_m_ni2\n"
"buffer_inv sc1\n"
"L_m_ni2:\n"
M_"v[100:103],v[28:31],a[92:95],v[100:103]\n"
T_"v[8:9],v195 offset:8192\n"
C_"v233,v74,v75\n"
M_"v[104:107],v[16:19],a[96:99],v[104:107]\n"
T_"v[10:11],v195 offset:8704\n"
C_"v234,v88,v89\n"
M_"v[104:107],v[20:23],a[100:103],v[104:107]\n"
C_"v235,v90,v91\n"
C_"v236,v76,v77\n"
M_"v[104:107],v[24:27],a[104:107],v[104:107]\n"
T_"v[12:13],v195 offset:12288\n"
C_"v237,v78,v79\n"
M_"v[104:107],v[28:31],a[108:111],v[104:107]\n"
C_"v238,v92,v93\n"
C_"v239,v94,v95\n"
M_"v[108:111],v[16:19],a[112:115],v[108:111]\n"
T_"v[14:15],v195 offset:12800\n"
M_"v[108:111],v[20:23],a[116:119],v[108:111]\n"
M_"v[108:111],v[24:27],a[120:123],v[108:111]\n"
M_"v[108:111],v[28:31],a[124:127],v[108:111]\n"
W_"lgkmcnt(15)\n"
M_"v[96:99],v[32:35],a[0:3],v[96:99]\n"
T_"v[16:17],v195 offset:16384\n"
M_"v[96:99],v[36:39],a[4:7],v[96:99]\n"
T_"v[18:19],v195 offset:16896\n"
M_"v[96:99],v[40:43],a[8:11],v[96:99]\n"
M_"v[96:99],v[44:47],a[12:15],v[96:99]\n"
T_"v[20:21],v195 offset:20480\n"
M_"v[100:103],v[32:35],a[16:19],v[100:103]\n"
M_"v[100:103],v[36:39],a[20:23],v[100:103]\n"
T_"v[22:23],v195 offset:20992\n"
M_"v[100:103],v[40:43],a[24:27],v[100:103]\n"
M_"v[100:103],v[44:47],a[28:31],v[100:103]\n"
T_"v[24:25],v195 offset:24576\n"
M_"v[104:107],v[32:35],a[32:35],v[104:107]\n"
T_"v[26:27],v195 offset:25088\n"
M_"v[104:107],v[36:39],a[36:39],v[104:107]\n"
M_"v[104:107],v[40:43],a[40:43],v[104:107]\n"
T_"v[28:29],v195 offset:28672\n"
M_"v[104:107],v[44:47],a[44:47],v[104:107]\n"
M_"v[108:111],v[32:35],a[48:51],v[108:111]\n"
T_"v[30:31],v195 offset:29184\n"
M_"v[108:111],v[36:39],a[52:55],v[108:111]\n"
M_"v[108:111],v[40:43],a[56:59],v[108:111]\n"
M_"v[108:111],v[44:47],a[60:63],v[108:111]\n"
W_"lgkmcnt(15)\n"
M_"v[96:99],v[48:51],a[128:131],v[96:99]\n"
T_"v[32:33],v195 offset:32768\n"
M_"v[96:99],v[52:55],a[132:135],v[96:99]\n"
T_"v[34:35],v195 offset:33280\n"
M_"v[96:99],v[56:59],a[136:139],v[96:99]\n"
M_"v[96:99],v[60:63],a[140:143],v[96:99]\n"
T_"v[36:37],v195 offset:36864\n"
M_"v[100:103],v[48:51],a[144:147],v[100:103]\n"
M_"v[100:103],v[52:55],a[148:151],v[100:103]\n"
T_"v[38:39],v195 offset:37376\n"
M_"v[100:103],v[56:59],a[152:155],v[100:103]\n"
M_"v[100:103],v[60:63],a[156:159],v[100:103]\n"
T_"v[40:41],v195 offset:40960\n"
M_"v[104:107],v[48:51],a[160:163],v[104:107]\n"
T_"v[42:43],v195 offset:41472\n"
M_"v[104:107],v[52:55],a[164:167],v[104:107]\n"
M_"v[104:107],v[56:59],a[168:171],v[104:107]\n"
T_"v[44:45],v195 offset:45056\n"
M_"v[104:107],v[60:63],a[172:175],v[104:107]\n"
M_"v[108:111],v[48:51],a[176:179],v[108:111]\n"
T_"v[46:47],v195 offset:45568\n"
M_"v[108:111],v[52:55],a[180:183],v[108:111]\n"
M_"v[108:111],v[56:59],a[184:187],v[108:111]\n"
W_"vmcnt(4)\n"
M_"v[108:111],v[60:63],a[188:191],v[108:111]\n"
W_"lgkmcnt(15)\n"
M_"v[112:115],v[0:3],a[192:195],0\n"
"v_xor_b32 v206,v206,v207\n"
T_"v[48:49],v195 offset:49152\n"
M_"v[112:115],v[4:7],a[196:199],v[112:115]\n"
T_"v[50:51],v195 offset:49664\n"
M_"v[112:115],v[8:11],a[200:203],v[112:115]\n"
"v_cmp_ne_u32 s[60:61],s32,v206\n"
T_"v[52:53],v195 offset:53248\n"
M_"v[112:115],v[12:15],a[204:207],v[112:115]\n"
M_"v[116:119],v[0:3],a[208:211],0\n"
"s_cmp_lg_u32 s11,7\n"
"s_cbranch_scc1 L_m_ni3\n"
"buffer_inv sc1\n"
"L_m_ni3:\n"
M_"v[116:119],v[4:7],a[212:215],v[116:119]\n"
"s_cmp_lg_u64 s[60:61],0\n"
"s_cbranch_scc1 L_slow_PT\n"
"L_back_PT:\n"
M_"v[116:119],v[8:11],a[216:219],v[116:119]\n"
T_"v[54:55],v195 offset:53760\n"
T_"v[56:57],v195 offset:57344\n"
M_"v[116:119],v[12:15],a[220:223],v[116:119]\n"
T_"v[58:59],v195 offset:57856\n"
M_"v[120:123],v[0:3],a[224:227],0\n"
T_"v[60:61],v195 offset:61440\n"
M_"v[120:123],v[4:7],a[228:231],v[120:123]\n"
T_"v[62:63],v195 offset:61952\n"
M_"v[120:123],v[8:11],a[232:235],v[120:123]\n"
M_"v[120:123],v[12:15],a[236:239],v[120:123]\n"
"v_readlane_b32 s60,v207,0\n"
M_"v[124:127],v[0:3],a[240:243],0\n"
M_"v[124:127],v[4:7],a[244:247],v[124:127]\n"
"v_readlane_b32 s61,v207,1\n"
M_"v[124:127],v[8:11],a[248:251],v[124:127]\n"
M_"v[124:127],v[12:15],a[252:255],v[124:127]\n"
W_"lgkmcnt(15)\n"
"s_sub_u32 s57,s60,s61\n"
M_"v[112:115],v[16:19],a[64:67],v[112:115]\n"
M_"v[112:115],v[20:23],a[68:71],v[112:115]\n"
M_"v[112:115],v[24:27],a[72:75],v[112:115]\n"
M_"v[112:115],v[28:31],a[76:79],v[112:115]\n"
M_"v[116:119],v[16:19],a[80:83],v[116:119]\n"
W_"vmcnt(0) lgkmcnt(0)\n"
"s_barrier\n"
M_"v[116:119],v[20:23],a[84:87],v[116:119]\n"
"buffer_store_dword v254,off,s[52:55],s78 sc1\n"
"s_cmp_gt_i32 s57,170\n"
"s_cbranch_scc1 L_m_g\n"
"s_cmp_lt_i32 s57,-170\n"
"s_cbranch_scc1 L_m_t\n"
M_"v[116:119],v[24:27],a[88:91],v[116:119]\n"
"s_mov_b64 s[48:49],s[24:25]\n"
"v_xor_b32 v192,0x10000,v192\n"
M_"v[116:119],v[28:31],a[92:95],v[116:119]\n"
"v_xor_b32 v193,0x10000,v193\n"
M_"v[120:123],v[16:19],a[96:99],v[120:123]\n"
"s_mov_b32 s50,0x1c0000\n"
"v_xor_b32 v194,0x10000,v194\n"
M_"v[120:123],v[20:23],a[100:103],v[120:123]\n"
"v_xor_b32 v195,0x10000,v195\n"
M_"v[120:123],v[24:27],a[104:107],v[120:123]\n"
A_"s75,s68,0\n"
T_"v[0:1],v192 offset:49152\n"
M_"v[120:123],v[28:31],a[108:111],v[120:123]\n"
T_"v[2:3],v192 offset:49664\n"
M_"v[124:127],v[16:19],a[112:115],v[124:127]\n"
A_"m0,s56,0\n"
M_"v[124:127],v[20:23],a[116:119],v[124:127]\n"
T_"v[4:5],v192 offset:53248\n"
M_"v[124:127],v[24:27],a[120:123],v[124:127]\n"
B_"v200,s[48:51],s75 offen offset:0  lds\n"
M_"v[124:127],v[28:31],a[124:127],v[124:127]\n"
T_"v[6:7],v192 offset:53760\n"
T_"v[8:9],v192 offset:57344\n"
M_"v[112:115],v[32:35],a[0:3],v[112:115]\n"
T_"v[10:11],v192 offset:57856\n"
M_"v[112:115],v[36:39],a[4:7],v[112:115]\n"
T_"v[12:13],v192 offset:61440\n"
M_"v[112:115],v[40:43],a[8:11],v[112:115]\n"
B_"v201,s[48:51],s75 offen offset:1024  lds\n"
M_"v[112:115],v[44:47],a[12:15],v[112:115]\n"
T_"v[14:15],v192 offset:61952\n"
T_"v[16:17],v193 offset:49152\n"
M_"v[116:119],v[32:35],a[16:19],v[116:119]\n"
T_"v[18:19],v193 offset:49664\n"
T_"v[20:21],v193 offset:53248\n"
M_"v[116:119],v[36:39],a[20:23],v[116:119]\n"
T_"v[22:23],v193 offset:53760\n"
M_"v[116:119],v[40:43],a[24:27],v[116:119]\n"
B_"v202,s[48:51],s75 offen offset:2048  lds\n"
M_"v[116:119],v[44:47],a[28:31],v[116:119]\n"
T_"v[24:25],v193 offset:57344\n"
M_"v[120:123],v[32:35],a[32:35],v[120:123]\n"
T_"v[26:27],v193 offset:57856\n"
M_"v[120:123],v[36:39],a[36:39],v[120:123]\n"
M_"v[120:123],v[40:43],a[40:43],v[120:123]\n"
T_"v[28:29],v193 offset:61440\n"
M_"v[120:123],v[44:47],a[44:47],v[120:123]\n"
B_"v203,s[48:51],s75 offen offset:3072  lds\n"
M_"v[124:127],v[32:35],a[48:51],v[124:127]\n"
T_"v[30:31],v193 offset:61952\n"
M_"v[124:127],v[36:39],a[52:55],v[124:127]\n"
M_"v[124:127],v[40:43],a[56:59],v[124:127]\n"
M_"v[124:127],v[44:47],a[60:63],v[124:127]\n"
A_"s75,s68,128\n"
M_"v[112:115],v[48:51],a[128:131],v[112:115]\n"
T_"v[32:33],v194 offset:49152\n"
M_"v[112:115],v[52:55],a[132:135],v[112:115]\n"
A_"m0,s56,147456\n"
T_"v[34:35],v194 offset:49664\n"
M_"v[112:115],v[56:59],a[136:139],v[112:115]\n"
"s_cmp_lg_u32 s11,0\n"
"s_cbranch_scc1 L_m_ni4\n"
"buffer_inv sc1\n"
"L_m_ni4:\n"
M_"v[112:115],v[60:63],a[140:143],v[112:115]\n"
B_"v200,s[48:51],s75 offen offset:0  lds\n"
M_"v[116:119],v[48:51],a[144:147],v[116:119]\n"
T_"v[36:37],v194 offset:53248\n"
M_"v[116:119],v[52:55],a[148:151],v[116:119]\n"
T_"v[38:39],v194 offset:53760\n"
M_"v[116:119],v[56:59],a[152:155],v[116:119]\n"
C_"v208,v96,v97\n"
M_"v[116:119],v[60:63],a[156:159],v[116:119]\n"
B_"v201,s[48:51],s75 offen offset:1024  lds\n"
M_"v[120:123],v[48:51],a[160:163],v[120:123]\n"
T_"v[40:41],v194 offset:57344\n"
T_"v[42:43],v194 offset:57856\n"
M_"v[120:123],v[52:55],a[164:167],v[120:123]\n"
C_"v209,v98,v99\n"
M_"v[120:123],v[56:59],a[168:171],v[120:123]\n"
T_"v[44:45],v194 offset:61440\n"
M_"v[120:123],v[60:63],a[172:175],v[120:123]\n"
C_"v210,v112,v113\n"
M_"v[124:127],v[48:51],a[176:179],v[124:127]\n"
B_"v202,s[48:51],s75 offen offset:2048  lds\n"
M_"v[124:127],v[52:55],a[180:183],v[124:127]\n"
T_"v[46:47],v194 offset:61952\n"
C_"v211,v114,v115\n"
M_"v[124:127],v[56:59],a[184:187],v[124:127]\n"
C_"v212,v100,v101\n"
C_"v213,v102,v103\n"
M_"v[124:127],v[60:63],a[188:191],v[124:127]\n"
W_"lgkmcnt(15)\n"
C_"v214,v116,v117\n"
M_"v[128:131],v[0:3],a[128:131],0\n"
B_"v203,s[48:51],s75 offen offset:3072  lds\n"
M_"v[128:131],v[4:7],a[132:135],v[128:131]\n"
T_"v[48:49],v195 offset:49152\n"
T_"v[50:51],v195 offset:49664\n"
M_"v[128:131],v[8:11],a[136:139],v[128:131]\n"
C_"v215,v118,v119\n"
C_"v216,v104,v105\n"
M_"v[128:131],v[12:15],a[140:143],v[128:131]\n"
T_"v[52:53],v195 offset:53248\n"
C_"v217,v106,v107\n"
M_"v[132:135],v[0:3],a[144:147],0\n"
A_"s75,s68,131072\n"
T_"v[54:55],v195 offset:53760\n"
C_"v218,v120,v121\n"
M_"v[132:135],v[4:7],a[148:151],v[132:135]\n"
C_"v219,v122,v123\n"
C_"v220,v108,v109\n"
M_"v[132:135],v[8:11],a[152:155],v[132:135]\n"
A_"m0,s56,16384\n"
T_"v[56:57],v195 offset:57344\n"
C_"v221,v110,v111\n"
M_"v[132:135],v[12:15],a[156:159],v[132:135]\n"
W_"lgkmcnt(13)\n"
C_"v222,v124,v125\n"
M_"v[144:147],v[16:19],a[128:131],0\n"
B_"v200,s[48:51],s75 offen offset:0  lds\n"
M_"v[144:147],v[20:23],a[132:135],v[144:147]\n"
T_"v[58:59],v195 offset:57856\n"
T_"v[60:61],v195 offset:61440\n"
M_"v[144:147],v[24:27],a[136:139],v[144:147]\n"
C_"v223,v126,v127\n"
M_"v[144:147],v[28:31],a[140:143],v[144:147]\n"
T_"v[62:63],v195 offset:61952\n"
M_"v[148:151],v[16:19],a[144:147],0\n"
M_"v[148:151],v[20:23],a[148:151],v[148:151]\n"
B_"v201,s[48:51],s75 offen offset:1024  lds\n"
M_"v[148:151],v[24:27],a[152:155],v[148:151]\n"
M_"v[148:151],v[28:31],a[156:159],v[148:151]\n"
W_"lgkmcnt(8)\n"
M_"v[160:163],v[32:35],a[128:131],0\n"
M_"v[160:163],v[36:39],a[132:135],v[160:163]\n"
B_"v202,s[48:51],s75 offen offset:2048  lds\n"
M_"v[160:163],v[40:43],a[136:139],v[160:163]\n"
M_"v[160:163],v[44:47],a[140:143],v[160:163]\n"
M_"v[164:167],v[32:35],a[144:147],0\n"
M_"v[164:167],v[36:39],a[148:151],v[164:167]\n"
B_"v203,s[48:51],s75 offen offset:3072  lds\n"
M_"v[164:167],v[40:43],a[152:155],v[164:167]\n"
M_"v[164:167],v[44:47],a[156:159],v[164:167]\n"
W_"lgkmcnt(0)\n"
M_"v[176:179],v[48:51],a[128:131],0\n"
M_"v[176:179],v[52:55],a[132:135],v[176:179]\n"
A_"s75,s68,262144\n"
M_"v[176:179],v[56:59],a[136:139],v[176:179]\n"
M_"v[176:179],v[60:63],a[140:143],v[176:179]\n"
A_"m0,s56,32768\n"
M_"v[180:183],v[48:51],a[144:147],0\n"
M_"v[180:183],v[52:55],a[148:151],v[180:183]\n"
B_"v200,s[48:51],s75 offen offset:0  lds\n"
M_"v[180:183],v[56:59],a[152:155],v[180:183]\n"
M_"v[180:183],v[60:63],a[156:159],v[180:183]\n"
M_"v[136:139],v[0:3],a[160:163],0\n"
"s_cmp_lg_u32 s11,1\n"
"s_cbranch_scc1 L_m_ni5\n"
"buffer_inv sc1\n"
"L_m_ni5:\n"
M_"v[136:139],v[4:7],a[164:167],v[136:139]\n"
M_"v[136:139],v[8:11],a[168:171],v[136:139]\n"
B_"v201,s[48:51],s75 offen offset:1024  lds\n"
M_"v[136:139],v[12:15],a[172:175],v[136:139]\n"
M_"v[140:143],v[0:3],a[176:179],0\n"
M_"v[140:143],v[4:7],a[180:183],v[140:143]\n"
M_"v[140:143],v[8:11],a[184:187],v[140:143]\n"
B_"v202,s[48:51],s75 offen offset:2048  lds\n"
M_"v[140:143],v[12:15],a[188:191],v[140:143]\n"
M_"v[152:155],v[16:19],a[160:163],0\n"
T_"v[0:1],v192 offset:16384\n"
M_"v[152:155],v[20:23],a[164:167],v[152:155]\n"
T_"v[2:3],v192 offset:16896\n"
M_"v[152:155],v[24:27],a[168:171],v[152:155]\n"
B_"v203,s[48:51],s75 offen offset:3072  lds\n"
M_"v[152:155],v[28:31],a[172:175],v[152:155]\n"
T_"v[4:5],v192 offset:20480\n"
M_"v[156:159],v[16:19],a[176:179],0\n"
T_"v[6:7],v192 offset:20992\n"
M_"v[156:159],v[20:23],a[180:183],v[156:159]\n"
M_"v[156:159],v[24:27],a[184:187],v[156:159]\n"
A_"s75,s16,393216\n"
T_"v[8:9],v192 offset:24576\n"
M_"v[156:159],v[28:31],a[188:191],v[156:159]\n"
M_"v[168:171],v[32:35],a[160:163],0\n"
A_"m0,s56,49152\n"
T_"v[10:11],v192 offset:25088\n"
M_"v[168:171],v[36:39],a[164:167],v[168:171]\n"
T_"v[12:13],v192 offset:28672\n"
M_"v[168:171],v[40:43],a[168:171],v[168:171]\n"
M_"v[168:171],v[44:47],a[172:175],v[168:171]\n"
B_"v200,s[48:51],s75 offen offset:0  lds\n"
M_"v[172:175],v[32:35],a[176:179],0\n"
T_"v[14:15],v192 offset:29184\n"
T_"v[16:17],v193 offset:16384\n"
M_"v[172:175],v[36:39],a[180:183],v[172:175]\n"
T_"v[18:19],v193 offset:16896\n"
T_"v[20:21],v193 offset:20480\n"
M_"v[172:175],v[40:43],a[184:187],v[172:175]\n"
T_"v[22:23],v193 offset:20992\n"
T_"v[24:25],v193 offset:24576\n"
M_"v[172:175],v[44:47],a[188:191],v[172:175]\n"
B_"v201,s[48:51],s75 offen offset:1024  lds\n"
M_"v[184:187],v[48:51],a[160:163],0\n"
T_"v[26:27],v193 offset:25088\n"
M_"v[184:187],v[52:55],a[164:167],v[184:187]\n"
T_"v[28:29],v193 offset:28672\n"
M_"v[184:187],v[56:59],a[168:171],v[184:187]\n"
M_"v[184:187],v[60:63],a[172:175],v[184:187]\n"
B_"v202,s[48:51],s75 offen offset:2048  lds\n"
M_"v[188:191],v[48:51],a[176:179],0\n"
"s_lshr_b32 s85,s11,3\n"
T_"v[30:31],v193 offset:29184\n"
T_"v[32:33],v194 offset:16384\n"
M_"v[188:191],v[52:55],a[180:183],v[188:191]\n"
"s_cmp_eq_u32 s85,8\n"
"s_cbranch_scc1 L_slow_PK\n"
"L_back_PK:\n"
M_"v[188:191],v[56:59],a[184:187],v[188:191]\n"
T_"v[34:35],v194 offset:16896\n"
T_"v[36:37],v194 offset:20480\n"
M_"v[188:191],v[60:63],a[188:191],v[188:191]\n"
W_"lgkmcnt(11)\n"
T_"v[38:39],v194 offset:20992\n"
T_"v[40:41],v194 offset:24576\n"
M_"v[128:131],v[0:3],a[64:67],v[128:131]\n"
B_"v203,s[48:51],s75 offen offset:3072  lds\n"
M_"v[128:131],v[4:7],a[68:71],v[128:131]\n"
"s_lshl_b32 s85,s31,8\n"
T_"v[42:43],v194 offset:25088\n"
T_"v[44:45],v194 offset:28672\n"
M_"v[128:131],v[8:11],a[72:75],v[128:131]\n"
A_"s85,s85,s30\n"
"v_accvgpr_write_b32 a128,v224\n"
M_"v[128:131],v[12:15],a[76:79],v[128:131]\n"
"s_cmp_eq_u32 s85,0\n"
"s_cselect_b32 s79,0x4f00000,s94\n"
T_"v[46:47],v194 offset:29184\n"
M_"v[132:135],v[0:3],a[80:83],v[132:135]\n"
"buffer_store_dword v254,off,s[52:55],s79 sc1\n"
M_"v[132:135],v[4:7],a[84:87],v[132:135]\n"
"v_mov_b32 v205,v243\n"
T_"v[48:49],v195 offset:16384\n"
M_"v[132:135],v[8:11],a[88:91],v[132:135]\n"
T_"v[50:51],v195 offset:16896\n"
T_"v[52:53],v195 offset:20480\n"
M_"v[132:135],v[12:15],a[92:95],v[132:135]\n"
W_"lgkmcnt(11)\n"
"v_accvgpr_write_b32 a129,v225\n"
T_"v[54:55],v195 offset:20992\n"
M_"v[144:147],v[16:19],a[64:67],v[144:147]\n"
A_"s36,s96,s87\n"
T_"v[56:57],v195 offset:24576\n"
"v_accvgpr_write_b32 a130,v226\n"
M_"v[144:147],v[20:23],a[68:71],v[144:147]\n"
T_"v[58:59],v195 offset:25088\n"
T_"v[60:61],v195 offset:28672\n"
M_"v[144:147],v[24:27],a[72:75],v[144:147]\n"
A_"s37,s98,s87\n"
"v_accvgpr_write_b32 a131,v227\n"
M_"v[144:147],v[28:31],a[76:79],v[144:147]\n"
T_"v[62:63],v195 offset:29184\n"
M_"v[148:151],v[16:19],a[80:83],v[148:151]\n"
A_"s57,s56,0x0\n"
"v_accvgpr_write_b32 a132,v208\n"
M_"v[148:151],v[20:23],a[84:87],v[148:151]\n"
M_"v[148:151],v[24:27],a[88:91],v[148:151]\n"
"s_cmp_lg_u32 s11,2\n"
"s_cbranch_scc1 L_m_ni6\n"
"buffer_inv sc1\n"
"L_m_ni6:\n"
M_"v[148:151],v[28:31],a[92:95],v[148:151]\n"
W_"lgkmcnt(8)\n"
"v_accvgpr_write_b32 a133,v209\n"
"s_sub_u32 s58,s34,1\n"
M_"v[160:163],v[32:35],a[64:67],v[160:163]\n"
"s_lshl_b32 s59,s34,1\n"
M_"v[160:163],v[36:39],a[68:71],v[160:163]\n"
"v_accvgpr_write_b32 a134,v210\n"
M_"v[160:163],v[40:43],a[72:75],v[160:163]\n"
"s_sub_u32 s59,13,s59\n"
M_"v[160:163],v[44:47],a[76:79],v[160:163]\n"
M_"v[164:167],v[32:35],a[80:83],v[164:167]\n"
"v_accvgpr_write_b32 a135,v211\n"
M_"v[164:167],v[36:39],a[84:87],v[164:167]\n"
"s_mov_b64 s[62:63],0\n"
M_"v[164:167],v[40:43],a[88:91],v[164:167]\n"
M_"v[164:167],v[44:47],a[92:95],v[164:167]\n"
W_"lgkmcnt(0)\n"
"v_add_u32 v245,0x10000,v244\n"
"v_accvgpr_write_b32 a144,v228\n"
M_"v[176:179],v[48:51],a[64:67],v[176:179]\n"
M_"v[176:179],v[52:55],a[68:71],v[176:179]\n"
"L_pt0_1:\n"
"s_cbranch_execz L_pr0\n"
"v_bfe_u32 v251,v244,4,4\n"
"v_accvgpr_write_b32 a145,v229\n"
M_"v[176:179],v[56:59],a[72:75],v[176:179]\n"
M_"v[176:179],v[60:63],a[76:79],v[176:179]\n"
"v_lshrrev_b32 v246,8,v244\n"
M_"v[180:183],v[48:51],a[80:83],v[180:183]\n"
"v_accvgpr_write_b32 a146,v230\n"
M_"v[180:183],v[52:55],a[84:87],v[180:183]\n"
"v_lshlrev_b32 v246,2,v246\n"
M_"v[180:183],v[56:59],a[88:91],v[180:183]\n"
"v_accvgpr_write_b32 a147,v231\n"
M_"v[180:183],v[60:63],a[92:95],v[180:183]\n"
M_"v[136:139],v[0:3],a[96:99],v[136:139]\n"
"v_sub_u32 v251,v251,v246\n"
M_"v[136:139],v[4:7],a[100:103],v[136:139]\n"
"v_accvgpr_write_b32 a148,v212\n"
M_"v[136:139],v[8:11],a[104:107],v[136:139]\n"
"s_lshl_b32 s60,s31,4\n"
M_"v[136:139],v[12:15],a[108:111],v[136:139]\n"
M_"v[140:143],v[0:3],a[112:115],v[140:143]\n"
"s_barrier\n"
M_"v[140:143],v[4:7],a[116:119],v[140:143]\n"
"v_add_u32 v251,s60,v251\n"
"v_accvgpr_write_b32 a149,v213\n"
M_"v[140:143],v[8:11],a[120:123],v[140:143]\n"
"s_cmp_eq_u32 s34,0\n"
"s_cselect_b32 s60,0xff800000,0\n"
"v_accvgpr_write_b32 a150,v214\n"
M_"v[140:143],v[12:15],a[124:127],v[140:143]\n"
A_"s75,s68,131200\n"
M_"v[152:155],v[16:19],a[96:99],v[152:155]\n"
"v_and_b32 v206,96,v244\n"
"v_and_b32 v207,16,v244\n"
M_"v[152:155],v[20:23],a[100:103],v[152:155]\n"
A_"m0,s56,114688\n"
T_"v[0:1],v192 offset:32768\n"
"v_accvgpr_write_b32 a151,v215\n"
M_"v[152:155],v[24:27],a[104:107],v[152:155]\n"
"s_and_b32 s10,s30,0xe6\n"
T_"v[2:3],v192 offset:33280\n"
"v_lshl_add_u32 v207,v206,2,v207\n"
M_"v[152:155],v[28:31],a[108:111],v[152:155]\n"
B_"v200,s[48:51],s75 offen offset:0  lds\n"
M_"v[156:159],v[16:19],a[112:115],v[156:159]\n"
"s_lshl4_add_u32 s10,s10,0x9e2000\n"
"v_mov_b32 v253,s60\n"
"v_add_lshl_u32 v207,s10,v207,3\n"
M_"v[156:159],v[20:23],a[116:119],v[156:159]\n"
"v_add_u32 v207,20,v207\n"
T_"v[4:5],v192 offset:36864\n"
M_"v[156:159],v[24:27],a[120:123],v[156:159]\n"
"buffer_load_dword v206,v207,s[52:55],0 offen sc1\n"
M_"v[156:159],v[28:31],a[124:127],v[156:159]\n"
B_"v201,s[48:51],s75 offen offset:1024  lds\n"
M_"v[168:171],v[32:35],a[96:99],v[168:171]\n"
"v_accvgpr_write_b32 a160,v232\n"
T_"v[6:7],v192 offset:37376\n"
M_"v[168:171],v[36:39],a[100:103],v[168:171]\n"
"v_bfe_i32 v246,v244,5,1\n"
"v_accvgpr_write_b32 a161,v233\n"
M_"v[168:171],v[40:43],a[104:107],v[168:171]\n"
T_"v[8:9],v192 offset:40960\n"
"v_and_b32 v246,0xaaaaaaaa,v246\n"
M_"v[168:171],v[44:47],a[108:111],v[168:171]\n"
B_"v202,s[48:51],s75 offen offset:2048  lds\n"
M_"v[172:175],v[32:35],a[112:115],v[172:175]\n"
T_"v[10:11],v192 offset:41472\n"
T_"v[12:13],v192 offset:45056\n"
M_"v[172:175],v[36:39],a[116:119],v[172:175]\n"
T_"v[14:15],v192 offset:45568\n"
T_"v[16:17],v193 offset:32768\n"
M_"v[172:175],v[40:43],a[120:123],v[172:175]\n"
"v_or_b32 v246,0x44444444,v246\n"
"v_accvgpr_write_b32 a162,v234\n"
M_"v[172:175],v[44:47],a[124:127],v[172:175]\n"
T_"v[18:19],v193 offset:33280\n"
"v_add_u32 v240,48,v244\n"
M_"v[184:187],v[48:51],a[96:99],v[184:187]\n"
B_"v203,s[48:51],s75 offen offset:3072  lds\n"
M_"v[184:187],v[52:55],a[100:103],v[184:187]\n"
"v_accvgpr_write_b32 a163,v235\n"
T_"v[20:21],v193 offset:36864\n"
M_"v[184:187],v[56:59],a[104:107],v[184:187]\n"
T_"v[22:23],v193 offset:37376\n"
"v_bfe_i32 v240,v240,5,1\n"
M_"v[184:187],v[60:63],a[108:111],v[184:187]\n"
A_"s75,s68,262272\n"
"v_accvgpr_write_b32 a164,v216\n"
T_"v[24:25],v193 offset:40960\n"
M_"v[188:191],v[48:51],a[112:115],v[188:191]\n"
"s_cmp_lg_u32 s11,3\n"
"s_cbranch_scc1 L_m_ni7\n"
"buffer_inv sc1\n"
"L_m_ni7:\n"
M_"v[188:191],v[52:55],a[116:119],v[188:191]\n"
A_"m0,s56,81920\n"
T_"v[26:27],v193 offset:41472\n"
T_"v[28:29],v193 offset:45056\n"
T_"v[30:31],v193 offset:45568\n"
M_"v[188:191],v[56:59],a[120:123],v[188:191]\n"
"v_and_b32 v240,0x3f803f80,v240\n"
T_"v[32:33],v194 offset:32768\n"
M_"v[188:191],v[60:63],a[124:127],v[188:191]\n"
W_"lgkmcnt(9)\n"
"v_accvgpr_write_b32 a165,v217\n"
T_"v[34:35],v194 offset:33280\n"
M_"v[128:131],v[0:3],a[0:3],v[128:131]\n"
"v_mov_b32 v241,v240\n"
"v_accvgpr_write_b32 a166,v218\n"
M_"v[128:131],v[4:7],a[4:7],v[128:131]\n"
T_"v[36:37],v194 offset:36864\n"
"v_mov_b32 v242,v240\n"
M_"v[128:131],v[8:11],a[8:11],v[128:131]\n"
T_"v[38:39],v194 offset:37376\n"
"v_mov_b32 v243,v240\n"
M_"v[128:131],v[12:15],a[12:15],v[128:131]\n"
B_"v200,s[48:51],s75 offen offset:0  lds\n"
M_"v[132:135],v[0:3],a[16:19],v[132:135]\n"
"s_lshl_b32 s60,s33,12\n"
"v_accvgpr_write_b32 a167,v219\n"
T_"v[40:41],v194 offset:40960\n"
M_"v[132:135],v[4:7],a[20:23],v[132:135]\n"
A_"s60,s60,0x3a00000\n"
T_"v[42:43],v194 offset:41472\n"
T_"v[44:45],v194 offset:45056\n"
T_"v[46:47],v194 offset:45568\n"
M_"v[132:135],v[8:11],a[24:27],v[132:135]\n"
"s_lshl3_add_u32 s96,s96,s60\n"
T_"v[48:49],v195 offset:32768\n"
"v_accvgpr_write_b32 a176,v236\n"
M_"v[132:135],v[12:15],a[28:31],v[132:135]\n"
W_"lgkmcnt(9)\n"
T_"v[50:51],v195 offset:33280\n"
"v_accvgpr_write_b32 a177,v237\n"
M_"v[144:147],v[16:19],a[0:3],v[144:147]\n"
B_"v201,s[48:51],s75 offen offset:1024  lds\n"
M_"v[144:147],v[20:23],a[4:7],v[144:147]\n"
"s_lshl3_add_u32 s97,s97,s60\n"
T_"v[52:53],v195 offset:36864\n"
T_"v[54:55],v195 offset:37376\n"
M_"v[144:147],v[24:27],a[8:11],v[144:147]\n"
"s_lshl3_add_u32 s98,s98,s60\n"
"v_accvgpr_write_b32 a178,v238\n"
T_"v[56:57],v195 offset:40960\n"
M_"v[144:147],v[28:31],a[12:15],v[144:147]\n"
"s_lshl3_add_u32 s99,s99,s60\n"
T_"v[58:59],v195 offset:41472\n"
T_"v[60:61],v195 offset:45056\n"
M_"v[148:151],v[16:19],a[16:19],v[148:151]\n"
B_"v202,s[48:51],s75 offen offset:2048  lds\n"
M_"v[148:151],v[20:23],a[20:23],v[148:151]\n"
"s_mov_b32 s60,0x5d800000\n"
T_"v[62:63],v195 offset:45568\n"
"v_accvgpr_write_b32 a179,v239\n"
M_"v[148:151],v[24:27],a[24:27],v[148:151]\n"
"v_accvgpr_write_b32 a180,v220\n"
"v_accvgpr_write_b32 a181,v221\n"
M_"v[148:151],v[28:31],a[28:31],v[148:151]\n"
W_"lgkmcnt(8)\n"
B_"v203,s[48:51],s75 offen offset:3072  lds\n"
M_"v[160:163],v[32:35],a[0:3],v[160:163]\n"
"v_accvgpr_write_b32 a182,v222\n"
M_"v[160:163],v[36:39],a[4:7],v[160:163]\n"
"v_cmp_le_i32 s[64:65],0,v251\n"
M_"v[160:163],v[40:43],a[8:11],v[160:163]\n"
"v_cmp_le_i32 s[66:67],1,v251\n"
M_"v[160:163],v[44:47],a[12:15],v[160:163]\n"
"v_accvgpr_write_b32 a183,v223\n"
"v_cmp_le_i32 s[68:69],2,v251\n"
M_"v[164:167],v[32:35],a[16:19],v[164:167]\n"
M_"v[164:167],v[36:39],a[20:23],v[164:167]\n"
"v_cmp_le_i32 s[70:71],3,v251\n"
M_"v[164:167],v[40:43],a[24:27],v[164:167]\n"
"v_cmp_le_i32 s[72:73],16,v251\n"
M_"v[164:167],v[44:47],a[28:31],v[164:167]\n"
W_"lgkmcnt(0)\n"
M_"v[176:179],v[48:51],a[0:3],v[176:179]\n"
"v_cmp_le_i32 s[74:75],17,v251\n"
M_"v[176:179],v[52:55],a[4:7],v[176:179]\n"
"v_cmp_le_i32 s[76:77],18,v251\n"
M_"v[176:179],v[56:59],a[8:11],v[176:179]\n"
M_"v[176:179],v[60:63],a[12:15],v[176:179]\n"
"v_cmp_le_i32 s[78:79],19,v251\n"
M_"v[180:183],v[48:51],a[16:19],v[180:183]\n"
"v_cmp_le_i32 s[80:81],32,v251\n"
M_"v[180:183],v[52:55],a[20:23],v[180:183]\n"
"v_cmp_le_i32 s[82:83],33,v251\n"
M_"v[180:183],v[56:59],a[24:27],v[180:183]\n"
M_"v[180:183],v[60:63],a[28:31],v[180:183]\n"
"v_cmp_le_i32 s[84:85],34,v251\n"
M_"v[136:139],v[0:3],a[32:35],v[136:139]\n"
"v_cmp_le_i32 s[86:87],35,v251\n"
M_"v[136:139],v[4:7],a[36:39],v[136:139]\n"
M_"v[136:139],v[8:11],a[40:43],v[136:139]\n"
"v_cmp_le_i32 s[88:89],48,v251\n"
M_"v[136:139],v[12:15],a[44:47],v[136:139]\n"
"v_cmp_le_i32 s[90:91],49,v251\n"
M_"v[140:143],v[0:3],a[48:51],v[140:143]\n"
M_"v[140:143],v[4:7],a[52:55],v[140:143]\n"
"v_cmp_le_i32 s[92:93],50,v251\n"
M_"v[140:143],v[8:11],a[56:59],v[140:143]\n"
"v_cmp_le_i32 s[94:95],51,v251\n"
M_"v[140:143],v[12:15],a[60:63],v[140:143]\n"
M_"v[152:155],v[16:19],a[32:35],v[152:155]\n"
T_"v[0:1],v192 offset:0\n"
M_"v[152:155],v[20:23],a[36:39],v[152:155]\n"
T_"v[2:3],v192 offset:512\n"
K_"v224,v253,0,s[64:65]\n"
M_"v[152:155],v[24:27],a[40:43],v[152:155]\n"
W_"vmcnt(7)\n"
K_"v225,v253,0,s[66:67]\n"
M_"v[152:155],v[28:31],a[44:47],v[152:155]\n"
"v_cmp_ne_u32 s[12:13],s32,v206\n"
T_"v[4:5],v192 offset:4096\n"
M_"v[156:159],v[16:19],a[48:51],v[156:159]\n"
"s_cmp_lg_u64 s[12:13],0\n"
"s_cbranch_scc1 L_slow_PH\n"
"L_back_PH:\n"
M_"v[156:159],v[20:23],a[52:55],v[156:159]\n"
T_"v[6:7],v192 offset:4608\n"
K_"v226,v253,0,s[68:69]\n"
M_"v[156:159],v[24:27],a[56:59],v[156:159]\n"
T_"v[8:9],v192 offset:8192\n"
K_"v227,v253,0,s[70:71]\n"
M_"v[156:159],v[28:31],a[60:63],v[156:159]\n"
K_"v228,v253,0,s[72:73]\n"
M_"v[168:171],v[32:35],a[32:35],v[168:171]\n"
A_"s10,s14,0\n"
T_"v[10:11],v192 offset:8704\n"
K_"v229,v253,0,s[74:75]\n"
M_"v[168:171],v[36:39],a[36:39],v[168:171]\n"
B_"a[64:67],v244,s[52:55],s10 offen offset:0 sc1\n"
M_"v[168:171],v[40:43],a[40:43],v[168:171]\n"
T_"v[12:13],v192 offset:12288\n"
K_"v230,v253,0,s[76:77]\n"
M_"v[168:171],v[44:47],a[44:47],v[168:171]\n"
T_"v[14:15],v192 offset:12800\n"
T_"v[16:17],v193 offset:0\n"
M_"v[172:175],v[32:35],a[48:51],v[172:175]\n"
T_"v[18:19],v193 offset:512\n"
T_"v[20:21],v193 offset:4096\n"
M_"v[172:175],v[36:39],a[52:55],v[172:175]\n"
B_"a[68:71],v244,s[52:55],s10 offen offset:1024 sc1\n"
M_"v[172:175],v[40:43],a[56:59],v[172:175]\n"
T_"v[22:23],v193 offset:4608\n"
T_"v[24:25],v193 offset:8192\n"
M_"v[172:175],v[44:47],a[60:63],v[172:175]\n"
K_"v231,v253,0,s[78:79]\n"
K_"v232,v253,0,s[80:81]\n"
M_"v[184:187],v[48:51],a[32:35],v[184:187]\n"
T_"v[26:27],v193 offset:8704\n"
K_"v233,v253,0,s[82:83]\n"
M_"v[184:187],v[52:55],a[36:39],v[184:187]\n"
B_"a[72:75],v244,s[52:55],s10 offen offset:2048 sc1\n"
M_"v[184:187],v[56:59],a[40:43],v[184:187]\n"
T_"v[28:29],v193 offset:12288\n"
K_"v234,v253,0,s[84:85]\n"
M_"v[184:187],v[60:63],a[44:47],v[184:187]\n"
T_"v[30:31],v193 offset:12800\n"
T_"v[32:33],v194 offset:0\n"
M_"v[188:191],v[48:51],a[48:51],v[188:191]\n"
T_"v[34:35],v194 offset:512\n"
T_"v[36:37],v194 offset:4096\n"
M_"v[188:191],v[52:55],a[52:55],v[188:191]\n"
B_"a[76:79],v244,s[52:55],s10 offen offset:3072 sc1\n"
M_"v[188:191],v[56:59],a[56:59],v[188:191]\n"
T_"v[38:39],v194 offset:4608\n"
T_"v[40:41],v194 offset:8192\n"
M_"v[188:191],v[60:63],a[60:63],v[188:191]\n"
W_"lgkmcnt(13)\n"
K_"v235,v253,0,s[86:87]\n"
K_"v236,v253,0,s[88:89]\n"
M_"v[128:131],v[0:3],a[192:195],v[128:131]\n"
A_"s10,s15,0\n"
T_"v[42:43],v194 offset:8704\n"
K_"v237,v253,0,s[90:91]\n"
M_"v[128:131],v[4:7],a[196:199],v[128:131]\n"
B_"a[80:83],v244,s[52:55],s10 offen offset:0 sc1\n"
M_"v[128:131],v[8:11],a[200:203],v[128:131]\n"
T_"v[44:45],v194 offset:12288\n"
K_"v238,v253,0,s[92:93]\n"
M_"v[128:131],v[12:15],a[204:207],v[128:131]\n"
T_"v[46:47],v194 offset:12800\n"
T_"v[48:49],v195 offset:0\n"
M_"v[132:135],v[0:3],a[208:211],v[132:135]\n"
T_"v[50:51],v195 offset:512\n"
T_"v[52:53],v195 offset:4096\n"
M_"v[132:135],v[4:7],a[212:215],v[132:135]\n"
B_"a[84:87],v244,s[52:55],s10 offen offset:1024 sc1\n"
M_"v[132:135],v[8:11],a[216:219],v[132:135]\n"
"buffer_load_dword v204,v205,s[44:47],0 offen\n"
M_"v[132:135],v[12:15],a[220:223],v[132:135]\n"
W_"lgkmcnt(11)\n"
T_"v[54:55],v195 offset:4608\n"
T_"v[56:57],v195 offset:8192\n"
M_"v[144:147],v[16:19],a[192:195],v[144:147]\n"
T_"v[58:59],v195 offset:8704\n"
K_"v239,v253,0,s[94:95]\n"
M_"v[144:147],v[20:23],a[196:199],v[144:147]\n"
B_"a[88:91],v244,s[52:55],s10 offen offset:2048 sc1\n"
M_"v[144:147],v[24:27],a[200:203],v[144:147]\n"
T_"v[60:61],v195 offset:12288\n"
"v_mov_b64 v[208:209],0\n"
M_"v[144:147],v[28:31],a[204:207],v[144:147]\n"
T_"v[62:63],v195 offset:12800\n"
"v_mov_b64 v[210:211],0\n"
M_"v[148:151],v[16:19],a[208:211],v[148:151]\n"
"v_mov_b64 v[212:213],0\n"
"v_mov_b64 v[214:215],0\n"
M_"v[148:151],v[20:23],a[212:215],v[148:151]\n"
B_"a[92:95],v244,s[52:55],s10 offen offset:3072 sc1\n"
M_"v[148:151],v[24:27],a[216:219],v[148:151]\n"
"v_mov_b64 v[216:217],0\n"
"v_mov_b64 v[218:219],0\n"
M_"v[148:151],v[28:31],a[220:223],v[148:151]\n"
W_"lgkmcnt(8)\n"
"v_mov_b64 v[220:221],0\n"
"v_mov_b64 v[222:223],0\n"
M_"v[160:163],v[32:35],a[192:195],v[160:163]\n"
A_"s10,s14,4096\n"
M_"v[160:163],v[36:39],a[196:199],v[160:163]\n"
B_"a[96:99],v244,s[52:55],s10 offen offset:0 sc1\n"
M_"v[160:163],v[40:43],a[200:203],v[160:163]\n"
M_"v[160:163],v[44:47],a[204:207],v[160:163]\n"
M_"v[164:167],v[32:35],a[208:211],v[164:167]\n"
M_"v[164:167],v[36:39],a[212:215],v[164:167]\n"
B_"a[100:103],v244,s[52:55],s10 offen offset:1024 sc1\n"
M_"v[164:167],v[40:43],a[216:219],v[164:167]\n"
M_"v[164:167],v[44:47],a[220:223],v[164:167]\n"
W_"lgkmcnt(0)\n"
M_"v[176:179],v[48:51],a[192:195],v[176:179]\n"
M_"v[176:179],v[52:55],a[196:199],v[176:179]\n"
B_"a[104:107],v244,s[52:55],s10 offen offset:2048 sc1\n"
M_"v[176:179],v[56:59],a[200:203],v[176:179]\n"
M_"v[176:179],v[60:63],a[204:207],v[176:179]\n"
M_"v[180:183],v[48:51],a[208:211],v[180:183]\n"
M_"v[180:183],v[52:55],a[212:215],v[180:183]\n"
B_"a[108:111],v244,s[52:55],s10 offen offset:3072 sc1\n"
M_"v[180:183],v[56:59],a[216:219],v[180:183]\n"
C_"v128,v128,v129\n"
M_"v[180:183],v[60:63],a[220:223],v[180:183]\n"
C_"v129,v130,v131\n"
M_"v[136:139],v[0:3],a[224:227],v[136:139]\n"
A_"s10,s15,4096\n"
C_"v130,v144,v145\n"
M_"v[136:139],v[4:7],a[228:231],v[136:139]\n"
B_"a[112:115],v244,s[52:55],s10 offen offset:0 sc1\n"
M_"v[136:139],v[8:11],a[232:235],v[136:139]\n"
C_"v131,v146,v147\n"
C_"v160,v160,v161\n"
M_"v[136:139],v[12:15],a[236:239],v[136:139]\n"
C_"v161,v162,v163\n"
M_"v[140:143],v[0:3],a[240:243],v[140:143]\n"
W_"vmcnt(14) lgkmcnt(0)\n"
"s_barrier\n"
C_"v162,v176,v177\n"
M_"v[140:143],v[4:7],a[244:247],v[140:143]\n"
B_"a[116:119],v244,s[52:55],s10 offen offset:1024 sc1\n"
M_"v[140:143],v[8:11],a[248:251],v[140:143]\n"
C_"v163,v178,v179\n"
"v_accvgpr_write_b32 a136,v128\n"
M_"v[140:143],v[12:15],a[252:255],v[140:143]\n"
"v_accvgpr_write_b32 a137,v129\n"
M_"v[152:155],v[16:19],a[224:227],v[152:155]\n"
"v_xor_b32 v192,0x10000,v192\n"
"v_accvgpr_write_b32 a138,v130\n"
M_"v[152:155],v[20:23],a[228:231],v[152:155]\n"
B_"a[120:123],v244,s[52:55],s10 offen offset:2048 sc1\n"
M_"v[152:155],v[24:27],a[232:235],v[152:155]\n"
"v_xor_b32 v193,0x10000,v193\n"
"v_xor_b32 v194,0x10000,v194\n"
M_"v[152:155],v[28:31],a[236:239],v[152:155]\n"
"v_xor_b32 v195,0x10000,v195\n"
"v_accvgpr_write_b32 a139,v131\n"
M_"v[156:159],v[16:19],a[240:243],v[156:159]\n"
T_"v[0:1],v192 offset:0\n"
"v_accvgpr_write_b32 a140,v160\n"
M_"v[156:159],v[20:23],a[244:247],v[156:159]\n"
B_"a[124:127],v244,s[52:55],s10 offen offset:3072 sc1\n"
M_"v[156:159],v[24:27],a[248:251],v[156:159]\n"
T_"v[2:3],v192 offset:512\n"
T_"v[4:5],v192 offset:4096\n"
M_"v[156:159],v[28:31],a[252:255],v[156:159]\n"
T_"v[6:7],v192 offset:4608\n"
"v_accvgpr_write_b32 a141,v161\n"
M_"v[168:171],v[32:35],a[224:227],v[168:171]\n"
A_"s10,s14,8192\n"
T_"v[8:9],v192 offset:8192\n"
"v_accvgpr_write_b32 a142,v162\n"
M_"v[168:171],v[36:39],a[228:231],v[168:171]\n"
B_"a[0:3],v244,s[52:55],s10 offen offset:0 sc1\n"
M_"v[168:171],v[40:43],a[232:235],v[168:171]\n"
T_"v[10:11],v192 offset:8704\n"
T_"v[12:13],v192 offset:12288\n"
M_"v[168:171],v[44:47],a[236:239],v[168:171]\n"
T_"v[14:15],v192 offset:12800\n"
T_"v[16:17],v193 offset:0\n"
M_"v[172:175],v[32:35],a[240:243],v[172:175]\n"
T_"v[18:19],v193 offset:512\n"
T_"v[20:21],v193 offset:4096\n"
M_"v[172:175],v[36:39],a[244:247],v[172:175]\n"
B_"a[4:7],v244,s[52:55],s10 offen offset:1024 sc1\n"
M_"v[172:175],v[40:43],a[248:251],v[172:175]\n"
T_"v[22:23],v193 offset:4608\n"
T_"v[24:25],v193 offset:8192\n"
M_"v[172:175],v[44:47],a[252:255],v[172:175]\n"
"v_accvgpr_write_b32 a143,v163\n"
C_"v132,v132,v133\n"
M_"v[184:187],v[48:51],a[224:227],v[184:187]\n"
T_"v[26:27],v193 offset:8704\n"
C_"v133,v134,v135\n"
M_"v[184:187],v[52:55],a[228:231],v[184:187]\n"
B_"a[8:11],v244,s[52:55],s10 offen offset:2048 sc1\n"
M_"v[184:187],v[56:59],a[232:235],v[184:187]\n"
T_"v[28:29],v193 offset:12288\n"
C_"v134,v148,v149\n"
M_"v[184:187],v[60:63],a[236:239],v[184:187]\n"
T_"v[30:31],v193 offset:12800\n"
T_"v[32:33],v194 offset:0\n"
M_"v[188:191],v[48:51],a[240:243],v[188:191]\n"
T_"v[34:35],v194 offset:512\n"
T_"v[36:37],v194 offset:4096\n"
M_"v[188:191],v[52:55],a[244:247],v[188:191]\n"
B_"a[12:15],v244,s[52:55],s10 offen offset:3072 sc1\n"
A_"s10,s56,0\n"
A_"m0,s56,65536\n"
M_"v[188:191],v[56:59],a[248:251],v[188:191]\n"
B_"v244,s[40:43],s10 offen offset:0 lds\n"
M_"v[188:191],v[60:63],a[252:255],v[188:191]\n"
W_"lgkmcnt(11)\n"
T_"v[38:39],v194 offset:4608\n"
W_"vmcnt(18)\n"
M_"v[64:67],v[0:3],a[64:67],0\n"
A_"s44,s40,0x1000000\n"
"s_addc_u32 s45,s41,0\n"
T_"v[40:41],v194 offset:8192\n"
M_"v[64:67],v[4:7],a[68:71],v[64:67]\n"
B_"v244,s[40:43],s10 offen offset:1024 lds\n"
M_"v[64:67],v[8:11],a[72:75],v[64:67]\n"
T_"v[42:43],v194 offset:8704\n"
T_"v[44:45],v194 offset:12288\n"
M_"v[64:67],v[12:15],a[76:79],v[64:67]\n"
W_"vmcnt(14)\n"
T_"v[46:47],v194 offset:12800\n"
T_"v[48:49],v195 offset:0\n"
M_"v[68:71],v[0:3],a[80:83],0\n"
B_"v244,s[40:43],s10 offen offset:2048 lds\n"
M_"v[68:71],v[4:7],a[84:87],v[68:71]\n"
T_"v[50:51],v195 offset:512\n"
T_"v[52:53],v195 offset:4096\n"
M_"v[68:71],v[8:11],a[88:91],v[68:71]\n"
T_"v[54:55],v195 offset:4608\n"
T_"v[56:57],v195 offset:8192\n"
M_"v[68:71],v[12:15],a[92:95],v[68:71]\n"
W_"lgkmcnt(13)\n"
B_"v244,s[40:43],s10 offen offset:3072 lds\n"
M_"v[72:75],v[16:19],a[64:67],0\n"
A_"s10,s56,16384\n"
T_"v[58:59],v195 offset:8704\n"
C_"v135,v150,v151\n"
M_"v[72:75],v[20:23],a[68:71],v[72:75]\n"
A_"m0,s56,98304\n"
T_"v[60:61],v195 offset:12288\n"
C_"v164,v164,v165\n"
M_"v[72:75],v[24:27],a[72:75],v[72:75]\n"
C_"v165,v166,v167\n"
C_"v166,v180,v181\n"
M_"v[72:75],v[28:31],a[76:79],v[72:75]\n"
B_"v244,s[40:43],s10 offen offset:0 lds\n"
M_"v[76:79],v[16:19],a[80:83],0\n"
T_"v[62:63],v195 offset:12800\n"
"v_xor_b32 v192,0x20000,v192\n"
M_"v[76:79],v[20:23],a[84:87],v[76:79]\n"
"s_mov_b32 s46,0x80000\n"
"v_xor_b32 v193,0x20000,v193\n"
"v_xor_b32 v194,0x20000,v194\n"
M_"v[76:79],v[24:27],a[88:91],v[76:79]\n"
"v_xor_b32 v195,0x20000,v195\n"
T_"v[0:1],v192 offset:16384\n"
M_"v[76:79],v[28:31],a[92:95],v[76:79]\n"
W_"lgkmcnt(9)\n"
B_"v244,s[40:43],s10 offen offset:1024 lds\n"
M_"v[80:83],v[32:35],a[64:67],0\n"
T_"v[2:3],v192 offset:16896\n"
T_"v[4:5],v192 offset:20480\n"
M_"v[80:83],v[36:39],a[68:71],v[80:83]\n"
T_"v[6:7],v192 offset:20992\n"
T_"v[8:9],v192 offset:24576\n"
M_"v[80:83],v[40:43],a[72:75],v[80:83]\n"
B_"v244,s[40:43],s10 offen offset:2048 lds\n"
M_"v[80:83],v[44:47],a[76:79],v[80:83]\n"
T_"v[10:11],v192 offset:25088\n"
T_"v[12:13],v192 offset:28672\n"
M_"v[84:87],v[32:35],a[80:83],0\n"
T_"v[14:15],v192 offset:29184\n"
T_"v[16:17],v193 offset:16384\n"
M_"v[84:87],v[36:39],a[84:87],v[84:87]\n"
B_"v244,s[40:43],s10 offen offset:3072 lds\n"
M_"v[84:87],v[40:43],a[88:91],v[84:87]\n"
A_"s10,s15,8192\n"
T_"v[18:19],v193 offset:16896\n"
T_"v[20:21],v193 offset:20480\n"
M_"v[84:87],v[44:47],a[92:95],v[84:87]\n"
W_"lgkmcnt(11)\n"
T_"v[22:23],v193 offset:20992\n"
T_"v[24:25],v193 offset:24576\n"
M_"v[88:91],v[48:51],a[64:67],0\n"
T_"v[26:27],v193 offset:25088\n"
C_"v167,v182,v183\n"
M_"v[88:91],v[52:55],a[68:71],v[88:91]\n"
B_"a[16:19],v244,s[52:55],s10 offen offset:0 sc1\n"
M_"v[88:91],v[56:59],a[72:75],v[88:91]\n"
T_"v[28:29],v193 offset:28672\n"
"v_accvgpr_write_b32 a152,v132\n"
M_"v[88:91],v[60:63],a[76:79],v[88:91]\n"
T_"v[30:31],v193 offset:29184\n"
T_"v[32:33],v194 offset:16384\n"
M_"v[92:95],v[48:51],a[80:83],0\n"
B_"a[20:23],v244,s[52:55],s10 offen offset:1024 sc1\n"
M_"v[92:95],v[52:55],a[84:87],v[92:95]\n"
T_"v[34:35],v194 offset:16896\n"
T_"v[36:37],v194 offset:20480\n"
M_"v[92:95],v[56:59],a[88:91],v[92:95]\n"
T_"v[38:39],v194 offset:20992\n"
T_"v[40:41],v194 offset:24576\n"
M_"v[92:95],v[60:63],a[92:95],v[92:95]\n"
W_"lgkmcnt(13)\n"
B_"a[24:27],v244,s[52:55],s10 offen offset:2048 sc1\n"
M_"v[96:99],v[0:3],a[64:67],0\n"
T_"v[42:43],v194 offset:25088\n"
"v_accvgpr_write_b32 a153,v133\n"
M_"v[96:99],v[4:7],a[68:71],v[96:99]\n"
T_"v[44:45],v194 offset:28672\n"
"v_accvgpr_write_b32 a154,v134\n"
M_"v[96:99],v[8:11],a[72:75],v[96:99]\n"
B_"a[28:31],v244,s[52:55],s10 offen offset:3072 sc1\n"
M_"v[96:99],v[12:15],a[76:79],v[96:99]\n"
A_"s10,s14,12288\n"
T_"v[46:47],v194 offset:29184\n"
T_"v[48:49],v195 offset:16384\n"
M_"v[100:103],v[0:3],a[80:83],0\n"
T_"v[50:51],v195 offset:16896\n"
T_"v[52:53],v195 offset:20480\n"
M_"v[100:103],v[4:7],a[84:87],v[100:103]\n"
T_"v[54:55],v195 offset:20992\n"
"v_accvgpr_write_b32 a155,v135\n"
M_"v[100:103],v[8:11],a[88:91],v[100:103]\n"
B_"a[32:35],v244,s[52:55],s10 offen offset:0 sc1\n"
M_"v[100:103],v[12:15],a[92:95],v[100:103]\n"
W_"lgkmcnt(12)\n"
T_"v[56:57],v195 offset:24576\n"
"v_accvgpr_write_b32 a156,v164\n"
M_"v[104:107],v[16:19],a[64:67],0\n"
T_"v[58:59],v195 offset:25088\n"
"v_accvgpr_write_b32 a157,v165\n"
M_"v[104:107],v[20:23],a[68:71],v[104:107]\n"
T_"v[60:61],v195 offset:28672\n"
"v_accvgpr_write_b32 a158,v166\n"
M_"v[104:107],v[24:27],a[72:75],v[104:107]\n"
B_"a[36:39],v244,s[52:55],s10 offen offset:1024 sc1\n"
M_"v[104:107],v[28:31],a[76:79],v[104:107]\n"
A_"s10,s15,12288\n"
T_"v[62:63],v195 offset:29184\n"
"v_xor_b32 v192,0x20000,v192\n"
M_"v[108:111],v[16:19],a[80:83],0\n"
"v_xor_b32 v193,0x20000,v193\n"
"v_xor_b32 v194,0x20000,v194\n"
M_"v[108:111],v[20:23],a[84:87],v[108:111]\n"
"v_xor_b32 v195,0x20000,v195\n"
T_"v[0:1],v192 offset:16384\n"
M_"v[108:111],v[24:27],a[88:91],v[108:111]\n"
B_"a[40:43],v244,s[52:55],s10 offen offset:0 sc1\n"
M_"v[108:111],v[28:31],a[92:95],v[108:111]\n"
W_"lgkmcnt(9)\n"
T_"v[2:3],v192 offset:16896\n"
T_"v[4:5],v192 offset:20480\n"
M_"v[112:115],v[32:35],a[64:67],0\n"
T_"v[6:7],v192 offset:20992\n"
T_"v[8:9],v192 offset:24576\n"
M_"v[112:115],v[36:39],a[68:71],v[112:115]\n"
T_"v[10:11],v192 offset:25088\n"
"v_accvgpr_write_b32 a159,v167\n"
M_"v[112:115],v[40:43],a[72:75],v[112:115]\n"
B_"a[44:47],v244,s[52:55],s10 offen offset:1024 sc1\n"
M_"v[112:115],v[44:47],a[76:79],v[112:115]\n"
T_"v[12:13],v192 offset:28672\n"
T_"v[14:15],v192 offset:29184\n"
M_"v[116:119],v[32:35],a[80:83],0\n"
T_"v[16:17],v193 offset:16384\n"
T_"v[18:19],v193 offset:16896\n"
M_"v[116:119],v[36:39],a[84:87],v[116:119]\n"
T_"v[20:21],v193 offset:20480\n"
T_"v[22:23],v193 offset:20992\n"
M_"v[116:119],v[40:43],a[88:91],v[116:119]\n"
T_"v[24:25],v193 offset:24576\n"
C_"v136,v136,v137\n"
M_"v[116:119],v[44:47],a[92:95],v[116:119]\n"
W_"lgkmcnt(13)\n"
C_"v137,v138,v139\n"
C_"v138,v152,v153\n"
M_"v[120:123],v[48:51],a[64:67],0\n"
T_"v[26:27],v193 offset:25088\n"
C_"v139,v154,v155\n"
M_"v[120:123],v[52:55],a[68:71],v[120:123]\n"
T_"v[28:29],v193 offset:28672\n"
C_"v168,v168,v169\n"
M_"v[120:123],v[56:59],a[72:75],v[120:123]\n"
"s_barrier\n"
M_"v[120:123],v[60:63],a[76:79],v[120:123]\n"
T_"v[30:31],v193 offset:29184\n"
T_"v[32:33],v194 offset:16384\n"
M_"v[124:127],v[48:51],a[80:83],0\n"
A_"s10,s56,32768\n"
T_"v[34:35],v194 offset:16896\n"
T_"v[36:37],v194 offset:20480\n"
M_"v[124:127],v[52:55],a[84:87],v[124:127]\n"
A_"m0,s56,0\n"
T_"v[38:39],v194 offset:20992\n"
C_"v169,v170,v171\n"
M_"v[124:127],v[56:59],a[88:91],v[124:127]\n"
B_"v244,s[40:43],s10 offen offset:0 lds\n"
M_"v[124:127],v[60:63],a[92:95],v[124:127]\n"
W_"lgkmcnt(12)\n"
T_"v[40:41],v194 offset:24576\n"
W_"vmcnt(25)\n"
M_"v[64:67],v[0:3],a[96:99],v[64:67]\n"
T_"v[42:43],v194 offset:25088\n"
C_"v170,v184,v185\n"
M_"v[64:67],v[4:7],a[100:103],v[64:67]\n"
T_"v[44:45],v194 offset:28672\n"
C_"v171,v186,v187\n"
M_"v[64:67],v[8:11],a[104:107],v[64:67]\n"
B_"v244,s[40:43],s10 offen offset:1024 lds\n"
M_"v[64:67],v[12:15],a[108:111],v[64:67]\n"
W_"vmcnt(22)\n"
T_"v[46:47],v194 offset:29184\n"
T_"v[48:49],v195 offset:16384\n"
M_"v[68:71],v[0:3],a[112:115],v[68:71]\n"
"s_mov_b64 s[48:49],s[18:19]\n"
T_"v[50:51],v195 offset:16896\n"
T_"v[52:53],v195 offset:20480\n"
M_"v[68:71],v[4:7],a[116:119],v[68:71]\n"
"s_mov_b32 s50,0x400000\n"
T_"v[54:55],v195 offset:20992\n"
"v_accvgpr_write_b32 a168,v136\n"
M_"v[68:71],v[8:11],a[120:123],v[68:71]\n"
"s_mov_b32 s51,0x20000\n"
T_"v[56:57],v195 offset:24576\n"
"v_accvgpr_write_b32 a169,v137\n"
M_"v[68:71],v[12:15],a[124:127],v[68:71]\n"
W_"lgkmcnt(13)\n"
"v_accvgpr_write_b32 a170,v138\n"
"v_accvgpr_write_b32 a171,v139\n"
M_"v[72:75],v[16:19],a[96:99],v[72:75]\n"
B_"v244,s[40:43],s10 offen offset:2048 lds\n"
M_"v[72:75],v[20:23],a[100:103],v[72:75]\n"
"s_and_b32 s13,s30,6\n"
T_"v[58:59],v195 offset:25088\n"
T_"v[60:61],v195 offset:28672\n"
M_"v[72:75],v[24:27],a[104:107],v[72:75]\n"
"s_lshl1_add_u32 s13,s13,s31\n"
"s_and_b32 s15,s34,1\n"
"v_accvgpr_write_b32 a172,v168\n"
M_"v[72:75],v[28:31],a[108:111],v[72:75]\n"
"s_lshl1_add_u32 s13,s13,s15\n"
T_"v[62:63],v195 offset:29184\n"
T_"v[0:1],v192 offset:32768\n"
M_"v[76:79],v[16:19],a[112:115],v[76:79]\n"
B_"v244,s[40:43],s10 offen offset:3072 lds\n"
M_"v[76:79],v[20:23],a[116:119],v[76:79]\n"
"s_lshl_b32 s14,s13,17\n"
"s_lshl_b32 s21,s13,12\n"
"s_bfe_u32 s13,s30,0x20003\n"
M_"v[76:79],v[24:27],a[120:123],v[76:79]\n"
"s_lshl2_add_u32 s18,s13,s31\n"
T_"v[2:3],v192 offset:33280\n"
T_"v[4:5],v192 offset:36864\n"
M_"v[76:79],v[28:31],a[124:127],v[76:79]\n"
W_"lgkmcnt(11)\n"
"s_lshr_b32 s13,s18,1\n"
T_"v[6:7],v192 offset:37376\n"
M_"v[80:83],v[32:35],a[96:99],v[80:83]\n"
"s_lshl_b32 s13,s13,7\n"
A_"s14,s14,s13\n"
T_"v[8:9],v192 offset:40960\n"
M_"v[80:83],v[36:39],a[100:103],v[80:83]\n"
"s_andn2_b32 s13,1,s34\n"
T_"v[10:11],v192 offset:41472\n"
T_"v[12:13],v192 offset:45056\n"
M_"v[80:83],v[40:43],a[104:107],v[80:83]\n"
"s_lshl4_add_u32 s13,s13,s31\n"
"v_accvgpr_write_b32 a173,v169\n"
"v_accvgpr_write_b32 a174,v170\n"
M_"v[80:83],v[44:47],a[108:111],v[80:83]\n"
"s_lshl_b32 s16,s13,12\n"
T_"v[14:15],v192 offset:45568\n"
T_"v[16:17],v193 offset:32768\n"
M_"v[84:87],v[32:35],a[112:115],v[84:87]\n"
"s_lshl_b32 s13,s30,7\n"
A_"s17,s13,0x4f10000\n"
T_"v[18:19],v193 offset:33280\n"
M_"v[84:87],v[36:39],a[116:119],v[84:87]\n"
A_"s12,s17,4\n"
T_"v[20:21],v193 offset:36864\n"
T_"v[22:23],v193 offset:37376\n"
M_"v[84:87],v[40:43],a[120:123],v[84:87]\n"
"s_andn2_b32 s20,s17,0xfff\n"
T_"v[24:25],v193 offset:40960\n"
"v_accvgpr_write_b32 a175,v171\n"
M_"v[84:87],v[44:47],a[124:127],v[84:87]\n"
W_"lgkmcnt(13)\n"
"s_cmp_eq_u32 s31,0\n"
"s_cselect_b32 s17,s17,0x7f000000\n"
M_"v[88:91],v[48:51],a[96:99],v[88:91]\n"
"s_mov_b32 s61,s17\n"
T_"v[26:27],v193 offset:41472\n"
C_"v140,v140,v141\n"
M_"v[88:91],v[52:55],a[100:103],v[88:91]\n"
"s_and_b32 s13,s30,1\n"
T_"v[28:29],v193 offset:45056\n"
C_"v141,v142,v143\n"
M_"v[88:91],v[56:59],a[104:107],v[88:91]\n"
"s_lshl1_add_u32 s13,s34,s13\n"
"s_bfe_u32 s15,s30,0x20001\n"
C_"v142,v156,v157\n"
M_"v[88:91],v[60:63],a[108:111],v[88:91]\n"
"s_lshl2_add_u32 s13,s13,s15\n"
T_"v[30:31],v193 offset:45568\n"
T_"v[32:33],v194 offset:32768\n"
M_"v[92:95],v[48:51],a[112:115],v[92:95]\n"
"s_lshl4_add_u32 s18,s13,s18\n"
T_"v[34:35],v194 offset:33280\n"
T_"v[36:37],v194 offset:36864\n"
M_"v[92:95],v[52:55],a[116:119],v[92:95]\n"
"s_lshl_b32 s18,s18,10\n"
T_"v[38:39],v194 offset:37376\n"
C_"v143,v158,v159\n"
M_"v[92:95],v[56:59],a[120:123],v[92:95]\n"
A_"s18,s18,0x200000\n"
"s_lshl_b32 s19,s34,18\n"
T_"v[40:41],v194 offset:40960\n"
M_"v[92:95],v[60:63],a[124:127],v[92:95]\n"
W_"lgkmcnt(13)\n"
"s_sub_u32 s19,0x1e0000,s19\n"
W_"vmcnt(20)\n"
M_"v[64:67],v[0:3],a[0:3],v[64:67]\n"
"s_lshl_b32 s13,s13,17\n"
T_"v[42:43],v194 offset:41472\n"
C_"v172,v172,v173\n"
M_"v[64:67],v[4:7],a[4:7],v[64:67]\n"
A_"s21,s21,s13\n"
A_"s21,s21,0x3a00000\n"
T_"v[44:45],v194 offset:45056\n"
M_"v[64:67],v[8:11],a[8:11],v[64:67]\n"
"s_xor_b32 s22,s21,0x40000\n"
C_"v173,v174,v175\n"
C_"v174,v188,v189\n"
M_"v[64:67],v[12:15],a[12:15],v[64:67]\n"
W_"vmcnt(8)\n"
"s_xor_b32 s23,s21,0x60000\n"
T_"v[46:47],v194 offset:45568\n"
M_"v[68:71],v[0:3],a[16:19],v[68:71]\n"
"s_xor_b32 s21,s21,0x20000\n"
"s_mov_b32 s24,1\n"
"v_lshrrev_b32 v197,6,v244\n"
M_"v[68:71],v[4:7],a[20:23],v[68:71]\n"
T_"v[48:49],v195 offset:32768\n"
"v_lshlrev_b32 v255,7,v197\n"
M_"v[68:71],v[8:11],a[24:27],v[68:71]\n"
"s_lshl_b32 s13,s31,1\n"
T_"v[50:51],v195 offset:33280\n"
"v_lshrrev_b32 v197,7,v244\n"
M_"v[68:71],v[12:15],a[28:31],v[68:71]\n"
W_"lgkmcnt(10)\n"
"v_xor_b32 v198,s13,v197\n"
T_"v[52:53],v195 offset:36864\n"
M_"v[72:75],v[16:19],a[0:3],v[72:75]\n"
"v_and_b32 v198,3,v198\n"
T_"v[54:55],v195 offset:37376\n"
M_"v[72:75],v[20:23],a[4:7],v[72:75]\n"
"v_lshl_add_u32 v255,v198,5,v255\n"
"v_bfe_u32 v198,v244,4,2\n"
M_"v[72:75],v[24:27],a[8:11],v[72:75]\n"
"v_lshl_add_u32 v255,v198,3,v255\n"
T_"v[56:57],v195 offset:40960\n"
M_"v[72:75],v[28:31],a[12:15],v[72:75]\n"
A_"s13,s31,32\n"
T_"v[58:59],v195 offset:41472\n"
T_"v[60:61],v195 offset:45056\n"
M_"v[76:79],v[16:19],a[16:19],v[76:79]\n"
"s_lshl_b32 s13,s13,12\n"
T_"v[62:63],v195 offset:45568\n"
T_"v[0:1],v192 offset:49152\n"
M_"v[76:79],v[20:23],a[20:23],v[76:79]\n"
"v_add_u32 v255,s13,v255\n"
"v_lshlrev_b32 v196,10,v197\n"
M_"v[76:79],v[24:27],a[24:27],v[76:79]\n"
T_"v[2:3],v192 offset:49664\n"
"v_and_b32 v197,6,v197\n"
M_"v[76:79],v[28:31],a[28:31],v[76:79]\n"
W_"lgkmcnt(10)\n"
"v_bfe_u32 v198,v244,4,3\n"
"v_xor_b32 v197,v197,v198\n"
M_"v[80:83],v[32:35],a[0:3],v[80:83]\n"
"v_lshl_add_u32 v196,v197,4,v196\n"
"v_add_u32 v197,7168,v196\n"
"v_add_u32 v198,14336,v196\n"
"v_add_u32 v199,21504,v196\n"
M_"v[80:83],v[36:39],a[4:7],v[80:83]\n"
T_"v[4:5],v192 offset:53248\n"
T_"v[6:7],v192 offset:53760\n"
M_"v[80:83],v[40:43],a[8:11],v[80:83]\n"
"s_xor_b32 s15,s14,0\n"
T_"v[8:9],v193 offset:49152\n"
T_"v[10:11],v193 offset:49664\n"
M_"v[80:83],v[44:47],a[12:15],v[80:83]\n"
"s_and_b32 s13,s16,0xffff\n"
T_"v[12:13],v193 offset:53248\n"
T_"v[14:15],v193 offset:53760\n"
M_"v[84:87],v[32:35],a[16:19],v[84:87]\n"
A_"m0,s13,131072\n"
T_"v[16:17],v194 offset:49152\n"
T_"v[18:19],v194 offset:49664\n"
M_"v[84:87],v[36:39],a[20:23],v[84:87]\n"
B_"v196,s[48:51],s15 offen offset:0  lds\n"
M_"v[84:87],v[40:43],a[24:27],v[84:87]\n"
T_"v[20:21],v194 offset:53248\n"
T_"v[22:23],v194 offset:53760\n"
M_"v[84:87],v[44:47],a[28:31],v[84:87]\n"
W_"lgkmcnt(12)\n"
T_"v[24:25],v195 offset:49152\n"
C_"v175,v190,v191\n"
M_"v[88:91],v[48:51],a[0:3],v[88:91]\n"
T_"v[26:27],v195 offset:49664\n"
"v_accvgpr_write_b32 a184,v140\n"
M_"v[88:91],v[52:55],a[4:7],v[88:91]\n"
B_"v197,s[48:51],s15 offen offset:1024  lds\n"
M_"v[88:91],v[56:59],a[8:11],v[88:91]\n"
T_"v[28:29],v195 offset:53248\n"
"v_accvgpr_write_b32 a185,v141\n"
M_"v[88:91],v[60:63],a[12:15],v[88:91]\n"
T_"v[30:31],v195 offset:53760\n"
T_"v[32:33],v192 offset:57344\n"
M_"v[92:95],v[48:51],a[16:19],v[92:95]\n"
T_"v[34:35],v192 offset:57856\n"
T_"v[36:37],v192 offset:61440\n"
M_"v[92:95],v[52:55],a[20:23],v[92:95]\n"
B_"v198,s[48:51],s15 offen offset:2048  lds\n"
M_"v[92:95],v[56:59],a[24:27],v[92:95]\n"
T_"v[38:39],v192 offset:61952\n"
T_"v[40:41],v193 offset:57344\n"
M_"v[92:95],v[60:63],a[28:31],v[92:95]\n"
W_"lgkmcnt(13)\n"
"v_accvgpr_write_b32 a186,v142\n"
W_"vmcnt(9)\n"
M_"v[64:67],v[0:3],a[32:35],v[64:67]\n"
T_"v[42:43],v193 offset:57856\n"
"v_accvgpr_write_b32 a187,v143\n"
M_"v[64:67],v[4:7],a[36:39],v[64:67]\n"
W_"vmcnt(7)\n"
T_"v[44:45],v193 offset:61440\n"
"v_accvgpr_write_b32 a188,v172\n"
M_"v[68:71],v[0:3],a[40:43],v[68:71]\n"
B_"v199,s[48:51],s15 offen offset:3072  lds\n"
M_"v[68:71],v[4:7],a[44:47],v[68:71]\n"
"s_xor_b32 s15,s14,128\n"
T_"v[46:47],v193 offset:61952\n"
T_"v[48:49],v194 offset:57344\n"
M_"v[72:75],v[8:11],a[32:35],v[72:75]\n"
"s_and_b32 s13,s16,0xffff\n"
T_"v[50:51],v194 offset:57856\n"
T_"v[52:53],v194 offset:61440\n"
M_"v[72:75],v[12:15],a[36:39],v[72:75]\n"
A_"m0,s13,147456\n"
T_"v[54:55],v194 offset:61952\n"
"v_accvgpr_write_b32 a189,v173\n"
M_"v[76:79],v[8:11],a[40:43],v[76:79]\n"
B_"v196,s[48:51],s15 offen offset:0  lds\n"
M_"v[76:79],v[12:15],a[44:47],v[76:79]\n"
W_"lgkmcnt(12)\n"
T_"v[56:57],v195 offset:57344\n"
"v_accvgpr_write_b32 a190,v174\n"
M_"v[80:83],v[16:19],a[32:35],v[80:83]\n"
T_"v[58:59],v195 offset:57856\n"
C_"v64,v64,v65\n"
M_"v[80:83],v[20:23],a[36:39],v[80:83]\n"
T_"v[60:61],v195 offset:61440\n"
C_"v65,v66,v67\n"
M_"v[84:87],v[16:19],a[40:43],v[84:87]\n"
B_"v197,s[48:51],s15 offen offset:1024  lds\n"
M_"v[84:87],v[20:23],a[44:47],v[84:87]\n"
T_"v[62:63],v195 offset:61952\n"
"v_xor_b32 v192,0x10000,v192\n"
M_"v[88:91],v[24:27],a[32:35],v[88:91]\n"
C_"v66,v72,v73\n"
"v_xor_b32 v193,0x10000,v193\n"
M_"v[88:91],v[28:31],a[36:39],v[88:91]\n"
C_"v67,v74,v75\n"
"v_xor_b32 v194,0x10000,v194\n"
M_"v[92:95],v[24:27],a[40:43],v[92:95]\n"
S_"v[64:67],v244,s[52:55],s36 offen offset:0 sc1\n"
M_"v[92:95],v[28:31],a[44:47],v[92:95]\n"
W_"lgkmcnt(8)\n"
"v_xor_b32 v195,0x10000,v195\n"
T_"v[0:1],v192 offset:49152\n"
M_"v[96:99],v[32:35],a[32:35],v[96:99]\n"
T_"v[2:3],v192 offset:49664\n"
T_"v[4:5],v192 offset:53248\n"
M_"v[96:99],v[36:39],a[36:39],v[96:99]\n"
T_"v[6:7],v192 offset:53760\n"
T_"v[8:9],v192 offset:57344\n"
M_"v[100:103],v[32:35],a[40:43],v[100:103]\n"
B_"v198,s[48:51],s15 offen offset:2048  lds\n"
M_"v[100:103],v[36:39],a[44:47],v[100:103]\n"
T_"v[10:11],v192 offset:57856\n"
T_"v[12:13],v192 offset:61440\n"
M_"v[104:107],v[40:43],a[32:35],v[104:107]\n"
T_"v[14:15],v192 offset:61952\n"
T_"v[16:17],v193 offset:49152\n"
M_"v[104:107],v[44:47],a[36:39],v[104:107]\n"
B_"v199,s[48:51],s15 offen offset:3072  lds\n"
M_"v[108:111],v[40:43],a[40:43],v[108:111]\n"
T_"v[18:19],v193 offset:49664\n"
T_"v[20:21],v193 offset:53248\n"
M_"v[108:111],v[44:47],a[44:47],v[108:111]\n"
W_"lgkmcnt(11)\n"
T_"v[22:23],v193 offset:53760\n"
C_"v68,v68,v69\n"
M_"v[112:115],v[48:51],a[32:35],v[112:115]\n"
"s_barrier\n"
M_"v[112:115],v[52:55],a[36:39],v[112:115]\n"
C_"v69,v70,v71\n"
T_"v[24:25],v193 offset:57344\n"
M_"v[116:119],v[48:51],a[40:43],v[116:119]\n"
C_"v70,v76,v77\n"
T_"v[26:27],v193 offset:57856\n"
M_"v[116:119],v[52:55],a[44:47],v[116:119]\n"
T_"v[28:29],v193 offset:61440\n"
T_"v[30:31],v193 offset:61952\n"
M_"v[120:123],v[56:59],a[32:35],v[120:123]\n"
C_"v71,v78,v79\n"
T_"v[32:33],v194 offset:49152\n"
M_"v[120:123],v[60:63],a[36:39],v[120:123]\n"
S_"v[68:71],v244,s[52:55],s37 offen offset:0 sc1\n"
M_"v[124:127],v[56:59],a[40:43],v[124:127]\n"
T_"v[34:35],v194 offset:49664\n"
T_"v[36:37],v194 offset:53248\n"
M_"v[124:127],v[60:63],a[44:47],v[124:127]\n"
W_"lgkmcnt(11)\n"
T_"v[38:39],v194 offset:53760\n"
D_"a[192:195],v245 offset:0\n"
M_"v[96:99],v[0:3],a[96:99],v[96:99]\n"
A_"s10,s56,0\n"
T_"v[40:41],v194 offset:57344\n"
T_"v[42:43],v194 offset:57856\n"
M_"v[96:99],v[4:7],a[100:103],v[96:99]\n"
A_"m0,s56,49152\n"
T_"v[44:45],v194 offset:61440\n"
"v_accvgpr_write_b32 a191,v175\n"
M_"v[96:99],v[8:11],a[104:107],v[96:99]\n"
B_"v244,s[44:47],s10 offen offset:0 lds\n"
M_"v[96:99],v[12:15],a[108:111],v[96:99]\n"
T_"v[46:47],v194 offset:61952\n"
T_"v[48:49],v195 offset:49152\n"
M_"v[100:103],v[0:3],a[112:115],v[100:103]\n"
D_"a[196:199],v245 offset:1024\n"
T_"v[50:51],v195 offset:49664\n"
M_"v[100:103],v[4:7],a[116:119],v[100:103]\n"
T_"v[52:53],v195 offset:53248\n"
T_"v[54:55],v195 offset:53760\n"
M_"v[100:103],v[8:11],a[120:123],v[100:103]\n"
B_"v244,s[44:47],s10 offen offset:1024 lds\n"
M_"v[100:103],v[12:15],a[124:127],v[100:103]\n"
W_"lgkmcnt(14)\n"
D_"a[200:203],v245 offset:2048\n"
T_"v[56:57],v195 offset:57344\n"
M_"v[104:107],v[16:19],a[96:99],v[104:107]\n"
T_"v[58:59],v195 offset:57856\n"
D_"a[204:207],v245 offset:3072\n"
M_"v[104:107],v[20:23],a[100:103],v[104:107]\n"
T_"v[60:61],v195 offset:61440\n"
C_"v80,v80,v81\n"
M_"v[104:107],v[24:27],a[104:107],v[104:107]\n"
B_"v244,s[44:47],s10 offen offset:2048 lds\n"
M_"v[104:107],v[28:31],a[108:111],v[104:107]\n"
T_"v[62:63],v195 offset:61952\n"
C_"v81,v82,v83\n"
M_"v[108:111],v[16:19],a[112:115],v[108:111]\n"
T_"v[0:1],v192 offset:16384\n"
C_"v82,v88,v89\n"
M_"v[108:111],v[20:23],a[116:119],v[108:111]\n"
T_"v[2:3],v192 offset:16896\n"
C_"v83,v90,v91\n"
M_"v[108:111],v[24:27],a[120:123],v[108:111]\n"
S_"v[80:83],v244,s[52:55],s36 offen offset:1024 sc1\n"
M_"v[108:111],v[28:31],a[124:127],v[108:111]\n"
W_"lgkmcnt(13)\n"
T_"v[4:5],v192 offset:20480\n"
D_"a[208:211],v245 offset:4096\n"
M_"v[112:115],v[32:35],a[96:99],v[112:115]\n"
T_"v[6:7],v192 offset:20992\n"
T_"v[8:9],v192 offset:24576\n"
M_"v[112:115],v[36:39],a[100:103],v[112:115]\n"
D_"a[212:215],v245 offset:5120\n"
T_"v[10:11],v192 offset:25088\n"
M_"v[112:115],v[40:43],a[104:107],v[112:115]\n"
B_"v244,s[44:47],s10 offen offset:3072 lds\n"
M_"v[112:115],v[44:47],a[108:111],v[112:115]\n"
T_"v[12:13],v192 offset:28672\n"
T_"v[14:15],v192 offset:29184\n"
M_"v[116:119],v[32:35],a[112:115],v[116:119]\n"
T_"v[16:17],v193 offset:16384\n"
T_"v[18:19],v193 offset:16896\n"
M_"v[116:119],v[36:39],a[116:119],v[116:119]\n"
D_"a[216:219],v245 offset:6144\n"
T_"v[20:21],v193 offset:20480\n"
M_"v[116:119],v[40:43],a[120:123],v[116:119]\n"
T_"v[22:23],v193 offset:20992\n"
D_"a[220:223],v245 offset:7168\n"
M_"v[116:119],v[44:47],a[124:127],v[116:119]\n"
W_"lgkmcnt(15)\n"
C_"v84,v84,v85\n"
C_"v85,v86,v87\n"
M_"v[120:123],v[48:51],a[96:99],v[120:123]\n"
T_"v[24:25],v193 offset:24576\n"
C_"v86,v92,v93\n"
M_"v[120:123],v[52:55],a[100:103],v[120:123]\n"
T_"v[26:27],v193 offset:25088\n"
T_"v[28:29],v193 offset:28672\n"
M_"v[120:123],v[56:59],a[104:107],v[120:123]\n"
C_"v87,v94,v95\n"
D_"a[224:227],v245 offset:8192\n"
M_"v[120:123],v[60:63],a[108:111],v[120:123]\n"
S_"v[84:87],v244,s[52:55],s37 offen offset:1024 sc1\n"
M_"v[124:127],v[48:51],a[112:115],v[124:127]\n"
A_"s10,s56,49152\n"
T_"v[30:31],v193 offset:29184\n"
T_"v[32:33],v194 offset:16384\n"
M_"v[124:127],v[52:55],a[116:119],v[124:127]\n"
A_"m0,s56,32768\n"
T_"v[34:35],v194 offset:16896\n"
T_"v[36:37],v194 offset:20480\n"
M_"v[124:127],v[56:59],a[120:123],v[124:127]\n"
T_"v[38:39],v194 offset:20992\n"
D_"a[228:231],v245 offset:9216\n"
M_"v[124:127],v[60:63],a[124:127],v[124:127]\n"
W_"lgkmcnt(15)\n"
T_"v[40:41],v194 offset:24576\n"
D_"a[232:235],v245 offset:10240\n"
M_"v[96:99],v[0:3],a[0:3],v[96:99]\n"
B_"v244,s[40:43],s10 offen offset:0 lds\n"
M_"v[96:99],v[4:7],a[4:7],v[96:99]\n"
T_"v[42:43],v194 offset:25088\n"
T_"v[44:45],v194 offset:28672\n"
M_"v[96:99],v[8:11],a[8:11],v[96:99]\n"
D_"a[236:239],v245 offset:11264\n"
M_"v[96:99],v[12:15],a[12:15],v[96:99]\n"
T_"v[46:47],v194 offset:29184\n"
T_"v[48:49],v195 offset:16384\n"
M_"v[100:103],v[0:3],a[16:19],v[100:103]\n"
B_"v244,s[40:43],s10 offen offset:1024 lds\n"
M_"v[100:103],v[4:7],a[20:23],v[100:103]\n"
T_"v[50:51],v195 offset:16896\n"
T_"v[52:53],v195 offset:20480\n"
M_"v[100:103],v[8:11],a[24:27],v[100:103]\n"
T_"v[54:55],v195 offset:20992\n"
T_"v[56:57],v195 offset:24576\n"
M_"v[100:103],v[12:15],a[28:31],v[100:103]\n"
W_"lgkmcnt(15)\n"
D_"a[240:243],v245 offset:12288\n"
D_"a[244:247],v245 offset:13312\n"
M_"v[104:107],v[16:19],a[0:3],v[104:107]\n"
B_"v244,s[40:43],s10 offen offset:2048 lds\n"
M_"v[104:107],v[20:23],a[4:7],v[104:107]\n"
T_"v[58:59],v195 offset:25088\n"
T_"v[60:61],v195 offset:28672\n"
M_"v[104:107],v[24:27],a[8:11],v[104:107]\n"
M_"v[104:107],v[28:31],a[12:15],v[104:107]\n"
B_"v244,s[40:43],s10 offen offset:3072 lds\n"
M_"v[108:111],v[16:19],a[16:19],v[108:111]\n"
A_"s40,s40,0x10000\n"
"s_addc_u32 s41,s41,0\n"
T_"v[62:63],v195 offset:29184\n"
M_"v[108:111],v[20:23],a[20:23],v[108:111]\n"
"s_mov_b32 s42,0x70000\n"
D_"a[248:251],v245 offset:14336\n"
M_"v[108:111],v[24:27],a[24:27],v[108:111]\n"
A_"s44,s44,0x4000\n"
"s_addc_u32 s45,s45,0\n"
C_"v96,v96,v97\n"
M_"v[108:111],v[28:31],a[28:31],v[108:111]\n"
W_"lgkmcnt(11)\n"
C_"v97,v98,v99\n"
C_"v98,v104,v105\n"
M_"v[112:115],v[32:35],a[0:3],v[112:115]\n"
"s_mov_b32 s46,0x7c000\n"
D_"a[252:255],v245 offset:15360\n"
C_"v99,v106,v107\n"
M_"v[112:115],v[36:39],a[4:7],v[112:115]\n"
S_"v[96:99],v244,s[52:55],s36 offen offset:2048 sc1\n"
M_"v[112:115],v[40:43],a[8:11],v[112:115]\n"
M_"v[112:115],v[44:47],a[12:15],v[112:115]\n"
M_"v[116:119],v[32:35],a[16:19],v[116:119]\n"
"v_mov_b32 v249,v196\n"
M_"v[116:119],v[36:39],a[20:23],v[116:119]\n"
M_"v[116:119],v[40:43],a[24:27],v[116:119]\n"
M_"v[116:119],v[44:47],a[28:31],v[116:119]\n"
W_"lgkmcnt(2)\n"
"v_mov_b32 v250,v197\n"
M_"v[120:123],v[48:51],a[0:3],v[120:123]\n"
M_"v[120:123],v[52:55],a[4:7],v[120:123]\n"
M_"v[120:123],v[56:59],a[8:11],v[120:123]\n"
M_"v[120:123],v[60:63],a[12:15],v[120:123]\n"
"v_mov_b32 v251,v198\n"
M_"v[124:127],v[48:51],a[16:19],v[124:127]\n"
M_"v[124:127],v[52:55],a[20:23],v[124:127]\n"
M_"v[124:127],v[56:59],a[24:27],v[124:127]\n"
"v_mov_b32 v253,v199\n"
C_"v100,v100,v101\n"
M_"v[124:127],v[60:63],a[28:31],v[124:127]\n"
C_"v101,v102,v103\n"
C_"v102,v108,v109\n"
W_"lgkmcnt(0)\n"
M_"v[32:35],a[192:195],a[160:163],0\n"
C_"v103,v110,v111\n"
M_"v[32:35],a[196:199],a[164:167],v[32:35]\n"
S_"v[100:103],v244,s[52:55],s37 offen offset:2048 sc1\n"
M_"v[32:35],a[200:203],a[168:171],v[32:35]\n"
M_"v[32:35],a[204:207],a[172:175],v[32:35]\n"
M_"v[48:51],a[192:195],a[176:179],0\n"
M_"v[48:51],a[196:199],a[180:183],v[48:51]\n"
M_"v[48:51],a[200:203],a[184:187],v[48:51]\n"
M_"v[48:51],a[204:207],a[188:191],v[48:51]\n"
"s_nop 3\n"
"v_max3_f32 v200,v32,v33,v34\n"
"v_max_f32 v200,v200,v35\n"
M_"v[16:19],a[192:195],a[144:147],0\n"
"v_mov_b32 v201,v200\n"
"s_nop 0\n"
M_"v[16:19],a[196:199],a[148:151],v[16:19]\n"
"v_permlane32_swap_b32 v200,v201\n"
"v_max_f32 v200,v200,v201\n"
M_"v[16:19],a[200:203],a[152:155],v[16:19]\n"
"v_mov_b32 v201,v200\n"
"s_nop 0\n"
M_"v[16:19],a[204:207],a[156:159],v[16:19]\n"
"v_permlane16_swap_b32 v200,v201\n"
"v_min_f32_e64 v202,-v200,-v201\n"
M_"v[0:3],a[192:195],a[128:131],v[224:227]\n"
"v_min_f32_e64 v203,-v200,-v201\n"
"v_mov_b32 v200,v202\n"
M_"v[0:3],a[196:199],a[132:135],v[0:3]\n"
"v_mov_b32 v201,v202\n"
"v_max3_f32 v204,v48,v49,v50\n"
M_"v[0:3],a[200:203],a[136:139],v[0:3]\n"
"v_max_f32 v204,v204,v51\n"
"v_mov_b32 v205,v204\n"
M_"v[0:3],a[204:207],a[140:143],v[0:3]\n"
"s_nop 0\n"
"v_permlane32_swap_b32 v204,v205\n"
"v_max_f32 v204,v204,v205\n"
M_"v[4:7],a[208:211],a[128:131],v[228:231]\n"
"v_mov_b32 v205,v204\n"
D_"a[192:195],v245 offset:32768\n"
M_"v[4:7],a[212:215],a[132:135],v[4:7]\n"
"s_nop 0\n"
"v_permlane16_swap_b32 v204,v205\n"
"v_min_f32_e64 v206,-v204,-v205\n"
M_"v[4:7],a[216:219],a[136:139],v[4:7]\n"
"v_min_f32_e64 v207,-v204,-v205\n"
"v_mov_b32 v204,v206\n"
M_"v[4:7],a[220:223],a[140:143],v[4:7]\n"
"v_mov_b32 v205,v206\n"
"v_max3_f32 v196,v16,v17,v18\n"
M_"v[8:11],a[224:227],a[128:131],v[232:235]\n"
"v_max_f32 v196,v196,v19\n"
"v_mov_b32 v197,v196\n"
M_"v[8:11],a[228:231],a[132:135],v[8:11]\n"
"s_nop 0\n"
D_"a[196:199],v245 offset:33792\n"
D_"a[200:203],v245 offset:34816\n"
M_"v[8:11],a[232:235],a[136:139],v[8:11]\n"
D_"a[204:207],v245 offset:35840\n"
"v_permlane32_swap_b32 v196,v197\n"
M_"v[8:11],a[236:239],a[140:143],v[8:11]\n"
C_"v112,v112,v113\n"
C_"v113,v114,v115\n"
M_"v[36:39],a[208:211],a[160:163],v[200:203]\n"
"v_max_f32 v196,v196,v197\n"
"v_mov_b32 v197,v196\n"
M_"v[36:39],a[212:215],a[164:167],v[36:39]\n"
"s_nop 0\n"
C_"v114,v120,v121\n"
"v_permlane16_swap_b32 v196,v197\n"
M_"v[36:39],a[216:219],a[168:171],v[36:39]\n"
C_"v115,v122,v123\n"
"v_min_f32_e64 v198,-v196,-v197\n"
M_"v[36:39],a[220:223],a[172:175],v[36:39]\n"
S_"v[112:115],v244,s[52:55],s36 offen offset:3072 sc1\n"
M_"v[52:55],a[208:211],a[176:179],v[204:207]\n"
"v_min_f32_e64 v199,-v196,-v197\n"
"v_mov_b32 v196,v198\n"
M_"v[52:55],a[212:215],a[180:183],v[52:55]\n"
"v_mov_b32 v197,v198\n"
"v_max3_f32 v192,v0,v1,v2\n"
M_"v[52:55],a[216:219],a[184:187],v[52:55]\n"
"v_max_f32 v192,v192,v3\n"
"v_mov_b32 v193,v192\n"
M_"v[52:55],a[220:223],a[188:191],v[52:55]\n"
"s_nop 0\n"
"v_permlane32_swap_b32 v192,v193\n"
"v_max_f32 v192,v192,v193\n"
M_"v[40:43],a[224:227],a[160:163],v[200:203]\n"
"v_mov_b32 v193,v192\n"
"v_add_f32 v32,v32,v202\n"
M_"v[40:43],a[228:231],a[164:167],v[40:43]\n"
"s_nop 0\n"
"v_permlane16_swap_b32 v192,v193\n"
"v_min_f32_e64 v194,-v192,-v193\n"
M_"v[40:43],a[232:235],a[168:171],v[40:43]\n"
"v_min_f32_e64 v195,-v192,-v193\n"
"v_mov_b32 v192,v194\n"
M_"v[40:43],a[236:239],a[172:175],v[40:43]\n"
"v_mov_b32 v193,v194\n"
"v_add_f32 v33,v33,v202\n"
M_"v[20:23],a[208:211],a[144:147],v[196:199]\n"
"v_add_f32 v34,v34,v202\n"
"v_add_f32 v35,v35,v202\n"
M_"v[20:23],a[212:215],a[148:151],v[20:23]\n"
"v_add_f32 v48,v48,v206\n"
"v_add_f32 v49,v49,v206\n"
M_"v[20:23],a[216:219],a[152:155],v[20:23]\n"
C_"v116,v116,v117\n"
C_"v117,v118,v119\n"
M_"v[20:23],a[220:223],a[156:159],v[20:23]\n"
C_"v118,v124,v125\n"
"v_add_f32 v50,v50,v206\n"
M_"v[56:59],a[224:227],a[176:179],v[204:207]\n"
C_"v119,v126,v127\n"
"v_add_f32 v51,v51,v206\n"
M_"v[56:59],a[228:231],a[180:183],v[56:59]\n"
S_"v[116:119],v244,s[52:55],s37 offen offset:3072 sc1\n"
M_"v[56:59],a[232:235],a[184:187],v[56:59]\n"
"v_add_f32 v16,v16,v198\n"
"v_add_f32 v17,v17,v198\n"
M_"v[56:59],a[236:239],a[188:191],v[56:59]\n"
"v_add_f32 v18,v18,v198\n"
"v_add_f32 v19,v19,v198\n"
M_"v[24:27],a[224:227],a[144:147],v[196:199]\n"
D_"a[208:211],v245 offset:36864\n"
D_"a[212:215],v245 offset:37888\n"
M_"v[24:27],a[228:231],a[148:151],v[24:27]\n"
D_"a[216:219],v245 offset:38912\n"
D_"a[220:223],v245 offset:39936\n"
M_"v[24:27],a[232:235],a[152:155],v[24:27]\n"
"v_add_f32 v0,v0,v194\n"
"v_add_f32 v1,v1,v194\n"
M_"v[24:27],a[236:239],a[156:159],v[24:27]\n"
"v_add_f32 v2,v2,v194\n"
"v_add_f32 v3,v3,v194\n"
M_"v[12:15],a[240:243],a[128:131],v[236:239]\n"
"v_add_f32 v4,v4,v194\n"
"v_add_f32 v5,v5,v194\n"
M_"v[12:15],a[244:247],a[132:135],v[12:15]\n"
"v_add_f32 v6,v6,v194\n"
"v_add_f32 v7,v7,v194\n"
M_"v[12:15],a[248:251],a[136:139],v[12:15]\n"
D_"a[224:227],v245 offset:40960\n"
D_"a[228:231],v245 offset:41984\n"
M_"v[12:15],a[252:255],a[140:143],v[12:15]\n"
D_"a[232:235],v245 offset:43008\n"
D_"a[236:239],v245 offset:44032\n"
M_"v[28:31],a[240:243],a[144:147],v[196:199]\n"
"v_add_f32 v8,v8,v194\n"
"v_add_f32 v9,v9,v194\n"
M_"v[28:31],a[244:247],a[148:151],v[28:31]\n"
"v_add_f32 v10,v10,v194\n"
"v_add_f32 v11,v11,v194\n"
M_"v[28:31],a[248:251],a[152:155],v[28:31]\n"
E_"v36,v36\n"
M_"v[28:31],a[252:255],a[156:159],v[28:31]\n"
E_"v37,v37\n"
M_"v[44:47],a[240:243],a[160:163],v[200:203]\n"
E_"v38,v38\n"
M_"v[44:47],a[244:247],a[164:167],v[44:47]\n"
E_"v39,v39\n"
M_"v[44:47],a[248:251],a[168:171],v[44:47]\n"
"v_add_f32 v12,v12,v194\n"
"v_add_f32 v13,v13,v194\n"
M_"v[44:47],a[252:255],a[172:175],v[44:47]\n"
"v_add_f32 v14,v14,v194\n"
"v_add_f32 v15,v15,v194\n"
M_"v[60:63],a[240:243],a[176:179],v[204:207]\n"
C_"v146,v36,v37\n"
C_"v147,v38,v39\n"
M_"v[60:63],a[244:247],a[180:183],v[60:63]\n"
M_"v[60:63],a[248:251],a[184:187],v[60:63]\n"
M_"v[60:63],a[252:255],a[188:191],v[60:63]\n"
D_"a[240:243],v245 offset:45056\n"
D_"a[244:247],v245 offset:46080\n"
D_"a[248:251],v245 offset:47104\n"
D_"a[252:255],v245 offset:48128\n"
W_"vmcnt(24) lgkmcnt(8)\n"
"L_m_att:\n"
"s_cmp_eq_u32 s34,0\n"
"s_cbranch_scc1 L_k2_a0\n"
M_"v[64:67],a[192:195],a[128:131],v[192:195]\n"
"s_barrier\n"
E_"v0,v0\n"
M_"v[64:67],a[196:199],a[132:135],v[64:67]\n"
"s_xor_b32 s57,s57,0x10000\n"
"v_xor_b32 v245,0x10000,v245\n"
E_"v1,v1\n"
M_"v[64:67],a[200:203],a[136:139],v[64:67]\n"
A_"m0,s57,16384\n"
C_"v128,v0,v1\n"
E_"v2,v2\n"
M_"v[64:67],a[204:207],a[140:143],v[64:67]\n"
B_"v244,s[44:47],s56 offen offset:0 lds\n"
E_"v3,v3\n"
M_"v[80:83],a[192:195],a[144:147],v[196:199]\n"
C_"v129,v2,v3\n"
E_"v4,v4\n"
M_"v[80:83],a[196:199],a[148:151],v[80:83]\n"
E_"v5,v5\n"
M_"v[80:83],a[200:203],a[152:155],v[80:83]\n"
C_"v130,v4,v5\n"
E_"v6,v6\n"
M_"v[80:83],a[204:207],a[156:159],v[80:83]\n"
E_"v7,v7\n"
M_"v[96:99],a[192:195],a[160:163],v[200:203]\n"
B_"v244,s[44:47],s56 offen offset:1024 lds\n"
C_"v131,v6,v7\n"
M_"v[96:99],a[196:199],a[164:167],v[96:99]\n"
E_"v8,v8\n"
E_"v9,v9\n"
M_"v[96:99],a[200:203],a[168:171],v[96:99]\n"
C_"v132,v8,v9\n"
E_"v10,v10\n"
M_"v[96:99],a[204:207],a[172:175],v[96:99]\n"
E_"v11,v11\n"
M_"v[112:115],a[192:195],a[176:179],v[204:207]\n"
C_"v133,v10,v11\n"
E_"v12,v12\n"
M_"v[112:115],a[196:199],a[180:183],v[112:115]\n"
B_"v244,s[44:47],s56 offen offset:2048 lds\n"
M_"v[112:115],a[200:203],a[184:187],v[112:115]\n"
E_"v13,v13\n"
M_"v[112:115],a[204:207],a[188:191],v[112:115]\n"
D_"a[192:195],v245 offset:0\n"
D_"a[196:199],v245 offset:1024\n"
D_"a[200:203],v245 offset:2048\n"
M_"v[68:71],a[208:211],a[128:131],v[192:195]\n"
D_"a[204:207],v245 offset:3072\n"
C_"v134,v12,v13\n"
E_"v14,v14\n"
M_"v[68:71],a[212:215],a[132:135],v[68:71]\n"
E_"v15,v15\n"
M_"v[68:71],a[216:219],a[136:139],v[68:71]\n"
B_"v244,s[44:47],s56 offen offset:3072 lds\n"
C_"v135,v14,v15\n"
M_"v[68:71],a[220:223],a[140:143],v[68:71]\n"
E_"v16,v16\n"
M_"v[84:87],a[208:211],a[144:147],v[196:199]\n"
E_"v17,v17\n"
M_"v[84:87],a[212:215],a[148:151],v[84:87]\n"
C_"v136,v16,v17\n"
E_"v18,v18\n"
M_"v[84:87],a[216:219],a[152:155],v[84:87]\n"
E_"v19,v19\n"
E_"v20,v20\n"
M_"v[84:87],a[220:223],a[156:159],v[84:87]\n"
C_"v137,v18,v19\n"
E_"v21,v21\n"
M_"v[100:103],a[208:211],a[160:163],v[200:203]\n"
A_"m0,s57,0\n"
C_"v138,v20,v21\n"
E_"v22,v22\n"
M_"v[100:103],a[212:215],a[164:167],v[100:103]\n"
B_"v244,s[40:43],s56 offen offset:0 lds\n"
M_"v[100:103],a[216:219],a[168:171],v[100:103]\n"
E_"v23,v23\n"
M_"v[100:103],a[220:223],a[172:175],v[100:103]\n"
C_"v139,v22,v23\n"
E_"v24,v24\n"
M_"v[116:119],a[208:211],a[176:179],v[204:207]\n"
E_"v25,v25\n"
E_"v26,v26\n"
M_"v[116:119],a[212:215],a[180:183],v[116:119]\n"
C_"v140,v24,v25\n"
E_"v27,v27\n"
M_"v[116:119],a[216:219],a[184:187],v[116:119]\n"
B_"v244,s[40:43],s56 offen offset:1024 lds\n"
C_"v141,v26,v27\n"
M_"v[116:119],a[220:223],a[188:191],v[116:119]\n"
W_"lgkmcnt(4)\n"
D_"a[208:211],v245 offset:4096\n"
D_"a[212:215],v245 offset:5120\n"
D_"a[216:219],v245 offset:6144\n"
M_"v[72:75],a[224:227],a[128:131],v[192:195]\n"
D_"a[220:223],v245 offset:7168\n"
E_"v28,v28\n"
M_"v[72:75],a[228:231],a[132:135],v[72:75]\n"
E_"v29,v29\n"
M_"v[72:75],a[232:235],a[136:139],v[72:75]\n"
C_"v142,v28,v29\n"
E_"v30,v30\n"
M_"v[72:75],a[236:239],a[140:143],v[72:75]\n"
B_"v244,s[40:43],s56 offen offset:2048 lds\n"
E_"v31,v31\n"
M_"v[88:91],a[224:227],a[144:147],v[196:199]\n"
C_"v143,v30,v31\n"
E_"v32,v32\n"
M_"v[88:91],a[228:231],a[148:151],v[88:91]\n"
E_"v33,v33\n"
M_"v[88:91],a[232:235],a[152:155],v[88:91]\n"
C_"v144,v32,v33\n"
E_"v34,v34\n"
M_"v[88:91],a[236:239],a[156:159],v[88:91]\n"
E_"v35,v35\n"
M_"v[104:107],a[224:227],a[160:163],v[200:203]\n"
C_"v145,v34,v35\n"
E_"v40,v40\n"
M_"v[104:107],a[228:231],a[164:167],v[104:107]\n"
B_"v244,s[40:43],s56 offen offset:3072 lds\n"
E_"v41,v41\n"
M_"v[104:107],a[232:235],a[168:171],v[104:107]\n"
C_"v148,v40,v41\n"
E_"v42,v42\n"
M_"v[104:107],a[236:239],a[172:175],v[104:107]\n"
E_"v43,v43\n"
M_"v[120:123],a[224:227],a[176:179],v[204:207]\n"
C_"v149,v42,v43\n"
E_"v44,v44\n"
M_"v[120:123],a[228:231],a[180:183],v[120:123]\n"
E_"v45,v45\n"
M_"v[120:123],a[232:235],a[184:187],v[120:123]\n"
A_"s56,s56,16384\n"
C_"v150,v44,v45\n"
E_"v46,v46\n"
M_"v[120:123],a[236:239],a[188:191],v[120:123]\n"
D_"a[224:227],v245 offset:8192\n"
D_"a[228:231],v245 offset:9216\n"
D_"a[232:235],v245 offset:10240\n"
M_"v[76:79],a[240:243],a[128:131],v[192:195]\n"
E_"v47,v47\n"
E_"v48,v48\n"
M_"v[76:79],a[244:247],a[132:135],v[76:79]\n"
D_"a[236:239],v245 offset:11264\n"
E_"v49,v49\n"
M_"v[76:79],a[248:251],a[136:139],v[76:79]\n"
C_"v151,v46,v47\n"
E_"v50,v50\n"
M_"v[76:79],a[252:255],a[140:143],v[76:79]\n"
C_"v152,v48,v49\n"
E_"v51,v51\n"
M_"v[92:95],a[240:243],a[144:147],v[196:199]\n"
C_"v153,v50,v51\n"
E_"v52,v52\n"
M_"v[92:95],a[244:247],a[148:151],v[92:95]\n"
E_"v53,v53\n"
M_"v[92:95],a[248:251],a[152:155],v[92:95]\n"
E_"v54,v54\n"
E_"v55,v55\n"
M_"v[92:95],a[252:255],a[156:159],v[92:95]\n"
C_"v154,v52,v53\n"
E_"v56,v56\n"
M_"v[108:111],a[240:243],a[160:163],v[200:203]\n"
C_"v155,v54,v55\n"
E_"v57,v57\n"
M_"v[108:111],a[244:247],a[164:167],v[108:111]\n"
C_"v156,v56,v57\n"
E_"v58,v58\n"
M_"v[108:111],a[248:251],a[168:171],v[108:111]\n"
E_"v59,v59\n"
M_"v[108:111],a[252:255],a[172:175],v[108:111]\n"
C_"v157,v58,v59\n"
E_"v60,v60\n"
M_"v[124:127],a[240:243],a[176:179],v[204:207]\n"
E_"v61,v61\n"
E_"v62,v62\n"
M_"v[124:127],a[244:247],a[180:183],v[124:127]\n"
C_"v158,v60,v61\n"
E_"v63,v63\n"
M_"v[124:127],a[248:251],a[184:187],v[124:127]\n"
C_"v159,v62,v63\n"
M_"v[124:127],a[252:255],a[188:191],v[124:127]\n"
D_"a[240:243],v245 offset:12288\n"
D_"a[244:247],v245 offset:13312\n"
D_"a[248:251],v245 offset:14336\n"
D_"a[252:255],v245 offset:15360\n"
W_"vmcnt(8) lgkmcnt(8)\n"
"s_cmp_lt_i32 s58,1\n"
"s_cbranch_scc1 L_k2_a1\n"
"v_smfmac_f32_16x16x64_bf16 v[208:211],v[240:243],v[128:135],v246\n"
"s_barrier\n"
E_"v64,v64\n"
"v_smfmac_f32_16x16x64_bf16 v[212:215],v[240:243],v[136:143],v246\n"
"buffer_store_dword v254,off,s[52:55],s17 offset:24 sc1\n"
M_"v[0:3],a[192:195],a[128:131],v[192:195]\n"
E_"v65,v65\n"
M_"v[0:3],a[196:199],a[132:135],v[0:3]\n"
A_"m0,s57,49152\n"
E_"v66,v66\n"
M_"v[0:3],a[200:203],a[136:139],v[0:3]\n"
C_"v160,v64,v65\n"
"v_add_f32 v209,v209,v64\n"
M_"v[0:3],a[204:207],a[140:143],v[0:3]\n"
B_"v244,s[44:47],s56 offen offset:0 lds\n"
M_"v[16:19],a[192:195],a[144:147],v[196:199]\n"
"v_add_f32 v210,v210,v65\n"
"v_add_f32 v209,v209,v66\n"
M_"v[16:19],a[196:199],a[148:151],v[16:19]\n"
E_"v67,v67\n"
M_"v[16:19],a[200:203],a[152:155],v[16:19]\n"
C_"v161,v66,v67\n"
"v_add_f32 v210,v210,v67\n"
M_"v[16:19],a[204:207],a[156:159],v[16:19]\n"
E_"v68,v68\n"
M_"v[32:35],a[192:195],a[160:163],v[200:203]\n"
E_"v69,v69\n"
M_"v[32:35],a[196:199],a[164:167],v[32:35]\n"
C_"v162,v68,v69\n"
"v_add_f32 v209,v209,v68\n"
M_"v[32:35],a[200:203],a[168:171],v[32:35]\n"
E_"v70,v70\n"
M_"v[32:35],a[204:207],a[172:175],v[32:35]\n"
"v_add_f32 v210,v210,v69\n"
"v_add_f32 v209,v209,v70\n"
M_"v[48:51],a[192:195],a[176:179],v[204:207]\n"
E_"v71,v71\n"
M_"v[48:51],a[196:199],a[180:183],v[48:51]\n"
C_"v163,v70,v71\n"
"v_add_f32 v210,v210,v71\n"
M_"v[48:51],a[200:203],a[184:187],v[48:51]\n"
E_"v72,v72\n"
M_"v[48:51],a[204:207],a[188:191],v[48:51]\n"
B_"v244,s[44:47],s56 offen offset:1024 lds\n"
M_"v[4:7],a[208:211],a[128:131],v[192:195]\n"
D_"a[192:195],v245 offset:49152\n"
D_"a[196:199],v245 offset:57344\n"
M_"v[4:7],a[212:215],a[132:135],v[4:7]\n"
D_"a[200:203],v245 offset:50176\n"
D_"a[204:207],v245 offset:58368\n"
M_"v[4:7],a[216:219],a[136:139],v[4:7]\n"
E_"v73,v73\n"
M_"v[4:7],a[220:223],a[140:143],v[4:7]\n"
C_"v164,v72,v73\n"
"v_add_f32 v209,v209,v72\n"
M_"v[20:23],a[208:211],a[144:147],v[196:199]\n"
E_"v74,v74\n"
M_"v[20:23],a[212:215],a[148:151],v[20:23]\n"
"v_add_f32 v210,v210,v73\n"
"v_add_f32 v209,v209,v74\n"
M_"v[20:23],a[216:219],a[152:155],v[20:23]\n"
E_"v75,v75\n"
M_"v[20:23],a[220:223],a[156:159],v[20:23]\n"
C_"v165,v74,v75\n"
"v_add_f32 v210,v210,v75\n"
M_"v[36:39],a[208:211],a[160:163],v[200:203]\n"
E_"v76,v76\n"
M_"v[36:39],a[212:215],a[164:167],v[36:39]\n"
E_"v77,v77\n"
M_"v[36:39],a[216:219],a[168:171],v[36:39]\n"
B_"v244,s[44:47],s56 offen offset:2048 lds\n"
M_"v[36:39],a[220:223],a[172:175],v[36:39]\n"
C_"v166,v76,v77\n"
"v_add_f32 v209,v209,v76\n"
M_"v[52:55],a[208:211],a[176:179],v[204:207]\n"
E_"v78,v78\n"
M_"v[52:55],a[212:215],a[180:183],v[52:55]\n"
"v_add_f32 v210,v210,v77\n"
"v_add_f32 v209,v209,v78\n"
M_"v[52:55],a[216:219],a[184:187],v[52:55]\n"
E_"v79,v79\n"
M_"v[52:55],a[220:223],a[188:191],v[52:55]\n"
W_"lgkmcnt(4)\n"
D_"a[208:211],v245 offset:51200\n"
D_"a[212:215],v245 offset:59392\n"
M_"v[8:11],a[224:227],a[128:131],v[192:195]\n"
D_"a[216:219],v245 offset:52224\n"
D_"a[220:223],v245 offset:60416\n"
M_"v[8:11],a[228:231],a[132:135],v[8:11]\n"
C_"v167,v78,v79\n"
"v_add_f32 v210,v210,v79\n"
M_"v[8:11],a[232:235],a[136:139],v[8:11]\n"
E_"v80,v80\n"
M_"v[8:11],a[236:239],a[140:143],v[8:11]\n"
E_"v81,v81\n"
M_"v[24:27],a[224:227],a[144:147],v[196:199]\n"
C_"v168,v80,v81\n"
"v_add_f32 v213,v213,v80\n"
M_"v[24:27],a[228:231],a[148:151],v[24:27]\n"
B_"v244,s[44:47],s56 offen offset:3072 lds\n"
M_"v[24:27],a[232:235],a[152:155],v[24:27]\n"
E_"v82,v82\n"
M_"v[24:27],a[236:239],a[156:159],v[24:27]\n"
"v_add_f32 v214,v214,v81\n"
"v_add_f32 v213,v213,v82\n"
M_"v[40:43],a[224:227],a[160:163],v[200:203]\n"
E_"v83,v83\n"
M_"v[40:43],a[228:231],a[164:167],v[40:43]\n"
C_"v169,v82,v83\n"
"v_add_f32 v214,v214,v83\n"
M_"v[40:43],a[232:235],a[168:171],v[40:43]\n"
E_"v84,v84\n"
M_"v[40:43],a[236:239],a[172:175],v[40:43]\n"
E_"v85,v85\n"
M_"v[56:59],a[224:227],a[176:179],v[204:207]\n"
C_"v170,v84,v85\n"
"v_add_f32 v213,v213,v84\n"
M_"v[56:59],a[228:231],a[180:183],v[56:59]\n"
E_"v86,v86\n"
M_"v[56:59],a[232:235],a[184:187],v[56:59]\n"
"v_add_f32 v214,v214,v85\n"
"v_add_f32 v213,v213,v86\n"
M_"v[56:59],a[236:239],a[188:191],v[56:59]\n"
D_"a[224:227],v245 offset:53248\n"
D_"a[228:231],v245 offset:61440\n"
M_"v[12:15],a[240:243],a[128:131],v[192:195]\n"
D_"a[232:235],v245 offset:54272\n"
D_"a[236:239],v245 offset:62464\n"
M_"v[12:15],a[244:247],a[132:135],v[12:15]\n"
A_"m0,s57,32768\n"
M_"v[12:15],a[248:251],a[136:139],v[12:15]\n"
E_"v87,v87\n"
M_"v[12:15],a[252:255],a[140:143],v[12:15]\n"
B_"v244,s[40:43],s56 offen offset:0 lds\n"
M_"v[28:31],a[240:243],a[144:147],v[196:199]\n"
C_"v171,v86,v87\n"
"v_add_f32 v214,v214,v87\n"
M_"v[28:31],a[244:247],a[148:151],v[28:31]\n"
E_"v88,v88\n"
M_"v[28:31],a[248:251],a[152:155],v[28:31]\n"
E_"v89,v89\n"
M_"v[28:31],a[252:255],a[156:159],v[28:31]\n"
C_"v172,v88,v89\n"
"v_add_f32 v213,v213,v88\n"
M_"v[44:47],a[240:243],a[160:163],v[200:203]\n"
E_"v90,v90\n"
M_"v[44:47],a[244:247],a[164:167],v[44:47]\n"
"v_add_f32 v214,v214,v89\n"
"v_add_f32 v213,v213,v90\n"
M_"v[44:47],a[248:251],a[168:171],v[44:47]\n"
E_"v91,v91\n"
M_"v[44:47],a[252:255],a[172:175],v[44:47]\n"
C_"v173,v90,v91\n"
"v_add_f32 v214,v214,v91\n"
M_"v[60:63],a[240:243],a[176:179],v[204:207]\n"
E_"v92,v92\n"
M_"v[60:63],a[244:247],a[180:183],v[60:63]\n"
E_"v93,v93\n"
M_"v[60:63],a[248:251],a[184:187],v[60:63]\n"
M_"v[60:63],a[252:255],a[188:191],v[60:63]\n"
W_"lgkmcnt(4)\n"
D_"a[240:243],v245 offset:55296\n"
D_"a[244:247],v245 offset:63488\n"
M_"a[0:3],a[192:195],v[128:131],0\n"
B_"v244,s[40:43],s56 offen offset:1024 lds\n"
M_"a[0:3],a[196:199],v[132:135],a[0:3]\n"
D_"a[248:251],v245 offset:56320\n"
D_"a[252:255],v245 offset:64512\n"
M_"a[4:7],a[192:195],v[136:139],0\n"
C_"v174,v92,v93\n"
"v_add_f32 v213,v213,v92\n"
M_"a[4:7],a[196:199],v[140:143],a[4:7]\n"
E_"v94,v94\n"
M_"a[8:11],a[192:195],v[144:147],0\n"
"v_add_f32 v214,v214,v93\n"
"v_add_f32 v213,v213,v94\n"
M_"a[8:11],a[196:199],v[148:151],a[8:11]\n"
E_"v95,v95\n"
M_"a[12:15],a[192:195],v[152:155],0\n"
C_"v175,v94,v95\n"
"v_add_f32 v214,v214,v95\n"
M_"a[12:15],a[196:199],v[156:159],a[12:15]\n"
D_"a[192:195],v245 offset:32768\n"
D_"a[196:199],v245 offset:33792\n"
M_"a[16:19],a[200:203],v[128:131],0\n"
E_"v96,v96\n"
M_"a[16:19],a[204:207],v[132:135],a[16:19]\n"
E_"v97,v97\n"
M_"a[20:23],a[200:203],v[136:139],0\n"
B_"v244,s[40:43],s56 offen offset:2048 lds\n"
M_"a[20:23],a[204:207],v[140:143],a[20:23]\n"
E_"v98,v98\n"
M_"a[24:27],a[200:203],v[144:147],0\n"
M_"a[24:27],a[204:207],v[148:151],a[24:27]\n"
E_"v99,v99\n"
M_"a[28:31],a[200:203],v[152:155],0\n"
C_"v176,v96,v97\n"
C_"v177,v98,v99\n"
M_"a[28:31],a[204:207],v[156:159],a[28:31]\n"
D_"a[200:203],v245 offset:34816\n"
D_"a[204:207],v245 offset:35840\n"
M_"a[32:35],a[208:211],v[128:131],0\n"
E_"v100,v100\n"
M_"a[32:35],a[212:215],v[132:135],a[32:35]\n"
E_"v101,v101\n"
M_"a[36:39],a[208:211],v[136:139],0\n"
E_"v102,v102\n"
M_"a[36:39],a[212:215],v[140:143],a[36:39]\n"
E_"v103,v103\n"
M_"a[40:43],a[208:211],v[144:147],0\n"
C_"v178,v100,v101\n"
C_"v179,v102,v103\n"
M_"a[40:43],a[212:215],v[148:151],a[40:43]\n"
E_"v104,v104\n"
M_"a[44:47],a[208:211],v[152:155],0\n"
B_"v244,s[40:43],s56 offen offset:3072 lds\n"
M_"a[44:47],a[212:215],v[156:159],a[44:47]\n"
D_"a[208:211],v245 offset:36864\n"
D_"a[212:215],v245 offset:37888\n"
M_"a[48:51],a[216:219],v[128:131],0\n"
E_"v105,v105\n"
M_"a[48:51],a[220:223],v[132:135],a[48:51]\n"
E_"v106,v106\n"
M_"a[52:55],a[216:219],v[136:139],0\n"
M_"a[52:55],a[220:223],v[140:143],a[52:55]\n"
E_"v107,v107\n"
M_"a[56:59],a[216:219],v[144:147],0\n"
C_"v180,v104,v105\n"
C_"v181,v106,v107\n"
M_"a[56:59],a[220:223],v[148:151],a[56:59]\n"
E_"v108,v108\n"
M_"a[60:63],a[216:219],v[152:155],0\n"
E_"v109,v109\n"
M_"a[60:63],a[220:223],v[156:159],a[60:63]\n"
W_"lgkmcnt(6)\n"
D_"a[216:219],v245 offset:38912\n"
D_"a[220:223],v245 offset:39936\n"
M_"a[64:67],a[224:227],v[128:131],0\n"
E_"v110,v110\n"
M_"a[64:67],a[228:231],v[132:135],a[64:67]\n"
A_"s56,s56,16384\n"
M_"a[68:71],a[224:227],v[136:139],0\n"
E_"v111,v111\n"
M_"a[68:71],a[228:231],v[140:143],a[68:71]\n"
C_"v182,v108,v109\n"
C_"v183,v110,v111\n"
M_"a[72:75],a[224:227],v[144:147],0\n"
E_"v112,v112\n"
M_"a[72:75],a[228:231],v[148:151],a[72:75]\n"
E_"v113,v113\n"
M_"a[76:79],a[224:227],v[152:155],0\n"
E_"v114,v114\n"
M_"a[76:79],a[228:231],v[156:159],a[76:79]\n"
D_"a[224:227],v245 offset:40960\n"
D_"a[228:231],v245 offset:41984\n"
M_"a[80:83],a[232:235],v[128:131],0\n"
E_"v115,v115\n"
M_"a[80:83],a[236:239],v[132:135],a[80:83]\n"
C_"v184,v112,v113\n"
C_"v185,v114,v115\n"
M_"a[84:87],a[232:235],v[136:139],0\n"
E_"v116,v116\n"
M_"a[84:87],a[236:239],v[140:143],a[84:87]\n"
E_"v117,v117\n"
M_"a[88:91],a[232:235],v[144:147],0\n"
E_"v118,v118\n"
M_"a[88:91],a[236:239],v[148:151],a[88:91]\n"
E_"v119,v119\n"
M_"a[92:95],a[232:235],v[152:155],0\n"
C_"v186,v116,v117\n"
C_"v187,v118,v119\n"
M_"a[92:95],a[236:239],v[156:159],a[92:95]\n"
D_"a[232:235],v245 offset:43008\n"
D_"a[236:239],v245 offset:44032\n"
M_"a[96:99],a[240:243],v[128:131],0\n"
M_"a[96:99],a[244:247],v[132:135],a[96:99]\n"
E_"v120,v120\n"
M_"a[100:103],a[240:243],v[136:139],0\n"
E_"v121,v121\n"
M_"a[100:103],a[244:247],v[140:143],a[100:103]\n"
E_"v122,v122\n"
M_"a[104:107],a[240:243],v[144:147],0\n"
E_"v123,v123\n"
M_"a[104:107],a[244:247],v[148:151],a[104:107]\n"
C_"v188,v120,v121\n"
C_"v189,v122,v123\n"
M_"a[108:111],a[240:243],v[152:155],0\n"
E_"v124,v124\n"
M_"a[108:111],a[244:247],v[156:159],a[108:111]\n"
D_"a[240:243],v245 offset:45056\n"
D_"a[244:247],v245 offset:46080\n"
M_"a[112:115],a[248:251],v[128:131],0\n"
E_"v125,v125\n"
M_"a[112:115],a[252:255],v[132:135],a[112:115]\n"
E_"v126,v126\n"
M_"a[116:119],a[248:251],v[136:139],0\n"
E_"v127,v127\n"
M_"a[116:119],a[252:255],v[140:143],a[116:119]\n"
C_"v190,v124,v125\n"
C_"v191,v126,v127\n"
M_"a[120:123],a[248:251],v[144:147],0\n"
M_"a[120:123],a[252:255],v[148:151],a[120:123]\n"
M_"a[124:127],a[248:251],v[152:155],0\n"
M_"a[124:127],a[252:255],v[156:159],a[124:127]\n"
D_"a[248:251],v245 offset:47104\n"
D_"a[252:255],v245 offset:48128\n"
"v_smfmac_f32_16x16x64_bf16 v[216:219],v[240:243],v[144:151],v246\n"
"v_smfmac_f32_16x16x64_bf16 v[220:223],v[240:243],v[152:159],v246\n"
W_"vmcnt(9) lgkmcnt(8)\n"
"s_branch L_k2_l4b\n"
".p2align 6\n"
"L_k2_loop4:\n"
M_"v[0:3],a[192:195],a[128:131],v[192:195]\n"
"s_barrier\n"
M_"v[0:3],a[196:199],a[132:135],v[0:3]\n"
A_"m0,s57,49152\n"
E_"v64,v64\n"
M_"v[0:3],a[200:203],a[136:139],v[0:3]\n"
E_"v65,v65\n"
M_"v[0:3],a[204:207],a[140:143],v[0:3]\n"
B_"v244,s[44:47],s56 offen offset:0 lds\n"
M_"v[16:19],a[192:195],a[144:147],v[196:199]\n"
C_"v160,v64,v65\n"
"v_add_f32 v209,v209,v64\n"
M_"v[16:19],a[196:199],a[148:151],v[16:19]\n"
E_"v66,v66\n"
M_"v[16:19],a[200:203],a[152:155],v[16:19]\n"
"v_add_f32 v210,v210,v65\n"
"v_add_f32 v209,v209,v66\n"
M_"v[16:19],a[204:207],a[156:159],v[16:19]\n"
E_"v67,v67\n"
M_"v[32:35],a[192:195],a[160:163],v[200:203]\n"
C_"v161,v66,v67\n"
"v_add_f32 v210,v210,v67\n"
M_"v[32:35],a[196:199],a[164:167],v[32:35]\n"
E_"v68,v68\n"
M_"v[32:35],a[200:203],a[168:171],v[32:35]\n"
E_"v69,v69\n"
M_"v[32:35],a[204:207],a[172:175],v[32:35]\n"
C_"v162,v68,v69\n"
"v_add_f32 v209,v209,v68\n"
M_"v[48:51],a[192:195],a[176:179],v[204:207]\n"
E_"v70,v70\n"
M_"v[48:51],a[196:199],a[180:183],v[48:51]\n"
"v_add_f32 v210,v210,v69\n"
"v_add_f32 v209,v209,v70\n"
M_"v[48:51],a[200:203],a[184:187],v[48:51]\n"
B_"v244,s[44:47],s56 offen offset:1024 lds\n"
M_"v[48:51],a[204:207],a[188:191],v[48:51]\n"
D_"a[192:195],v245 offset:49152\n"
D_"a[196:199],v245 offset:57344\n"
M_"v[4:7],a[208:211],a[128:131],v[192:195]\n"
D_"a[200:203],v245 offset:50176\n"
D_"a[204:207],v245 offset:58368\n"
M_"v[4:7],a[212:215],a[132:135],v[4:7]\n"
E_"v71,v71\n"
M_"v[4:7],a[216:219],a[136:139],v[4:7]\n"
C_"v163,v70,v71\n"
"v_add_f32 v210,v210,v71\n"
M_"v[4:7],a[220:223],a[140:143],v[4:7]\n"
E_"v72,v72\n"
M_"v[20:23],a[208:211],a[144:147],v[196:199]\n"
E_"v73,v73\n"
M_"v[20:23],a[212:215],a[148:151],v[20:23]\n"
C_"v164,v72,v73\n"
"v_add_f32 v209,v209,v72\n"
M_"v[20:23],a[216:219],a[152:155],v[20:23]\n"
E_"v74,v74\n"
M_"v[20:23],a[220:223],a[156:159],v[20:23]\n"
"v_add_f32 v210,v210,v73\n"
"v_add_f32 v209,v209,v74\n"
M_"v[36:39],a[208:211],a[160:163],v[200:203]\n"
E_"v75,v75\n"
M_"v[36:39],a[212:215],a[164:167],v[36:39]\n"
C_"v165,v74,v75\n"
"v_add_f32 v210,v210,v75\n"
M_"v[36:39],a[216:219],a[168:171],v[36:39]\n"
B_"v244,s[44:47],s56 offen offset:2048 lds\n"
M_"v[36:39],a[220:223],a[172:175],v[36:39]\n"
E_"v76,v76\n"
M_"v[52:55],a[208:211],a[176:179],v[204:207]\n"
E_"v77,v77\n"
M_"v[52:55],a[212:215],a[180:183],v[52:55]\n"
C_"v166,v76,v77\n"
"v_add_f32 v209,v209,v76\n"
M_"v[52:55],a[216:219],a[184:187],v[52:55]\n"
E_"v78,v78\n"
M_"v[52:55],a[220:223],a[188:191],v[52:55]\n"
W_"lgkmcnt(4)\n"
D_"a[208:211],v245 offset:51200\n"
D_"a[212:215],v245 offset:59392\n"
M_"v[8:11],a[224:227],a[128:131],v[192:195]\n"
D_"a[216:219],v245 offset:52224\n"
D_"a[220:223],v245 offset:60416\n"
M_"v[8:11],a[228:231],a[132:135],v[8:11]\n"
"v_add_f32 v210,v210,v77\n"
"v_add_f32 v209,v209,v78\n"
M_"v[8:11],a[232:235],a[136:139],v[8:11]\n"
E_"v79,v79\n"
M_"v[8:11],a[236:239],a[140:143],v[8:11]\n"
C_"v167,v78,v79\n"
"v_add_f32 v210,v210,v79\n"
M_"v[24:27],a[224:227],a[144:147],v[196:199]\n"
E_"v80,v80\n"
M_"v[24:27],a[228:231],a[148:151],v[24:27]\n"
B_"v244,s[44:47],s56 offen offset:3072 lds\n"
M_"v[24:27],a[232:235],a[152:155],v[24:27]\n"
E_"v81,v81\n"
M_"v[24:27],a[236:239],a[156:159],v[24:27]\n"
C_"v168,v80,v81\n"
"v_add_f32 v213,v213,v80\n"
M_"v[40:43],a[224:227],a[160:163],v[200:203]\n"
E_"v82,v82\n"
M_"v[40:43],a[228:231],a[164:167],v[40:43]\n"
"v_add_f32 v214,v214,v81\n"
"v_add_f32 v213,v213,v82\n"
M_"v[40:43],a[232:235],a[168:171],v[40:43]\n"
E_"v83,v83\n"
M_"v[40:43],a[236:239],a[172:175],v[40:43]\n"
C_"v169,v82,v83\n"
"v_add_f32 v214,v214,v83\n"
M_"v[56:59],a[224:227],a[176:179],v[204:207]\n"
E_"v84,v84\n"
M_"v[56:59],a[228:231],a[180:183],v[56:59]\n"
E_"v85,v85\n"
M_"v[56:59],a[232:235],a[184:187],v[56:59]\n"
C_"v170,v84,v85\n"
"v_add_f32 v213,v213,v84\n"
M_"v[56:59],a[236:239],a[188:191],v[56:59]\n"
D_"a[224:227],v245 offset:53248\n"
D_"a[228:231],v245 offset:61440\n"
M_"v[12:15],a[240:243],a[128:131],v[192:195]\n"
D_"a[232:235],v245 offset:54272\n"
D_"a[236:239],v245 offset:62464\n"
M_"v[12:15],a[244:247],a[132:135],v[12:15]\n"
A_"m0,s57,32768\n"
E_"v86,v86\n"
M_"v[12:15],a[248:251],a[136:139],v[12:15]\n"
"v_add_f32 v214,v214,v85\n"
"v_add_f32 v213,v213,v86\n"
M_"v[12:15],a[252:255],a[140:143],v[12:15]\n"
B_"v244,s[40:43],s56 offen offset:0 lds\n"
M_"v[28:31],a[240:243],a[144:147],v[196:199]\n"
E_"v87,v87\n"
M_"v[28:31],a[244:247],a[148:151],v[28:31]\n"
C_"v171,v86,v87\n"
"v_add_f32 v214,v214,v87\n"
M_"v[28:31],a[248:251],a[152:155],v[28:31]\n"
E_"v88,v88\n"
M_"v[28:31],a[252:255],a[156:159],v[28:31]\n"
E_"v89,v89\n"
M_"v[44:47],a[240:243],a[160:163],v[200:203]\n"
C_"v172,v88,v89\n"
"v_add_f32 v213,v213,v88\n"
M_"v[44:47],a[244:247],a[164:167],v[44:47]\n"
E_"v90,v90\n"
M_"v[44:47],a[248:251],a[168:171],v[44:47]\n"
"v_add_f32 v214,v214,v89\n"
"v_add_f32 v213,v213,v90\n"
M_"v[44:47],a[252:255],a[172:175],v[44:47]\n"
E_"v91,v91\n"
M_"v[60:63],a[240:243],a[176:179],v[204:207]\n"
C_"v173,v90,v91\n"
"v_add_f32 v214,v214,v91\n"
M_"v[60:63],a[244:247],a[180:183],v[60:63]\n"
E_"v92,v92\n"
M_"v[60:63],a[248:251],a[184:187],v[60:63]\n"
E_"v93,v93\n"
M_"v[60:63],a[252:255],a[188:191],v[60:63]\n"
W_"lgkmcnt(4)\n"
D_"a[240:243],v245 offset:55296\n"
D_"a[244:247],v245 offset:63488\n"
M_"a[0:3],a[192:195],v[128:131],a[0:3]\n"
B_"v244,s[40:43],s56 offen offset:1024 lds\n"
M_"a[0:3],a[196:199],v[132:135],a[0:3]\n"
D_"a[248:251],v245 offset:56320\n"
D_"a[252:255],v245 offset:64512\n"
M_"a[4:7],a[192:195],v[136:139],a[4:7]\n"
C_"v174,v92,v93\n"
"v_add_f32 v213,v213,v92\n"
M_"a[4:7],a[196:199],v[140:143],a[4:7]\n"
E_"v94,v94\n"
M_"a[8:11],a[192:195],v[144:147],a[8:11]\n"
"v_add_f32 v214,v214,v93\n"
"v_add_f32 v213,v213,v94\n"
M_"a[8:11],a[196:199],v[148:151],a[8:11]\n"
E_"v95,v95\n"
M_"a[12:15],a[192:195],v[152:155],a[12:15]\n"
C_"v175,v94,v95\n"
"v_add_f32 v214,v214,v95\n"
M_"a[12:15],a[196:199],v[156:159],a[12:15]\n"
D_"a[192:195],v245 offset:32768\n"
D_"a[196:199],v245 offset:33792\n"
M_"a[16:19],a[200:203],v[128:131],a[16:19]\n"
M_"a[16:19],a[204:207],v[132:135],a[16:19]\n"
E_"v96,v96\n"
M_"a[20:23],a[200:203],v[136:139],a[20:23]\n"
B_"v244,s[40:43],s56 offen offset:2048 lds\n"
M_"a[20:23],a[204:207],v[140:143],a[20:23]\n"
E_"v97,v97\n"
M_"a[24:27],a[200:203],v[144:147],a[24:27]\n"
E_"v98,v98\n"
M_"a[24:27],a[204:207],v[148:151],a[24:27]\n"
E_"v99,v99\n"
M_"a[28:31],a[200:203],v[152:155],a[28:31]\n"
C_"v176,v96,v97\n"
C_"v177,v98,v99\n"
M_"a[28:31],a[204:207],v[156:159],a[28:31]\n"
D_"a[200:203],v245 offset:34816\n"
D_"a[204:207],v245 offset:35840\n"
M_"a[32:35],a[208:211],v[128:131],a[32:35]\n"
E_"v100,v100\n"
M_"a[32:35],a[212:215],v[132:135],a[32:35]\n"
E_"v101,v101\n"
M_"a[36:39],a[208:211],v[136:139],a[36:39]\n"
E_"v102,v102\n"
M_"a[36:39],a[212:215],v[140:143],a[36:39]\n"
E_"v103,v103\n"
M_"a[40:43],a[208:211],v[144:147],a[40:43]\n"
C_"v178,v100,v101\n"
C_"v179,v102,v103\n"
M_"a[40:43],a[212:215],v[148:151],a[40:43]\n"
E_"v104,v104\n"
M_"a[44:47],a[208:211],v[152:155],a[44:47]\n"
B_"v244,s[40:43],s56 offen offset:3072 lds\n"
M_"a[44:47],a[212:215],v[156:159],a[44:47]\n"
D_"a[208:211],v245 offset:36864\n"
D_"a[212:215],v245 offset:37888\n"
M_"a[48:51],a[216:219],v[128:131],a[48:51]\n"
M_"a[48:51],a[220:223],v[132:135],a[48:51]\n"
E_"v105,v105\n"
M_"a[52:55],a[216:219],v[136:139],a[52:55]\n"
E_"v106,v106\n"
M_"a[52:55],a[220:223],v[140:143],a[52:55]\n"
E_"v107,v107\n"
M_"a[56:59],a[216:219],v[144:147],a[56:59]\n"
C_"v180,v104,v105\n"
C_"v181,v106,v107\n"
M_"a[56:59],a[220:223],v[148:151],a[56:59]\n"
E_"v108,v108\n"
M_"a[60:63],a[216:219],v[152:155],a[60:63]\n"
E_"v109,v109\n"
M_"a[60:63],a[220:223],v[156:159],a[60:63]\n"
W_"lgkmcnt(6)\n"
D_"a[216:219],v245 offset:38912\n"
D_"a[220:223],v245 offset:39936\n"
M_"a[64:67],a[224:227],v[128:131],a[64:67]\n"
E_"v110,v110\n"
M_"a[64:67],a[228:231],v[132:135],a[64:67]\n"
A_"s56,s56,16384\n"
M_"a[68:71],a[224:227],v[136:139],a[68:71]\n"
E_"v111,v111\n"
M_"a[68:71],a[228:231],v[140:143],a[68:71]\n"
C_"v182,v108,v109\n"
C_"v183,v110,v111\n"
M_"a[72:75],a[224:227],v[144:147],a[72:75]\n"
E_"v112,v112\n"
M_"a[72:75],a[228:231],v[148:151],a[72:75]\n"
E_"v113,v113\n"
M_"a[76:79],a[224:227],v[152:155],a[76:79]\n"
E_"v114,v114\n"
M_"a[76:79],a[228:231],v[156:159],a[76:79]\n"
D_"a[224:227],v245 offset:40960\n"
D_"a[228:231],v245 offset:41984\n"
M_"a[80:83],a[232:235],v[128:131],a[80:83]\n"
E_"v115,v115\n"
M_"a[80:83],a[236:239],v[132:135],a[80:83]\n"
C_"v184,v112,v113\n"
C_"v185,v114,v115\n"
M_"a[84:87],a[232:235],v[136:139],a[84:87]\n"
E_"v116,v116\n"
M_"a[84:87],a[236:239],v[140:143],a[84:87]\n"
E_"v117,v117\n"
M_"a[88:91],a[232:235],v[144:147],a[88:91]\n"
E_"v118,v118\n"
M_"a[88:91],a[236:239],v[148:151],a[88:91]\n"
M_"a[92:95],a[232:235],v[152:155],a[92:95]\n"
E_"v119,v119\n"
M_"a[92:95],a[236:239],v[156:159],a[92:95]\n"
D_"a[232:235],v245 offset:43008\n"
D_"a[236:239],v245 offset:44032\n"
M_"a[96:99],a[240:243],v[128:131],a[96:99]\n"
C_"v186,v116,v117\n"
C_"v187,v118,v119\n"
M_"a[96:99],a[244:247],v[132:135],a[96:99]\n"
E_"v120,v120\n"
M_"a[100:103],a[240:243],v[136:139],a[100:103]\n"
E_"v121,v121\n"
M_"a[100:103],a[244:247],v[140:143],a[100:103]\n"
E_"v122,v122\n"
M_"a[104:107],a[240:243],v[144:147],a[104:107]\n"
E_"v123,v123\n"
M_"a[104:107],a[244:247],v[148:151],a[104:107]\n"
C_"v188,v120,v121\n"
C_"v189,v122,v123\n"
M_"a[108:111],a[240:243],v[152:155],a[108:111]\n"
E_"v124,v124\n"
M_"a[108:111],a[244:247],v[156:159],a[108:111]\n"
D_"a[240:243],v245 offset:45056\n"
D_"a[244:247],v245 offset:46080\n"
M_"a[112:115],a[248:251],v[128:131],a[112:115]\n"
E_"v125,v125\n"
M_"a[112:115],a[252:255],v[132:135],a[112:115]\n"
E_"v126,v126\n"
M_"a[116:119],a[248:251],v[136:139],a[116:119]\n"
E_"v127,v127\n"
M_"a[116:119],a[252:255],v[140:143],a[116:119]\n"
C_"v190,v124,v125\n"
C_"v191,v126,v127\n"
M_"a[120:123],a[248:251],v[144:147],a[120:123]\n"
M_"a[120:123],a[252:255],v[148:151],a[120:123]\n"
M_"a[124:127],a[248:251],v[152:155],a[124:127]\n"
M_"a[124:127],a[252:255],v[156:159],a[124:127]\n"
D_"a[248:251],v245 offset:47104\n"
D_"a[252:255],v245 offset:48128\n"
"v_smfmac_f32_16x16x64_bf16 v[216:219],v[240:243],v[144:151],v246\n"
"v_smfmac_f32_16x16x64_bf16 v[220:223],v[240:243],v[152:159],v246\n"
W_"vmcnt(8) lgkmcnt(8)\n"
"L_k2_l4b:\n"
M_"v[64:67],a[192:195],a[128:131],v[192:195]\n"
"s_barrier\n"
"v_xor_b32 v245,0x10000,v245\n"
M_"v[64:67],a[196:199],a[132:135],v[64:67]\n"
"s_xor_b32 s57,s57,0x10000\n"
E_"v0,v0\n"
M_"v[64:67],a[200:203],a[136:139],v[64:67]\n"
E_"v1,v1\n"
M_"v[64:67],a[204:207],a[140:143],v[64:67]\n"
A_"m0,s57,16384\n"
C_"v128,v0,v1\n"
"v_add_f32 v209,v209,v0\n"
M_"v[80:83],a[192:195],a[144:147],v[196:199]\n"
E_"v2,v2\n"
M_"v[80:83],a[196:199],a[148:151],v[80:83]\n"
B_"v244,s[44:47],s56 offen offset:0 lds\n"
M_"v[80:83],a[200:203],a[152:155],v[80:83]\n"
"v_add_f32 v210,v210,v1\n"
"v_add_f32 v209,v209,v2\n"
M_"v[80:83],a[204:207],a[156:159],v[80:83]\n"
E_"v3,v3\n"
M_"v[96:99],a[192:195],a[160:163],v[200:203]\n"
C_"v129,v2,v3\n"
"v_add_f32 v210,v210,v3\n"
M_"v[96:99],a[196:199],a[164:167],v[96:99]\n"
E_"v4,v4\n"
M_"v[96:99],a[200:203],a[168:171],v[96:99]\n"
E_"v5,v5\n"
M_"v[96:99],a[204:207],a[172:175],v[96:99]\n"
C_"v130,v4,v5\n"
"v_add_f32 v209,v209,v4\n"
M_"v[112:115],a[192:195],a[176:179],v[204:207]\n"
E_"v6,v6\n"
M_"v[112:115],a[196:199],a[180:183],v[112:115]\n"
"v_add_f32 v210,v210,v5\n"
"v_add_f32 v209,v209,v6\n"
M_"v[112:115],a[200:203],a[184:187],v[112:115]\n"
E_"v7,v7\n"
M_"v[112:115],a[204:207],a[188:191],v[112:115]\n"
D_"a[192:195],v245 offset:16384\n"
D_"a[196:199],v245 offset:24576\n"
M_"v[68:71],a[208:211],a[128:131],v[192:195]\n"
B_"v244,s[44:47],s56 offen offset:1024 lds\n"
M_"v[68:71],a[212:215],a[132:135],v[68:71]\n"
D_"a[200:203],v245 offset:17408\n"
D_"a[204:207],v245 offset:25600\n"
M_"v[68:71],a[216:219],a[136:139],v[68:71]\n"
C_"v131,v6,v7\n"
"v_add_f32 v210,v210,v7\n"
M_"v[68:71],a[220:223],a[140:143],v[68:71]\n"
E_"v8,v8\n"
M_"v[84:87],a[208:211],a[144:147],v[196:199]\n"
E_"v9,v9\n"
M_"v[84:87],a[212:215],a[148:151],v[84:87]\n"
C_"v132,v8,v9\n"
"v_add_f32 v209,v209,v8\n"
M_"v[84:87],a[216:219],a[152:155],v[84:87]\n"
E_"v10,v10\n"
M_"v[84:87],a[220:223],a[156:159],v[84:87]\n"
"v_add_f32 v210,v210,v9\n"
"v_add_f32 v209,v209,v10\n"
M_"v[100:103],a[208:211],a[160:163],v[200:203]\n"
E_"v11,v11\n"
M_"v[100:103],a[212:215],a[164:167],v[100:103]\n"
C_"v133,v10,v11\n"
"v_add_f32 v210,v210,v11\n"
M_"v[100:103],a[216:219],a[168:171],v[100:103]\n"
E_"v12,v12\n"
M_"v[100:103],a[220:223],a[172:175],v[100:103]\n"
E_"v13,v13\n"
M_"v[116:119],a[208:211],a[176:179],v[204:207]\n"
B_"v244,s[44:47],s56 offen offset:2048 lds\n"
M_"v[116:119],a[212:215],a[180:183],v[116:119]\n"
C_"v134,v12,v13\n"
"v_add_f32 v209,v209,v12\n"
M_"v[116:119],a[216:219],a[184:187],v[116:119]\n"
E_"v14,v14\n"
M_"v[116:119],a[220:223],a[188:191],v[116:119]\n"
W_"lgkmcnt(4)\n"
D_"a[208:211],v245 offset:18432\n"
D_"a[212:215],v245 offset:26624\n"
M_"v[72:75],a[224:227],a[128:131],v[192:195]\n"
D_"a[216:219],v245 offset:19456\n"
D_"a[220:223],v245 offset:27648\n"
M_"v[72:75],a[228:231],a[132:135],v[72:75]\n"
"v_add_f32 v210,v210,v13\n"
"v_add_f32 v209,v209,v14\n"
M_"v[72:75],a[232:235],a[136:139],v[72:75]\n"
E_"v15,v15\n"
M_"v[72:75],a[236:239],a[140:143],v[72:75]\n"
C_"v135,v14,v15\n"
"v_add_f32 v210,v210,v15\n"
M_"v[88:91],a[224:227],a[144:147],v[196:199]\n"
E_"v16,v16\n"
M_"v[88:91],a[228:231],a[148:151],v[88:91]\n"
E_"v17,v17\n"
M_"v[88:91],a[232:235],a[152:155],v[88:91]\n"
C_"v136,v16,v17\n"
"v_add_f32 v213,v213,v16\n"
M_"v[88:91],a[236:239],a[156:159],v[88:91]\n"
B_"v244,s[44:47],s56 offen offset:3072 lds\n"
M_"v[104:107],a[224:227],a[160:163],v[200:203]\n"
E_"v18,v18\n"
M_"v[104:107],a[228:231],a[164:167],v[104:107]\n"
"v_add_f32 v214,v214,v17\n"
"v_add_f32 v213,v213,v18\n"
M_"v[104:107],a[232:235],a[168:171],v[104:107]\n"
E_"v19,v19\n"
M_"v[104:107],a[236:239],a[172:175],v[104:107]\n"
C_"v137,v18,v19\n"
"v_add_f32 v214,v214,v19\n"
M_"v[120:123],a[224:227],a[176:179],v[204:207]\n"
E_"v20,v20\n"
M_"v[120:123],a[228:231],a[180:183],v[120:123]\n"
E_"v21,v21\n"
M_"v[120:123],a[232:235],a[184:187],v[120:123]\n"
C_"v138,v20,v21\n"
"v_add_f32 v213,v213,v20\n"
M_"v[120:123],a[236:239],a[188:191],v[120:123]\n"
D_"a[224:227],v245 offset:20480\n"
D_"a[228:231],v245 offset:28672\n"
M_"v[76:79],a[240:243],a[128:131],v[192:195]\n"
D_"a[232:235],v245 offset:21504\n"
D_"a[236:239],v245 offset:29696\n"
M_"v[76:79],a[244:247],a[132:135],v[76:79]\n"
E_"v22,v22\n"
M_"v[76:79],a[248:251],a[136:139],v[76:79]\n"
A_"m0,s57,0\n"
"v_add_f32 v214,v214,v21\n"
"v_add_f32 v213,v213,v22\n"
M_"v[76:79],a[252:255],a[140:143],v[76:79]\n"
E_"v23,v23\n"
M_"v[92:95],a[240:243],a[144:147],v[196:199]\n"
B_"v244,s[40:43],s56 offen offset:0 lds\n"
M_"v[92:95],a[244:247],a[148:151],v[92:95]\n"
C_"v139,v22,v23\n"
"v_add_f32 v214,v214,v23\n"
M_"v[92:95],a[248:251],a[152:155],v[92:95]\n"
E_"v24,v24\n"
M_"v[92:95],a[252:255],a[156:159],v[92:95]\n"
E_"v25,v25\n"
M_"v[108:111],a[240:243],a[160:163],v[200:203]\n"
C_"v140,v24,v25\n"
"v_add_f32 v213,v213,v24\n"
M_"v[108:111],a[244:247],a[164:167],v[108:111]\n"
E_"v26,v26\n"
M_"v[108:111],a[248:251],a[168:171],v[108:111]\n"
"v_add_f32 v214,v214,v25\n"
"v_add_f32 v213,v213,v26\n"
M_"v[108:111],a[252:255],a[172:175],v[108:111]\n"
E_"v27,v27\n"
M_"v[124:127],a[240:243],a[176:179],v[204:207]\n"
C_"v141,v26,v27\n"
"v_add_f32 v214,v214,v27\n"
M_"v[124:127],a[244:247],a[180:183],v[124:127]\n"
E_"v28,v28\n"
M_"v[124:127],a[248:251],a[184:187],v[124:127]\n"
E_"v29,v29\n"
M_"v[124:127],a[252:255],a[188:191],v[124:127]\n"
W_"lgkmcnt(4)\n"
D_"a[240:243],v245 offset:22528\n"
D_"a[244:247],v245 offset:30720\n"
M_"a[0:3],a[192:195],v[160:163],a[0:3]\n"
B_"v244,s[40:43],s56 offen offset:1024 lds\n"
M_"a[0:3],a[196:199],v[164:167],a[0:3]\n"
D_"a[248:251],v245 offset:23552\n"
D_"a[252:255],v245 offset:31744\n"
M_"a[4:7],a[192:195],v[168:171],a[4:7]\n"
M_"a[4:7],a[196:199],v[172:175],a[4:7]\n"
C_"v142,v28,v29\n"
"v_add_f32 v213,v213,v28\n"
M_"a[8:11],a[192:195],v[176:179],a[8:11]\n"
E_"v30,v30\n"
M_"a[8:11],a[196:199],v[180:183],a[8:11]\n"
"v_add_f32 v214,v214,v29\n"
"v_add_f32 v213,v213,v30\n"
M_"a[12:15],a[192:195],v[184:187],a[12:15]\n"
E_"v31,v31\n"
M_"a[12:15],a[196:199],v[188:191],a[12:15]\n"
D_"a[192:195],v245 offset:0\n"
D_"a[196:199],v245 offset:1024\n"
M_"a[16:19],a[200:203],v[160:163],a[16:19]\n"
C_"v143,v30,v31\n"
"v_add_f32 v214,v214,v31\n"
M_"a[16:19],a[204:207],v[164:167],a[16:19]\n"
E_"v32,v32\n"
M_"a[20:23],a[200:203],v[168:171],a[20:23]\n"
E_"v33,v33\n"
M_"a[20:23],a[204:207],v[172:175],a[20:23]\n"
B_"v244,s[40:43],s56 offen offset:2048 lds\n"
M_"a[24:27],a[200:203],v[176:179],a[24:27]\n"
E_"v34,v34\n"
M_"a[24:27],a[204:207],v[180:183],a[24:27]\n"
E_"v35,v35\n"
M_"a[28:31],a[200:203],v[184:187],a[28:31]\n"
C_"v144,v32,v33\n"
C_"v145,v34,v35\n"
M_"a[28:31],a[204:207],v[188:191],a[28:31]\n"
D_"a[200:203],v245 offset:2048\n"
D_"a[204:207],v245 offset:3072\n"
M_"a[32:35],a[208:211],v[160:163],a[32:35]\n"
E_"v36,v36\n"
M_"a[32:35],a[212:215],v[164:167],a[32:35]\n"
M_"a[36:39],a[208:211],v[168:171],a[36:39]\n"
E_"v37,v37\n"
M_"a[36:39],a[212:215],v[172:175],a[36:39]\n"
E_"v38,v38\n"
M_"a[40:43],a[208:211],v[176:179],a[40:43]\n"
E_"v39,v39\n"
M_"a[40:43],a[212:215],v[180:183],a[40:43]\n"
C_"v146,v36,v37\n"
C_"v147,v38,v39\n"
M_"a[44:47],a[208:211],v[184:187],a[44:47]\n"
B_"v244,s[40:43],s56 offen offset:3072 lds\n"
M_"a[44:47],a[212:215],v[188:191],a[44:47]\n"
D_"a[208:211],v245 offset:4096\n"
D_"a[212:215],v245 offset:5120\n"
M_"a[48:51],a[216:219],v[160:163],a[48:51]\n"
E_"v40,v40\n"
M_"a[48:51],a[220:223],v[164:167],a[48:51]\n"
E_"v41,v41\n"
M_"a[52:55],a[216:219],v[168:171],a[52:55]\n"
E_"v42,v42\n"
M_"a[52:55],a[220:223],v[172:175],a[52:55]\n"
E_"v43,v43\n"
M_"a[56:59],a[216:219],v[176:179],a[56:59]\n"
C_"v148,v40,v41\n"
C_"v149,v42,v43\n"
M_"a[56:59],a[220:223],v[180:183],a[56:59]\n"
E_"v44,v44\n"
M_"a[60:63],a[216:219],v[184:187],a[60:63]\n"
E_"v45,v45\n"
M_"a[60:63],a[220:223],v[188:191],a[60:63]\n"
W_"lgkmcnt(6)\n"
D_"a[216:219],v245 offset:6144\n"
D_"a[220:223],v245 offset:7168\n"
M_"a[64:67],a[224:227],v[160:163],a[64:67]\n"
E_"v46,v46\n"
M_"a[64:67],a[228:231],v[164:167],a[64:67]\n"
A_"s56,s56,16384\n"
M_"a[68:71],a[224:227],v[168:171],a[68:71]\n"
E_"v47,v47\n"
M_"a[68:71],a[228:231],v[172:175],a[68:71]\n"
C_"v150,v44,v45\n"
C_"v151,v46,v47\n"
M_"a[72:75],a[224:227],v[176:179],a[72:75]\n"
E_"v48,v48\n"
M_"a[72:75],a[228:231],v[180:183],a[72:75]\n"
E_"v49,v49\n"
M_"a[76:79],a[224:227],v[184:187],a[76:79]\n"
E_"v50,v50\n"
M_"a[76:79],a[228:231],v[188:191],a[76:79]\n"
D_"a[224:227],v245 offset:8192\n"
D_"a[228:231],v245 offset:9216\n"
M_"a[80:83],a[232:235],v[160:163],a[80:83]\n"
E_"v51,v51\n"
M_"a[80:83],a[236:239],v[164:167],a[80:83]\n"
C_"v152,v48,v49\n"
C_"v153,v50,v51\n"
M_"a[84:87],a[232:235],v[168:171],a[84:87]\n"
E_"v52,v52\n"
M_"a[84:87],a[236:239],v[172:175],a[84:87]\n"
M_"a[88:91],a[232:235],v[176:179],a[88:91]\n"
E_"v53,v53\n"
M_"a[88:91],a[236:239],v[180:183],a[88:91]\n"
E_"v54,v54\n"
M_"a[92:95],a[232:235],v[184:187],a[92:95]\n"
E_"v55,v55\n"
M_"a[92:95],a[236:239],v[188:191],a[92:95]\n"
D_"a[232:235],v245 offset:10240\n"
D_"a[236:239],v245 offset:11264\n"
M_"a[96:99],a[240:243],v[160:163],a[96:99]\n"
C_"v154,v52,v53\n"
C_"v155,v54,v55\n"
M_"a[96:99],a[244:247],v[164:167],a[96:99]\n"
E_"v56,v56\n"
M_"a[100:103],a[240:243],v[168:171],a[100:103]\n"
E_"v57,v57\n"
M_"a[100:103],a[244:247],v[172:175],a[100:103]\n"
E_"v58,v58\n"
M_"a[104:107],a[240:243],v[176:179],a[104:107]\n"
E_"v59,v59\n"
M_"a[104:107],a[244:247],v[180:183],a[104:107]\n"
C_"v156,v56,v57\n"
C_"v157,v58,v59\n"
M_"a[108:111],a[240:243],v[184:187],a[108:111]\n"
E_"v60,v60\n"
M_"a[108:111],a[244:247],v[188:191],a[108:111]\n"
D_"a[240:243],v245 offset:12288\n"
D_"a[244:247],v245 offset:13312\n"
M_"a[112:115],a[248:251],v[160:163],a[112:115]\n"
E_"v61,v61\n"
M_"a[112:115],a[252:255],v[164:167],a[112:115]\n"
E_"v62,v62\n"
M_"a[116:119],a[248:251],v[168:171],a[116:119]\n"
E_"v63,v63\n"
M_"a[116:119],a[252:255],v[172:175],a[116:119]\n"
C_"v158,v60,v61\n"
C_"v159,v62,v63\n"
M_"a[120:123],a[248:251],v[176:179],a[120:123]\n"
M_"a[120:123],a[252:255],v[180:183],a[120:123]\n"
"s_sub_u32 s58,s58,1\n"
M_"a[124:127],a[248:251],v[184:187],a[124:127]\n"
"s_cmp_gt_i32 s58,0\n"
M_"a[124:127],a[252:255],v[188:191],a[124:127]\n"
D_"a[248:251],v245 offset:14336\n"
D_"a[252:255],v245 offset:15360\n"
"v_smfmac_f32_16x16x64_bf16 v[216:219],v[240:243],v[176:183],v246\n"
W_"vmcnt(8) lgkmcnt(8)\n"
"v_smfmac_f32_16x16x64_bf16 v[220:223],v[240:243],v[184:191],v246\n"
"s_cbranch_scc1 L_k2_loop4\n"
M_"v[16:19],a[192:195],a[144:147],v[196:199]\n"
"s_barrier\n"
"v_mov_b32 v248,0xff800000\n"
K_"v224,v248,v192,s[64:65]\n"
M_"v[16:19],a[196:199],a[148:151],v[16:19]\n"
A_"m0,s57,49152\n"
K_"v225,v248,v192,s[66:67]\n"
K_"v226,v248,v192,s[68:69]\n"
M_"v[16:19],a[200:203],a[152:155],v[16:19]\n"
K_"v227,v248,v192,s[70:71]\n"
K_"v228,v248,v192,s[72:73]\n"
M_"v[16:19],a[204:207],a[156:159],v[16:19]\n"
B_"v244,s[44:47],s56 offen offset:0 lds\n"
M_"v[32:35],a[192:195],a[160:163],v[200:203]\n"
K_"v229,v248,v192,s[74:75]\n"
K_"v230,v248,v192,s[76:77]\n"
M_"v[32:35],a[196:199],a[164:167],v[32:35]\n"
K_"v231,v248,v192,s[78:79]\n"
K_"v232,v248,v192,s[80:81]\n"
M_"v[32:35],a[200:203],a[168:171],v[32:35]\n"
K_"v233,v248,v192,s[82:83]\n"
K_"v234,v248,v192,s[84:85]\n"
M_"v[32:35],a[204:207],a[172:175],v[32:35]\n"
K_"v235,v248,v192,s[86:87]\n"
K_"v236,v248,v192,s[88:89]\n"
M_"v[48:51],a[192:195],a[176:179],v[204:207]\n"
K_"v237,v248,v192,s[90:91]\n"
K_"v238,v248,v192,s[92:93]\n"
M_"v[48:51],a[196:199],a[180:183],v[48:51]\n"
K_"v239,v248,v192,s[94:95]\n"
"v_add_f32 v209,v209,v210\n"
M_"v[48:51],a[200:203],a[184:187],v[48:51]\n"
"v_mov_b32 v210,v209\n"
"v_add_f32 v213,v213,v214\n"
M_"v[48:51],a[204:207],a[188:191],v[48:51]\n"
"v_permlane32_swap_b32 v209,v210\n"
"v_mov_b32 v214,v213\n"
M_"v[0:3],a[192:195],a[128:131],v[224:227]\n"
"v_add_f32 v209,v209,v210\n"
"v_permlane32_swap_b32 v213,v214\n"
M_"v[0:3],a[196:199],a[132:135],v[0:3]\n"
"v_mov_b32 v210,v209\n"
"v_add_f32 v213,v213,v214\n"
M_"v[0:3],a[200:203],a[136:139],v[0:3]\n"
B_"v244,s[44:47],s56 offen offset:1024 lds\n"
M_"v[0:3],a[204:207],a[140:143],v[0:3]\n"
D_"a[192:195],v245 offset:49152\n"
D_"a[196:199],v245 offset:57344\n"
M_"v[20:23],a[208:211],a[144:147],v[196:199]\n"
D_"a[200:203],v245 offset:50176\n"
D_"a[204:207],v245 offset:58368\n"
M_"v[20:23],a[212:215],a[148:151],v[20:23]\n"
"v_permlane16_swap_b32 v209,v210\n"
"v_mov_b32 v214,v213\n"
M_"v[20:23],a[216:219],a[152:155],v[20:23]\n"
"v_add_f32 v209,v209,v210\n"
"v_permlane16_swap_b32 v213,v214\n"
M_"v[20:23],a[220:223],a[156:159],v[20:23]\n"
"v_add_f32 v208,v208,v209\n"
"v_add_f32 v213,v213,v214\n"
M_"v[36:39],a[208:211],a[160:163],v[200:203]\n"
E_"v64,v64\n"
M_"v[36:39],a[212:215],a[164:167],v[36:39]\n"
E_"v65,v65\n"
M_"v[36:39],a[216:219],a[168:171],v[36:39]\n"
C_"v160,v64,v65\n"
"v_add_f32 v212,v212,v213\n"
M_"v[36:39],a[220:223],a[172:175],v[36:39]\n"
E_"v66,v66\n"
M_"v[52:55],a[208:211],a[176:179],v[204:207]\n"
E_"v67,v67\n"
M_"v[52:55],a[212:215],a[180:183],v[52:55]\n"
E_"v68,v68\n"
M_"v[52:55],a[216:219],a[184:187],v[52:55]\n"
B_"v244,s[44:47],s56 offen offset:2048 lds\n"
M_"v[52:55],a[220:223],a[188:191],v[52:55]\n"
E_"v69,v69\n"
M_"v[4:7],a[208:211],a[128:131],v[228:231]\n"
C_"v161,v66,v67\n"
C_"v162,v68,v69\n"
M_"v[4:7],a[212:215],a[132:135],v[4:7]\n"
E_"v70,v70\n"
M_"v[4:7],a[216:219],a[136:139],v[4:7]\n"
E_"v71,v71\n"
M_"v[4:7],a[220:223],a[140:143],v[4:7]\n"
W_"lgkmcnt(4)\n"
D_"a[208:211],v245 offset:51200\n"
D_"a[212:215],v245 offset:59392\n"
M_"v[24:27],a[224:227],a[144:147],v[196:199]\n"
D_"a[216:219],v245 offset:52224\n"
D_"a[220:223],v245 offset:60416\n"
M_"v[24:27],a[228:231],a[148:151],v[24:27]\n"
E_"v72,v72\n"
M_"v[24:27],a[232:235],a[152:155],v[24:27]\n"
E_"v73,v73\n"
M_"v[24:27],a[236:239],a[156:159],v[24:27]\n"
C_"v163,v70,v71\n"
C_"v164,v72,v73\n"
M_"v[40:43],a[224:227],a[160:163],v[200:203]\n"
E_"v74,v74\n"
M_"v[40:43],a[228:231],a[164:167],v[40:43]\n"
B_"v244,s[44:47],s56 offen offset:3072 lds\n"
M_"v[40:43],a[232:235],a[168:171],v[40:43]\n"
E_"v75,v75\n"
M_"v[40:43],a[236:239],a[172:175],v[40:43]\n"
E_"v76,v76\n"
M_"v[56:59],a[224:227],a[176:179],v[204:207]\n"
E_"v77,v77\n"
M_"v[56:59],a[228:231],a[180:183],v[56:59]\n"
C_"v165,v74,v75\n"
C_"v166,v76,v77\n"
M_"v[56:59],a[232:235],a[184:187],v[56:59]\n"
E_"v78,v78\n"
M_"v[56:59],a[236:239],a[188:191],v[56:59]\n"
E_"v79,v79\n"
M_"v[8:11],a[224:227],a[128:131],v[232:235]\n"
E_"v80,v80\n"
M_"v[8:11],a[228:231],a[132:135],v[8:11]\n"
E_"v81,v81\n"
M_"v[8:11],a[232:235],a[136:139],v[8:11]\n"
C_"v167,v78,v79\n"
C_"v168,v80,v81\n"
M_"v[8:11],a[236:239],a[140:143],v[8:11]\n"
D_"a[224:227],v245 offset:53248\n"
D_"a[228:231],v245 offset:61440\n"
M_"v[28:31],a[240:243],a[144:147],v[196:199]\n"
D_"a[232:235],v245 offset:54272\n"
D_"a[236:239],v245 offset:62464\n"
M_"v[28:31],a[244:247],a[148:151],v[28:31]\n"
A_"m0,s57,32768\n"
E_"v82,v82\n"
M_"v[28:31],a[248:251],a[152:155],v[28:31]\n"
E_"v83,v83\n"
M_"v[28:31],a[252:255],a[156:159],v[28:31]\n"
B_"v244,s[40:43],s56 offen offset:0 lds\n"
M_"v[44:47],a[240:243],a[160:163],v[200:203]\n"
E_"v84,v84\n"
M_"v[44:47],a[244:247],a[164:167],v[44:47]\n"
E_"v85,v85\n"
M_"v[44:47],a[248:251],a[168:171],v[44:47]\n"
C_"v169,v82,v83\n"
C_"v170,v84,v85\n"
M_"v[44:47],a[252:255],a[172:175],v[44:47]\n"
E_"v86,v86\n"
M_"v[60:63],a[240:243],a[176:179],v[204:207]\n"
E_"v87,v87\n"
M_"v[60:63],a[244:247],a[180:183],v[60:63]\n"
E_"v88,v88\n"
M_"v[60:63],a[248:251],a[184:187],v[60:63]\n"
E_"v89,v89\n"
M_"v[60:63],a[252:255],a[188:191],v[60:63]\n"
C_"v171,v86,v87\n"
C_"v172,v88,v89\n"
M_"v[12:15],a[240:243],a[128:131],v[236:239]\n"
E_"v90,v90\n"
M_"v[12:15],a[244:247],a[132:135],v[12:15]\n"
E_"v91,v91\n"
M_"v[12:15],a[248:251],a[136:139],v[12:15]\n"
E_"v92,v92\n"
M_"v[12:15],a[252:255],a[140:143],v[12:15]\n"
W_"lgkmcnt(4)\n"
D_"a[240:243],v245 offset:55296\n"
D_"a[244:247],v245 offset:63488\n"
M_"a[0:3],a[192:195],v[128:131],a[0:3]\n"
B_"v244,s[40:43],s56 offen offset:1024 lds\n"
M_"a[0:3],a[196:199],v[132:135],a[0:3]\n"
D_"a[248:251],v245 offset:56320\n"
D_"a[252:255],v245 offset:64512\n"
M_"a[4:7],a[192:195],v[136:139],a[4:7]\n"
K_"v224,v248,v196,s[64:65]\n"
K_"v225,v248,v196,s[66:67]\n"
M_"a[4:7],a[196:199],v[140:143],a[4:7]\n"
K_"v226,v248,v196,s[68:69]\n"
K_"v227,v248,v196,s[70:71]\n"
M_"a[8:11],a[192:195],v[144:147],a[8:11]\n"
K_"v228,v248,v196,s[72:73]\n"
K_"v229,v248,v196,s[74:75]\n"
M_"a[8:11],a[196:199],v[148:151],a[8:11]\n"
K_"v230,v248,v196,s[76:77]\n"
K_"v231,v248,v196,s[78:79]\n"
M_"a[12:15],a[192:195],v[152:155],a[12:15]\n"
K_"v232,v248,v196,s[80:81]\n"
K_"v233,v248,v196,s[82:83]\n"
M_"a[12:15],a[196:199],v[156:159],a[12:15]\n"
D_"a[192:195],v245 offset:32768\n"
D_"a[196:199],v245 offset:33792\n"
M_"a[16:19],a[200:203],v[128:131],a[16:19]\n"
K_"v234,v248,v196,s[84:85]\n"
K_"v235,v248,v196,s[86:87]\n"
M_"a[16:19],a[204:207],v[132:135],a[16:19]\n"
K_"v236,v248,v196,s[88:89]\n"
K_"v237,v248,v196,s[90:91]\n"
M_"a[20:23],a[200:203],v[136:139],a[20:23]\n"
B_"v244,s[40:43],s56 offen offset:2048 lds\n"
M_"a[20:23],a[204:207],v[140:143],a[20:23]\n"
K_"v238,v248,v196,s[92:93]\n"
K_"v239,v248,v196,s[94:95]\n"
M_"a[24:27],a[200:203],v[144:147],a[24:27]\n"
E_"v93,v93\n"
M_"a[24:27],a[204:207],v[148:151],a[24:27]\n"
C_"v173,v90,v91\n"
C_"v174,v92,v93\n"
M_"a[28:31],a[200:203],v[152:155],a[28:31]\n"
E_"v94,v94\n"
M_"a[28:31],a[204:207],v[156:159],a[28:31]\n"
D_"a[200:203],v245 offset:34816\n"
D_"a[204:207],v245 offset:35840\n"
M_"a[32:35],a[208:211],v[128:131],a[32:35]\n"
E_"v95,v95\n"
M_"a[32:35],a[212:215],v[132:135],a[32:35]\n"
E_"v96,v96\n"
M_"a[36:39],a[208:211],v[136:139],a[36:39]\n"
E_"v97,v97\n"
M_"a[36:39],a[212:215],v[140:143],a[36:39]\n"
C_"v175,v94,v95\n"
C_"v176,v96,v97\n"
M_"a[40:43],a[208:211],v[144:147],a[40:43]\n"
E_"v98,v98\n"
M_"a[40:43],a[212:215],v[148:151],a[40:43]\n"
E_"v99,v99\n"
M_"a[44:47],a[208:211],v[152:155],a[44:47]\n"
B_"v244,s[40:43],s56 offen offset:3072 lds\n"
M_"a[44:47],a[212:215],v[156:159],a[44:47]\n"
D_"a[208:211],v245 offset:36864\n"
D_"a[212:215],v245 offset:37888\n"
M_"a[48:51],a[216:219],v[128:131],a[48:51]\n"
E_"v100,v100\n"
M_"a[48:51],a[220:223],v[132:135],a[48:51]\n"
E_"v101,v101\n"
M_"a[52:55],a[216:219],v[136:139],a[52:55]\n"
C_"v177,v98,v99\n"
C_"v178,v100,v101\n"
M_"a[52:55],a[220:223],v[140:143],a[52:55]\n"
E_"v102,v102\n"
M_"a[56:59],a[216:219],v[144:147],a[56:59]\n"
E_"v103,v103\n"
M_"a[56:59],a[220:223],v[148:151],a[56:59]\n"
E_"v104,v104\n"
M_"a[60:63],a[216:219],v[152:155],a[60:63]\n"
E_"v105,v105\n"
M_"a[60:63],a[220:223],v[156:159],a[60:63]\n"
W_"lgkmcnt(6)\n"
D_"a[216:219],v245 offset:38912\n"
D_"a[220:223],v245 offset:39936\n"
M_"a[64:67],a[224:227],v[128:131],a[64:67]\n"
C_"v179,v102,v103\n"
C_"v180,v104,v105\n"
M_"a[64:67],a[228:231],v[132:135],a[64:67]\n"
A_"s56,s56,16384\n"
E_"v106,v106\n"
M_"a[68:71],a[224:227],v[136:139],a[68:71]\n"
E_"v107,v107\n"
M_"a[68:71],a[228:231],v[140:143],a[68:71]\n"
E_"v108,v108\n"
M_"a[72:75],a[224:227],v[144:147],a[72:75]\n"
E_"v109,v109\n"
M_"a[72:75],a[228:231],v[148:151],a[72:75]\n"
C_"v181,v106,v107\n"
C_"v182,v108,v109\n"
M_"a[76:79],a[224:227],v[152:155],a[76:79]\n"
E_"v110,v110\n"
M_"a[76:79],a[228:231],v[156:159],a[76:79]\n"
D_"a[224:227],v245 offset:40960\n"
D_"a[228:231],v245 offset:41984\n"
M_"a[80:83],a[232:235],v[128:131],a[80:83]\n"
E_"v111,v111\n"
M_"a[80:83],a[236:239],v[132:135],a[80:83]\n"
E_"v112,v112\n"
M_"a[84:87],a[232:235],v[136:139],a[84:87]\n"
E_"v113,v113\n"
M_"a[84:87],a[236:239],v[140:143],a[84:87]\n"
C_"v183,v110,v111\n"
C_"v184,v112,v113\n"
M_"a[88:91],a[232:235],v[144:147],a[88:91]\n"
E_"v114,v114\n"
M_"a[88:91],a[236:239],v[148:151],a[88:91]\n"
E_"v115,v115\n"
M_"a[92:95],a[232:235],v[152:155],a[92:95]\n"
E_"v116,v116\n"
M_"a[92:95],a[236:239],v[156:159],a[92:95]\n"
D_"a[232:235],v245 offset:43008\n"
D_"a[236:239],v245 offset:44032\n"
M_"a[96:99],a[240:243],v[128:131],a[96:99]\n"
E_"v117,v117\n"
M_"a[96:99],a[244:247],v[132:135],a[96:99]\n"
C_"v185,v114,v115\n"
C_"v186,v116,v117\n"
M_"a[100:103],a[240:243],v[136:139],a[100:103]\n"
E_"v118,v118\n"
M_"a[100:103],a[244:247],v[140:143],a[100:103]\n"
E_"v119,v119\n"
M_"a[104:107],a[240:243],v[144:147],a[104:107]\n"
E_"v120,v120\n"
M_"a[104:107],a[244:247],v[148:151],a[104:107]\n"
E_"v121,v121\n"
M_"a[108:111],a[240:243],v[152:155],a[108:111]\n"
C_"v187,v118,v119\n"
C_"v188,v120,v121\n"
M_"a[108:111],a[244:247],v[156:159],a[108:111]\n"
D_"a[240:243],v245 offset:45056\n"
D_"a[244:247],v245 offset:46080\n"
M_"a[112:115],a[248:251],v[128:131],a[112:115]\n"
E_"v122,v122\n"
M_"a[112:115],a[252:255],v[132:135],a[112:115]\n"
E_"v123,v123\n"
M_"a[116:119],a[248:251],v[136:139],a[116:119]\n"
E_"v124,v124\n"
M_"a[116:119],a[252:255],v[140:143],a[116:119]\n"
E_"v125,v125\n"
M_"a[120:123],a[248:251],v[144:147],a[120:123]\n"
C_"v189,v122,v123\n"
C_"v190,v124,v125\n"
M_"a[120:123],a[252:255],v[148:151],a[120:123]\n"
E_"v126,v126\n"
M_"a[124:127],a[248:251],v[152:155],a[124:127]\n"
E_"v127,v127\n"
M_"a[124:127],a[252:255],v[156:159],a[124:127]\n"
D_"a[248:251],v245 offset:47104\n"
D_"a[252:255],v245 offset:48128\n"
"v_smfmac_f32_16x16x64_bf16 v[216:219],v[240:243],v[144:151],v246\n"
C_"v191,v126,v127\n"
"v_smfmac_f32_16x16x64_bf16 v[220:223],v[240:243],v[152:159],v246\n"
W_"vmcnt(8) lgkmcnt(8)\n"
"s_cmp_eq_u32 s34,7\n"
"s_cbranch_scc1 L_k2_a7\n"
"L_k2_t2:\n"
M_"v[96:99],a[192:195],a[160:163],v[200:203]\n"
"s_barrier\n"
E_"v0,v0\n"
M_"v[96:99],a[196:199],a[164:167],v[96:99]\n"
"s_xor_b32 s57,s57,0x10000\n"
"v_xor_b32 v245,0x10000,v245\n"
M_"v[96:99],a[200:203],a[168:171],v[96:99]\n"
A_"m0,s57,16384\n"
E_"v1,v1\n"
M_"v[96:99],a[204:207],a[172:175],v[96:99]\n"
B_"v244,s[44:47],s56 offen offset:0 lds\n"
M_"v[112:115],a[192:195],a[176:179],v[204:207]\n"
E_"v2,v2\n"
M_"v[112:115],a[196:199],a[180:183],v[112:115]\n"
E_"v3,v3\n"
M_"v[112:115],a[200:203],a[184:187],v[112:115]\n"
C_"v128,v0,v1\n"
C_"v129,v2,v3\n"
M_"v[112:115],a[204:207],a[188:191],v[112:115]\n"
E_"v4,v4\n"
M_"v[80:83],a[192:195],a[144:147],v[224:227]\n"
E_"v5,v5\n"
M_"v[80:83],a[196:199],a[148:151],v[80:83]\n"
B_"v244,s[44:47],s56 offen offset:1024 lds\n"
M_"v[80:83],a[200:203],a[152:155],v[80:83]\n"
E_"v6,v6\n"
M_"v[80:83],a[204:207],a[156:159],v[80:83]\n"
D_"a[192:195],v245 offset:16384\n"
D_"a[196:199],v245 offset:24576\n"
M_"v[100:103],a[208:211],a[160:163],v[200:203]\n"
D_"a[200:203],v245 offset:17408\n"
D_"a[204:207],v245 offset:25600\n"
M_"v[100:103],a[212:215],a[164:167],v[100:103]\n"
E_"v7,v7\n"
M_"v[100:103],a[216:219],a[168:171],v[100:103]\n"
C_"v130,v4,v5\n"
C_"v131,v6,v7\n"
M_"v[100:103],a[220:223],a[172:175],v[100:103]\n"
E_"v8,v8\n"
M_"v[116:119],a[208:211],a[176:179],v[204:207]\n"
B_"v244,s[44:47],s56 offen offset:2048 lds\n"
M_"v[116:119],a[212:215],a[180:183],v[116:119]\n"
E_"v9,v9\n"
M_"v[116:119],a[216:219],a[184:187],v[116:119]\n"
E_"v10,v10\n"
M_"v[116:119],a[220:223],a[188:191],v[116:119]\n"
E_"v11,v11\n"
M_"v[84:87],a[208:211],a[144:147],v[228:231]\n"
C_"v132,v8,v9\n"
C_"v133,v10,v11\n"
M_"v[84:87],a[212:215],a[148:151],v[84:87]\n"
E_"v12,v12\n"
M_"v[84:87],a[216:219],a[152:155],v[84:87]\n"
B_"v244,s[44:47],s56 offen offset:3072 lds\n"
M_"v[84:87],a[220:223],a[156:159],v[84:87]\n"
W_"lgkmcnt(4)\n"
D_"a[208:211],v245 offset:18432\n"
D_"a[212:215],v245 offset:26624\n"
M_"v[104:107],a[224:227],a[160:163],v[200:203]\n"
D_"a[216:219],v245 offset:19456\n"
D_"a[220:223],v245 offset:27648\n"
M_"v[104:107],a[228:231],a[164:167],v[104:107]\n"
E_"v13,v13\n"
M_"v[104:107],a[232:235],a[168:171],v[104:107]\n"
E_"v14,v14\n"
M_"v[104:107],a[236:239],a[172:175],v[104:107]\n"
E_"v15,v15\n"
M_"v[120:123],a[224:227],a[176:179],v[204:207]\n"
C_"v134,v12,v13\n"
C_"v135,v14,v15\n"
M_"v[120:123],a[228:231],a[180:183],v[120:123]\n"
A_"m0,s57,0\n"
E_"v16,v16\n"
M_"v[120:123],a[232:235],a[184:187],v[120:123]\n"
B_"v244,s[40:43],s56 offen offset:0 lds\n"
M_"v[120:123],a[236:239],a[188:191],v[120:123]\n"
E_"v17,v17\n"
M_"v[88:91],a[224:227],a[144:147],v[232:235]\n"
E_"v18,v18\n"
M_"v[88:91],a[228:231],a[148:151],v[88:91]\n"
E_"v19,v19\n"
M_"v[88:91],a[232:235],a[152:155],v[88:91]\n"
C_"v136,v16,v17\n"
C_"v137,v18,v19\n"
M_"v[88:91],a[236:239],a[156:159],v[88:91]\n"
D_"a[224:227],v245 offset:20480\n"
D_"a[228:231],v245 offset:28672\n"
M_"v[108:111],a[240:243],a[160:163],v[200:203]\n"
B_"v244,s[40:43],s56 offen offset:1024 lds\n"
M_"v[108:111],a[244:247],a[164:167],v[108:111]\n"
D_"a[232:235],v245 offset:21504\n"
D_"a[236:239],v245 offset:29696\n"
M_"v[108:111],a[248:251],a[168:171],v[108:111]\n"
E_"v20,v20\n"
M_"v[108:111],a[252:255],a[172:175],v[108:111]\n"
E_"v21,v21\n"
M_"v[124:127],a[240:243],a[176:179],v[204:207]\n"
E_"v22,v22\n"
M_"v[124:127],a[244:247],a[180:183],v[124:127]\n"
E_"v23,v23\n"
M_"v[124:127],a[248:251],a[184:187],v[124:127]\n"
C_"v138,v20,v21\n"
C_"v139,v22,v23\n"
M_"v[124:127],a[252:255],a[188:191],v[124:127]\n"
B_"v244,s[40:43],s56 offen offset:2048 lds\n"
M_"v[92:95],a[240:243],a[144:147],v[236:239]\n"
E_"v24,v24\n"
M_"v[92:95],a[244:247],a[148:151],v[92:95]\n"
E_"v25,v25\n"
M_"v[92:95],a[248:251],a[152:155],v[92:95]\n"
E_"v26,v26\n"
M_"v[92:95],a[252:255],a[156:159],v[92:95]\n"
W_"lgkmcnt(4)\n"
D_"a[240:243],v245 offset:22528\n"
D_"a[244:247],v245 offset:30720\n"
M_"a[0:3],a[192:195],v[160:163],a[0:3]\n"
D_"a[248:251],v245 offset:23552\n"
D_"a[252:255],v245 offset:31744\n"
M_"a[0:3],a[196:199],v[164:167],a[0:3]\n"
B_"v244,s[40:43],s56 offen offset:3072 lds\n"
M_"a[4:7],a[192:195],v[168:171],a[4:7]\n"
E_"v27,v27\n"
M_"a[4:7],a[196:199],v[172:175],a[4:7]\n"
C_"v140,v24,v25\n"
C_"v141,v26,v27\n"
M_"a[8:11],a[192:195],v[176:179],a[8:11]\n"
E_"v28,v28\n"
M_"a[8:11],a[196:199],v[180:183],a[8:11]\n"
E_"v29,v29\n"
M_"a[12:15],a[192:195],v[184:187],a[12:15]\n"
E_"v30,v30\n"
M_"a[12:15],a[196:199],v[188:191],a[12:15]\n"
A_"s56,s56,16384\n"
D_"a[192:195],v245 offset:0\n"
D_"a[196:199],v245 offset:1024\n"
M_"a[16:19],a[200:203],v[160:163],a[16:19]\n"
E_"v31,v31\n"
M_"a[16:19],a[204:207],v[164:167],a[16:19]\n"
"v_mov_b32 v64,-1\n"
C_"v142,v28,v29\n"
M_"a[20:23],a[200:203],v[168:171],a[20:23]\n"
"v_mov_b32 v65,-1\n"
C_"v143,v30,v31\n"
M_"a[20:23],a[204:207],v[172:175],a[20:23]\n"
E_"v32,v32\n"
M_"a[24:27],a[200:203],v[176:179],a[24:27]\n"
"v_mov_b32 v66,-1\n"
"v_mov_b32 v67,-1\n"
M_"a[24:27],a[204:207],v[180:183],a[24:27]\n"
E_"v33,v33\n"
M_"a[28:31],a[200:203],v[184:187],a[28:31]\n"
"s_lshl3_add_u32 s13,s30,s31\n"
E_"v34,v34\n"
M_"a[28:31],a[204:207],v[188:191],a[28:31]\n"
"s_lshl_b32 s13,s13,13\n"
D_"a[200:203],v245 offset:2048\n"
D_"a[204:207],v245 offset:3072\n"
M_"a[32:35],a[208:211],v[160:163],a[32:35]\n"
A_"s13,s13,0x5000000\n"
E_"v35,v35\n"
M_"a[32:35],a[212:215],v[164:167],a[32:35]\n"
S_"v[64:67],v244,s[52:55],s13 offen offset:0 sc1\n"
M_"a[36:39],a[208:211],v[168:171],a[36:39]\n"
C_"v144,v32,v33\n"
C_"v145,v34,v35\n"
M_"a[36:39],a[212:215],v[172:175],a[36:39]\n"
E_"v36,v36\n"
M_"a[40:43],a[208:211],v[176:179],a[40:43]\n"
E_"v37,v37\n"
M_"a[40:43],a[212:215],v[180:183],a[40:43]\n"
E_"v38,v38\n"
M_"a[44:47],a[208:211],v[184:187],a[44:47]\n"
E_"v39,v39\n"
M_"a[44:47],a[212:215],v[188:191],a[44:47]\n"
D_"a[208:211],v245 offset:4096\n"
D_"a[212:215],v245 offset:5120\n"
M_"a[48:51],a[216:219],v[160:163],a[48:51]\n"
S_"v[64:67],v244,s[52:55],s13 offen offset:1024 sc1\n"
M_"a[48:51],a[220:223],v[164:167],a[48:51]\n"
C_"v146,v36,v37\n"
C_"v147,v38,v39\n"
M_"a[52:55],a[216:219],v[168:171],a[52:55]\n"
E_"v40,v40\n"
M_"a[52:55],a[220:223],v[172:175],a[52:55]\n"
E_"v41,v41\n"
M_"a[56:59],a[216:219],v[176:179],a[56:59]\n"
E_"v42,v42\n"
M_"a[56:59],a[220:223],v[180:183],a[56:59]\n"
E_"v43,v43\n"
M_"a[60:63],a[216:219],v[184:187],a[60:63]\n"
S_"v[64:67],v244,s[52:55],s13 offen offset:2048 sc1\n"
M_"a[60:63],a[220:223],v[188:191],a[60:63]\n"
W_"lgkmcnt(6)\n"
D_"a[216:219],v245 offset:6144\n"
D_"a[220:223],v245 offset:7168\n"
M_"a[64:67],a[224:227],v[160:163],a[64:67]\n"
C_"v148,v40,v41\n"
C_"v149,v42,v43\n"
M_"a[64:67],a[228:231],v[164:167],a[64:67]\n"
E_"v44,v44\n"
M_"a[68:71],a[224:227],v[168:171],a[68:71]\n"
E_"v45,v45\n"
M_"a[68:71],a[228:231],v[172:175],a[68:71]\n"
E_"v46,v46\n"
M_"a[72:75],a[224:227],v[176:179],a[72:75]\n"
E_"v47,v47\n"
M_"a[72:75],a[228:231],v[180:183],a[72:75]\n"
A_"s13,s13,4096\n"
C_"v150,v44,v45\n"
C_"v151,v46,v47\n"
M_"a[76:79],a[224:227],v[184:187],a[76:79]\n"
S_"v[64:67],v244,s[52:55],s13 offen offset:0 sc1\n"
M_"a[76:79],a[228:231],v[188:191],a[76:79]\n"
D_"a[224:227],v245 offset:8192\n"
D_"a[228:231],v245 offset:9216\n"
M_"a[80:83],a[232:235],v[160:163],a[80:83]\n"
E_"v48,v48\n"
M_"a[80:83],a[236:239],v[164:167],a[80:83]\n"
E_"v49,v49\n"
M_"a[84:87],a[232:235],v[168:171],a[84:87]\n"
E_"v50,v50\n"
M_"a[84:87],a[236:239],v[172:175],a[84:87]\n"
E_"v51,v51\n"
M_"a[88:91],a[232:235],v[176:179],a[88:91]\n"
S_"v[64:67],v244,s[52:55],s13 offen offset:1024 sc1\n"
M_"a[88:91],a[236:239],v[180:183],a[88:91]\n"
C_"v152,v48,v49\n"
C_"v153,v50,v51\n"
M_"a[92:95],a[232:235],v[184:187],a[92:95]\n"
E_"v52,v52\n"
M_"a[92:95],a[236:239],v[188:191],a[92:95]\n"
D_"a[232:235],v245 offset:10240\n"
D_"a[236:239],v245 offset:11264\n"
M_"a[96:99],a[240:243],v[160:163],a[96:99]\n"
E_"v53,v53\n"
M_"a[96:99],a[244:247],v[164:167],a[96:99]\n"
E_"v54,v54\n"
M_"a[100:103],a[240:243],v[168:171],a[100:103]\n"
E_"v55,v55\n"
M_"a[100:103],a[244:247],v[172:175],a[100:103]\n"
S_"v[64:67],v244,s[52:55],s13 offen offset:2048 sc1\n"
M_"a[104:107],a[240:243],v[176:179],a[104:107]\n"
C_"v154,v52,v53\n"
C_"v155,v54,v55\n"
M_"a[104:107],a[244:247],v[180:183],a[104:107]\n"
E_"v56,v56\n"
M_"a[108:111],a[240:243],v[184:187],a[108:111]\n"
E_"v57,v57\n"
M_"a[108:111],a[244:247],v[188:191],a[108:111]\n"
D_"a[240:243],v245 offset:12288\n"
D_"a[244:247],v245 offset:13312\n"
M_"a[112:115],a[248:251],v[160:163],a[112:115]\n"
E_"v58,v58\n"
M_"a[112:115],a[252:255],v[164:167],a[112:115]\n"
E_"v59,v59\n"
M_"a[116:119],a[248:251],v[168:171],a[116:119]\n"
C_"v156,v56,v57\n"
C_"v157,v58,v59\n"
M_"a[116:119],a[252:255],v[172:175],a[116:119]\n"
E_"v60,v60\n"
M_"a[120:123],a[248:251],v[176:179],a[120:123]\n"
E_"v61,v61\n"
M_"a[120:123],a[252:255],v[180:183],a[120:123]\n"
E_"v62,v62\n"
M_"a[124:127],a[248:251],v[184:187],a[124:127]\n"
E_"v63,v63\n"
M_"a[124:127],a[252:255],v[188:191],a[124:127]\n"
D_"a[248:251],v245 offset:14336\n"
D_"a[252:255],v245 offset:15360\n"
"v_smfmac_f32_16x16x64_bf16 v[208:211],v[240:243],v[160:167],v246\n"
C_"v158,v60,v61\n"
C_"v159,v62,v63\n"
"v_smfmac_f32_16x16x64_bf16 v[212:215],v[240:243],v[168:175],v246\n"
"v_smfmac_f32_16x16x64_bf16 v[216:219],v[240:243],v[176:183],v246\n"
"v_smfmac_f32_16x16x64_bf16 v[220:223],v[240:243],v[184:191],v246\n"
W_"vmcnt(14) lgkmcnt(8)\n"
M_"v[32:35],a[192:195],a[160:163],v[200:203]\n"
"s_barrier\n"
E_"v80,v80\n"
M_"v[32:35],a[196:199],a[164:167],v[32:35]\n"
"v_xor_b32 v192,0,v255\n"
"v_xor_b32 v193,32,v255\n"
M_"v[32:35],a[200:203],a[168:171],v[32:35]\n"
E_"v81,v81\n"
M_"v[32:35],a[204:207],a[172:175],v[32:35]\n"
"v_xor_b32 v194,64,v255\n"
"v_xor_b32 v195,96,v255\n"
M_"v[48:51],a[192:195],a[176:179],v[204:207]\n"
"v_bfe_u32 v255,v244,4,5\n"
"v_lshlrev_b32 v255,7,v255\n"
M_"v[48:51],a[196:199],a[180:183],v[48:51]\n"
T_"a[128:129],v192 offset:0\n"
E_"v82,v82\n"
M_"v[48:51],a[200:203],a[184:187],v[48:51]\n"
T_"a[130:131],v192 offset:2048\n"
C_"v168,v80,v81\n"
M_"v[48:51],a[204:207],a[188:191],v[48:51]\n"
D_"a[192:195],v245 offset:49152\n"
D_"a[196:199],v245 offset:57344\n"
M_"v[36:39],a[208:211],a[160:163],v[200:203]\n"
D_"a[200:203],v245 offset:50176\n"
D_"a[204:207],v245 offset:58368\n"
M_"v[36:39],a[212:215],a[164:167],v[36:39]\n"
T_"a[132:133],v193 offset:0\n"
T_"a[134:135],v193 offset:2048\n"
M_"v[36:39],a[216:219],a[168:171],v[36:39]\n"
T_"a[136:137],v194 offset:0\n"
T_"a[138:139],v194 offset:2048\n"
M_"v[36:39],a[220:223],a[172:175],v[36:39]\n"
T_"a[140:141],v195 offset:0\n"
T_"a[142:143],v195 offset:2048\n"
M_"v[52:55],a[208:211],a[176:179],v[204:207]\n"
A_"m0,s57,49152\n"
E_"v83,v83\n"
M_"v[52:55],a[212:215],a[180:183],v[52:55]\n"
B_"v244,s[44:47],s56 offen offset:0 lds\n"
M_"v[52:55],a[216:219],a[184:187],v[52:55]\n"
E_"v84,v84\n"
M_"v[52:55],a[220:223],a[188:191],v[52:55]\n"
W_"lgkmcnt(12)\n"
D_"a[208:211],v245 offset:51200\n"
D_"a[212:215],v245 offset:59392\n"
D_"a[216:219],v245 offset:52224\n"
M_"v[40:43],a[224:227],a[160:163],v[200:203]\n"
B_"v244,s[44:47],s56 offen offset:1024 lds\n"
M_"v[40:43],a[228:231],a[164:167],v[40:43]\n"
D_"a[220:223],v245 offset:60416\n"
C_"v169,v82,v83\n"
M_"v[40:43],a[232:235],a[168:171],v[40:43]\n"
E_"v85,v85\n"
M_"v[40:43],a[236:239],a[172:175],v[40:43]\n"
E_"v86,v86\n"
M_"v[56:59],a[224:227],a[176:179],v[204:207]\n"
B_"v244,s[44:47],s56 offen offset:2048 lds\n"
M_"v[56:59],a[228:231],a[180:183],v[56:59]\n"
E_"v87,v87\n"
M_"v[56:59],a[232:235],a[184:187],v[56:59]\n"
C_"v170,v84,v85\n"
C_"v171,v86,v87\n"
M_"v[56:59],a[236:239],a[188:191],v[56:59]\n"
D_"a[224:227],v245 offset:53248\n"
D_"a[228:231],v245 offset:61440\n"
M_"v[44:47],a[240:243],a[160:163],v[200:203]\n"
B_"v244,s[44:47],s56 offen offset:3072 lds\n"
M_"v[44:47],a[244:247],a[164:167],v[44:47]\n"
D_"a[232:235],v245 offset:54272\n"
E_"v88,v88\n"
M_"v[44:47],a[248:251],a[168:171],v[44:47]\n"
E_"v89,v89\n"
M_"v[44:47],a[252:255],a[172:175],v[44:47]\n"
D_"a[236:239],v245 offset:62464\n"
C_"v172,v88,v89\n"
M_"v[60:63],a[240:243],a[176:179],v[204:207]\n"
E_"v90,v90\n"
M_"v[60:63],a[244:247],a[180:183],v[60:63]\n"
T_"a[144:145],v192 offset:16384\n"
T_"a[146:147],v192 offset:18432\n"
M_"v[60:63],a[248:251],a[184:187],v[60:63]\n"
E_"v91,v91\n"
M_"v[60:63],a[252:255],a[188:191],v[60:63]\n"
W_"lgkmcnt(6)\n"
D_"a[240:243],v245 offset:55296\n"
D_"a[244:247],v245 offset:63488\n"
M_"a[0:3],a[192:195],v[128:131],a[0:3]\n"
D_"a[248:251],v245 offset:56320\n"
D_"a[252:255],v245 offset:64512\n"
M_"a[0:3],a[196:199],v[132:135],a[0:3]\n"
T_"a[148:149],v193 offset:16384\n"
T_"a[150:151],v193 offset:18432\n"
M_"a[4:7],a[192:195],v[136:139],a[4:7]\n"
T_"a[152:153],v194 offset:16384\n"
T_"a[154:155],v194 offset:18432\n"
M_"a[4:7],a[196:199],v[140:143],a[4:7]\n"
T_"a[156:157],v195 offset:16384\n"
T_"a[158:159],v195 offset:18432\n"
C_"v173,v90,v91\n"
M_"a[8:11],a[192:195],v[144:147],a[8:11]\n"
A_"m0,s57,32768\n"
E_"v92,v92\n"
M_"a[8:11],a[196:199],v[148:151],a[8:11]\n"
B_"v244,s[40:43],s56 offen offset:0 lds\n"
M_"a[12:15],a[192:195],v[152:155],a[12:15]\n"
E_"v93,v93\n"
M_"a[12:15],a[196:199],v[156:159],a[12:15]\n"
D_"a[192:195],v245 offset:32768\n"
D_"a[196:199],v245 offset:33792\n"
M_"a[16:19],a[200:203],v[128:131],a[16:19]\n"
B_"v244,s[40:43],s56 offen offset:1024 lds\n"
M_"a[16:19],a[204:207],v[132:135],a[16:19]\n"
E_"v94,v94\n"
M_"a[20:23],a[200:203],v[136:139],a[20:23]\n"
E_"v95,v95\n"
M_"a[20:23],a[204:207],v[140:143],a[20:23]\n"
C_"v174,v92,v93\n"
C_"v175,v94,v95\n"
M_"a[24:27],a[200:203],v[144:147],a[24:27]\n"
B_"v244,s[40:43],s56 offen offset:2048 lds\n"
M_"a[24:27],a[204:207],v[148:151],a[24:27]\n"
E_"v96,v96\n"
M_"a[28:31],a[200:203],v[152:155],a[28:31]\n"
E_"v97,v97\n"
M_"a[28:31],a[204:207],v[156:159],a[28:31]\n"
D_"a[200:203],v245 offset:34816\n"
D_"a[204:207],v245 offset:35840\n"
M_"a[32:35],a[208:211],v[128:131],a[32:35]\n"
B_"v244,s[40:43],s56 offen offset:3072 lds\n"
M_"a[32:35],a[212:215],v[132:135],a[32:35]\n"
E_"v98,v98\n"
M_"a[36:39],a[208:211],v[136:139],a[36:39]\n"
E_"v99,v99\n"
M_"a[36:39],a[212:215],v[140:143],a[36:39]\n"
C_"v176,v96,v97\n"
C_"v177,v98,v99\n"
M_"a[40:43],a[208:211],v[144:147],a[40:43]\n"
A_"s56,s56,16384\n"
E_"v100,v100\n"
M_"a[40:43],a[212:215],v[148:151],a[40:43]\n"
W_"lgkmcnt(4)\n"
E_"v101,v101\n"
M_"a[44:47],a[208:211],v[152:155],a[44:47]\n"
"s_xor_b32 s15,s14,32768\n"
E_"v102,v102\n"
M_"a[44:47],a[212:215],v[156:159],a[44:47]\n"
"s_and_b32 s13,s16,0xffff\n"
D_"a[208:211],v245 offset:36864\n"
D_"a[212:215],v245 offset:37888\n"
C_"v178,v100,v101\n"
M_"a[48:51],a[216:219],v[128:131],a[48:51]\n"
A_"m0,s13,131072\n"
E_"v103,v103\n"
M_"a[48:51],a[220:223],v[132:135],a[48:51]\n"
B_"v249,s[48:51],s15 offen offset:0  lds\n"
M_"a[52:55],a[216:219],v[136:139],a[52:55]\n"
E_"v104,v104\n"
M_"a[52:55],a[220:223],v[140:143],a[52:55]\n"
E_"v105,v105\n"
M_"a[56:59],a[216:219],v[144:147],a[56:59]\n"
B_"v250,s[48:51],s15 offen offset:1024  lds\n"
M_"a[56:59],a[220:223],v[148:151],a[56:59]\n"
C_"v179,v102,v103\n"
C_"v180,v104,v105\n"
M_"a[60:63],a[216:219],v[152:155],a[60:63]\n"
E_"v106,v106\n"
M_"a[60:63],a[220:223],v[156:159],a[60:63]\n"
D_"a[216:219],v245 offset:38912\n"
D_"a[220:223],v245 offset:39936\n"
M_"a[64:67],a[224:227],v[128:131],a[64:67]\n"
B_"v251,s[48:51],s15 offen offset:2048  lds\n"
M_"a[64:67],a[228:231],v[132:135],a[64:67]\n"
E_"v107,v107\n"
M_"a[68:71],a[224:227],v[136:139],a[68:71]\n"
E_"v108,v108\n"
M_"a[68:71],a[228:231],v[140:143],a[68:71]\n"
B_"v253,s[48:51],s15 offen offset:3072  lds\n"
M_"a[72:75],a[224:227],v[144:147],a[72:75]\n"
E_"v109,v109\n"
M_"a[72:75],a[228:231],v[148:151],a[72:75]\n"
C_"v181,v106,v107\n"
C_"v182,v108,v109\n"
M_"a[76:79],a[224:227],v[152:155],a[76:79]\n"
E_"v110,v110\n"
M_"a[76:79],a[228:231],v[156:159],a[76:79]\n"
"s_xor_b32 s15,s14,32896\n"
D_"a[224:227],v245 offset:40960\n"
D_"a[228:231],v245 offset:41984\n"
M_"a[80:83],a[232:235],v[128:131],a[80:83]\n"
"s_and_b32 s13,s16,0xffff\n"
E_"v111,v111\n"
M_"a[80:83],a[236:239],v[132:135],a[80:83]\n"
A_"m0,s13,147456\n"
E_"v112,v112\n"
M_"a[84:87],a[232:235],v[136:139],a[84:87]\n"
B_"v249,s[48:51],s15 offen offset:0  lds\n"
M_"a[84:87],a[236:239],v[140:143],a[84:87]\n"
C_"v183,v110,v111\n"
E_"v113,v113\n"
M_"a[88:91],a[232:235],v[144:147],a[88:91]\n"
E_"v114,v114\n"
M_"a[88:91],a[236:239],v[148:151],a[88:91]\n"
E_"v115,v115\n"
M_"a[92:95],a[232:235],v[152:155],a[92:95]\n"
B_"v250,s[48:51],s15 offen offset:1024  lds\n"
M_"a[92:95],a[236:239],v[156:159],a[92:95]\n"
D_"a[232:235],v245 offset:43008\n"
D_"a[236:239],v245 offset:44032\n"
M_"a[96:99],a[240:243],v[128:131],a[96:99]\n"
C_"v184,v112,v113\n"
C_"v185,v114,v115\n"
M_"a[96:99],a[244:247],v[132:135],a[96:99]\n"
E_"v116,v116\n"
M_"a[100:103],a[240:243],v[136:139],a[100:103]\n"
B_"v251,s[48:51],s15 offen offset:2048  lds\n"
M_"a[100:103],a[244:247],v[140:143],a[100:103]\n"
E_"v117,v117\n"
M_"a[104:107],a[240:243],v[144:147],a[104:107]\n"
E_"v118,v118\n"
M_"a[104:107],a[244:247],v[148:151],a[104:107]\n"
C_"v186,v116,v117\n"
E_"v119,v119\n"
M_"a[108:111],a[240:243],v[152:155],a[108:111]\n"
B_"v253,s[48:51],s15 offen offset:3072  lds\n"
M_"a[108:111],a[244:247],v[156:159],a[108:111]\n"
D_"a[240:243],v245 offset:45056\n"
D_"a[244:247],v245 offset:46080\n"
M_"a[112:115],a[248:251],v[128:131],a[112:115]\n"
E_"v120,v120\n"
M_"a[112:115],a[252:255],v[132:135],a[112:115]\n"
E_"v121,v121\n"
M_"a[116:119],a[248:251],v[136:139],a[116:119]\n"
C_"v187,v118,v119\n"
C_"v188,v120,v121\n"
M_"a[116:119],a[252:255],v[140:143],a[116:119]\n"
E_"v122,v122\n"
M_"a[120:123],a[248:251],v[144:147],a[120:123]\n"
E_"v123,v123\n"
M_"a[120:123],a[252:255],v[148:151],a[120:123]\n"
E_"v124,v124\n"
M_"a[124:127],a[248:251],v[152:155],a[124:127]\n"
E_"v125,v125\n"
M_"a[124:127],a[252:255],v[156:159],a[124:127]\n"
D_"a[248:251],v245 offset:47104\n"
D_"a[252:255],v245 offset:48128\n"
C_"v189,v122,v123\n"
"v_smfmac_f32_16x16x64_bf16 v[208:211],v[240:243],v[128:135],v246\n"
E_"v126,v126\n"
"v_smfmac_f32_16x16x64_bf16 v[212:215],v[240:243],v[136:143],v246\n"
E_"v127,v127\n"
"v_smfmac_f32_16x16x64_bf16 v[216:219],v[240:243],v[144:151],v246\n"
C_"v190,v124,v125\n"
C_"v191,v126,v127\n"
"v_smfmac_f32_16x16x64_bf16 v[220:223],v[240:243],v[152:159],v246\n"
W_"vmcnt(16) lgkmcnt(8)\n"
"L_k2_t4:\n"
M_"v[96:99],a[192:195],a[160:163],v[200:203]\n"
"s_barrier\n"
E_"v32,v32\n"
M_"v[96:99],a[196:199],a[164:167],v[96:99]\n"
"s_xor_b32 s57,s57,0x10000\n"
"v_xor_b32 v245,0x10000,v245\n"
E_"v33,v33\n"
M_"v[96:99],a[200:203],a[168:171],v[96:99]\n"
A_"m0,s57,16384\n"
C_"v144,v32,v33\n"
E_"v34,v34\n"
M_"v[96:99],a[204:207],a[172:175],v[96:99]\n"
B_"v244,s[44:47],s56 offen offset:0 lds\n"
M_"v[112:115],a[192:195],a[176:179],v[204:207]\n"
E_"v35,v35\n"
M_"v[112:115],a[196:199],a[180:183],v[112:115]\n"
E_"v36,v36\n"
M_"v[112:115],a[200:203],a[184:187],v[112:115]\n"
C_"v145,v34,v35\n"
E_"v37,v37\n"
M_"v[112:115],a[204:207],a[188:191],v[112:115]\n"
D_"a[192:195],v245 offset:16384\n"
D_"a[196:199],v245 offset:24576\n"
D_"a[200:203],v245 offset:17408\n"
M_"v[100:103],a[208:211],a[160:163],v[200:203]\n"
D_"a[204:207],v245 offset:25600\n"
C_"v146,v36,v37\n"
"v_add_f32 v208,v208,v211\n"
M_"v[100:103],a[212:215],a[164:167],v[100:103]\n"
E_"v38,v38\n"
M_"v[100:103],a[216:219],a[168:171],v[100:103]\n"
B_"v244,s[44:47],s56 offen offset:1024 lds\n"
M_"v[100:103],a[220:223],a[172:175],v[100:103]\n"
E_"v39,v39\n"
M_"v[116:119],a[208:211],a[176:179],v[204:207]\n"
C_"v147,v38,v39\n"
E_"v40,v40\n"
M_"v[116:119],a[212:215],a[180:183],v[116:119]\n"
E_"v41,v41\n"
M_"v[116:119],a[216:219],a[184:187],v[116:119]\n"
C_"v148,v40,v41\n"
E_"v42,v42\n"
M_"v[116:119],a[220:223],a[188:191],v[116:119]\n"
W_"lgkmcnt(4)\n"
D_"a[208:211],v245 offset:18432\n"
D_"a[212:215],v245 offset:26624\n"
D_"a[216:219],v245 offset:19456\n"
M_"v[104:107],a[224:227],a[160:163],v[200:203]\n"
D_"a[220:223],v245 offset:27648\n"
E_"v43,v43\n"
M_"v[104:107],a[228:231],a[164:167],v[104:107]\n"
B_"v244,s[44:47],s56 offen offset:2048 lds\n"
M_"v[104:107],a[232:235],a[168:171],v[104:107]\n"
C_"v149,v42,v43\n"
E_"v44,v44\n"
M_"v[104:107],a[236:239],a[172:175],v[104:107]\n"
E_"v45,v45\n"
M_"v[120:123],a[224:227],a[176:179],v[204:207]\n"
C_"v150,v44,v45\n"
E_"v46,v46\n"
M_"v[120:123],a[228:231],a[180:183],v[120:123]\n"
E_"v47,v47\n"
M_"v[120:123],a[232:235],a[184:187],v[120:123]\n"
C_"v151,v46,v47\n"
E_"v48,v48\n"
M_"v[120:123],a[236:239],a[188:191],v[120:123]\n"
D_"a[224:227],v245 offset:20480\n"
D_"a[228:231],v245 offset:28672\n"
D_"a[232:235],v245 offset:21504\n"
M_"v[108:111],a[240:243],a[160:163],v[200:203]\n"
B_"v244,s[44:47],s56 offen offset:3072 lds\n"
D_"a[236:239],v245 offset:29696\n"
M_"v[108:111],a[244:247],a[164:167],v[108:111]\n"
E_"v49,v49\n"
M_"v[108:111],a[248:251],a[168:171],v[108:111]\n"
C_"v152,v48,v49\n"
E_"v50,v50\n"
M_"v[108:111],a[252:255],a[172:175],v[108:111]\n"
E_"v51,v51\n"
M_"v[124:127],a[240:243],a[176:179],v[204:207]\n"
C_"v153,v50,v51\n"
E_"v52,v52\n"
M_"v[124:127],a[244:247],a[180:183],v[124:127]\n"
E_"v53,v53\n"
M_"v[124:127],a[248:251],a[184:187],v[124:127]\n"
C_"v154,v52,v53\n"
E_"v54,v54\n"
M_"v[124:127],a[252:255],a[188:191],v[124:127]\n"
W_"lgkmcnt(4)\n"
D_"a[240:243],v245 offset:22528\n"
D_"a[244:247],v245 offset:30720\n"
D_"a[248:251],v245 offset:23552\n"
M_"a[4:7],a[192:195],v[168:171],a[4:7]\n"
A_"m0,s57,0\n"
D_"a[252:255],v245 offset:31744\n"
E_"v55,v55\n"
M_"a[4:7],a[196:199],v[172:175],a[4:7]\n"
B_"v244,s[40:43],s56 offen offset:0 lds\n"
C_"v155,v54,v55\n"
M_"a[8:11],a[192:195],v[176:179],a[8:11]\n"
E_"v56,v56\n"
M_"a[8:11],a[196:199],v[180:183],a[8:11]\n"
E_"v57,v57\n"
M_"a[12:15],a[192:195],v[184:187],a[12:15]\n"
C_"v156,v56,v57\n"
E_"v58,v58\n"
M_"a[12:15],a[196:199],v[188:191],a[12:15]\n"
D_"a[192:195],v245 offset:0\n"
E_"v59,v59\n"
M_"a[20:23],a[200:203],v[168:171],a[20:23]\n"
D_"a[196:199],v245 offset:1024\n"
E_"v60,v60\n"
M_"a[20:23],a[204:207],v[172:175],a[20:23]\n"
C_"v157,v58,v59\n"
E_"v61,v61\n"
M_"a[24:27],a[200:203],v[176:179],a[24:27]\n"
B_"v244,s[40:43],s56 offen offset:1024 lds\n"
C_"v158,v60,v61\n"
M_"a[24:27],a[204:207],v[180:183],a[24:27]\n"
E_"v62,v62\n"
M_"a[28:31],a[200:203],v[184:187],a[28:31]\n"
E_"v63,v63\n"
M_"a[28:31],a[204:207],v[188:191],a[28:31]\n"
D_"a[200:203],v245 offset:2048\n"
D_"a[204:207],v245 offset:3072\n"
C_"v159,v62,v63\n"
M_"a[36:39],a[208:211],v[168:171],a[36:39]\n"
"v_rcp_f32 v247,v208\n"
"v_cmp_nge_f32 vcc,s60,v208\n"
M_"a[36:39],a[212:215],v[172:175],a[36:39]\n"
"s_or_b64 s[62:63],s[62:63],vcc\n"
R_"v0,a0\n"
R_"v1,a1\n"
R_"v2,a2\n"
M_"a[40:43],a[208:211],v[176:179],a[40:43]\n"
R_"v3,a3\n"
R_"v4,a16\n"
R_"v5,a17\n"
M_"a[40:43],a[212:215],v[180:183],a[40:43]\n"
B_"v244,s[40:43],s56 offen offset:2048 lds\n"
R_"v6,a18\n"
M_"a[44:47],a[208:211],v[184:187],a[44:47]\n"
R_"v7,a19\n"
U_"v0,v247,v0\n"
U_"v1,v247,v1\n"
M_"a[44:47],a[212:215],v[188:191],a[44:47]\n"
D_"a[208:211],v245 offset:4096\n"
D_"a[212:215],v245 offset:5120\n"
U_"v2,v247,v2\n"
M_"a[52:55],a[216:219],v[168:171],a[52:55]\n"
U_"v3,v247,v3\n"
U_"v4,v247,v4\n"
U_"v5,v247,v5\n"
M_"a[52:55],a[220:223],v[172:175],a[52:55]\n"
U_"v6,v247,v6\n"
U_"v7,v247,v7\n"
C_"v0,v0,v1\n"
M_"a[56:59],a[216:219],v[176:179],a[56:59]\n"
C_"v1,v2,v3\n"
C_"v2,v4,v5\n"
C_"v3,v6,v7\n"
M_"a[56:59],a[220:223],v[180:183],a[56:59]\n"
S_"v[0:3],v244,s[52:55],s96 offen offset:0 sc1\n"
M_"a[60:63],a[216:219],v[184:187],a[60:63]\n"
B_"v244,s[40:43],s56 offen offset:3072 lds\n"
R_"v8,a32\n"
M_"a[60:63],a[220:223],v[188:191],a[60:63]\n"
W_"lgkmcnt(6)\n"
D_"a[216:219],v245 offset:6144\n"
D_"a[220:223],v245 offset:7168\n"
R_"v9,a33\n"
M_"a[68:71],a[224:227],v[168:171],a[68:71]\n"
R_"v10,a34\n"
R_"v11,a35\n"
R_"v12,a48\n"
M_"a[68:71],a[228:231],v[172:175],a[68:71]\n"
R_"v13,a49\n"
R_"v14,a50\n"
R_"v15,a51\n"
M_"a[72:75],a[224:227],v[176:179],a[72:75]\n"
U_"v8,v247,v8\n"
U_"v9,v247,v9\n"
U_"v10,v247,v10\n"
M_"a[72:75],a[228:231],v[180:183],a[72:75]\n"
U_"v11,v247,v11\n"
U_"v12,v247,v12\n"
U_"v13,v247,v13\n"
M_"a[76:79],a[224:227],v[184:187],a[76:79]\n"
U_"v14,v247,v14\n"
U_"v15,v247,v15\n"
C_"v8,v8,v9\n"
M_"a[76:79],a[228:231],v[188:191],a[76:79]\n"
A_"s56,s56,16384\n"
D_"a[224:227],v245 offset:8192\n"
D_"a[228:231],v245 offset:9216\n"
C_"v9,v10,v11\n"
M_"a[84:87],a[232:235],v[168:171],a[84:87]\n"
C_"v10,v12,v13\n"
C_"v11,v14,v15\n"
M_"a[84:87],a[236:239],v[172:175],a[84:87]\n"
S_"v[8:11],v244,s[52:55],s96 offen offset:1024 sc1\n"
R_"v0,a64\n"
M_"a[88:91],a[232:235],v[176:179],a[88:91]\n"
R_"v1,a65\n"
R_"v2,a66\n"
R_"v3,a67\n"
M_"a[88:91],a[236:239],v[180:183],a[88:91]\n"
R_"v4,a80\n"
R_"v5,a81\n"
M_"a[92:95],a[232:235],v[184:187],a[92:95]\n"
R_"v6,a82\n"
R_"v7,a83\n"
U_"v0,v247,v0\n"
M_"a[92:95],a[236:239],v[188:191],a[92:95]\n"
D_"a[232:235],v245 offset:10240\n"
D_"a[236:239],v245 offset:11264\n"
U_"v1,v247,v1\n"
M_"a[100:103],a[240:243],v[168:171],a[100:103]\n"
U_"v2,v247,v2\n"
U_"v3,v247,v3\n"
U_"v4,v247,v4\n"
M_"a[100:103],a[244:247],v[172:175],a[100:103]\n"
U_"v5,v247,v5\n"
U_"v6,v247,v6\n"
U_"v7,v247,v7\n"
M_"a[104:107],a[240:243],v[176:179],a[104:107]\n"
C_"v0,v0,v1\n"
C_"v1,v2,v3\n"
C_"v2,v4,v5\n"
M_"a[104:107],a[244:247],v[180:183],a[104:107]\n"
C_"v3,v6,v7\n"
M_"a[108:111],a[240:243],v[184:187],a[108:111]\n"
S_"v[0:3],v244,s[52:55],s96 offen offset:2048 sc1\n"
R_"v8,a96\n"
M_"a[108:111],a[244:247],v[188:191],a[108:111]\n"
D_"a[240:243],v245 offset:12288\n"
D_"a[244:247],v245 offset:13312\n"
R_"v9,a97\n"
M_"a[116:119],a[248:251],v[168:171],a[116:119]\n"
R_"v10,a98\n"
R_"v11,a99\n"
R_"v12,a112\n"
M_"a[116:119],a[252:255],v[172:175],a[116:119]\n"
R_"v13,a113\n"
R_"v14,a114\n"
R_"v15,a115\n"
M_"a[120:123],a[248:251],v[176:179],a[120:123]\n"
U_"v8,v247,v8\n"
U_"v9,v247,v9\n"
U_"v10,v247,v10\n"
M_"a[120:123],a[252:255],v[180:183],a[120:123]\n"
U_"v11,v247,v11\n"
U_"v12,v247,v12\n"
M_"a[124:127],a[248:251],v[184:187],a[124:127]\n"
U_"v13,v247,v13\n"
U_"v14,v247,v14\n"
U_"v15,v247,v15\n"
M_"a[124:127],a[252:255],v[188:191],a[124:127]\n"
D_"a[248:251],v245 offset:14336\n"
D_"a[252:255],v245 offset:15360\n"
C_"v8,v8,v9\n"
"v_smfmac_f32_16x16x64_bf16 v[212:215],v[240:243],v[168:175],v246\n"
C_"v9,v10,v11\n"
C_"v10,v12,v13\n"
C_"v11,v14,v15\n"
"v_smfmac_f32_16x16x64_bf16 v[216:219],v[240:243],v[176:183],v246\n"
S_"v[8:11],v244,s[52:55],s96 offen offset:3072 sc1\n"
"v_smfmac_f32_16x16x64_bf16 v[220:223],v[240:243],v[184:191],v246\n"
W_"vmcnt(12) lgkmcnt(8)\n"
M_"v[32:35],a[192:195],a[160:163],v[200:203]\n"
"s_barrier\n"
E_"v96,v96\n"
E_"v97,v97\n"
M_"v[32:35],a[196:199],a[164:167],v[32:35]\n"
T_"a[0:1],v192 offset:0\n"
T_"a[2:3],v192 offset:2048\n"
E_"v98,v98\n"
M_"v[32:35],a[200:203],a[168:171],v[32:35]\n"
T_"a[16:17],v193 offset:0\n"
T_"a[18:19],v193 offset:2048\n"
E_"v99,v99\n"
M_"v[32:35],a[204:207],a[172:175],v[32:35]\n"
T_"a[32:33],v194 offset:0\n"
T_"a[34:35],v194 offset:2048\n"
C_"v176,v96,v97\n"
C_"v177,v98,v99\n"
M_"v[48:51],a[192:195],a[176:179],v[204:207]\n"
T_"a[48:49],v195 offset:0\n"
T_"a[50:51],v195 offset:2048\n"
E_"v100,v100\n"
M_"v[48:51],a[196:199],a[180:183],v[48:51]\n"
T_"a[64:65],v192 offset:16384\n"
T_"a[66:67],v192 offset:18432\n"
E_"v101,v101\n"
M_"v[48:51],a[200:203],a[184:187],v[48:51]\n"
T_"a[80:81],v193 offset:16384\n"
T_"a[82:83],v193 offset:18432\n"
C_"v178,v100,v101\n"
"v_add_f32 v212,v212,v215\n"
M_"v[48:51],a[204:207],a[188:191],v[48:51]\n"
D_"a[192:195],v245 offset:49152\n"
D_"a[196:199],v245 offset:57344\n"
D_"a[200:203],v245 offset:50176\n"
D_"a[204:207],v245 offset:58368\n"
M_"v[36:39],a[208:211],a[160:163],v[200:203]\n"
T_"a[96:97],v194 offset:16384\n"
T_"a[98:99],v194 offset:18432\n"
T_"a[112:113],v195 offset:16384\n"
T_"a[114:115],v195 offset:18432\n"
M_"v[36:39],a[212:215],a[164:167],v[36:39]\n"
"buffer_load_dword v196,v255,s[52:55],s20 offen offset:24 sc1\n"
E_"v102,v102\n"
M_"v[36:39],a[216:219],a[168:171],v[36:39]\n"
A_"m0,s57,49152\n"
E_"v103,v103\n"
E_"v104,v104\n"
M_"v[36:39],a[220:223],a[172:175],v[36:39]\n"
B_"v244,s[44:47],s56 offen offset:0 lds\n"
E_"v105,v105\n"
M_"v[52:55],a[208:211],a[176:179],v[204:207]\n"
C_"v179,v102,v103\n"
C_"v180,v104,v105\n"
E_"v106,v106\n"
M_"v[52:55],a[212:215],a[180:183],v[52:55]\n"
B_"v244,s[44:47],s56 offen offset:1024 lds\n"
E_"v107,v107\n"
M_"v[52:55],a[216:219],a[184:187],v[52:55]\n"
E_"v108,v108\n"
E_"v109,v109\n"
M_"v[52:55],a[220:223],a[188:191],v[52:55]\n"
W_"lgkmcnt(15)\n"
D_"a[208:211],v245 offset:51200\n"
D_"a[212:215],v245 offset:59392\n"
D_"a[216:219],v245 offset:52224\n"
D_"a[220:223],v245 offset:60416\n"
M_"v[40:43],a[224:227],a[160:163],v[200:203]\n"
B_"v244,s[44:47],s56 offen offset:2048 lds\n"
C_"v181,v106,v107\n"
E_"v110,v110\n"
M_"v[40:43],a[228:231],a[164:167],v[40:43]\n"
E_"v111,v111\n"
E_"v112,v112\n"
M_"v[40:43],a[232:235],a[168:171],v[40:43]\n"
B_"v244,s[44:47],s56 offen offset:3072 lds\n"
C_"v182,v108,v109\n"
C_"v183,v110,v111\n"
M_"v[40:43],a[236:239],a[172:175],v[40:43]\n"
E_"v113,v113\n"
E_"v114,v114\n"
M_"v[56:59],a[224:227],a[176:179],v[204:207]\n"
E_"v115,v115\n"
E_"v116,v116\n"
M_"v[56:59],a[228:231],a[180:183],v[56:59]\n"
W_"lgkmcnt(4)\n"
C_"v184,v112,v113\n"
C_"v185,v114,v115\n"
E_"v117,v117\n"
M_"v[56:59],a[232:235],a[184:187],v[56:59]\n"
"s_xor_b32 s15,s14,65536\n"
E_"v118,v118\n"
E_"v119,v119\n"
M_"v[56:59],a[236:239],a[188:191],v[56:59]\n"
"s_and_b32 s13,s16,0xffff\n"
D_"a[224:227],v245 offset:53248\n"
D_"a[228:231],v245 offset:61440\n"
D_"a[232:235],v245 offset:54272\n"
D_"a[236:239],v245 offset:62464\n"
M_"v[44:47],a[240:243],a[160:163],v[200:203]\n"
A_"m0,s13,131072\n"
C_"v186,v116,v117\n"
C_"v187,v118,v119\n"
E_"v120,v120\n"
M_"v[44:47],a[244:247],a[164:167],v[44:47]\n"
B_"v249,s[48:51],s15 offen offset:0  lds\n"
E_"v121,v121\n"
M_"v[44:47],a[248:251],a[168:171],v[44:47]\n"
B_"v250,s[48:51],s15 offen offset:1024  lds\n"
E_"v122,v122\n"
M_"v[44:47],a[252:255],a[172:175],v[44:47]\n"
E_"v123,v123\n"
E_"v124,v124\n"
M_"v[60:63],a[240:243],a[176:179],v[204:207]\n"
B_"v251,s[48:51],s15 offen offset:2048  lds\n"
C_"v188,v120,v121\n"
C_"v189,v122,v123\n"
M_"v[60:63],a[244:247],a[180:183],v[60:63]\n"
E_"v125,v125\n"
E_"v126,v126\n"
M_"v[60:63],a[248:251],a[184:187],v[60:63]\n"
E_"v127,v127\n"
"v_rcp_f32 v248,v212\n"
M_"v[60:63],a[252:255],a[188:191],v[60:63]\n"
W_"lgkmcnt(4)\n"
D_"a[240:243],v245 offset:55296\n"
D_"a[244:247],v245 offset:63488\n"
D_"a[248:251],v245 offset:56320\n"
D_"a[252:255],v245 offset:64512\n"
M_"a[8:11],a[192:195],v[144:147],a[8:11]\n"
B_"v253,s[48:51],s15 offen offset:3072  lds\n"
C_"v190,v124,v125\n"
C_"v191,v126,v127\n"
M_"a[8:11],a[196:199],v[148:151],a[8:11]\n"
"v_cmp_nge_f32 vcc,s60,v212\n"
"s_or_b64 s[62:63],s[62:63],vcc\n"
R_"v16,a4\n"
R_"v17,a5\n"
M_"a[12:15],a[192:195],v[152:155],a[12:15]\n"
"s_xor_b32 s15,s14,65664\n"
R_"v18,a6\n"
R_"v19,a7\n"
R_"v20,a20\n"
R_"v21,a21\n"
M_"a[12:15],a[196:199],v[156:159],a[12:15]\n"
"s_and_b32 s13,s16,0xffff\n"
D_"a[192:195],v245 offset:32768\n"
D_"a[196:199],v245 offset:33792\n"
R_"v22,a22\n"
R_"v23,a23\n"
M_"a[24:27],a[200:203],v[144:147],a[24:27]\n"
A_"m0,s13,147456\n"
U_"v16,v248,v16\n"
U_"v17,v248,v17\n"
U_"v18,v248,v18\n"
U_"v19,v248,v19\n"
M_"a[24:27],a[204:207],v[148:151],a[24:27]\n"
B_"v249,s[48:51],s15 offen offset:0  lds\n"
U_"v20,v248,v20\n"
U_"v21,v248,v21\n"
M_"a[28:31],a[200:203],v[152:155],a[28:31]\n"
B_"v250,s[48:51],s15 offen offset:1024  lds\n"
U_"v22,v248,v22\n"
U_"v23,v248,v23\n"
M_"a[28:31],a[204:207],v[156:159],a[28:31]\n"
D_"a[200:203],v245 offset:34816\n"
D_"a[204:207],v245 offset:35840\n"
C_"v16,v16,v17\n"
C_"v17,v18,v19\n"
M_"a[40:43],a[208:211],v[144:147],a[40:43]\n"
B_"v251,s[48:51],s15 offen offset:2048  lds\n"
C_"v18,v20,v21\n"
C_"v19,v22,v23\n"
M_"a[40:43],a[212:215],v[148:151],a[40:43]\n"
S_"v[16:19],v244,s[52:55],s97 offen offset:0 sc1\n"
R_"v24,a36\n"
R_"v25,a37\n"
M_"a[44:47],a[208:211],v[152:155],a[44:47]\n"
R_"v26,a38\n"
R_"v27,a39\n"
R_"v28,a52\n"
R_"v29,a53\n"
M_"a[44:47],a[212:215],v[156:159],a[44:47]\n"
B_"v253,s[48:51],s15 offen offset:3072  lds\n"
D_"a[208:211],v245 offset:36864\n"
D_"a[212:215],v245 offset:37888\n"
M_"a[56:59],a[216:219],v[144:147],a[56:59]\n"
R_"v30,a54\n"
R_"v31,a55\n"
U_"v24,v248,v24\n"
U_"v25,v248,v25\n"
M_"a[56:59],a[220:223],v[148:151],a[56:59]\n"
U_"v26,v248,v26\n"
U_"v27,v248,v27\n"
U_"v28,v248,v28\n"
U_"v29,v248,v29\n"
M_"a[60:63],a[216:219],v[152:155],a[60:63]\n"
A_"m0,s57,32768\n"
U_"v30,v248,v30\n"
U_"v31,v248,v31\n"
C_"v24,v24,v25\n"
C_"v25,v26,v27\n"
M_"a[60:63],a[220:223],v[156:159],a[60:63]\n"
W_"lgkmcnt(6)\n"
D_"a[216:219],v245 offset:38912\n"
D_"a[220:223],v245 offset:39936\n"
C_"v26,v28,v29\n"
C_"v27,v30,v31\n"
M_"a[72:75],a[224:227],v[144:147],a[72:75]\n"
B_"v244,s[40:43],s56 offen offset:0 lds\n"
M_"a[72:75],a[228:231],v[148:151],a[72:75]\n"
B_"v244,s[40:43],s56 offen offset:1024 lds\n"
S_"v[24:27],v244,s[52:55],s97 offen offset:1024 sc1\n"
M_"a[76:79],a[224:227],v[152:155],a[76:79]\n"
R_"v16,a68\n"
R_"v17,a69\n"
R_"v18,a70\n"
R_"v19,a71\n"
M_"a[76:79],a[228:231],v[156:159],a[76:79]\n"
D_"a[224:227],v245 offset:40960\n"
D_"a[228:231],v245 offset:41984\n"
R_"v20,a84\n"
R_"v21,a85\n"
M_"a[88:91],a[232:235],v[144:147],a[88:91]\n"
B_"v244,s[40:43],s56 offen offset:2048 lds\n"
R_"v22,a86\n"
R_"v23,a87\n"
M_"a[88:91],a[236:239],v[148:151],a[88:91]\n"
U_"v16,v248,v16\n"
U_"v17,v248,v17\n"
U_"v18,v248,v18\n"
U_"v19,v248,v19\n"
M_"a[92:95],a[232:235],v[152:155],a[92:95]\n"
B_"v244,s[40:43],s56 offen offset:3072 lds\n"
U_"v20,v248,v20\n"
U_"v21,v248,v21\n"
M_"a[92:95],a[236:239],v[156:159],a[92:95]\n"
D_"a[232:235],v245 offset:43008\n"
D_"a[236:239],v245 offset:44032\n"
U_"v22,v248,v22\n"
U_"v23,v248,v23\n"
M_"a[104:107],a[240:243],v[144:147],a[104:107]\n"
C_"v16,v16,v17\n"
C_"v17,v18,v19\n"
C_"v18,v20,v21\n"
C_"v19,v22,v23\n"
M_"a[104:107],a[244:247],v[148:151],a[104:107]\n"
S_"v[16:19],v244,s[52:55],s97 offen offset:2048 sc1\n"
R_"v24,a100\n"
R_"v25,a101\n"
M_"a[108:111],a[240:243],v[152:155],a[108:111]\n"
A_"s56,s56,16384\n"
R_"v26,a102\n"
R_"v27,a103\n"
R_"v28,a116\n"
R_"v29,a117\n"
M_"a[108:111],a[244:247],v[156:159],a[108:111]\n"
D_"a[240:243],v245 offset:45056\n"
D_"a[244:247],v245 offset:46080\n"
R_"v30,a118\n"
R_"v31,a119\n"
M_"a[120:123],a[248:251],v[144:147],a[120:123]\n"
U_"v24,v248,v24\n"
U_"v25,v248,v25\n"
U_"v26,v248,v26\n"
U_"v27,v248,v27\n"
M_"a[120:123],a[252:255],v[148:151],a[120:123]\n"
U_"v28,v248,v28\n"
U_"v29,v248,v29\n"
U_"v30,v248,v30\n"
U_"v31,v248,v31\n"
M_"a[124:127],a[248:251],v[152:155],a[124:127]\n"
C_"v24,v24,v25\n"
C_"v25,v26,v27\n"
C_"v26,v28,v29\n"
C_"v27,v30,v31\n"
M_"a[124:127],a[252:255],v[156:159],a[124:127]\n"
S_"v[24:27],v244,s[52:55],s97 offen offset:3072 sc1\n"
D_"a[248:251],v245 offset:47104\n"
D_"a[252:255],v245 offset:48128\n"
"v_smfmac_f32_16x16x64_bf16 v[216:219],v[240:243],v[144:151],v246\n"
"s_or_b64 s[62:63],s[62:63],%8\n"
"s_cselect_b32 s25,1,0\n"
"s_cselect_b32 s12,s12,0x7f000000\n"
"s_mov_b64 s[62:63],0\n"
"v_smfmac_f32_16x16x64_bf16 v[220:223],v[240:243],v[152:159],v246\n"
"buffer_store_dword v254,off,s[52:55],s12 sc1\n"
W_"vmcnt(22) lgkmcnt(8)\n"
M_"v[96:99],a[192:195],a[160:163],v[200:203]\n"
"s_barrier\n"
E_"v32,v32\n"
M_"v[96:99],a[196:199],a[164:167],v[96:99]\n"
"s_getpc_b64 s[10:11]\n"
"L_ca:\n"
A_"s10,s10,L_k2_u1-L_ca\n"
"s_addc_u32 s11,s11,0\n"
M_"v[96:99],a[200:203],a[168:171],v[96:99]\n"
"s_lshl_b32 s13,s31,10\n"
"v_xor_b32 v245,0x10000,v245\n"
E_"v33,v33\n"
M_"v[96:99],a[204:207],a[172:175],v[96:99]\n"
"v_add_lshl_u32 v64,s13,v244,3\n"
E_"v34,v34\n"
M_"v[112:115],a[192:195],a[176:179],v[204:207]\n"
"global_load_dword v64,v64,s[10:11]\n"
C_"v144,v32,v33\n"
M_"v[112:115],a[196:199],a[180:183],v[112:115]\n"
"s_xor_b32 s57,s57,0x10000\n"
E_"v35,v35\n"
E_"v36,v36\n"
M_"v[112:115],a[200:203],a[184:187],v[112:115]\n"
A_"m0,s57,16384\n"
C_"v145,v34,v35\n"
E_"v37,v37\n"
M_"v[112:115],a[204:207],a[188:191],v[112:115]\n"
B_"v244,s[44:47],s56 offen offset:0 lds\n"
D_"a[192:195],v245 offset:16384\n"
M_"v[100:103],a[208:211],a[160:163],v[200:203]\n"
B_"v244,s[44:47],s56 offen offset:1024 lds\n"
D_"a[196:199],v245 offset:24576\n"
M_"v[100:103],a[212:215],a[164:167],v[100:103]\n"
B_"v244,s[44:47],s56 offen offset:2048 lds\n"
D_"a[200:203],v245 offset:17408\n"
M_"v[100:103],a[216:219],a[168:171],v[100:103]\n"
B_"v244,s[44:47],s56 offen offset:3072 lds\n"
D_"a[204:207],v245 offset:25600\n"
M_"v[100:103],a[220:223],a[172:175],v[100:103]\n"
A_"m0,s57,0\n"
C_"v146,v36,v37\n"
E_"v38,v38\n"
M_"v[116:119],a[208:211],a[176:179],v[204:207]\n"
B_"v244,s[40:43],s56 offen offset:0 lds\n"
M_"v[116:119],a[212:215],a[180:183],v[116:119]\n"
B_"v244,s[40:43],s56 offen offset:1024 lds\n"
M_"v[116:119],a[216:219],a[184:187],v[116:119]\n"
B_"v244,s[40:43],s56 offen offset:2048 lds\n"
M_"v[116:119],a[220:223],a[188:191],v[116:119]\n"
W_"lgkmcnt(4)\n"
D_"a[208:211],v245 offset:18432\n"
D_"a[212:215],v245 offset:26624\n"
D_"a[216:219],v245 offset:19456\n"
M_"v[104:107],a[224:227],a[160:163],v[200:203]\n"
B_"v244,s[40:43],s56 offen offset:3072 lds\n"
E_"v39,v39\n"
M_"v[104:107],a[228:231],a[164:167],v[104:107]\n"
A_"s56,s56,16384\n"
D_"a[220:223],v245 offset:27648\n"
E_"v40,v40\n"
M_"v[104:107],a[232:235],a[168:171],v[104:107]\n"
W_"vmcnt(30)\n"
"v_cmp_ne_u32 s[10:11],s32,v196\n"
E_"v41,v41\n"
M_"v[104:107],a[236:239],a[172:175],v[104:107]\n"
"s_cmp_lg_u64 s[10:11],0\n"
"s_cbranch_scc1 L_slow_PF\n"
"L_back_PF:\n"
C_"v147,v38,v39\n"
M_"v[120:123],a[224:227],a[176:179],v[204:207]\n"
"s_mov_b32 s13,s18\n"
C_"v148,v40,v41\n"
E_"v42,v42\n"
M_"v[120:123],a[228:231],a[180:183],v[120:123]\n"
B_"v[128:131],v244,s[52:55],s13 offen sc1\n"
A_"s13,s13,0x200000\n"
M_"v[120:123],a[232:235],a[184:187],v[120:123]\n"
B_"v[132:135],v244,s[52:55],s13 offen sc1\n"
A_"s13,s13,0x200000\n"
M_"v[120:123],a[236:239],a[188:191],v[120:123]\n"
B_"v[136:139],v244,s[52:55],s13 offen sc1\n"
D_"a[224:227],v245 offset:20480\n"
M_"v[108:111],a[240:243],a[160:163],v[200:203]\n"
A_"s13,s13,0x200000\n"
D_"a[228:231],v245 offset:28672\n"
D_"a[232:235],v245 offset:21504\n"
D_"a[236:239],v245 offset:29696\n"
M_"v[108:111],a[244:247],a[164:167],v[108:111]\n"
B_"v[140:143],v244,s[52:55],s13 offen sc1\n"
M_"v[108:111],a[248:251],a[168:171],v[108:111]\n"
W_"vmcnt(21)\n"
E_"v43,v43\n"
M_"v[108:111],a[252:255],a[172:175],v[108:111]\n"
T_"a[4:5],v192 offset:0\n"
T_"a[6:7],v192 offset:2048\n"
T_"a[20:21],v193 offset:0\n"
C_"v149,v42,v43\n"
M_"v[124:127],a[240:243],a[176:179],v[204:207]\n"
T_"a[22:23],v193 offset:2048\n"
T_"a[36:37],v194 offset:0\n"
T_"a[38:39],v194 offset:2048\n"
M_"v[124:127],a[244:247],a[180:183],v[124:127]\n"
T_"a[52:53],v195 offset:0\n"
E_"v44,v44\n"
M_"v[124:127],a[248:251],a[184:187],v[124:127]\n"
T_"a[54:55],v195 offset:2048\n"
T_"a[68:69],v192 offset:16384\n"
T_"a[70:71],v192 offset:18432\n"
M_"v[124:127],a[252:255],a[188:191],v[124:127]\n"
W_"lgkmcnt(14)\n"
D_"a[240:243],v245 offset:22528\n"
D_"a[244:247],v245 offset:30720\n"
D_"a[248:251],v245 offset:23552\n"
M_"a[8:11],a[192:195],v[176:179],a[8:11]\n"
D_"a[252:255],v245 offset:31744\n"
T_"a[84:85],v193 offset:16384\n"
T_"a[86:87],v193 offset:18432\n"
M_"a[8:11],a[196:199],v[180:183],a[8:11]\n"
T_"a[100:101],v194 offset:16384\n"
T_"a[102:103],v194 offset:18432\n"
T_"a[116:117],v195 offset:16384\n"
M_"a[12:15],a[192:195],v[184:187],a[12:15]\n"
T_"a[118:119],v195 offset:18432\n"
E_"v45,v45\n"
M_"a[12:15],a[196:199],v[188:191],a[12:15]\n"
A_"s13,s18,s19\n"
D_"a[192:195],v245 offset:0\n"
D_"a[196:199],v245 offset:1024\n"
C_"v150,v44,v45\n"
M_"a[24:27],a[200:203],v[176:179],a[24:27]\n"
B_"v[160:163],v244,s[52:55],s13 offen sc1\n"
A_"s13,s13,0x200000\n"
M_"a[24:27],a[204:207],v[180:183],a[24:27]\n"
B_"v[164:167],v244,s[52:55],s13 offen sc1\n"
A_"s13,s13,0x200000\n"
M_"a[28:31],a[200:203],v[184:187],a[28:31]\n"
B_"v[168:171],v244,s[52:55],s13 offen sc1\n"
E_"v46,v46\n"
M_"a[28:31],a[204:207],v[188:191],a[28:31]\n"
A_"s13,s13,0x200000\n"
D_"a[200:203],v245 offset:2048\n"
E_"v47,v47\n"
M_"a[40:43],a[208:211],v[176:179],a[40:43]\n"
B_"v[172:175],v244,s[52:55],s13 offen sc1\n"
D_"a[204:207],v245 offset:3072\n"
M_"a[40:43],a[212:215],v[180:183],a[40:43]\n"
W_"vmcnt(17)\n"
"s_barrier\n"
E_"v48,v48\n"
M_"a[44:47],a[208:211],v[184:187],a[44:47]\n"
"buffer_store_dword v254,off,s[52:55],s17 sc1\n"
C_"v151,v46,v47\n"
M_"a[44:47],a[212:215],v[188:191],a[44:47]\n"
W_"lgkmcnt(4)\n"
D_"a[208:211],v245 offset:4096\n"
E_"v49,v49\n"
M_"a[56:59],a[216:219],v[176:179],a[56:59]\n"
"s_xor_b32 s15,s14,98304\n"
D_"a[212:215],v245 offset:5120\n"
E_"v50,v50\n"
M_"a[56:59],a[220:223],v[180:183],a[56:59]\n"
"s_and_b32 s13,s16,0xffff\n"
C_"v152,v48,v49\n"
E_"v51,v51\n"
M_"a[60:63],a[216:219],v[184:187],a[60:63]\n"
A_"m0,s13,131072\n"
C_"v153,v50,v51\n"
E_"v52,v52\n"
M_"a[60:63],a[220:223],v[188:191],a[60:63]\n"
D_"a[216:219],v245 offset:6144\n"
E_"v53,v53\n"
M_"a[72:75],a[224:227],v[176:179],a[72:75]\n"
B_"v249,s[48:51],s15 offen offset:0  lds\n"
D_"a[220:223],v245 offset:7168\n"
M_"a[72:75],a[228:231],v[180:183],a[72:75]\n"
B_"v250,s[48:51],s15 offen offset:1024  lds\n"
E_"v54,v54\n"
M_"a[76:79],a[224:227],v[184:187],a[76:79]\n"
B_"v251,s[48:51],s15 offen offset:2048  lds\n"
C_"v154,v52,v53\n"
M_"a[76:79],a[228:231],v[188:191],a[76:79]\n"
B_"v253,s[48:51],s15 offen offset:3072  lds\n"
D_"a[224:227],v245 offset:8192\n"
M_"a[88:91],a[232:235],v[176:179],a[88:91]\n"
"s_xor_b32 s15,s14,98432\n"
D_"a[228:231],v245 offset:9216\n"
E_"v55,v55\n"
M_"a[88:91],a[236:239],v[180:183],a[88:91]\n"
"s_and_b32 s13,s16,0xffff\n"
C_"v155,v54,v55\n"
E_"v56,v56\n"
M_"a[92:95],a[232:235],v[184:187],a[92:95]\n"
A_"m0,s13,147456\n"
E_"v57,v57\n"
M_"a[92:95],a[236:239],v[188:191],a[92:95]\n"
B_"v249,s[48:51],s15 offen offset:0  lds\n"
D_"a[232:235],v245 offset:10240\n"
M_"a[104:107],a[240:243],v[176:179],a[104:107]\n"
B_"v250,s[48:51],s15 offen offset:1024  lds\n"
D_"a[236:239],v245 offset:11264\n"
M_"a[104:107],a[244:247],v[180:183],a[104:107]\n"
B_"v251,s[48:51],s15 offen offset:2048  lds\n"
C_"v156,v56,v57\n"
M_"a[108:111],a[240:243],v[184:187],a[108:111]\n"
B_"v253,s[48:51],s15 offen offset:3072  lds\n"
M_"a[108:111],a[244:247],v[188:191],a[108:111]\n"
D_"a[240:243],v245 offset:12288\n"
E_"v58,v58\n"
M_"a[120:123],a[248:251],v[176:179],a[120:123]\n"
E_"v59,v59\n"
E_"v60,v60\n"
M_"a[120:123],a[252:255],v[180:183],a[120:123]\n"
"s_sub_u32 s59,s59,1\n"
D_"a[244:247],v245 offset:13312\n"
E_"v61,v61\n"
M_"a[124:127],a[248:251],v[184:187],a[124:127]\n"
"s_cmp_gt_i32 s59,0\n"
C_"v157,v58,v59\n"
E_"v62,v62\n"
M_"a[124:127],a[252:255],v[188:191],a[124:127]\n"
D_"a[248:251],v245 offset:14336\n"
D_"a[252:255],v245 offset:15360\n"
C_"v158,v60,v61\n"
"v_smfmac_f32_16x16x64_bf16 v[216:219],v[240:243],v[176:183],v246\n"
E_"v63,v63\n"
W_"vmcnt(25) lgkmcnt(8)\n"
"v_smfmac_f32_16x16x64_bf16 v[220:223],v[240:243],v[184:191],v246\n"
C_"v159,v62,v63\n"
"s_cbranch_scc0 L_k2_u1\n"
"s_branch L_k2_loop2\n"
".p2align 6\n"
"L_k2_loop2:\n"
M_"v[32:35],a[192:195],a[160:163],v[200:203]\n"
"s_barrier\n"
M_"v[32:35],a[196:199],a[164:167],v[32:35]\n"
A_"m0,s57,49152\n"
E_"v96,v96\n"
M_"v[32:35],a[200:203],a[168:171],v[32:35]\n"
B_"v244,s[44:47],s56 offen offset:0 lds\n"
M_"v[32:35],a[204:207],a[172:175],v[32:35]\n"
E_"v97,v97\n"
M_"v[48:51],a[192:195],a[176:179],v[204:207]\n"
E_"v98,v98\n"
M_"v[48:51],a[196:199],a[180:183],v[48:51]\n"
E_"v99,v99\n"
M_"v[48:51],a[200:203],a[184:187],v[48:51]\n"
C_"v176,v96,v97\n"
C_"v177,v98,v99\n"
M_"v[48:51],a[204:207],a[188:191],v[48:51]\n"
B_"v244,s[44:47],s56 offen offset:1024 lds\n"
M_"v[36:39],a[208:211],a[160:163],v[200:203]\n"
D_"a[192:195],v245 offset:49152\n"
D_"a[196:199],v245 offset:57344\n"
M_"v[36:39],a[212:215],a[164:167],v[36:39]\n"
D_"a[200:203],v245 offset:50176\n"
D_"a[204:207],v245 offset:58368\n"
M_"v[36:39],a[216:219],a[168:171],v[36:39]\n"
E_"v100,v100\n"
M_"v[36:39],a[220:223],a[172:175],v[36:39]\n"
E_"v101,v101\n"
M_"v[52:55],a[208:211],a[176:179],v[204:207]\n"
E_"v102,v102\n"
M_"v[52:55],a[212:215],a[180:183],v[52:55]\n"
B_"v244,s[44:47],s56 offen offset:2048 lds\n"
M_"v[52:55],a[216:219],a[184:187],v[52:55]\n"
E_"v103,v103\n"
M_"v[52:55],a[220:223],a[188:191],v[52:55]\n"
W_"lgkmcnt(4)\n"
D_"a[208:211],v245 offset:51200\n"
D_"a[212:215],v245 offset:59392\n"
M_"v[40:43],a[224:227],a[160:163],v[200:203]\n"
D_"a[216:219],v245 offset:52224\n"
D_"a[220:223],v245 offset:60416\n"
M_"v[40:43],a[228:231],a[164:167],v[40:43]\n"
C_"v178,v100,v101\n"
C_"v179,v102,v103\n"
M_"v[40:43],a[232:235],a[168:171],v[40:43]\n"
B_"v244,s[44:47],s56 offen offset:3072 lds\n"
M_"v[40:43],a[236:239],a[172:175],v[40:43]\n"
E_"v104,v104\n"
M_"v[56:59],a[224:227],a[176:179],v[204:207]\n"
E_"v105,v105\n"
M_"v[56:59],a[228:231],a[180:183],v[56:59]\n"
E_"v106,v106\n"
M_"v[56:59],a[232:235],a[184:187],v[56:59]\n"
E_"v107,v107\n"
M_"v[56:59],a[236:239],a[188:191],v[56:59]\n"
D_"a[224:227],v245 offset:53248\n"
D_"a[228:231],v245 offset:61440\n"
M_"v[44:47],a[240:243],a[160:163],v[200:203]\n"
A_"m0,s57,32768\n"
D_"a[232:235],v245 offset:54272\n"
D_"a[236:239],v245 offset:62464\n"
M_"v[44:47],a[244:247],a[164:167],v[44:47]\n"
B_"v244,s[40:43],s56 offen offset:0 lds\n"
M_"v[44:47],a[248:251],a[168:171],v[44:47]\n"
C_"v180,v104,v105\n"
C_"v181,v106,v107\n"
M_"v[44:47],a[252:255],a[172:175],v[44:47]\n"
E_"v108,v108\n"
M_"v[60:63],a[240:243],a[176:179],v[204:207]\n"
E_"v109,v109\n"
M_"v[60:63],a[244:247],a[180:183],v[60:63]\n"
E_"v110,v110\n"
M_"v[60:63],a[248:251],a[184:187],v[60:63]\n"
E_"v111,v111\n"
M_"v[60:63],a[252:255],a[188:191],v[60:63]\n"
W_"lgkmcnt(4)\n"
D_"a[240:243],v245 offset:55296\n"
D_"a[244:247],v245 offset:63488\n"
M_"a[8:11],a[192:195],v[144:147],a[8:11]\n"
B_"v244,s[40:43],s56 offen offset:1024 lds\n"
M_"a[8:11],a[196:199],v[148:151],a[8:11]\n"
D_"a[248:251],v245 offset:56320\n"
D_"a[252:255],v245 offset:64512\n"
M_"a[12:15],a[192:195],v[152:155],a[12:15]\n"
C_"v182,v108,v109\n"
C_"v183,v110,v111\n"
M_"a[12:15],a[196:199],v[156:159],a[12:15]\n"
D_"a[192:195],v245 offset:32768\n"
D_"a[196:199],v245 offset:33792\n"
M_"a[24:27],a[200:203],v[144:147],a[24:27]\n"
B_"v244,s[40:43],s56 offen offset:2048 lds\n"
M_"a[24:27],a[204:207],v[148:151],a[24:27]\n"
E_"v112,v112\n"
M_"a[28:31],a[200:203],v[152:155],a[28:31]\n"
E_"v113,v113\n"
M_"a[28:31],a[204:207],v[156:159],a[28:31]\n"
D_"a[200:203],v245 offset:34816\n"
D_"a[204:207],v245 offset:35840\n"
M_"a[40:43],a[208:211],v[144:147],a[40:43]\n"
E_"v114,v114\n"
M_"a[40:43],a[212:215],v[148:151],a[40:43]\n"
E_"v115,v115\n"
M_"a[44:47],a[208:211],v[152:155],a[44:47]\n"
B_"v244,s[40:43],s56 offen offset:3072 lds\n"
M_"a[44:47],a[212:215],v[156:159],a[44:47]\n"
D_"a[208:211],v245 offset:36864\n"
D_"a[212:215],v245 offset:37888\n"
M_"a[56:59],a[216:219],v[144:147],a[56:59]\n"
C_"v184,v112,v113\n"
C_"v185,v114,v115\n"
M_"a[56:59],a[220:223],v[148:151],a[56:59]\n"
E_"v116,v116\n"
M_"a[60:63],a[216:219],v[152:155],a[60:63]\n"
E_"v117,v117\n"
M_"a[60:63],a[220:223],v[156:159],a[60:63]\n"
W_"lgkmcnt(6)\n"
D_"a[216:219],v245 offset:38912\n"
D_"a[220:223],v245 offset:39936\n"
M_"a[72:75],a[224:227],v[144:147],a[72:75]\n"
A_"s56,s56,16384\n"
E_"v118,v118\n"
M_"a[72:75],a[228:231],v[148:151],a[72:75]\n"
E_"v119,v119\n"
M_"a[76:79],a[224:227],v[152:155],a[76:79]\n"
C_"v186,v116,v117\n"
C_"v187,v118,v119\n"
M_"a[76:79],a[228:231],v[156:159],a[76:79]\n"
D_"a[224:227],v245 offset:40960\n"
D_"a[228:231],v245 offset:41984\n"
M_"a[88:91],a[232:235],v[144:147],a[88:91]\n"
E_"v120,v120\n"
M_"a[88:91],a[236:239],v[148:151],a[88:91]\n"
E_"v121,v121\n"
M_"a[92:95],a[232:235],v[152:155],a[92:95]\n"
E_"v122,v122\n"
M_"a[92:95],a[236:239],v[156:159],a[92:95]\n"
D_"a[232:235],v245 offset:43008\n"
D_"a[236:239],v245 offset:44032\n"
M_"a[104:107],a[240:243],v[144:147],a[104:107]\n"
E_"v123,v123\n"
M_"a[104:107],a[244:247],v[148:151],a[104:107]\n"
C_"v188,v120,v121\n"
C_"v189,v122,v123\n"
M_"a[108:111],a[240:243],v[152:155],a[108:111]\n"
E_"v124,v124\n"
M_"a[108:111],a[244:247],v[156:159],a[108:111]\n"
D_"a[240:243],v245 offset:45056\n"
D_"a[244:247],v245 offset:46080\n"
M_"a[120:123],a[248:251],v[144:147],a[120:123]\n"
E_"v125,v125\n"
M_"a[120:123],a[252:255],v[148:151],a[120:123]\n"
E_"v126,v126\n"
M_"a[124:127],a[248:251],v[152:155],a[124:127]\n"
E_"v127,v127\n"
M_"a[124:127],a[252:255],v[156:159],a[124:127]\n"
D_"a[248:251],v245 offset:47104\n"
D_"a[252:255],v245 offset:48128\n"
"v_smfmac_f32_16x16x64_bf16 v[216:219],v[240:243],v[144:151],v246\n"
C_"v190,v124,v125\n"
C_"v191,v126,v127\n"
"v_smfmac_f32_16x16x64_bf16 v[220:223],v[240:243],v[152:159],v246\n"
W_"vmcnt(8) lgkmcnt(8)\n"
M_"v[96:99],a[192:195],a[160:163],v[200:203]\n"
"s_barrier\n"
"v_xor_b32 v245,0x10000,v245\n"
M_"v[96:99],a[196:199],a[164:167],v[96:99]\n"
"s_xor_b32 s57,s57,0x10000\n"
E_"v32,v32\n"
M_"v[96:99],a[200:203],a[168:171],v[96:99]\n"
A_"m0,s57,16384\n"
E_"v33,v33\n"
M_"v[96:99],a[204:207],a[172:175],v[96:99]\n"
B_"v244,s[44:47],s56 offen offset:0 lds\n"
M_"v[112:115],a[192:195],a[176:179],v[204:207]\n"
E_"v34,v34\n"
M_"v[112:115],a[196:199],a[180:183],v[112:115]\n"
E_"v35,v35\n"
M_"v[112:115],a[200:203],a[184:187],v[112:115]\n"
C_"v144,v32,v33\n"
C_"v145,v34,v35\n"
M_"v[112:115],a[204:207],a[188:191],v[112:115]\n"
D_"a[192:195],v245 offset:16384\n"
D_"a[196:199],v245 offset:24576\n"
M_"v[100:103],a[208:211],a[160:163],v[200:203]\n"
B_"v244,s[44:47],s56 offen offset:1024 lds\n"
M_"v[100:103],a[212:215],a[164:167],v[100:103]\n"
D_"a[200:203],v245 offset:17408\n"
D_"a[204:207],v245 offset:25600\n"
M_"v[100:103],a[216:219],a[168:171],v[100:103]\n"
E_"v36,v36\n"
M_"v[100:103],a[220:223],a[172:175],v[100:103]\n"
E_"v37,v37\n"
M_"v[116:119],a[208:211],a[176:179],v[204:207]\n"
E_"v38,v38\n"
M_"v[116:119],a[212:215],a[180:183],v[116:119]\n"
E_"v39,v39\n"
M_"v[116:119],a[216:219],a[184:187],v[116:119]\n"
B_"v244,s[44:47],s56 offen offset:2048 lds\n"
M_"v[116:119],a[220:223],a[188:191],v[116:119]\n"
W_"lgkmcnt(4)\n"
D_"a[208:211],v245 offset:18432\n"
D_"a[212:215],v245 offset:26624\n"
M_"v[104:107],a[224:227],a[160:163],v[200:203]\n"
D_"a[216:219],v245 offset:19456\n"
D_"a[220:223],v245 offset:27648\n"
M_"v[104:107],a[228:231],a[164:167],v[104:107]\n"
C_"v146,v36,v37\n"
C_"v147,v38,v39\n"
M_"v[104:107],a[232:235],a[168:171],v[104:107]\n"
E_"v40,v40\n"
M_"v[104:107],a[236:239],a[172:175],v[104:107]\n"
B_"v244,s[44:47],s56 offen offset:3072 lds\n"
M_"v[120:123],a[224:227],a[176:179],v[204:207]\n"
E_"v41,v41\n"
M_"v[120:123],a[228:231],a[180:183],v[120:123]\n"
E_"v42,v42\n"
M_"v[120:123],a[232:235],a[184:187],v[120:123]\n"
E_"v43,v43\n"
M_"v[120:123],a[236:239],a[188:191],v[120:123]\n"
D_"a[224:227],v245 offset:20480\n"
D_"a[228:231],v245 offset:28672\n"
M_"v[108:111],a[240:243],a[160:163],v[200:203]\n"
D_"a[232:235],v245 offset:21504\n"
D_"a[236:239],v245 offset:29696\n"
M_"v[108:111],a[244:247],a[164:167],v[108:111]\n"
A_"m0,s57,0\n"
C_"v148,v40,v41\n"
C_"v149,v42,v43\n"
M_"v[108:111],a[248:251],a[168:171],v[108:111]\n"
B_"v244,s[40:43],s56 offen offset:0 lds\n"
M_"v[108:111],a[252:255],a[172:175],v[108:111]\n"
E_"v44,v44\n"
M_"v[124:127],a[240:243],a[176:179],v[204:207]\n"
E_"v45,v45\n"
M_"v[124:127],a[244:247],a[180:183],v[124:127]\n"
E_"v46,v46\n"
M_"v[124:127],a[248:251],a[184:187],v[124:127]\n"
E_"v47,v47\n"
M_"v[124:127],a[252:255],a[188:191],v[124:127]\n"
W_"lgkmcnt(4)\n"
D_"a[240:243],v245 offset:22528\n"
D_"a[244:247],v245 offset:30720\n"
M_"a[8:11],a[192:195],v[176:179],a[8:11]\n"
B_"v244,s[40:43],s56 offen offset:1024 lds\n"
M_"a[8:11],a[196:199],v[180:183],a[8:11]\n"
D_"a[248:251],v245 offset:23552\n"
D_"a[252:255],v245 offset:31744\n"
M_"a[12:15],a[192:195],v[184:187],a[12:15]\n"
C_"v150,v44,v45\n"
C_"v151,v46,v47\n"
M_"a[12:15],a[196:199],v[188:191],a[12:15]\n"
D_"a[192:195],v245 offset:0\n"
D_"a[196:199],v245 offset:1024\n"
M_"a[24:27],a[200:203],v[176:179],a[24:27]\n"
E_"v48,v48\n"
M_"a[24:27],a[204:207],v[180:183],a[24:27]\n"
B_"v244,s[40:43],s56 offen offset:2048 lds\n"
M_"a[28:31],a[200:203],v[184:187],a[28:31]\n"
E_"v49,v49\n"
M_"a[28:31],a[204:207],v[188:191],a[28:31]\n"
D_"a[200:203],v245 offset:2048\n"
D_"a[204:207],v245 offset:3072\n"
M_"a[40:43],a[208:211],v[176:179],a[40:43]\n"
E_"v50,v50\n"
M_"a[40:43],a[212:215],v[180:183],a[40:43]\n"
E_"v51,v51\n"
M_"a[44:47],a[208:211],v[184:187],a[44:47]\n"
B_"v244,s[40:43],s56 offen offset:3072 lds\n"
M_"a[44:47],a[212:215],v[188:191],a[44:47]\n"
D_"a[208:211],v245 offset:4096\n"
D_"a[212:215],v245 offset:5120\n"
M_"a[56:59],a[216:219],v[176:179],a[56:59]\n"
C_"v152,v48,v49\n"
C_"v153,v50,v51\n"
M_"a[56:59],a[220:223],v[180:183],a[56:59]\n"
E_"v52,v52\n"
M_"a[60:63],a[216:219],v[184:187],a[60:63]\n"
E_"v53,v53\n"
M_"a[60:63],a[220:223],v[188:191],a[60:63]\n"
W_"lgkmcnt(6)\n"
D_"a[216:219],v245 offset:6144\n"
D_"a[220:223],v245 offset:7168\n"
M_"a[72:75],a[224:227],v[176:179],a[72:75]\n"
A_"s56,s56,16384\n"
E_"v54,v54\n"
M_"a[72:75],a[228:231],v[180:183],a[72:75]\n"
E_"v55,v55\n"
M_"a[76:79],a[224:227],v[184:187],a[76:79]\n"
C_"v154,v52,v53\n"
C_"v155,v54,v55\n"
M_"a[76:79],a[228:231],v[188:191],a[76:79]\n"
D_"a[224:227],v245 offset:8192\n"
D_"a[228:231],v245 offset:9216\n"
M_"a[88:91],a[232:235],v[176:179],a[88:91]\n"
E_"v56,v56\n"
M_"a[88:91],a[236:239],v[180:183],a[88:91]\n"
E_"v57,v57\n"
M_"a[92:95],a[232:235],v[184:187],a[92:95]\n"
E_"v58,v58\n"
M_"a[92:95],a[236:239],v[188:191],a[92:95]\n"
D_"a[232:235],v245 offset:10240\n"
D_"a[236:239],v245 offset:11264\n"
M_"a[104:107],a[240:243],v[176:179],a[104:107]\n"
E_"v59,v59\n"
M_"a[104:107],a[244:247],v[180:183],a[104:107]\n"
C_"v156,v56,v57\n"
C_"v157,v58,v59\n"
M_"a[108:111],a[240:243],v[184:187],a[108:111]\n"
E_"v60,v60\n"
M_"a[108:111],a[244:247],v[188:191],a[108:111]\n"
D_"a[240:243],v245 offset:12288\n"
D_"a[244:247],v245 offset:13312\n"
M_"a[120:123],a[248:251],v[176:179],a[120:123]\n"
E_"v61,v61\n"
M_"a[120:123],a[252:255],v[180:183],a[120:123]\n"
"s_sub_u32 s59,s59,1\n"
E_"v62,v62\n"
M_"a[124:127],a[248:251],v[184:187],a[124:127]\n"
"s_cmp_gt_i32 s59,0\n"
E_"v63,v63\n"
M_"a[124:127],a[252:255],v[188:191],a[124:127]\n"
D_"a[248:251],v245 offset:14336\n"
D_"a[252:255],v245 offset:15360\n"
"v_smfmac_f32_16x16x64_bf16 v[216:219],v[240:243],v[176:183],v246\n"
W_"vmcnt(8) lgkmcnt(8)\n"
C_"v158,v60,v61\n"
C_"v159,v62,v63\n"
"v_smfmac_f32_16x16x64_bf16 v[220:223],v[240:243],v[184:191],v246\n"
"s_cbranch_scc1 L_k2_loop2\n"
"L_k2_u1:\n"
M_"v[48:51],a[192:195],a[176:179],v[204:207]\n"
"s_barrier\n"
"v_mov_b32 v248,0xff800000\n"
K_"v224,v248,v200,s[64:65]\n"
K_"v225,v248,v200,s[66:67]\n"
M_"v[48:51],a[196:199],a[180:183],v[48:51]\n"
A_"m0,s57,49152\n"
K_"v226,v248,v200,s[68:69]\n"
K_"v227,v248,v200,s[70:71]\n"
K_"v228,v248,v200,s[72:73]\n"
M_"v[48:51],a[200:203],a[184:187],v[48:51]\n"
B_"v244,s[44:47],s56 offen offset:0 lds\n"
K_"v229,v248,v200,s[74:75]\n"
M_"v[48:51],a[204:207],a[188:191],v[48:51]\n"
K_"v230,v248,v200,s[76:77]\n"
K_"v231,v248,v200,s[78:79]\n"
K_"v232,v248,v200,s[80:81]\n"
M_"v[32:35],a[192:195],a[160:163],v[224:227]\n"
B_"v244,s[44:47],s56 offen offset:1024 lds\n"
K_"v233,v248,v200,s[82:83]\n"
M_"v[32:35],a[196:199],a[164:167],v[32:35]\n"
K_"v234,v248,v200,s[84:85]\n"
K_"v235,v248,v200,s[86:87]\n"
K_"v236,v248,v200,s[88:89]\n"
M_"v[32:35],a[200:203],a[168:171],v[32:35]\n"
K_"v237,v248,v200,s[90:91]\n"
K_"v238,v248,v200,s[92:93]\n"
K_"v239,v248,v200,s[94:95]\n"
M_"v[32:35],a[204:207],a[172:175],v[32:35]\n"
B_"v244,s[44:47],s56 offen offset:2048 lds\n"
D_"a[192:195],v245 offset:49152\n"
M_"v[52:55],a[208:211],a[176:179],v[204:207]\n"
D_"a[196:199],v245 offset:57344\n"
D_"a[200:203],v245 offset:50176\n"
D_"a[204:207],v245 offset:58368\n"
M_"v[52:55],a[212:215],a[180:183],v[52:55]\n"
E_"v96,v96\n"
M_"v[52:55],a[216:219],a[184:187],v[52:55]\n"
B_"v244,s[44:47],s56 offen offset:3072 lds\n"
M_"v[52:55],a[220:223],a[188:191],v[52:55]\n"
E_"v97,v97\n"
M_"v[36:39],a[208:211],a[160:163],v[228:231]\n"
C_"v176,v96,v97\n"
E_"v98,v98\n"
M_"v[36:39],a[212:215],a[164:167],v[36:39]\n"
E_"v99,v99\n"
M_"v[36:39],a[216:219],a[168:171],v[36:39]\n"
A_"s56,s56,16384\n"
C_"v177,v98,v99\n"
E_"v100,v100\n"
M_"v[36:39],a[220:223],a[172:175],v[36:39]\n"
W_"lgkmcnt(4)\n"
D_"a[208:211],v245 offset:51200\n"
D_"a[212:215],v245 offset:59392\n"
D_"a[216:219],v245 offset:52224\n"
M_"v[56:59],a[224:227],a[176:179],v[204:207]\n"
"s_xor_b32 m0,s57,0x14000\n"
D_"a[220:223],v245 offset:60416\n"
E_"v101,v101\n"
M_"v[56:59],a[228:231],a[180:183],v[56:59]\n"
B_"v244,s[44:47],s56 offen offset:0 lds\n"
C_"v178,v100,v101\n"
M_"v[56:59],a[232:235],a[184:187],v[56:59]\n"
B_"v244,s[44:47],s56 offen offset:1024 lds\n"
M_"v[56:59],a[236:239],a[188:191],v[56:59]\n"
E_"v102,v102\n"
M_"v[40:43],a[224:227],a[160:163],v[232:235]\n"
E_"v103,v103\n"
M_"v[40:43],a[228:231],a[164:167],v[40:43]\n"
B_"v244,s[44:47],s56 offen offset:2048 lds\n"
C_"v179,v102,v103\n"
M_"v[40:43],a[232:235],a[168:171],v[40:43]\n"
E_"v104,v104\n"
M_"v[40:43],a[236:239],a[172:175],v[40:43]\n"
D_"a[224:227],v245 offset:53248\n"
D_"a[228:231],v245 offset:61440\n"
D_"a[232:235],v245 offset:54272\n"
M_"v[60:63],a[240:243],a[176:179],v[204:207]\n"
B_"v244,s[44:47],s56 offen offset:3072 lds\n"
D_"a[236:239],v245 offset:62464\n"
M_"v[60:63],a[244:247],a[180:183],v[60:63]\n"
E_"v105,v105\n"
M_"v[60:63],a[248:251],a[184:187],v[60:63]\n"
C_"v180,v104,v105\n"
E_"v106,v106\n"
M_"v[60:63],a[252:255],a[188:191],v[60:63]\n"
"s_xor_b32 s15,s14,131072\n"
E_"v107,v107\n"
M_"v[44:47],a[240:243],a[160:163],v[236:239]\n"
"s_xor_b32 m0,s16,0\n"
C_"v181,v106,v107\n"
E_"v108,v108\n"
M_"v[44:47],a[244:247],a[164:167],v[44:47]\n"
B_"v249,s[48:51],s15 offen offset:0  lds\n"
M_"v[44:47],a[248:251],a[168:171],v[44:47]\n"
E_"v109,v109\n"
M_"v[44:47],a[252:255],a[172:175],v[44:47]\n"
W_"lgkmcnt(4)\n"
D_"a[240:243],v245 offset:55296\n"
D_"a[244:247],v245 offset:63488\n"
D_"a[248:251],v245 offset:56320\n"
M_"a[8:11],a[192:195],v[144:147],a[8:11]\n"
B_"v250,s[48:51],s15 offen offset:1024  lds\n"
M_"a[8:11],a[196:199],v[148:151],a[8:11]\n"
D_"a[252:255],v245 offset:64512\n"
E_"v110,v110\n"
M_"a[12:15],a[192:195],v[152:155],a[12:15]\n"
K_"v224,v248,v204,s[64:65]\n"
K_"v225,v248,v204,s[66:67]\n"
K_"v226,v248,v204,s[68:69]\n"
M_"a[12:15],a[196:199],v[156:159],a[12:15]\n"
B_"v251,s[48:51],s15 offen offset:2048  lds\n"
D_"a[192:195],v245 offset:32768\n"
M_"a[24:27],a[200:203],v[144:147],a[24:27]\n"
D_"a[196:199],v245 offset:33792\n"
K_"v227,v248,v204,s[70:71]\n"
K_"v228,v248,v204,s[72:73]\n"
M_"a[24:27],a[204:207],v[148:151],a[24:27]\n"
K_"v229,v248,v204,s[74:75]\n"
K_"v230,v248,v204,s[76:77]\n"
K_"v231,v248,v204,s[78:79]\n"
M_"a[28:31],a[200:203],v[152:155],a[28:31]\n"
B_"v253,s[48:51],s15 offen offset:3072  lds\n"
K_"v232,v248,v204,s[80:81]\n"
M_"a[28:31],a[204:207],v[156:159],a[28:31]\n"
D_"a[200:203],v245 offset:34816\n"
D_"a[204:207],v245 offset:35840\n"
K_"v233,v248,v204,s[82:83]\n"
M_"a[40:43],a[208:211],v[144:147],a[40:43]\n"
K_"v234,v248,v204,s[84:85]\n"
K_"v235,v248,v204,s[86:87]\n"
K_"v236,v248,v204,s[88:89]\n"
M_"a[40:43],a[212:215],v[148:151],a[40:43]\n"
B_"v[196:199],v255,s[52:55],s20 offen sc1\n"
K_"v237,v248,v204,s[90:91]\n"
M_"a[44:47],a[208:211],v[152:155],a[44:47]\n"
K_"v238,v248,v204,s[92:93]\n"
K_"v239,v248,v204,s[94:95]\n"
C_"v182,v108,v109\n"
M_"a[44:47],a[212:215],v[156:159],a[44:47]\n"
D_"a[208:211],v245 offset:36864\n"
E_"v111,v111\n"
M_"a[56:59],a[216:219],v[144:147],a[56:59]\n"
"s_xor_b32 s15,s14,131200\n"
D_"a[212:215],v245 offset:37888\n"
E_"v112,v112\n"
M_"a[56:59],a[220:223],v[148:151],a[56:59]\n"
"s_xor_b32 m0,s16,32768\n"
C_"v183,v110,v111\n"
E_"v113,v113\n"
M_"a[60:63],a[216:219],v[152:155],a[60:63]\n"
B_"v249,s[48:51],s15 offen offset:0  lds\n"
C_"v184,v112,v113\n"
M_"a[60:63],a[220:223],v[156:159],a[60:63]\n"
W_"lgkmcnt(6)\n"
D_"a[216:219],v245 offset:38912\n"
E_"v114,v114\n"
M_"a[72:75],a[224:227],v[144:147],a[72:75]\n"
B_"v250,s[48:51],s15 offen offset:1024  lds\n"
D_"a[220:223],v245 offset:39936\n"
M_"a[72:75],a[228:231],v[148:151],a[72:75]\n"
E_"v115,v115\n"
M_"a[76:79],a[224:227],v[152:155],a[76:79]\n"
C_"v185,v114,v115\n"
E_"v116,v116\n"
M_"a[76:79],a[228:231],v[156:159],a[76:79]\n"
B_"v251,s[48:51],s15 offen offset:2048  lds\n"
D_"a[224:227],v245 offset:40960\n"
M_"a[88:91],a[232:235],v[144:147],a[88:91]\n"
D_"a[228:231],v245 offset:41984\n"
E_"v117,v117\n"
M_"a[88:91],a[236:239],v[148:151],a[88:91]\n"
C_"v186,v116,v117\n"
E_"v118,v118\n"
M_"a[92:95],a[232:235],v[152:155],a[92:95]\n"
B_"v253,s[48:51],s15 offen offset:3072  lds\n"
M_"a[92:95],a[236:239],v[156:159],a[92:95]\n"
D_"a[232:235],v245 offset:43008\n"
E_"v119,v119\n"
M_"a[104:107],a[240:243],v[144:147],a[104:107]\n"
D_"a[236:239],v245 offset:44032\n"
E_"v120,v120\n"
M_"a[104:107],a[244:247],v[148:151],a[104:107]\n"
C_"v187,v118,v119\n"
E_"v121,v121\n"
M_"a[108:111],a[240:243],v[152:155],a[108:111]\n"
C_"v188,v120,v121\n"
E_"v122,v122\n"
M_"a[108:111],a[244:247],v[156:159],a[108:111]\n"
D_"a[240:243],v245 offset:45056\n"
E_"v123,v123\n"
M_"a[120:123],a[248:251],v[144:147],a[120:123]\n"
D_"a[244:247],v245 offset:46080\n"
E_"v124,v124\n"
M_"a[120:123],a[252:255],v[148:151],a[120:123]\n"
C_"v189,v122,v123\n"
E_"v125,v125\n"
M_"a[124:127],a[248:251],v[152:155],a[124:127]\n"
C_"v190,v124,v125\n"
E_"v126,v126\n"
M_"a[124:127],a[252:255],v[156:159],a[124:127]\n"
D_"a[248:251],v245 offset:47104\n"
D_"a[252:255],v245 offset:48128\n"
"v_smfmac_f32_16x16x64_bf16 v[216:219],v[240:243],v[144:151],v246\n"
E_"v127,v127\n"
"v_smfmac_f32_16x16x64_bf16 v[220:223],v[240:243],v[152:159],v246\n"
C_"v191,v126,v127\n"
W_"vmcnt(17) lgkmcnt(8)\n"
M_"v[112:115],a[192:195],a[176:179],v[224:227]\n"
"s_barrier\n"
E_"v32,v32\n"
M_"v[112:115],a[196:199],a[180:183],v[112:115]\n"
"s_xor_b32 s57,s57,0x10000\n"
"v_xor_b32 v245,0x10000,v245\n"
E_"v33,v33\n"
M_"v[112:115],a[200:203],a[184:187],v[112:115]\n"
"s_xor_b32 s15,s14,163840\n"
C_"v144,v32,v33\n"
E_"v34,v34\n"
M_"v[112:115],a[204:207],a[188:191],v[112:115]\n"
"s_xor_b32 m0,s16,65536\n"
D_"a[192:195],v245 offset:16384\n"
D_"a[196:199],v245 offset:24576\n"
D_"a[200:203],v245 offset:17408\n"
M_"v[116:119],a[208:211],a[176:179],v[228:231]\n"
B_"v249,s[48:51],s15 offen offset:0  lds\n"
D_"a[204:207],v245 offset:25600\n"
M_"v[116:119],a[212:215],a[180:183],v[116:119]\n"
B_"v250,s[48:51],s15 offen offset:1024  lds\n"
E_"v35,v35\n"
M_"v[116:119],a[216:219],a[184:187],v[116:119]\n"
C_"v145,v34,v35\n"
E_"v36,v36\n"
M_"v[116:119],a[220:223],a[188:191],v[116:119]\n"
W_"lgkmcnt(4)\n"
D_"a[208:211],v245 offset:18432\n"
D_"a[212:215],v245 offset:26624\n"
D_"a[216:219],v245 offset:19456\n"
M_"v[120:123],a[224:227],a[176:179],v[232:235]\n"
B_"v251,s[48:51],s15 offen offset:2048  lds\n"
D_"a[220:223],v245 offset:27648\n"
M_"v[120:123],a[228:231],a[180:183],v[120:123]\n"
B_"v253,s[48:51],s15 offen offset:3072  lds\n"
M_"v[120:123],a[232:235],a[184:187],v[120:123]\n"
E_"v37,v37\n"
M_"v[120:123],a[236:239],a[188:191],v[120:123]\n"
"s_mov_b64 s[40:41],s[52:53]\n"
D_"a[224:227],v245 offset:20480\n"
D_"a[228:231],v245 offset:28672\n"
D_"a[232:235],v245 offset:21504\n"
M_"v[124:127],a[240:243],a[176:179],v[236:239]\n"
W_"vmcnt(8)\n"
D_"a[236:239],v245 offset:29696\n"
"v_cmp_ne_u32 vcc,s32,v196\n"
C_"v146,v36,v37\n"
M_"v[124:127],a[244:247],a[180:183],v[124:127]\n"
"v_cmp_eq_u32 s[10:11],s32,v197\n"
E_"v38,v38\n"
M_"v[124:127],a[248:251],a[184:187],v[124:127]\n"
"s_or_b64 s[10:11],s[10:11],vcc\n"
"v_cmp_ne_u32 vcc,s32,v198\n"
E_"v39,v39\n"
M_"v[124:127],a[252:255],a[188:191],v[124:127]\n"
W_"lgkmcnt(4)\n"
D_"a[240:243],v245 offset:22528\n"
D_"a[244:247],v245 offset:30720\n"
D_"a[248:251],v245 offset:23552\n"
D_"a[252:255],v245 offset:31744\n"
M_"a[8:11],a[192:195],v[176:179],a[8:11]\n"
"s_and_b64 vcc,vcc,s[10:11]\n"
"s_cselect_b32 s24,1,0\n"
"s_cselect_b32 s42,0,0x6000000\n"
C_"v147,v38,v39\n"
M_"a[8:11],a[196:199],v[180:183],a[8:11]\n"
B_"v[0:3],v244,s[40:43],s21 offen offset:0 sc1\n"
M_"a[12:15],a[192:195],v[184:187],a[12:15]\n"
B_"v[4:7],v244,s[40:43],s22 offen offset:0 sc1\n"
M_"a[12:15],a[196:199],v[188:191],a[12:15]\n"
B_"v[8:11],v244,s[40:43],s23 offen offset:0 sc1\n"
M_"a[24:27],a[200:203],v[176:179],a[24:27]\n"
E_"v40,v40\n"
M_"a[24:27],a[204:207],v[180:183],a[24:27]\n"
B_"v[12:15],v244,s[40:43],s21 offen offset:1024 sc1\n"
M_"a[28:31],a[200:203],v[184:187],a[28:31]\n"
E_"v41,v41\n"
M_"a[28:31],a[204:207],v[188:191],a[28:31]\n"
C_"v148,v40,v41\n"
E_"v42,v42\n"
M_"a[40:43],a[208:211],v[176:179],a[40:43]\n"
B_"v[16:19],v244,s[40:43],s22 offen offset:1024 sc1\n"
M_"a[40:43],a[212:215],v[180:183],a[40:43]\n"
E_"v43,v43\n"
E_"v44,v44\n"
M_"a[44:47],a[208:211],v[184:187],a[44:47]\n"
B_"v[20:23],v244,s[40:43],s23 offen offset:1024 sc1\n"
C_"v149,v42,v43\n"
M_"a[44:47],a[212:215],v[188:191],a[44:47]\n"
E_"v45,v45\n"
M_"a[56:59],a[216:219],v[176:179],a[56:59]\n"
B_"v[24:27],v244,s[40:43],s21 offen offset:2048 sc1\n"
C_"v150,v44,v45\n"
M_"a[56:59],a[220:223],v[180:183],a[56:59]\n"
E_"v46,v46\n"
M_"a[60:63],a[216:219],v[184:187],a[60:63]\n"
E_"v47,v47\n"
M_"a[60:63],a[220:223],v[188:191],a[60:63]\n"
W_"lgkmcnt(0)\n"
C_"v151,v46,v47\n"
E_"v48,v48\n"
M_"a[72:75],a[224:227],v[176:179],a[72:75]\n"
B_"v[28:31],v244,s[40:43],s22 offen offset:2048 sc1\n"
M_"a[72:75],a[228:231],v[180:183],a[72:75]\n"
B_"v[64:67],v244,s[40:43],s23 offen offset:2048 sc1\n"
M_"a[76:79],a[224:227],v[184:187],a[76:79]\n"
E_"v49,v49\n"
M_"a[76:79],a[228:231],v[188:191],a[76:79]\n"
B_"v[68:71],v244,s[40:43],s21 offen offset:3072 sc1\n"
E_"v50,v50\n"
M_"a[88:91],a[232:235],v[176:179],a[88:91]\n"
C_"v152,v48,v49\n"
E_"v51,v51\n"
M_"a[88:91],a[236:239],v[180:183],a[88:91]\n"
C_"v153,v50,v51\n"
E_"v52,v52\n"
M_"a[92:95],a[232:235],v[184:187],a[92:95]\n"
B_"v[72:75],v244,s[40:43],s22 offen offset:3072 sc1\n"
M_"a[92:95],a[236:239],v[188:191],a[92:95]\n"
E_"v53,v53\n"
M_"a[104:107],a[240:243],v[176:179],a[104:107]\n"
B_"v[76:79],v244,s[40:43],s23 offen offset:3072 sc1\n"
C_"v154,v52,v53\n"
M_"a[104:107],a[244:247],v[180:183],a[104:107]\n"
E_"v54,v54\n"
M_"a[108:111],a[240:243],v[184:187],a[108:111]\n"
E_"v55,v55\n"
M_"a[108:111],a[244:247],v[188:191],a[108:111]\n"
C_"v155,v54,v55\n"
E_"v56,v56\n"
M_"a[120:123],a[248:251],v[176:179],a[120:123]\n"
E_"v57,v57\n"
M_"a[120:123],a[252:255],v[180:183],a[120:123]\n"
E_"v58,v58\n"
E_"v59,v59\n"
M_"a[124:127],a[248:251],v[184:187],a[124:127]\n"
C_"v156,v56,v57\n"
E_"v60,v60\n"
M_"a[124:127],a[252:255],v[188:191],a[124:127]\n"
C_"v157,v58,v59\n"
E_"v61,v61\n"
"v_smfmac_f32_16x16x64_bf16 v[216:219],v[240:243],v[176:183],v246\n"
C_"v158,v60,v61\n"
E_"v62,v62\n"
"v_smfmac_f32_16x16x64_bf16 v[220:223],v[240:243],v[184:191],v246\n"
E_"v63,v63\n"
"s_nop 0\n"
C_"v159,v62,v63\n"
"v_smfmac_f32_16x16x64_bf16 v[216:219],v[240:243],v[144:151],v246\n"
"s_barrier\n"
D_"a[192:195],v245 offset:49152\n"
D_"a[196:199],v245 offset:57344\n"
D_"a[200:203],v245 offset:50176\n"
"v_smfmac_f32_16x16x64_bf16 v[220:223],v[240:243],v[152:159],v246\n"
W_"lgkmcnt(1)\n"
D_"a[204:207],v245 offset:58368\n"
D_"a[208:211],v245 offset:51200\n"
M_"a[8:11],a[192:195],v[144:147],a[8:11]\n"
"s_xor_b32 s21,s21,4096\n"
D_"a[212:215],v245 offset:59392\n"
D_"a[216:219],v245 offset:52224\n"
D_"a[220:223],v245 offset:60416\n"
M_"a[8:11],a[196:199],v[148:151],a[8:11]\n"
"s_xor_b32 s22,s22,4096\n"
D_"a[224:227],v245 offset:53248\n"
D_"a[228:231],v245 offset:61440\n"
D_"a[232:235],v245 offset:54272\n"
M_"a[12:15],a[192:195],v[152:155],a[12:15]\n"
"s_xor_b32 s23,s23,4096\n"
D_"a[236:239],v245 offset:62464\n"
D_"a[240:243],v245 offset:55296\n"
D_"a[244:247],v245 offset:63488\n"
M_"a[12:15],a[196:199],v[156:159],a[12:15]\n"
W_"lgkmcnt(10)\n"
D_"a[248:251],v245 offset:56320\n"
D_"a[252:255],v245 offset:64512\n"
M_"a[24:27],a[200:203],v[144:147],a[24:27]\n"
B_"v[80:83],v244,s[40:43],s21 offen offset:0 sc1\n"
"v_bfe_u32 v212,v244,6,2\n"
M_"a[24:27],a[204:207],v[148:151],a[24:27]\n"
B_"v[84:87],v244,s[40:43],s22 offen offset:0 sc1\n"
"v_lshrrev_b32 v213,8,v244\n"
M_"a[28:31],a[200:203],v[152:155],a[28:31]\n"
B_"v[88:91],v244,s[40:43],s23 offen offset:0 sc1\n"
"v_cmp_eq_u32 s[10:11],v212,v213\n"
M_"a[28:31],a[204:207],v[156:159],a[28:31]\n"
W_"lgkmcnt(10)\n"
"v_mov_b32 v208,0x3f80\n"
"v_mov_b32 v209,0\n"
M_"a[40:43],a[208:211],v[144:147],a[40:43]\n"
B_"v[92:95],v244,s[40:43],s21 offen offset:1024 sc1\n"
"v_and_b32 v212,48,v244\n"
M_"a[40:43],a[212:215],v[148:151],a[40:43]\n"
K_"v208,0,v208,s[10:11]\n"
"v_lshlrev_b64 v[210:211],v212,v[208:209]\n"
"v_mov_b32 v208,0\n"
M_"a[44:47],a[208:211],v[152:155],a[44:47]\n"
"v_mov_b32 v212,0\n"
E_"v112,v112\n"
M_"a[44:47],a[212:215],v[156:159],a[44:47]\n"
W_"lgkmcnt(8)\n"
E_"v113,v113\n"
M_"a[56:59],a[216:219],v[144:147],a[56:59]\n"
"s_xor_b32 s15,s14,163968\n"
C_"v184,v112,v113\n"
E_"v114,v114\n"
M_"a[56:59],a[220:223],v[148:151],a[56:59]\n"
"s_xor_b32 m0,s16,98304\n"
"v_mov_b32 v213,0\n"
E_"v115,v115\n"
M_"a[60:63],a[216:219],v[152:155],a[60:63]\n"
B_"v249,s[48:51],s15 offen offset:0  lds\n"
M_"a[60:63],a[220:223],v[156:159],a[60:63]\n"
W_"lgkmcnt(6)\n"
C_"v185,v114,v115\n"
E_"v116,v116\n"
M_"a[72:75],a[224:227],v[144:147],a[72:75]\n"
B_"v250,s[48:51],s15 offen offset:1024  lds\n"
M_"a[72:75],a[228:231],v[148:151],a[72:75]\n"
B_"v251,s[48:51],s15 offen offset:2048  lds\n"
M_"a[76:79],a[224:227],v[152:155],a[76:79]\n"
E_"v117,v117\n"
M_"a[76:79],a[228:231],v[156:159],a[76:79]\n"
W_"lgkmcnt(4)\n"
C_"v186,v116,v117\n"
E_"v118,v118\n"
M_"a[88:91],a[232:235],v[144:147],a[88:91]\n"
B_"v253,s[48:51],s15 offen offset:3072  lds\n"
M_"a[88:91],a[236:239],v[148:151],a[88:91]\n"
E_"v119,v119\n"
M_"a[92:95],a[232:235],v[152:155],a[92:95]\n"
B_"v[236:239],v255,s[52:55],s20 offen sc1\n"
M_"a[92:95],a[236:239],v[156:159],a[92:95]\n"
W_"lgkmcnt(2)\n"
C_"v187,v118,v119\n"
E_"v120,v120\n"
M_"a[104:107],a[240:243],v[144:147],a[104:107]\n"
E_"v121,v121\n"
M_"a[104:107],a[244:247],v[148:151],a[104:107]\n"
C_"v188,v120,v121\n"
E_"v122,v122\n"
M_"a[108:111],a[240:243],v[152:155],a[108:111]\n"
E_"v123,v123\n"
M_"a[108:111],a[244:247],v[156:159],a[108:111]\n"
W_"lgkmcnt(0)\n"
C_"v189,v122,v123\n"
E_"v124,v124\n"
M_"a[120:123],a[248:251],v[144:147],a[120:123]\n"
E_"v125,v125\n"
M_"a[120:123],a[252:255],v[148:151],a[120:123]\n"
C_"v190,v124,v125\n"
E_"v126,v126\n"
M_"a[124:127],a[248:251],v[152:155],a[124:127]\n"
E_"v127,v127\n"
M_"a[124:127],a[252:255],v[156:159],a[124:127]\n"
C_"v191,v126,v127\n"
"L_k2_u4:\n"
"s_nop 1\n"
"v_smfmac_f32_16x16x64_bf16 v[220:223],v[240:243],v[184:191],v246\n"
"s_barrier\n"
"v_xor_b32 v245,0x10000,v245\n"
D_"a[192:195],v245 offset:16384\n"
D_"a[196:199],v245 offset:24576\n"
D_"a[200:203],v245 offset:17408\n"
D_"a[204:207],v245 offset:25600\n"
D_"a[208:211],v245 offset:18432\n"
W_"lgkmcnt(3)\n"
M_"a[12:15],a[192:195],v[184:187],a[12:15]\n"
"s_xor_b32 s57,s57,0x10000\n"
D_"a[212:215],v245 offset:26624\n"
D_"a[216:219],v245 offset:19456\n"
D_"a[220:223],v245 offset:27648\n"
D_"a[224:227],v245 offset:20480\n"
D_"a[228:231],v245 offset:28672\n"
D_"a[232:235],v245 offset:21504\n"
D_"a[236:239],v245 offset:29696\n"
M_"a[12:15],a[196:199],v[188:191],a[12:15]\n"
W_"lgkmcnt(8)\n"
D_"a[240:243],v245 offset:22528\n"
D_"a[244:247],v245 offset:30720\n"
D_"a[248:251],v245 offset:23552\n"
D_"a[252:255],v245 offset:31744\n"
"v_add_f32 v216,v216,v219\n"
"v_rcp_f32 v247,v216\n"
M_"a[28:31],a[200:203],v[184:187],a[28:31]\n"
"v_cmp_nge_f32 vcc,s60,v216\n"
"s_or_b64 s[62:63],s[62:63],vcc\n"
R_"v32,a8\n"
R_"v33,a9\n"
R_"v34,a10\n"
R_"v35,a11\n"
R_"v36,a24\n"
M_"a[28:31],a[204:207],v[188:191],a[28:31]\n"
W_"lgkmcnt(10)\n"
R_"v37,a25\n"
R_"v38,a26\n"
R_"v39,a27\n"
U_"v32,v247,v32\n"
U_"v33,v247,v33\n"
U_"v34,v247,v34\n"
M_"a[44:47],a[208:211],v[184:187],a[44:47]\n"
U_"v35,v247,v35\n"
U_"v36,v247,v36\n"
U_"v37,v247,v37\n"
U_"v38,v247,v38\n"
U_"v39,v247,v39\n"
C_"v32,v32,v33\n"
C_"v33,v34,v35\n"
M_"a[44:47],a[212:215],v[188:191],a[44:47]\n"
W_"lgkmcnt(8)\n"
C_"v34,v36,v37\n"
C_"v35,v38,v39\n"
S_"v[32:35],v244,s[52:55],s98 offen offset:0 sc1\n"
R_"v40,a40\n"
R_"v41,a41\n"
M_"a[60:63],a[216:219],v[184:187],a[60:63]\n"
R_"v42,a42\n"
R_"v43,a43\n"
R_"v44,a56\n"
R_"v45,a57\n"
R_"v46,a58\n"
R_"v47,a59\n"
U_"v40,v247,v40\n"
M_"a[60:63],a[220:223],v[188:191],a[60:63]\n"
W_"lgkmcnt(6)\n"
U_"v41,v247,v41\n"
U_"v42,v247,v42\n"
U_"v43,v247,v43\n"
U_"v44,v247,v44\n"
U_"v45,v247,v45\n"
U_"v46,v247,v46\n"
U_"v47,v247,v47\n"
M_"a[76:79],a[224:227],v[184:187],a[76:79]\n"
C_"v40,v40,v41\n"
C_"v41,v42,v43\n"
C_"v42,v44,v45\n"
C_"v43,v46,v47\n"
S_"v[40:43],v244,s[52:55],s98 offen offset:1024 sc1\n"
M_"a[76:79],a[228:231],v[188:191],a[76:79]\n"
W_"lgkmcnt(4)\n"
R_"v32,a72\n"
R_"v33,a73\n"
R_"v34,a74\n"
R_"v35,a75\n"
R_"v36,a88\n"
R_"v37,a89\n"
R_"v38,a90\n"
M_"a[92:95],a[232:235],v[184:187],a[92:95]\n"
R_"v39,a91\n"
U_"v32,v247,v32\n"
U_"v33,v247,v33\n"
U_"v34,v247,v34\n"
U_"v35,v247,v35\n"
U_"v36,v247,v36\n"
U_"v37,v247,v37\n"
M_"a[92:95],a[236:239],v[188:191],a[92:95]\n"
W_"lgkmcnt(2)\n"
U_"v38,v247,v38\n"
U_"v39,v247,v39\n"
C_"v32,v32,v33\n"
C_"v33,v34,v35\n"
C_"v34,v36,v37\n"
C_"v35,v38,v39\n"
M_"a[108:111],a[240:243],v[184:187],a[108:111]\n"
S_"v[32:35],v244,s[52:55],s98 offen offset:2048 sc1\n"
R_"v40,a104\n"
R_"v41,a105\n"
R_"v42,a106\n"
R_"v43,a107\n"
R_"v44,a120\n"
M_"a[108:111],a[244:247],v[188:191],a[108:111]\n"
W_"lgkmcnt(0)\n"
R_"v45,a121\n"
R_"v46,a122\n"
R_"v47,a123\n"
U_"v40,v247,v40\n"
U_"v41,v247,v41\n"
U_"v42,v247,v42\n"
U_"v43,v247,v43\n"
M_"a[124:127],a[248:251],v[184:187],a[124:127]\n"
U_"v44,v247,v44\n"
U_"v45,v247,v45\n"
U_"v46,v247,v46\n"
U_"v47,v247,v47\n"
C_"v40,v40,v41\n"
C_"v41,v42,v43\n"
C_"v42,v44,v45\n"
M_"a[124:127],a[252:255],v[188:191],a[124:127]\n"
C_"v43,v46,v47\n"
S_"v[40:43],v244,s[52:55],s98 offen offset:3072 sc1\n"
W_"vmcnt(63)\n"
"s_cmp_eq_u32 s24,0\n"
"s_cbranch_scc1 L_p3_2nd_back\n"
W_"vmcnt(4)\n"
"s_branch L_p3_2nd\n"
"L_p3_2nd_back:\n"
"v_add_f32 v220,v220,v223\n"
"v_rcp_f32 v248,v220\n"
"v_cmp_nge_f32 vcc,s60,v220\n"
"s_or_b64 s[62:63],s[62:63],vcc\n"
M_"v[200:203],v[210:213],v[128:131],0\n"
R_"v48,a12\n"
R_"v49,a13\n"
R_"v50,a14\n"
R_"v51,a15\n"
R_"v52,a28\n"
M_"v[200:203],v[210:213],v[132:135],v[200:203]\n"
R_"v53,a29\n"
R_"v54,a30\n"
R_"v55,a31\n"
U_"v48,v248,v48\n"
U_"v49,v248,v49\n"
M_"v[200:203],v[210:213],v[136:139],v[200:203]\n"
U_"v50,v248,v50\n"
U_"v51,v248,v51\n"
U_"v52,v248,v52\n"
U_"v53,v248,v53\n"
U_"v54,v248,v54\n"
M_"v[200:203],v[210:213],v[140:143],v[200:203]\n"
U_"v55,v248,v55\n"
C_"v48,v48,v49\n"
C_"v49,v50,v51\n"
C_"v50,v52,v53\n"
C_"v51,v54,v55\n"
M_"v[204:207],v[208:211],v[128:131],0\n"
S_"v[48:51],v244,s[52:55],s99 offen offset:0 sc1\n"
R_"v56,a44\n"
R_"v57,a45\n"
R_"v58,a46\n"
R_"v59,a47\n"
M_"v[204:207],v[208:211],v[132:135],v[204:207]\n"
R_"v60,a60\n"
R_"v61,a61\n"
R_"v62,a62\n"
R_"v63,a63\n"
U_"v56,v248,v56\n"
M_"v[204:207],v[208:211],v[136:139],v[204:207]\n"
U_"v57,v248,v57\n"
U_"v58,v248,v58\n"
U_"v59,v248,v59\n"
U_"v60,v248,v60\n"
U_"v61,v248,v61\n"
M_"v[204:207],v[208:211],v[140:143],v[204:207]\n"
U_"v62,v248,v62\n"
U_"v63,v248,v63\n"
C_"v56,v56,v57\n"
C_"v57,v58,v59\n"
C_"v58,v60,v61\n"
M_"v[128:131],v[210:213],v[160:163],0\n"
C_"v59,v62,v63\n"
S_"v[56:59],v244,s[52:55],s99 offen offset:1024 sc1\n"
R_"v48,a76\n"
R_"v49,a77\n"
R_"v50,a78\n"
M_"v[128:131],v[210:213],v[164:167],v[128:131]\n"
R_"v51,a79\n"
R_"v52,a92\n"
R_"v53,a93\n"
R_"v54,a94\n"
R_"v55,a95\n"
M_"v[128:131],v[210:213],v[168:171],v[128:131]\n"
U_"v48,v248,v48\n"
U_"v49,v248,v49\n"
U_"v50,v248,v50\n"
U_"v51,v248,v51\n"
U_"v52,v248,v52\n"
M_"v[128:131],v[210:213],v[172:175],v[128:131]\n"
U_"v53,v248,v53\n"
U_"v54,v248,v54\n"
U_"v55,v248,v55\n"
C_"v48,v48,v49\n"
C_"v49,v50,v51\n"
M_"v[132:135],v[208:211],v[160:163],0\n"
C_"v50,v52,v53\n"
C_"v51,v54,v55\n"
S_"v[48:51],v244,s[52:55],s99 offen offset:2048 sc1\n"
R_"v56,a108\n"
R_"v57,a109\n"
M_"v[132:135],v[208:211],v[164:167],v[132:135]\n"
R_"v58,a110\n"
R_"v59,a111\n"
R_"v60,a124\n"
R_"v61,a125\n"
R_"v62,a126\n"
M_"v[132:135],v[208:211],v[168:171],v[132:135]\n"
R_"v63,a127\n"
U_"v56,v248,v56\n"
U_"v57,v248,v57\n"
U_"v58,v248,v58\n"
U_"v59,v248,v59\n"
M_"v[132:135],v[208:211],v[172:175],v[132:135]\n"
U_"v60,v248,v60\n"
U_"v61,v248,v61\n"
U_"v62,v248,v62\n"
U_"v63,v248,v63\n"
C_"v56,v56,v57\n"
C_"v57,v58,v59\n"
C_"v58,v60,v61\n"
C_"v59,v62,v63\n"
S_"v[56:59],v244,s[52:55],s99 offen offset:3072 sc1\n"
"s_or_b64 s[62:63],s[62:63],%8\n"
"s_cselect_b32 s35,2,0\n"
"s_or_b32 s25,s25,s35\n"
"s_cbranch_scc1 L_m_exit\n"
"s_cmp_lg_u32 s24,0\n"
"s_cbranch_scc1 L_p3_slow\n"
"L_p3_go:\n"
W_"vmcnt(26)\n"
M_"v[96:99],a[128:131],v[0:3],0\n"
A_"s76,s17,8\n"
M_"v[100:103],a[132:135],v[0:3],0\n"
M_"v[104:107],a[136:139],v[0:3],0\n"
"s_mov_b32 s75,0xbfb8aa3b\n"
T_"a[160:161],v192 offset:0\n"
"s_xor_b32 s15,s14,196608\n"
M_"v[108:111],a[140:143],v[0:3],0\n"
"s_xor_b32 m0,s16,114688\n"
T_"a[162:163],v192 offset:2048\n"
M_"v[144:147],a[128:131],v[4:7],0\n"
B_"v249,s[48:51],s15 offen offset:0  lds\n"
M_"v[148:151],a[132:135],v[4:7],0\n"
"s_xor_b32 s65,s21,0x21000\n"
T_"a[164:165],v193 offset:0\n"
U_"v136,s75,v200\n"
M_"v[152:155],a[136:139],v[4:7],0\n"
"s_lshl3_add_u32 s64,s19,s65\n"
T_"a[166:167],v193 offset:2048\n"
U_"v137,s75,v201\n"
M_"v[156:159],a[140:143],v[4:7],0\n"
U_"v138,s75,v202\n"
M_"v[32:35],a[128:131],v[8:11],0\n"
B_"v250,s[48:51],s15 offen offset:1024  lds\n"
M_"v[36:39],a[132:135],v[8:11],0\n"
T_"a[168:169],v194 offset:0\n"
T_"a[170:171],v194 offset:2048\n"
M_"v[40:43],a[136:139],v[8:11],0\n"
U_"v139,s75,v203\n"
U_"v140,s75,v204\n"
M_"v[44:47],a[140:143],v[8:11],0\n"
B_"v251,s[48:51],s15 offen offset:2048  lds\n"
M_"v[112:115],a[144:147],v[0:3],0\n"
B_"v253,s[48:51],s15 offen offset:3072  lds\n"
M_"v[116:119],a[148:151],v[0:3],0\n"
T_"a[172:173],v195 offset:0\n"
T_"a[174:175],v195 offset:2048\n"
M_"v[120:123],a[152:155],v[0:3],0\n"
U_"v141,s75,v205\n"
U_"v142,s75,v206\n"
M_"v[124:127],a[156:159],v[0:3],0\n"
W_"lgkmcnt(0)\n"
U_"v143,s75,v207\n"
M_"v[176:179],a[144:147],v[4:7],0\n"
E_"v136,v136\n"
M_"v[180:183],a[148:151],v[4:7],0\n"
T_"a[176:177],v192 offset:16384\n"
T_"a[178:179],v192 offset:18432\n"
M_"v[184:187],a[152:155],v[4:7],0\n"
"s_xor_b32 s15,s14,196736\n"
E_"v137,v137\n"
M_"v[188:191],a[156:159],v[4:7],0\n"
"s_xor_b32 m0,s16,16384\n"
T_"a[180:181],v193 offset:16384\n"
M_"v[48:51],a[144:147],v[8:11],0\n"
B_"v249,s[48:51],s15 offen offset:0  lds\n"
M_"v[52:55],a[148:151],v[8:11],0\n"
E_"v138,v138\n"
M_"v[56:59],a[152:155],v[8:11],0\n"
T_"a[182:183],v193 offset:18432\n"
T_"a[184:185],v194 offset:16384\n"
M_"v[60:63],a[156:159],v[8:11],0\n"
W_"vmcnt(28)\n"
T_"a[186:187],v194 offset:18432\n"
M_"v[96:99],a[0:3],v[12:15],v[96:99]\n"
B_"v250,s[48:51],s15 offen offset:1024  lds\n"
M_"v[100:103],a[16:19],v[12:15],v[100:103]\n"
E_"v139,v139\n"
M_"v[104:107],a[32:35],v[12:15],v[104:107]\n"
T_"a[188:189],v195 offset:16384\n"
T_"a[190:191],v195 offset:18432\n"
M_"v[108:111],a[48:51],v[12:15],v[108:111]\n"
B_"v251,s[48:51],s15 offen offset:2048  lds\n"
M_"v[144:147],a[0:3],v[16:19],v[144:147]\n"
B_"v253,s[48:51],s15 offen offset:3072  lds\n"
M_"v[148:151],a[16:19],v[16:19],v[148:151]\n"
E_"v140,v140\n"
M_"v[152:155],a[32:35],v[16:19],v[152:155]\n"
"s_and_b32 s73,s16,0x10000\n"
E_"v141,v141\n"
M_"v[156:159],a[48:51],v[16:19],v[156:159]\n"
"s_or_b32 s73,s73,0x20000\n"
"v_xor_b32 v224,s73,v192\n"
"v_xor_b32 v225,s73,v193\n"
M_"v[32:35],a[0:3],v[20:23],v[32:35]\n"
E_"v142,v142\n"
M_"v[36:39],a[16:19],v[20:23],v[36:39]\n"
"v_xor_b32 v226,s73,v194\n"
"v_xor_b32 v227,s73,v195\n"
M_"v[40:43],a[32:35],v[20:23],v[40:43]\n"
"v_xor_b32 v228,0x10000,v224\n"
"v_xor_b32 v229,0x10000,v225\n"
M_"v[44:47],a[48:51],v[20:23],v[44:47]\n"
"v_xor_b32 v230,0x10000,v226\n"
"v_xor_b32 v231,0x10000,v227\n"
M_"v[112:115],a[64:67],v[12:15],v[112:115]\n"
E_"v143,v143\n"
M_"v[116:119],a[80:83],v[12:15],v[116:119]\n"
"s_xor_b32 s15,s14,229376\n"
T_"a[192:193],v224 offset:0\n"
T_"a[194:195],v224 offset:2048\n"
M_"v[120:123],a[96:99],v[12:15],v[120:123]\n"
"s_xor_b32 m0,s16,49152\n"
T_"a[196:197],v225 offset:0\n"
T_"a[198:199],v225 offset:2048\n"
M_"v[124:127],a[112:115],v[12:15],v[124:127]\n"
B_"v249,s[48:51],s15 offen offset:0  lds\n"
M_"v[176:179],a[64:67],v[16:19],v[176:179]\n"
T_"a[200:201],v226 offset:0\n"
T_"a[202:203],v226 offset:2048\n"
M_"v[180:183],a[80:83],v[16:19],v[180:183]\n"
T_"a[204:205],v227 offset:0\n"
T_"a[206:207],v227 offset:2048\n"
M_"v[184:187],a[96:99],v[16:19],v[184:187]\n"
"v_add_f32 v136,1.0,v136\n"
"v_add_f32 v137,1.0,v137\n"
M_"v[188:191],a[112:115],v[16:19],v[188:191]\n"
B_"v250,s[48:51],s15 offen offset:1024  lds\n"
M_"v[48:51],a[64:67],v[20:23],v[48:51]\n"
"v_add_f32 v138,1.0,v138\n"
"v_add_f32 v139,1.0,v139\n"
M_"v[52:55],a[80:83],v[20:23],v[52:55]\n"
B_"v251,s[48:51],s15 offen offset:2048  lds\n"
M_"v[56:59],a[96:99],v[20:23],v[56:59]\n"
B_"v253,s[48:51],s15 offen offset:3072  lds\n"
M_"v[60:63],a[112:115],v[20:23],v[60:63]\n"
W_"vmcnt(32)\n"
T_"a[208:209],v224 offset:32768\n"
T_"a[210:211],v224 offset:34816\n"
M_"v[96:99],a[4:7],v[24:27],v[96:99]\n"
T_"a[212:213],v225 offset:32768\n"
"v_add_f32 v140,1.0,v140\n"
M_"v[100:103],a[20:23],v[24:27],v[100:103]\n"
T_"a[214:215],v225 offset:34816\n"
"v_add_f32 v141,1.0,v141\n"
M_"v[104:107],a[36:39],v[24:27],v[104:107]\n"
B_"v[160:163],v244,s[52:55],s22 offen offset:1024 sc1\n"
M_"v[108:111],a[52:55],v[24:27],v[108:111]\n"
T_"a[216:217],v226 offset:32768\n"
"v_add_f32 v142,1.0,v142\n"
M_"v[144:147],a[4:7],v[28:31],v[144:147]\n"
T_"a[218:219],v226 offset:34816\n"
"v_add_f32 v143,1.0,v143\n"
M_"v[148:151],a[20:23],v[28:31],v[148:151]\n"
"v_rcp_f32 v136,v136\n"
M_"v[152:155],a[36:39],v[28:31],v[152:155]\n"
B_"v[164:167],v244,s[52:55],s23 offen offset:1024 sc1\n"
M_"v[156:159],a[52:55],v[28:31],v[156:159]\n"
T_"a[220:221],v227 offset:32768\n"
T_"a[222:223],v227 offset:34816\n"
M_"v[32:35],a[4:7],v[64:67],v[32:35]\n"
B_"v[168:171],v244,s[52:55],s21 offen offset:2048 sc1\n"
M_"v[36:39],a[20:23],v[64:67],v[36:39]\n"
B_"v[172:175],v244,s[52:55],s22 offen offset:2048 sc1\n"
M_"v[40:43],a[36:39],v[64:67],v[40:43]\n"
W_"lgkmcnt(8)\n"
"v_and_b32 v215,16,v244\n"
"v_and_b32 v247,96,v244\n"
M_"v[44:47],a[52:55],v[64:67],v[44:47]\n"
T_"a[224:225],v228 offset:0\n"
"v_lshl_add_u32 v215,v247,2,v215\n"
M_"v[112:115],a[68:71],v[24:27],v[112:115]\n"
"s_and_b32 s74,s30,6\n"
"v_rcp_f32 v137,v137\n"
M_"v[116:119],a[84:87],v[24:27],v[116:119]\n"
"s_lshl_b32 s74,s74,4\n"
"v_rcp_f32 v138,v138\n"
M_"v[120:123],a[100:103],v[24:27],v[120:123]\n"
T_"a[226:227],v228 offset:2048\n"
T_"a[228:229],v229 offset:0\n"
M_"v[124:127],a[116:119],v[24:27],v[124:127]\n"
T_"a[230:231],v229 offset:2048\n"
"v_add_lshl_u32 v215,s74,v215,3\n"
M_"v[176:179],a[68:71],v[28:31],v[176:179]\n"
"v_rcp_f32 v139,v139\n"
M_"v[180:183],a[84:87],v[28:31],v[180:183]\n"
"s_xor_b32 s74,s31,1\n"
"v_rcp_f32 v140,v140\n"
M_"v[184:187],a[100:103],v[28:31],v[184:187]\n"
W_"vmcnt(16)\n"
"s_cmp_eq_u32 s24,1\n"
"s_cbranch_scc1 L_p3_noc2n\n"
"s_barrier\n"
"buffer_store_dword v254,off,s[52:55],s76 sc1\n"
"L_p3_noc2n:\n"
M_"v[188:191],a[116:119],v[28:31],v[188:191]\n"
"s_mul_i32 s74,s74,3072\n"
T_"a[232:233],v230 offset:0\n"
T_"a[234:235],v230 offset:2048\n"
M_"v[48:51],a[68:71],v[64:67],v[48:51]\n"
"s_xor_b32 s15,s14,229504\n"
T_"a[236:237],v231 offset:0\n"
T_"a[238:239],v231 offset:2048\n"
M_"v[52:55],a[84:87],v[64:67],v[52:55]\n"
"s_and_b32 s13,s16,0xffff\n"
"v_rcp_f32 v141,v141\n"
M_"v[56:59],a[100:103],v[64:67],v[56:59]\n"
A_"m0,s13,131072\n"
"v_rcp_f32 v142,v142\n"
M_"v[60:63],a[116:119],v[64:67],v[60:63]\n"
A_"s74,s74,0\n"
M_"v[96:99],a[160:163],v[68:71],v[96:99]\n"
B_"v249,s[48:51],s15 offen offset:0  lds\n"
M_"v[100:103],a[164:167],v[68:71],v[100:103]\n"
T_"a[240:241],v228 offset:32768\n"
"v_add_u32 v240,s74,v244\n"
M_"v[104:107],a[168:171],v[68:71],v[104:107]\n"
"s_xor_b32 s74,s31,2\n"
"v_rcp_f32 v143,v143\n"
M_"v[108:111],a[172:175],v[68:71],v[108:111]\n"
"s_mul_i32 s74,s74,3072\n"
T_"a[242:243],v228 offset:34816\n"
U_"v136,v200,v136\n"
M_"v[144:147],a[160:163],v[72:75],v[144:147]\n"
B_"v250,s[48:51],s15 offen offset:1024  lds\n"
M_"v[148:151],a[164:167],v[72:75],v[148:151]\n"
A_"s74,s74,1024\n"
T_"a[244:245],v229 offset:32768\n"
T_"a[246:247],v229 offset:34816\n"
M_"v[152:155],a[168:171],v[72:75],v[152:155]\n"
B_"v251,s[48:51],s15 offen offset:2048  lds\n"
M_"v[156:159],a[172:175],v[72:75],v[156:159]\n"
B_"v253,s[48:51],s15 offen offset:3072  lds\n"
M_"v[32:35],a[160:163],v[76:79],v[32:35]\n"
T_"a[248:249],v230 offset:32768\n"
T_"a[250:251],v230 offset:34816\n"
M_"v[36:39],a[164:167],v[76:79],v[36:39]\n"
T_"a[252:253],v231 offset:32768\n"
"v_add_u32 v241,s74,v244\n"
M_"v[40:43],a[168:171],v[76:79],v[40:43]\n"
"s_xor_b32 s74,s31,3\n"
T_"a[254:255],v231 offset:34816\n"
U_"v137,v201,v137\n"
M_"v[44:47],a[172:175],v[76:79],v[44:47]\n"
U_"v138,v202,v138\n"
U_"v139,v203,v139\n"
M_"v[112:115],a[176:179],v[68:71],v[112:115]\n"
B_"v[0:3],v244,s[52:55],s23 offen offset:2048 sc1\n"
M_"v[116:119],a[180:183],v[68:71],v[116:119]\n"
W_"lgkmcnt(8)\n"
U_"v140,v204,v140\n"
U_"v141,v205,v141\n"
M_"v[120:123],a[184:187],v[68:71],v[120:123]\n"
"s_mul_i32 s74,s74,3072\n"
U_"v142,v206,v142\n"
U_"v143,v207,v143\n"
M_"v[124:127],a[188:191],v[68:71],v[124:127]\n"
W_"vmcnt(17)\n"
T_"a[8:9],v228 offset:49152\n"
U_"v216,s75,v128\n"
M_"v[176:179],a[176:179],v[72:75],v[176:179]\n"
B_"v[4:7],v244,s[52:55],s21 offen offset:3072 sc1\n"
M_"v[180:183],a[180:183],v[72:75],v[180:183]\n"
A_"s74,s74,2048\n"
T_"a[10:11],v228 offset:51200\n"
"v_add_u32 v242,s74,v244\n"
M_"v[184:187],a[184:187],v[72:75],v[184:187]\n"
"s_mul_i32 s74,s31,3072\n"
T_"a[24:25],v229 offset:49152\n"
"v_add_u32 v243,s74,v244\n"
M_"v[188:191],a[188:191],v[72:75],v[188:191]\n"
"s_lshl3_add_u32 s66,s30,s31\n"
T_"a[26:27],v229 offset:51200\n"
U_"v217,s75,v129\n"
M_"v[48:51],a[176:179],v[76:79],v[48:51]\n"
B_"v[8:11],v244,s[52:55],s22 offen offset:3072 sc1\n"
M_"v[52:55],a[180:183],v[76:79],v[52:55]\n"
B_"v[12:15],v244,s[52:55],s23 offen offset:3072 sc1\n"
M_"v[56:59],a[184:187],v[76:79],v[56:59]\n"
"s_lshl_b32 s66,s66,13\n"
T_"a[40:41],v230 offset:49152\n"
T_"a[42:43],v230 offset:51200\n"
M_"v[60:63],a[188:191],v[76:79],v[60:63]\n"
T_"a[56:57],v231 offset:49152\n"
M_"v[96:99],a[192:195],v[80:83],v[96:99]\n"
A_"s66,s66,0x5000000\n"
U_"v218,s75,v130\n"
T_"a[58:59],v231 offset:51200\n"
M_"v[100:103],a[196:199],v[80:83],v[100:103]\n"
A_"s72,s66,4096\n"
U_"v219,s75,v131\n"
U_"v220,s75,v132\n"
M_"v[104:107],a[200:203],v[80:83],v[104:107]\n"
U_"v221,s75,v133\n"
U_"v222,s75,v134\n"
M_"v[108:111],a[204:207],v[80:83],v[108:111]\n"
"s_bfe_u32 s74,s18,0x4000a\n"
U_"v223,s75,v135\n"
M_"v[144:147],a[192:195],v[84:87],v[144:147]\n"
W_"vmcnt(16)\n"
E_"v216,v216\n"
M_"v[148:151],a[196:199],v[84:87],v[148:151]\n"
"s_xor_b32 s71,s65,0x0\n"
E_"v217,v217\n"
M_"v[152:155],a[200:203],v[84:87],v[152:155]\n"
B_"v[16:19],v244,s[52:55],s71 offen offset:0 sc1\n"
M_"v[156:159],a[204:207],v[84:87],v[156:159]\n"
"s_lshl_b32 s74,s74,6\n"
T_"a[72:73],v224 offset:16384\n"
T_"a[74:75],v224 offset:18432\n"
M_"v[32:35],a[192:195],v[88:91],v[32:35]\n"
"s_and_b32 s69,s18,0x1fc000\n"
E_"v218,v218\n"
M_"v[36:39],a[196:199],v[88:91],v[36:39]\n"
A_"s69,s69,s74\n"
T_"a[88:89],v225 offset:16384\n"
T_"a[90:91],v225 offset:18432\n"
M_"v[40:43],a[200:203],v[88:91],v[40:43]\n"
B_"v[20:23],v244,s[52:55],s71 offen offset:1024 sc1\n"
M_"v[44:47],a[204:207],v[88:91],v[44:47]\n"
E_"v219,v219\n"
M_"v[112:115],a[208:211],v[80:83],v[112:115]\n"
A_"s70,s69,s19\n"
T_"a[104:105],v226 offset:16384\n"
T_"a[106:107],v226 offset:18432\n"
M_"v[116:119],a[212:215],v[80:83],v[116:119]\n"
B_"v[24:27],v244,s[52:55],s71 offen offset:2048 sc1\n"
M_"v[120:123],a[216:219],v[80:83],v[120:123]\n"
B_"v[28:31],v244,s[52:55],s71 offen offset:3072 sc1\n"
M_"v[124:127],a[220:223],v[80:83],v[124:127]\n"
T_"a[120:121],v227 offset:16384\n"
T_"a[122:123],v227 offset:18432\n"
M_"v[176:179],a[208:211],v[84:87],v[176:179]\n"
"v_bfe_u32 v245,v244,4,4\n"
"v_lshlrev_b32 v245,10,v245\n"
M_"v[180:183],a[212:215],v[84:87],v[180:183]\n"
W_"lgkmcnt(8)\n"
E_"v220,v220\n"
M_"v[184:187],a[216:219],v[84:87],v[184:187]\n"
W_"vmcnt(16)\n"
"v_lshrrev_b32 v247,8,v244\n"
"v_lshl_add_u32 v245,v247,3,v245\n"
M_"v[188:191],a[220:223],v[84:87],v[188:191]\n"
"s_xor_b32 s71,s65,0x1000\n"
E_"v221,v221\n"
M_"v[48:51],a[208:211],v[88:91],v[48:51]\n"
B_"v[64:67],v244,s[52:55],s71 offen offset:0 sc1\n"
M_"v[52:55],a[212:215],v[88:91],v[52:55]\n"
"s_mov_b64 s[44:45],s[8:9]\n"
T_"a[12:13],v224 offset:49152\n"
T_"a[14:15],v224 offset:51200\n"
M_"v[56:59],a[216:219],v[88:91],v[56:59]\n"
"s_mov_b32 s46,0x200000\n"
E_"v222,v222\n"
M_"v[60:63],a[220:223],v[88:91],v[60:63]\n"
T_"a[28:29],v225 offset:49152\n"
W_"vmcnt(15)\n"
M_"v[96:99],a[224:227],v[92:95],v[96:99]\n"
B_"v[68:71],v244,s[52:55],s71 offen offset:1024 sc1\n"
M_"v[100:103],a[228:231],v[92:95],v[100:103]\n"
"s_mov_b32 s47,0x20000\n"
T_"a[30:31],v225 offset:51200\n"
T_"a[44:45],v226 offset:49152\n"
M_"v[104:107],a[232:235],v[92:95],v[104:107]\n"
"s_mov_b64 s[56:57],s[26:27]\n"
E_"v223,v223\n"
M_"v[108:111],a[236:239],v[92:95],v[108:111]\n"
B_"v[72:75],v244,s[52:55],s71 offen offset:2048 sc1\n"
M_"v[144:147],a[224:227],v[160:163],v[144:147]\n"
B_"v[76:79],v244,s[52:55],s71 offen offset:3072 sc1\n"
M_"v[148:151],a[228:231],v[160:163],v[148:151]\n"
"s_mov_b32 s58,0x200000\n"
T_"a[46:47],v226 offset:51200\n"
T_"a[60:61],v227 offset:49152\n"
M_"v[152:155],a[232:235],v[160:163],v[152:155]\n"
"s_mov_b32 s59,0x20000\n"
T_"a[62:63],v227 offset:51200\n"
"v_add_f32 v216,1.0,v216\n"
M_"v[156:159],a[236:239],v[160:163],v[156:159]\n"
"v_add_f32 v217,1.0,v217\n"
"v_add_f32 v218,1.0,v218\n"
M_"v[32:35],a[224:227],v[164:167],v[32:35]\n"
"v_bfe_u32 v247,v244,8,1\n"
"v_add_f32 v219,1.0,v219\n"
M_"v[36:39],a[228:231],v[164:167],v[36:39]\n"
"v_mad_u32_u24 v245,v247,24,v245\n"
"v_add_f32 v220,1.0,v220\n"
M_"v[40:43],a[232:235],v[164:167],v[40:43]\n"
"v_add_f32 v221,1.0,v221\n"
"v_add_f32 v222,1.0,v222\n"
M_"v[44:47],a[236:239],v[164:167],v[44:47]\n"
"v_add_u32 v199,0x180,v244\n"
"v_add_f32 v223,1.0,v223\n"
M_"v[112:115],a[240:243],v[92:95],v[112:115]\n"
"buffer_load_dword v255,v215,s[52:55],s20 offen offset:8 sc1\n"
M_"v[116:119],a[244:247],v[92:95],v[116:119]\n"
"v_rcp_f32 v216,v216\n"
M_"v[120:123],a[248:251],v[92:95],v[120:123]\n"
"v_rcp_f32 v217,v217\n"
M_"v[124:127],a[252:255],v[92:95],v[124:127]\n"
"v_bfe_i32 v199,v199,8,1\n"
"v_and_b32 v199,0x3f80,v199\n"
M_"v[176:179],a[240:243],v[160:163],v[176:179]\n"
"v_rcp_f32 v218,v218\n"
M_"v[180:183],a[244:247],v[160:163],v[180:183]\n"
"v_and_b32 v197,16,v244\n"
M_"v[184:187],a[248:251],v[160:163],v[184:187]\n"
W_"vmcnt(13)\n"
"v_rcp_f32 v219,v219\n"
M_"v[188:191],a[252:255],v[160:163],v[188:191]\n"
T_"a[76:77],v192 offset:0\n"
"v_lshlrev_b32 v199,v197,v199\n"
M_"v[48:51],a[240:243],v[164:167],v[48:51]\n"
T_"a[78:79],v192 offset:2048\n"
M_"v[52:55],a[244:247],v[164:167],v[52:55]\n"
"v_rcp_f32 v220,v220\n"
M_"v[56:59],a[248:251],v[164:167],v[56:59]\n"
T_"a[92:93],v193 offset:0\n"
"v_bfe_i32 v198,v244,6,1\n"
M_"v[60:63],a[252:255],v[164:167],v[60:63]\n"
T_"a[94:95],v193 offset:2048\n"
W_"vmcnt(12)\n"
M_"v[96:99],a[8:11],v[168:171],v[96:99]\n"
T_"a[108:109],v194 offset:0\n"
"v_and_b32 v198,v198,v199\n"
M_"v[100:103],a[24:27],v[168:171],v[100:103]\n"
"v_rcp_f32 v221,v221\n"
M_"v[104:107],a[40:43],v[168:171],v[104:107]\n"
T_"a[110:111],v194 offset:2048\n"
"v_xor_b32 v196,v198,v199\n"
M_"v[108:111],a[56:59],v[168:171],v[108:111]\n"
T_"a[124:125],v195 offset:0\n"
"v_bfe_i32 v199,v244,9,1\n"
M_"v[144:147],a[8:11],v[172:175],v[144:147]\n"
T_"a[126:127],v195 offset:2048\n"
"v_and_b32 v208,v196,v199\n"
M_"v[148:151],a[24:27],v[172:175],v[148:151]\n"
"v_rcp_f32 v222,v222\n"
M_"v[152:155],a[40:43],v[172:175],v[152:155]\n"
"v_rcp_f32 v223,v223\n"
M_"v[156:159],a[56:59],v[172:175],v[156:159]\n"
"v_and_b32 v210,v198,v199\n"
M_"v[32:35],a[8:11],v[0:3],v[32:35]\n"
"v_mov_b32 v209,v196\n"
U_"v128,v128,v216\n"
M_"v[36:39],a[24:27],v[0:3],v[36:39]\n"
M_"v[40:43],a[40:43],v[0:3],v[40:43]\n"
"v_mov_b32 v211,v198\n"
U_"v129,v129,v217\n"
M_"v[44:47],a[56:59],v[0:3],v[44:47]\n"
W_"lgkmcnt(15)\n"
U_"v130,v130,v218\n"
M_"v[112:115],a[72:75],v[168:171],v[112:115]\n"
"v_xor_b32 v197,v196,v208\n"
U_"v131,v131,v219\n"
M_"v[116:119],a[88:91],v[168:171],v[116:119]\n"
M_"v[120:123],a[104:107],v[168:171],v[120:123]\n"
"v_xor_b32 v199,v198,v210\n"
U_"v132,v132,v220\n"
M_"v[124:127],a[120:123],v[168:171],v[124:127]\n"
U_"v133,v133,v221\n"
M_"v[176:179],a[72:75],v[172:175],v[176:179]\n"
M_"v[180:183],a[88:91],v[172:175],v[180:183]\n"
U_"v134,v134,v222\n"
M_"v[184:187],a[104:107],v[172:175],v[184:187]\n"
W_"lgkmcnt(0)\n"
U_"v135,v135,v223\n"
M_"v[188:191],a[120:123],v[172:175],v[188:191]\n"
"buffer_load_dword v214,v215,s[52:55],s20 offen offset:8 sc1\n"
M_"v[48:51],a[72:75],v[0:3],v[48:51]\n"
M_"v[52:55],a[88:91],v[0:3],v[52:55]\n"
M_"v[56:59],a[104:107],v[0:3],v[56:59]\n"
M_"v[60:63],a[120:123],v[0:3],v[60:63]\n"
W_"vmcnt(10)\n"
M_"v[104:107],a[44:47],v[4:7],v[104:107]\n"
"s_barrier\n"
M_"v[108:111],a[60:63],v[4:7],v[108:111]\n"
M_"v[112:115],a[76:79],v[4:7],v[112:115]\n"
W_"vmcnt(1)\n"
"v_cmp_ne_u32 vcc,s32,v255\n"
M_"v[116:119],a[92:95],v[4:7],v[116:119]\n"
"s_cmp_lg_u64 vcc,0\n"
"s_cbranch_scc1 L_slow_PCn\n"
"L_back_PCn:\n"
M_"v[120:123],a[108:111],v[4:7],v[120:123]\n"
C_"v104,v104,v105\n"
C_"v105,v106,v107\n"
M_"v[124:127],a[124:127],v[4:7],v[124:127]\n"
C_"v106,v108,v109\n"
C_"v107,v110,v111\n"
M_"v[96:99],a[12:15],v[4:7],v[96:99]\n"
"ds_write_b128 v240,v[104:107] offset:0\n"
C_"v112,v112,v113\n"
M_"v[100:103],a[28:31],v[4:7],v[100:103]\n"
C_"v113,v114,v115\n"
C_"v114,v116,v117\n"
M_"v[152:155],a[44:47],v[8:11],v[152:155]\n"
"s_xor_b32 s71,s64,0x20000\n"
C_"v115,v118,v119\n"
C_"v120,v120,v121\n"
M_"v[156:159],a[60:63],v[8:11],v[156:159]\n"
B_"v[80:83],v244,s[52:55],s71 offen offset:0 sc1\n"
M_"v[176:179],a[76:79],v[8:11],v[176:179]\n"
"ds_write_b128 v241,v[112:115] offset:0\n"
C_"v121,v122,v123\n"
M_"v[180:183],a[92:95],v[8:11],v[180:183]\n"
C_"v122,v124,v125\n"
C_"v123,v126,v127\n"
M_"v[184:187],a[108:111],v[8:11],v[184:187]\n"
"ds_write_b128 v242,v[120:123] offset:0\n"
C_"v152,v152,v153\n"
M_"v[188:191],a[124:127],v[8:11],v[188:191]\n"
B_"v[84:87],v244,s[52:55],s71 offen offset:1024 sc1\n"
M_"v[144:147],a[12:15],v[8:11],v[144:147]\n"
C_"v153,v154,v155\n"
C_"v154,v156,v157\n"
M_"v[148:151],a[28:31],v[8:11],v[148:151]\n"
C_"v155,v158,v159\n"
C_"v176,v176,v177\n"
M_"v[40:43],a[44:47],v[12:15],v[40:43]\n"
"ds_write_b128 v240,v[152:155] offset:12288\n"
C_"v177,v178,v179\n"
M_"v[44:47],a[60:63],v[12:15],v[44:47]\n"
B_"v[88:91],v244,s[52:55],s71 offen offset:2048 sc1\n"
M_"v[48:51],a[76:79],v[12:15],v[48:51]\n"
B_"v[92:95],v244,s[52:55],s71 offen offset:3072 sc1\n"
M_"v[52:55],a[92:95],v[12:15],v[52:55]\n"
"s_xor_b32 s71,s64,0x21000\n"
C_"v178,v180,v181\n"
C_"v179,v182,v183\n"
M_"v[56:59],a[108:111],v[12:15],v[56:59]\n"
"ds_write_b128 v241,v[176:179] offset:12288\n"
C_"v184,v184,v185\n"
M_"v[60:63],a[124:127],v[12:15],v[60:63]\n"
C_"v185,v186,v187\n"
C_"v186,v188,v189\n"
M_"v[32:35],a[12:15],v[12:15],v[32:35]\n"
B_"v[160:163],v244,s[52:55],s71 offen offset:0 sc1\n"
M_"v[36:39],a[28:31],v[12:15],v[36:39]\n"
C_"v187,v190,v191\n"
C_"v40,v40,v41\n"
M_"v[216:219],a[136:139],v[16:19],0\n"
"ds_write_b128 v242,v[184:187] offset:12288\n"
C_"v41,v42,v43\n"
M_"v[216:219],a[32:35],v[20:23],v[216:219]\n"
C_"v42,v44,v45\n"
C_"v43,v46,v47\n"
M_"v[216:219],a[36:39],v[24:27],v[216:219]\n"
B_"v[164:167],v244,s[52:55],s71 offen offset:1024 sc1\n"
M_"v[216:219],a[168:171],v[28:31],v[216:219]\n"
B_"v[168:171],v244,s[52:55],s71 offen offset:2048 sc1\n"
M_"v[220:223],a[140:143],v[16:19],0\n"
B_"v[172:175],v244,s[52:55],s71 offen offset:3072 sc1\n"
M_"v[220:223],a[48:51],v[20:23],v[220:223]\n"
"s_xor_b32 s71,s64,0x40000\n"
"ds_write_b128 v240,v[40:43] offset:24576\n"
C_"v48,v48,v49\n"
M_"v[220:223],a[52:55],v[24:27],v[220:223]\n"
C_"v49,v50,v51\n"
C_"v50,v52,v53\n"
M_"v[220:223],a[172:175],v[28:31],v[220:223]\n"
C_"v51,v54,v55\n"
C_"v56,v56,v57\n"
M_"v[224:227],a[144:147],v[16:19],0\n"
B_"v[0:3],v244,s[52:55],s71 offen offset:0 sc1\n"
M_"v[224:227],a[64:67],v[20:23],v[224:227]\n"
"ds_write_b128 v241,v[48:51] offset:24576\n"
C_"v57,v58,v59\n"
M_"v[224:227],a[68:71],v[24:27],v[224:227]\n"
C_"v58,v60,v61\n"
C_"v59,v62,v63\n"
M_"v[224:227],a[176:179],v[28:31],v[224:227]\n"
"ds_write_b128 v242,v[56:59] offset:24576\n"
M_"v[228:231],a[148:151],v[16:19],0\n"
B_"v[4:7],v244,s[52:55],s71 offen offset:1024 sc1\n"
M_"v[228:231],a[80:83],v[20:23],v[228:231]\n"
B_"v[8:11],v244,s[52:55],s71 offen offset:2048 sc1\n"
M_"v[228:231],a[84:87],v[24:27],v[228:231]\n"
B_"v[12:15],v244,s[52:55],s71 offen offset:3072 sc1\n"
M_"v[228:231],a[180:183],v[28:31],v[228:231]\n"
M_"v[232:235],a[152:155],v[16:19],0\n"
M_"v[232:235],a[96:99],v[20:23],v[232:235]\n"
W_"lgkmcnt(0)\n"
"s_barrier\n"
D_"v[104:107],v243 offset:0\n"
M_"v[232:235],a[100:103],v[24:27],v[232:235]\n"
D_"v[108:111],v243 offset:1024\n"
D_"v[112:115],v243 offset:2048\n"
M_"v[232:235],a[184:187],v[28:31],v[232:235]\n"
D_"v[176:179],v243 offset:12288\n"
D_"v[180:183],v243 offset:13312\n"
M_"v[236:239],a[156:159],v[16:19],0\n"
D_"v[184:187],v243 offset:14336\n"
M_"v[236:239],a[112:115],v[20:23],v[236:239]\n"
D_"v[40:43],v243 offset:24576\n"
D_"v[44:47],v243 offset:25600\n"
M_"v[236:239],a[116:119],v[24:27],v[236:239]\n"
D_"v[48:51],v243 offset:26624\n"
M_"v[236:239],a[188:191],v[28:31],v[236:239]\n"
M_"v[200:203],a[128:131],v[16:19],0\n"
M_"v[200:203],a[0:3],v[20:23],v[200:203]\n"
M_"v[200:203],a[4:7],v[24:27],v[200:203]\n"
M_"v[200:203],a[160:163],v[28:31],v[200:203]\n"
M_"v[204:207],a[132:135],v[16:19],0\n"
M_"v[204:207],a[16:19],v[20:23],v[204:207]\n"
W_"lgkmcnt(6)\n"
"v_swap_b32 v106,v112\n"
"v_swap_b32 v107,v113\n"
M_"v[204:207],a[20:23],v[24:27],v[204:207]\n"
M_"v[204:207],a[164:167],v[28:31],v[204:207]\n"
"v_smfmac_f32_16x16x64_bf16 v[96:99],v[196:199],v[104:111],v246\n"
"v_smfmac_f32_16x16x64_bf16 v[100:103],v[208:211],v[108:115],v246\n"
"s_xor_b32 s71,s64,0x41000\n"
M_"v[216:219],a[200:203],v[64:67],v[216:219]\n"
B_"v[16:19],v244,s[52:55],s71 offen offset:0 sc1\n"
M_"v[216:219],a[232:235],v[68:71],v[216:219]\n"
M_"v[216:219],a[40:43],v[72:75],v[216:219]\n"
"s_nop 1\n"
C_"v96,v96,v97\n"
C_"v97,v98,v99\n"
M_"v[216:219],a[44:47],v[76:79],v[216:219]\n"
C_"v98,v100,v101\n"
C_"v99,v102,v103\n"
M_"v[220:223],a[204:207],v[64:67],v[220:223]\n"
B_"v[20:23],v244,s[52:55],s71 offen offset:1024 sc1\n"
M_"v[220:223],a[236:239],v[68:71],v[220:223]\n"
W_"lgkmcnt(3)\n"
"v_swap_b32 v178,v184\n"
"v_swap_b32 v179,v185\n"
M_"v[220:223],a[56:59],v[72:75],v[220:223]\n"
"v_min_u32 v96,-2,v96\n"
"v_min_u32 v97,-2,v97\n"
M_"v[220:223],a[60:63],v[76:79],v[220:223]\n"
"v_min_u32 v98,-2,v98\n"
"v_min_u32 v99,-2,v99\n"
"v_smfmac_f32_16x16x64_bf16 v[144:147],v[196:199],v[176:183],v246\n"
B_"v[24:27],v244,s[52:55],s71 offen offset:2048 sc1\n"
"v_smfmac_f32_16x16x64_bf16 v[148:151],v[208:211],v[180:187],v246\n"
B_"v[28:31],v244,s[52:55],s71 offen offset:3072 sc1\n"
M_"v[224:227],a[208:211],v[64:67],v[224:227]\n"
"s_xor_b32 s74,s66,0x20000\n"
C_"v216,v216,v217\n"
C_"v217,v218,v219\n"
M_"v[224:227],a[240:243],v[68:71],v[224:227]\n"
M_"v[224:227],a[72:75],v[72:75],v[224:227]\n"
S_"v[96:99],v244,s[52:55],s74 offen sc1\n"
M_"v[224:227],a[76:79],v[76:79],v[224:227]\n"
C_"v218,v220,v221\n"
C_"v219,v222,v223\n"
M_"v[228:231],a[212:215],v[64:67],v[228:231]\n"
"ds_write_b128 v240,v[216:219] offset:36864\n"
C_"v144,v144,v145\n"
M_"v[228:231],a[244:247],v[68:71],v[228:231]\n"
W_"lgkmcnt(1)\n"
"v_swap_b32 v42,v48\n"
"v_swap_b32 v43,v49\n"
M_"v[228:231],a[88:91],v[72:75],v[228:231]\n"
C_"v145,v146,v147\n"
C_"v146,v148,v149\n"
M_"v[228:231],a[92:95],v[76:79],v[228:231]\n"
C_"v147,v150,v151\n"
"v_min_u32 v144,-2,v144\n"
"v_smfmac_f32_16x16x64_bf16 v[32:35],v[196:199],v[40:47],v246\n"
"v_min_u32 v145,-2,v145\n"
"v_min_u32 v146,-2,v146\n"
"v_smfmac_f32_16x16x64_bf16 v[36:39],v[208:211],v[44:51],v246\n"
"v_min_u32 v147,-2,v147\n"
"s_xor_b32 s74,s66,0x40400\n"
M_"v[232:235],a[216:219],v[64:67],v[232:235]\n"
S_"v[144:147],v244,s[52:55],s74 offen sc1\n"
M_"v[232:235],a[248:251],v[68:71],v[232:235]\n"
C_"v224,v224,v225\n"
C_"v225,v226,v227\n"
M_"v[232:235],a[104:107],v[72:75],v[232:235]\n"
C_"v32,v32,v33\n"
C_"v33,v34,v35\n"
M_"v[232:235],a[108:111],v[76:79],v[232:235]\n"
C_"v226,v228,v229\n"
C_"v227,v230,v231\n"
M_"v[236:239],a[220:223],v[64:67],v[236:239]\n"
"ds_write_b128 v241,v[224:227] offset:36864\n"
C_"v34,v36,v37\n"
M_"v[236:239],a[252:255],v[68:71],v[236:239]\n"
C_"v35,v38,v39\n"
"v_min_u32 v32,-2,v32\n"
M_"v[236:239],a[120:123],v[72:75],v[236:239]\n"
"v_min_u32 v33,-2,v33\n"
"v_min_u32 v34,-2,v34\n"
M_"v[236:239],a[124:127],v[76:79],v[236:239]\n"
"v_min_u32 v35,-2,v35\n"
"s_xor_b32 s74,s66,0x60800\n"
M_"v[200:203],a[192:195],v[64:67],v[200:203]\n"
S_"v[32:35],v244,s[52:55],s74 offen sc1\n"
M_"v[200:203],a[224:227],v[68:71],v[200:203]\n"
M_"v[200:203],a[8:11],v[72:75],v[200:203]\n"
C_"v232,v232,v233\n"
C_"v233,v234,v235\n"
M_"v[200:203],a[12:15],v[76:79],v[200:203]\n"
C_"v234,v236,v237\n"
C_"v235,v238,v239\n"
M_"v[204:207],a[196:199],v[64:67],v[204:207]\n"
"ds_write_b128 v242,v[232:235] offset:36864\n"
M_"v[204:207],a[228:231],v[68:71],v[204:207]\n"
M_"v[204:207],a[24:27],v[72:75],v[204:207]\n"
M_"v[204:207],a[28:31],v[76:79],v[204:207]\n"
W_"vmcnt(15)\n"
M_"v[104:107],a[136:139],v[80:83],0\n"
M_"v[104:107],a[32:35],v[84:87],v[104:107]\n"
"s_xor_b32 s71,s64,0x60000\n"
B_"v[64:67],v244,s[52:55],s71 offen offset:0 sc1\n"
M_"v[104:107],a[36:39],v[88:91],v[104:107]\n"
W_"lgkmcnt(0)\n"
"s_barrier\n"
D_"v[216:219],v243 offset:36864\n"
M_"v[104:107],a[168:171],v[92:95],v[104:107]\n"
D_"v[220:223],v243 offset:37888\n"
D_"v[224:227],v243 offset:38912\n"
M_"v[108:111],a[140:143],v[80:83],0\n"
M_"v[108:111],a[48:51],v[84:87],v[108:111]\n"
B_"v[68:71],v244,s[52:55],s71 offen offset:1024 sc1\n"
M_"v[108:111],a[52:55],v[88:91],v[108:111]\n"
M_"v[108:111],a[172:175],v[92:95],v[108:111]\n"
M_"v[112:115],a[144:147],v[80:83],0\n"
M_"v[112:115],a[64:67],v[84:87],v[112:115]\n"
B_"v[72:75],v244,s[52:55],s71 offen offset:2048 sc1\n"
M_"v[112:115],a[68:71],v[88:91],v[112:115]\n"
M_"v[112:115],a[176:179],v[92:95],v[112:115]\n"
B_"v[76:79],v244,s[52:55],s71 offen offset:3072 sc1\n"
M_"v[116:119],a[148:151],v[80:83],0\n"
M_"v[116:119],a[80:83],v[84:87],v[116:119]\n"
M_"v[116:119],a[84:87],v[88:91],v[116:119]\n"
M_"v[116:119],a[180:183],v[92:95],v[116:119]\n"
M_"v[120:123],a[152:155],v[80:83],0\n"
M_"v[120:123],a[96:99],v[84:87],v[120:123]\n"
M_"v[120:123],a[100:103],v[88:91],v[120:123]\n"
B_"v[32:35],v245,s[44:47],s69 offen\n"
M_"v[120:123],a[184:187],v[92:95],v[120:123]\n"
M_"v[124:127],a[156:159],v[80:83],0\n"
M_"v[124:127],a[112:115],v[84:87],v[124:127]\n"
W_"lgkmcnt(0)\n"
"v_swap_b32 v218,v224\n"
"v_swap_b32 v219,v225\n"
M_"v[124:127],a[116:119],v[88:91],v[124:127]\n"
B_"v[36:39],v245,s[44:47],s70 offen\n"
M_"v[124:127],a[188:191],v[92:95],v[124:127]\n"
"v_smfmac_f32_16x16x64_bf16 v[200:203],v[196:199],v[216:223],v246\n"
"v_smfmac_f32_16x16x64_bf16 v[204:207],v[208:211],v[220:227],v246\n"
M_"v[96:99],a[128:131],v[80:83],0\n"
M_"v[96:99],a[0:3],v[84:87],v[96:99]\n"
M_"v[96:99],a[4:7],v[88:91],v[96:99]\n"
M_"v[96:99],a[160:163],v[92:95],v[96:99]\n"
M_"v[100:103],a[132:135],v[80:83],0\n"
M_"v[100:103],a[16:19],v[84:87],v[100:103]\n"
M_"v[100:103],a[20:23],v[88:91],v[100:103]\n"
M_"v[100:103],a[164:167],v[92:95],v[100:103]\n"
W_"vmcnt(17)\n"
M_"v[104:107],a[200:203],v[160:163],v[104:107]\n"
M_"v[104:107],a[232:235],v[164:167],v[104:107]\n"
"s_xor_b32 s71,s64,0x61000\n"
B_"v[80:83],v244,s[52:55],s71 offen offset:0 sc1\n"
M_"v[104:107],a[40:43],v[168:171],v[104:107]\n"
M_"v[104:107],a[44:47],v[172:175],v[104:107]\n"
M_"v[108:111],a[204:207],v[160:163],v[108:111]\n"
M_"v[108:111],a[236:239],v[164:167],v[108:111]\n"
B_"v[84:87],v244,s[52:55],s71 offen offset:1024 sc1\n"
M_"v[108:111],a[56:59],v[168:171],v[108:111]\n"
M_"v[108:111],a[60:63],v[172:175],v[108:111]\n"
M_"v[112:115],a[208:211],v[160:163],v[112:115]\n"
M_"v[112:115],a[240:243],v[164:167],v[112:115]\n"
B_"v[88:91],v244,s[52:55],s71 offen offset:2048 sc1\n"
M_"v[112:115],a[72:75],v[168:171],v[112:115]\n"
C_"v104,v104,v105\n"
C_"v105,v106,v107\n"
M_"v[112:115],a[76:79],v[172:175],v[112:115]\n"
B_"v[92:95],v244,s[52:55],s71 offen offset:3072 sc1\n"
M_"v[116:119],a[212:215],v[160:163],v[116:119]\n"
M_"v[116:119],a[244:247],v[164:167],v[116:119]\n"
C_"v106,v108,v109\n"
C_"v107,v110,v111\n"
M_"v[116:119],a[88:91],v[168:171],v[116:119]\n"
"ds_write_b128 v240,v[104:107] offset:49152\n"
M_"v[116:119],a[92:95],v[172:175],v[116:119]\n"
M_"v[120:123],a[216:219],v[160:163],v[120:123]\n"
M_"v[120:123],a[248:251],v[164:167],v[120:123]\n"
M_"v[120:123],a[104:107],v[168:171],v[120:123]\n"
C_"v112,v112,v113\n"
C_"v113,v114,v115\n"
M_"v[120:123],a[108:111],v[172:175],v[120:123]\n"
M_"v[124:127],a[220:223],v[160:163],v[124:127]\n"
M_"v[124:127],a[252:255],v[164:167],v[124:127]\n"
C_"v114,v116,v117\n"
C_"v115,v118,v119\n"
M_"v[124:127],a[120:123],v[168:171],v[124:127]\n"
"ds_write_b128 v241,v[112:115] offset:49152\n"
M_"v[124:127],a[124:127],v[172:175],v[124:127]\n"
M_"v[96:99],a[192:195],v[160:163],v[96:99]\n"
M_"v[96:99],a[224:227],v[164:167],v[96:99]\n"
M_"v[96:99],a[8:11],v[168:171],v[96:99]\n"
C_"v120,v120,v121\n"
C_"v121,v122,v123\n"
M_"v[96:99],a[12:15],v[172:175],v[96:99]\n"
"s_nop 1\n"
C_"v122,v124,v125\n"
C_"v123,v126,v127\n"
M_"v[100:103],a[196:199],v[160:163],v[100:103]\n"
"ds_write_b128 v242,v[120:123] offset:49152\n"
M_"v[100:103],a[228:231],v[164:167],v[100:103]\n"
M_"v[100:103],a[24:27],v[168:171],v[100:103]\n"
M_"v[100:103],a[28:31],v[172:175],v[100:103]\n"
W_"vmcnt(17)\n"
M_"v[152:155],a[136:139],v[0:3],0\n"
M_"v[152:155],a[32:35],v[4:7],v[152:155]\n"
"s_xor_b32 s71,s64,0x0\n"
B_"v[160:163],v244,s[52:55],s71 offen offset:0 sc1\n"
M_"v[152:155],a[36:39],v[8:11],v[152:155]\n"
W_"lgkmcnt(0)\n"
"s_barrier\n"
D_"v[104:107],v243 offset:49152\n"
M_"v[152:155],a[168:171],v[12:15],v[152:155]\n"
D_"v[108:111],v243 offset:50176\n"
D_"v[112:115],v243 offset:51200\n"
M_"v[156:159],a[140:143],v[0:3],0\n"
M_"v[156:159],a[48:51],v[4:7],v[156:159]\n"
B_"v[164:167],v244,s[52:55],s71 offen offset:1024 sc1\n"
M_"v[156:159],a[52:55],v[8:11],v[156:159]\n"
M_"v[156:159],a[172:175],v[12:15],v[156:159]\n"
M_"v[176:179],a[144:147],v[0:3],0\n"
M_"v[176:179],a[64:67],v[4:7],v[176:179]\n"
B_"v[168:171],v244,s[52:55],s71 offen offset:2048 sc1\n"
M_"v[176:179],a[68:71],v[8:11],v[176:179]\n"
M_"v[176:179],a[176:179],v[12:15],v[176:179]\n"
B_"v[172:175],v244,s[52:55],s71 offen offset:3072 sc1\n"
M_"v[180:183],a[148:151],v[0:3],0\n"
M_"v[180:183],a[80:83],v[4:7],v[180:183]\n"
M_"v[180:183],a[84:87],v[8:11],v[180:183]\n"
M_"v[180:183],a[180:183],v[12:15],v[180:183]\n"
M_"v[184:187],a[152:155],v[0:3],0\n"
M_"v[184:187],a[96:99],v[4:7],v[184:187]\n"
M_"v[184:187],a[100:103],v[8:11],v[184:187]\n"
M_"v[184:187],a[184:187],v[12:15],v[184:187]\n"
M_"v[188:191],a[156:159],v[0:3],0\n"
M_"v[188:191],a[112:115],v[4:7],v[188:191]\n"
W_"lgkmcnt(0)\n"
"v_swap_b32 v106,v112\n"
"v_swap_b32 v107,v113\n"
M_"v[188:191],a[116:119],v[8:11],v[188:191]\n"
M_"v[188:191],a[188:191],v[12:15],v[188:191]\n"
"v_smfmac_f32_16x16x64_bf16 v[96:99],v[196:199],v[104:111],v246\n"
"v_smfmac_f32_16x16x64_bf16 v[100:103],v[208:211],v[108:115],v246\n"
M_"v[144:147],a[128:131],v[0:3],0\n"
M_"v[144:147],a[0:3],v[4:7],v[144:147]\n"
M_"v[144:147],a[4:7],v[8:11],v[144:147]\n"
"s_nop 3\n"
C_"v96,v96,v97\n"
C_"v97,v98,v99\n"
M_"v[144:147],a[160:163],v[12:15],v[144:147]\n"
C_"v98,v100,v101\n"
C_"v99,v102,v103\n"
"v_min_u32 v96,-2,v96\n"
M_"v[148:151],a[132:135],v[0:3],0\n"
"v_min_u32 v97,-2,v97\n"
"v_min_u32 v98,-2,v98\n"
M_"v[148:151],a[16:19],v[4:7],v[148:151]\n"
"v_min_u32 v99,-2,v99\n"
"s_xor_b32 s74,s66,0x21000\n"
M_"v[148:151],a[20:23],v[8:11],v[148:151]\n"
S_"v[96:99],v244,s[52:55],s74 offen sc1\n"
M_"v[148:151],a[164:167],v[12:15],v[148:151]\n"
W_"vmcnt(18)\n"
M_"v[152:155],a[200:203],v[16:19],v[152:155]\n"
M_"v[152:155],a[232:235],v[20:23],v[152:155]\n"
"s_xor_b32 s71,s64,0x1000\n"
M_"v[152:155],a[40:43],v[24:27],v[152:155]\n"
B_"v[0:3],v244,s[52:55],s71 offen offset:0 sc1\n"
M_"v[152:155],a[44:47],v[28:31],v[152:155]\n"
M_"v[156:159],a[204:207],v[16:19],v[156:159]\n"
M_"v[156:159],a[236:239],v[20:23],v[156:159]\n"
M_"v[156:159],a[56:59],v[24:27],v[156:159]\n"
B_"v[4:7],v244,s[52:55],s71 offen offset:1024 sc1\n"
M_"v[156:159],a[60:63],v[28:31],v[156:159]\n"
M_"v[176:179],a[208:211],v[16:19],v[176:179]\n"
M_"v[176:179],a[240:243],v[20:23],v[176:179]\n"
M_"v[176:179],a[72:75],v[24:27],v[176:179]\n"
B_"v[8:11],v244,s[52:55],s71 offen offset:2048 sc1\n"
C_"v152,v152,v153\n"
M_"v[176:179],a[76:79],v[28:31],v[176:179]\n"
B_"v[12:15],v244,s[52:55],s71 offen offset:3072 sc1\n"
C_"v153,v154,v155\n"
M_"v[180:183],a[212:215],v[16:19],v[180:183]\n"
M_"v[180:183],a[244:247],v[20:23],v[180:183]\n"
C_"v154,v156,v157\n"
C_"v155,v158,v159\n"
M_"v[180:183],a[88:91],v[24:27],v[180:183]\n"
"ds_write_b128 v240,v[152:155] offset:0\n"
M_"v[180:183],a[92:95],v[28:31],v[180:183]\n"
M_"v[184:187],a[216:219],v[16:19],v[184:187]\n"
M_"v[184:187],a[248:251],v[20:23],v[184:187]\n"
M_"v[184:187],a[104:107],v[24:27],v[184:187]\n"
C_"v176,v176,v177\n"
C_"v177,v178,v179\n"
M_"v[184:187],a[108:111],v[28:31],v[184:187]\n"
M_"v[188:191],a[220:223],v[16:19],v[188:191]\n"
M_"v[188:191],a[252:255],v[20:23],v[188:191]\n"
C_"v178,v180,v181\n"
C_"v179,v182,v183\n"
M_"v[188:191],a[120:123],v[24:27],v[188:191]\n"
B_"v[216:219],v244,s[52:55],s66 offen offset:0 sc1\n"
M_"v[188:191],a[124:127],v[28:31],v[188:191]\n"
"ds_write_b128 v241,v[176:179] offset:0\n"
M_"v[144:147],a[192:195],v[16:19],v[144:147]\n"
M_"v[144:147],a[224:227],v[20:23],v[144:147]\n"
M_"v[144:147],a[8:11],v[24:27],v[144:147]\n"
C_"v184,v184,v185\n"
C_"v185,v186,v187\n"
M_"v[144:147],a[12:15],v[28:31],v[144:147]\n"
B_"v[220:223],v244,s[52:55],s66 offen offset:1024 sc1\n"
M_"v[148:151],a[196:199],v[16:19],v[148:151]\n"
C_"v186,v188,v189\n"
C_"v187,v190,v191\n"
M_"v[148:151],a[228:231],v[20:23],v[148:151]\n"
"ds_write_b128 v242,v[184:187] offset:0\n"
M_"v[148:151],a[24:27],v[24:27],v[148:151]\n"
B_"v[224:227],v244,s[52:55],s66 offen offset:2048 sc1\n"
M_"v[148:151],a[28:31],v[28:31],v[148:151]\n"
W_"vmcnt(18)\n"
M_"v[104:107],a[136:139],v[64:67],0\n"
W_"vmcnt(16)\n"
M_"v[104:107],a[32:35],v[68:71],v[104:107]\n"
"v_permlane16_swap_b32 v32,v34\n"
M_"v[104:107],a[36:39],v[72:75],v[104:107]\n"
W_"lgkmcnt(0)\n"
"s_barrier\n"
D_"v[176:179],v243 offset:0\n"
D_"v[180:183],v243 offset:1024\n"
M_"v[104:107],a[168:171],v[76:79],v[104:107]\n"
W_"vmcnt(12)\n"
D_"v[184:187],v243 offset:2048\n"
"v_permlane16_swap_b32 v33,v35\n"
M_"v[104:107],a[200:203],v[80:83],v[104:107]\n"
"v_permlane16_swap_b32 v36,v38\n"
M_"v[104:107],a[232:235],v[84:87],v[104:107]\n"
"v_permlane16_swap_b32 v37,v39\n"
M_"v[104:107],a[40:43],v[88:91],v[104:107]\n"
M_"v[104:107],a[44:47],v[92:95],v[104:107]\n"
"v_lshlrev_b32 v40,16,v32\n"
M_"v[108:111],a[140:143],v[64:67],0\n"
"v_and_b32 v41,0xffff0000,v32\n"
M_"v[108:111],a[48:51],v[68:71],v[108:111]\n"
M_"v[108:111],a[52:55],v[72:75],v[108:111]\n"
"v_lshlrev_b32 v42,16,v33\n"
M_"v[108:111],a[172:175],v[76:79],v[108:111]\n"
"v_and_b32 v43,0xffff0000,v33\n"
M_"v[108:111],a[204:207],v[80:83],v[108:111]\n"
"v_lshlrev_b32 v44,16,v34\n"
M_"v[108:111],a[236:239],v[84:87],v[108:111]\n"
M_"v[108:111],a[56:59],v[88:91],v[108:111]\n"
"v_and_b32 v45,0xffff0000,v34\n"
M_"v[108:111],a[60:63],v[92:95],v[108:111]\n"
"v_lshlrev_b32 v46,16,v35\n"
M_"v[112:115],a[144:147],v[64:67],0\n"
M_"v[112:115],a[64:67],v[68:71],v[112:115]\n"
"v_and_b32 v47,0xffff0000,v35\n"
M_"v[112:115],a[68:71],v[72:75],v[112:115]\n"
C_"v104,v104,v105\n"
C_"v105,v106,v107\n"
M_"v[112:115],a[176:179],v[76:79],v[112:115]\n"
"v_lshlrev_b32 v48,16,v36\n"
M_"v[112:115],a[208:211],v[80:83],v[112:115]\n"
"v_and_b32 v49,0xffff0000,v36\n"
M_"v[112:115],a[240:243],v[84:87],v[112:115]\n"
W_"lgkmcnt(0)\n"
C_"v106,v108,v109\n"
C_"v107,v110,v111\n"
"v_swap_b32 v178,v184\n"
M_"v[112:115],a[72:75],v[88:91],v[112:115]\n"
"v_swap_b32 v179,v185\n"
"ds_write_b128 v240,v[104:107] offset:12288\n"
"v_lshlrev_b32 v50,16,v37\n"
M_"v[112:115],a[76:79],v[92:95],v[112:115]\n"
"v_and_b32 v51,0xffff0000,v37\n"
"v_smfmac_f32_16x16x64_bf16 v[144:147],v[196:199],v[176:183],v246\n"
"v_lshlrev_b32 v52,16,v38\n"
"v_smfmac_f32_16x16x64_bf16 v[148:151],v[208:211],v[180:187],v246\n"
"v_and_b32 v53,0xffff0000,v38\n"
M_"v[116:119],a[148:151],v[64:67],0\n"
M_"v[116:119],a[80:83],v[68:71],v[116:119]\n"
"v_lshlrev_b32 v54,16,v39\n"
M_"v[116:119],a[84:87],v[72:75],v[116:119]\n"
"v_and_b32 v55,0xffff0000,v39\n"
C_"v144,v144,v145\n"
C_"v145,v146,v147\n"
M_"v[116:119],a[180:183],v[76:79],v[116:119]\n"
C_"v146,v148,v149\n"
C_"v147,v150,v151\n"
M_"v[116:119],a[212:215],v[80:83],v[116:119]\n"
"v_min_u32 v144,-2,v144\n"
"v_min_u32 v145,-2,v145\n"
"v_min_u32 v146,-2,v146\n"
M_"v[116:119],a[244:247],v[84:87],v[116:119]\n"
"v_min_u32 v147,-2,v147\n"
"s_xor_b32 s74,s66,0x41400\n"
M_"v[116:119],a[88:91],v[88:91],v[116:119]\n"
S_"v[144:147],v244,s[52:55],s74 offen sc1\n"
M_"v[116:119],a[92:95],v[92:95],v[116:119]\n"
M_"v[120:123],a[152:155],v[64:67],0\n"
M_"v[120:123],a[96:99],v[68:71],v[120:123]\n"
M_"v[120:123],a[100:103],v[72:75],v[120:123]\n"
C_"v112,v112,v113\n"
C_"v113,v114,v115\n"
M_"v[120:123],a[184:187],v[76:79],v[120:123]\n"
M_"v[120:123],a[216:219],v[80:83],v[120:123]\n"
M_"v[120:123],a[248:251],v[84:87],v[120:123]\n"
C_"v114,v116,v117\n"
C_"v115,v118,v119\n"
M_"v[120:123],a[104:107],v[88:91],v[120:123]\n"
"ds_write_b128 v241,v[112:115] offset:12288\n"
M_"v[120:123],a[108:111],v[92:95],v[120:123]\n"
M_"v[124:127],a[156:159],v[64:67],0\n"
M_"v[124:127],a[112:115],v[68:71],v[124:127]\n"
M_"v[124:127],a[116:119],v[72:75],v[124:127]\n"
M_"v[124:127],a[188:191],v[76:79],v[124:127]\n"
M_"v[124:127],a[220:223],v[80:83],v[124:127]\n"
M_"v[124:127],a[252:255],v[84:87],v[124:127]\n"
M_"v[124:127],a[120:123],v[88:91],v[124:127]\n"
M_"v[124:127],a[124:127],v[92:95],v[124:127]\n"
M_"v[96:99],a[128:131],v[64:67],0\n"
M_"v[96:99],a[0:3],v[68:71],v[96:99]\n"
M_"v[96:99],a[4:7],v[72:75],v[96:99]\n"
C_"v120,v120,v121\n"
C_"v121,v122,v123\n"
M_"v[96:99],a[160:163],v[76:79],v[96:99]\n"
"s_nop 1\n"
C_"v122,v124,v125\n"
C_"v123,v126,v127\n"
M_"v[96:99],a[192:195],v[80:83],v[96:99]\n"
"ds_write_b128 v242,v[120:123] offset:12288\n"
M_"v[96:99],a[224:227],v[84:87],v[96:99]\n"
M_"v[96:99],a[8:11],v[88:91],v[96:99]\n"
B_"v[228:231],v244,s[52:55],s72 offen offset:0 sc1\n"
M_"v[96:99],a[12:15],v[92:95],v[96:99]\n"
M_"v[100:103],a[132:135],v[64:67],0\n"
M_"v[100:103],a[16:19],v[68:71],v[100:103]\n"
W_"lgkmcnt(0)\n"
"s_barrier\n"
D_"v[104:107],v243 offset:12288\n"
M_"v[100:103],a[20:23],v[72:75],v[100:103]\n"
D_"v[108:111],v243 offset:13312\n"
D_"v[112:115],v243 offset:14336\n"
M_"v[100:103],a[164:167],v[76:79],v[100:103]\n"
M_"v[100:103],a[196:199],v[80:83],v[100:103]\n"
M_"v[100:103],a[228:231],v[84:87],v[100:103]\n"
M_"v[100:103],a[24:27],v[88:91],v[100:103]\n"
M_"v[100:103],a[28:31],v[92:95],v[100:103]\n"
W_"vmcnt(10)\n"
M_"v[152:155],a[136:139],v[160:163],0\n"
M_"v[152:155],a[32:35],v[164:167],v[152:155]\n"
M_"v[152:155],a[36:39],v[168:171],v[152:155]\n"
W_"lgkmcnt(0)\n"
"v_swap_b32 v106,v112\n"
"v_swap_b32 v107,v113\n"
M_"v[152:155],a[168:171],v[172:175],v[152:155]\n"
W_"vmcnt(5)\n"
M_"v[152:155],a[200:203],v[0:3],v[152:155]\n"
"v_smfmac_f32_16x16x64_bf16 v[96:99],v[196:199],v[104:111],v246\n"
"v_smfmac_f32_16x16x64_bf16 v[100:103],v[208:211],v[108:115],v246\n"
M_"v[152:155],a[232:235],v[4:7],v[152:155]\n"
M_"v[152:155],a[40:43],v[8:11],v[152:155]\n"
M_"v[152:155],a[44:47],v[12:15],v[152:155]\n"
"s_nop 3\n"
C_"v96,v96,v97\n"
C_"v97,v98,v99\n"
C_"v98,v100,v101\n"
M_"v[156:159],a[140:143],v[160:163],0\n"
C_"v99,v102,v103\n"
"v_min_u32 v96,-2,v96\n"
M_"v[156:159],a[48:51],v[164:167],v[156:159]\n"
"v_min_u32 v97,-2,v97\n"
"v_min_u32 v98,-2,v98\n"
M_"v[156:159],a[52:55],v[168:171],v[156:159]\n"
"v_min_u32 v99,-2,v99\n"
"s_xor_b32 s74,s66,0x61800\n"
M_"v[156:159],a[172:175],v[172:175],v[156:159]\n"
S_"v[96:99],v244,s[52:55],s74 offen sc1\n"
M_"v[156:159],a[204:207],v[0:3],v[156:159]\n"
W_"vmcnt(3)\n"
"v_max3_u32 v247,v216,v217,v218\n"
"v_max3_u32 v248,v219,v220,v221\n"
M_"v[156:159],a[236:239],v[4:7],v[156:159]\n"
"v_max3_u32 v247,v247,v222,v223\n"
M_"v[156:159],a[56:59],v[8:11],v[156:159]\n"
"v_max3_u32 v248,v248,v224,v225\n"
"v_max3_u32 v247,v247,v226,v227\n"
M_"v[156:159],a[60:63],v[12:15],v[156:159]\n"
"v_max_u32 v247,v247,v248\n"
M_"v[176:179],a[144:147],v[160:163],0\n"
"v_cmp_eq_u32 vcc,-1,v247\n"
M_"v[176:179],a[64:67],v[164:167],v[176:179]\n"
"s_cmp_lg_u64 vcc,0\n"
"s_cbranch_scc1 L_slow_XEn\n"
"L_back_XEn:\n"
M_"v[176:179],a[68:71],v[168:171],v[176:179]\n"
C_"v152,v152,v153\n"
C_"v153,v154,v155\n"
M_"v[176:179],a[176:179],v[172:175],v[176:179]\n"
"v_swap_b32 v218,v224\n"
M_"v[176:179],a[208:211],v[0:3],v[176:179]\n"
"v_swap_b32 v219,v225\n"
M_"v[176:179],a[240:243],v[4:7],v[176:179]\n"
C_"v154,v156,v157\n"
C_"v155,v158,v159\n"
M_"v[176:179],a[72:75],v[8:11],v[176:179]\n"
"ds_write_b128 v240,v[152:155] offset:24576\n"
M_"v[176:179],a[76:79],v[12:15],v[176:179]\n"
"v_smfmac_f32_16x16x64_bf16 v[200:203],v[196:199],v[216:223],v246\n"
"v_smfmac_f32_16x16x64_bf16 v[204:207],v[208:211],v[220:227],v246\n"
M_"v[180:183],a[148:151],v[160:163],0\n"
M_"v[180:183],a[80:83],v[164:167],v[180:183]\n"
M_"v[180:183],a[84:87],v[168:171],v[180:183]\n"
B_"v[232:235],v244,s[52:55],s72 offen offset:1024 sc1\n"
M_"v[180:183],a[180:183],v[172:175],v[180:183]\n"
"s_nop 1\n"
"v_fma_f32 v40,v200,v136,v40\n"
M_"v[180:183],a[212:215],v[0:3],v[180:183]\n"
"v_fma_f32 v41,v201,v137,v41\n"
M_"v[180:183],a[244:247],v[4:7],v[180:183]\n"
"v_fma_f32 v42,v202,v138,v42\n"
M_"v[180:183],a[88:91],v[8:11],v[180:183]\n"
B_"v[236:239],v244,s[52:55],s72 offen offset:2048 sc1\n"
M_"v[180:183],a[92:95],v[12:15],v[180:183]\n"
"v_fma_f32 v43,v203,v139,v43\n"
"v_fma_f32 v44,v204,v140,v44\n"
M_"v[184:187],a[152:155],v[160:163],0\n"
"v_fma_f32 v45,v205,v141,v45\n"
M_"v[184:187],a[96:99],v[164:167],v[184:187]\n"
"v_fma_f32 v46,v206,v142,v46\n"
M_"v[184:187],a[100:103],v[168:171],v[184:187]\n"
C_"v176,v176,v177\n"
C_"v177,v178,v179\n"
"v_fma_f32 v47,v207,v143,v47\n"
M_"v[184:187],a[184:187],v[172:175],v[184:187]\n"
C_"v40,v40,v41\n"
M_"v[184:187],a[216:219],v[0:3],v[184:187]\n"
C_"v41,v42,v43\n"
M_"v[184:187],a[248:251],v[4:7],v[184:187]\n"
C_"v178,v180,v181\n"
C_"v179,v182,v183\n"
M_"v[184:187],a[104:107],v[8:11],v[184:187]\n"
"ds_write_b128 v241,v[176:179] offset:24576\n"
C_"v42,v44,v45\n"
C_"v43,v46,v47\n"
M_"v[184:187],a[108:111],v[12:15],v[184:187]\n"
"v_permlane16_swap_b32 v40,v42\n"
M_"v[188:191],a[156:159],v[160:163],0\n"
"v_permlane16_swap_b32 v41,v43\n"
M_"v[188:191],a[112:115],v[164:167],v[188:191]\n"
S_"v[40:43],v245,s[56:59],s69 offen sc1\n"
M_"v[188:191],a[116:119],v[168:171],v[188:191]\n"
M_"v[188:191],a[188:191],v[172:175],v[188:191]\n"
M_"v[188:191],a[220:223],v[0:3],v[188:191]\n"
M_"v[188:191],a[252:255],v[4:7],v[188:191]\n"
M_"v[188:191],a[120:123],v[8:11],v[188:191]\n"
M_"v[188:191],a[124:127],v[12:15],v[188:191]\n"
M_"v[144:147],a[128:131],v[160:163],0\n"
M_"v[144:147],a[0:3],v[164:167],v[144:147]\n"
M_"v[144:147],a[4:7],v[168:171],v[144:147]\n"
C_"v184,v184,v185\n"
C_"v185,v186,v187\n"
M_"v[144:147],a[160:163],v[172:175],v[144:147]\n"
"s_nop 1\n"
C_"v186,v188,v189\n"
C_"v187,v190,v191\n"
M_"v[144:147],a[192:195],v[0:3],v[144:147]\n"
"ds_write_b128 v242,v[184:187] offset:24576\n"
M_"v[144:147],a[224:227],v[4:7],v[144:147]\n"
M_"v[144:147],a[8:11],v[8:11],v[144:147]\n"
M_"v[144:147],a[12:15],v[12:15],v[144:147]\n"
M_"v[148:151],a[132:135],v[160:163],0\n"
W_"lgkmcnt(0)\n"
"s_barrier\n"
D_"v[176:179],v243 offset:24576\n"
M_"v[148:151],a[16:19],v[164:167],v[148:151]\n"
D_"v[180:183],v243 offset:25600\n"
D_"v[184:187],v243 offset:26624\n"
M_"v[148:151],a[20:23],v[168:171],v[148:151]\n"
M_"v[148:151],a[164:167],v[172:175],v[148:151]\n"
M_"v[148:151],a[196:199],v[0:3],v[148:151]\n"
M_"v[148:151],a[228:231],v[4:7],v[148:151]\n"
B_"v[216:219],v244,s[52:55],s72 offen offset:2048 sc1\n"
M_"v[148:151],a[24:27],v[8:11],v[148:151]\n"
M_"v[148:151],a[28:31],v[12:15],v[148:151]\n"
W_"lgkmcnt(0)\n"
"v_swap_b32 v178,v184\n"
"v_swap_b32 v179,v185\n"
"s_nop 1\n"
"v_smfmac_f32_16x16x64_bf16 v[144:147],v[196:199],v[176:183],v246\n"
"v_smfmac_f32_16x16x64_bf16 v[148:151],v[208:211],v[180:187],v246\n"
W_"vmcnt(2)\n"
"v_max3_u32 v247,v228,v229,v230\n"
"v_max3_u32 v248,v231,v232,v233\n"
"v_max3_u32 v247,v247,v234,v235\n"
"v_max3_u32 v248,v248,v236,v237\n"
"v_max3_u32 v247,v247,v238,v239\n"
"v_max_u32 v247,v247,v248\n"
"v_cmp_eq_u32 vcc,-1,v247\n"
"s_cmp_lg_u64 vcc,0\n"
"s_cbranch_scc1 L_slow_XLn\n"
"L_back_XLn:\n"
"v_swap_b32 v230,v236\n"
"v_swap_b32 v231,v237\n"
"s_nop 1\n"
"v_smfmac_f32_16x16x64_bf16 v[144:147],v[196:199],v[228:235],v246\n"
"v_smfmac_f32_16x16x64_bf16 v[148:151],v[208:211],v[232:239],v246\n"
"s_nop 6\n"
"v_fma_f32 v48,v144,v128,v48\n"
"v_fma_f32 v49,v145,v129,v49\n"
"v_fma_f32 v50,v146,v130,v50\n"
"v_fma_f32 v51,v147,v131,v51\n"
"v_fma_f32 v52,v148,v132,v52\n"
"v_fma_f32 v53,v149,v133,v53\n"
"v_fma_f32 v54,v150,v134,v54\n"
"v_fma_f32 v55,v151,v135,v55\n"
C_"v48,v48,v49\n"
C_"v49,v50,v51\n"
C_"v50,v52,v53\n"
C_"v51,v54,v55\n"
"s_nop 0\n"
"v_permlane16_swap_b32 v48,v50\n"
"v_permlane16_swap_b32 v49,v51\n"
S_"v[48:51],v245,s[56:59],s70 offen sc1\n"
W_"vmcnt(1)\n"
"L_p3_end:\n"
"s_endpgm\n"
"L_p3_slow:\n"
W_"vmcnt(11)\n"
"s_cmp_eq_u32 s24,2\n"
"s_cbranch_scc1 L_p3_go\n"
"L_p3_slow2:\n"
A_"s76,s17,8\n"
B_"v[236:239],v255,s[52:55],s20 offen sc1\n"
W_"vmcnt(0)\n"
"s_barrier\n"
"buffer_store_dword v254,off,s[52:55],s76 sc1\n"
"s_mov_b32 s35,0x40000\n"
"L_p3_spin:\n"
"v_cmp_ne_u32 vcc,s32,v236\n"
"v_cmp_eq_u32 s[10:11],s32,v237\n"
"s_or_b64 s[10:11],s[10:11],vcc\n"
"v_cmp_ne_u32 vcc,s32,v238\n"
"s_and_b64 vcc,vcc,s[10:11]\n"
"s_cbranch_scc0 L_p3_rdy\n"
B_"v[236:239],v255,s[52:55],s20 offen sc1\n"
W_"vmcnt(0)\n"
"s_sub_u32 s35,s35,1\n"
"s_cbranch_scc0 L_p3_spin\n"
"L_p3_rdy:\n"
"s_xor_b32 s21,s21,4096\n"
"s_xor_b32 s22,s22,4096\n"
"s_xor_b32 s23,s23,4096\n"
B_"v[0:3],v244,s[52:55],s21 offen offset:0 sc1\n"
B_"v[4:7],v244,s[52:55],s22 offen offset:0 sc1\n"
B_"v[8:11],v244,s[52:55],s23 offen offset:0 sc1\n"
B_"v[12:15],v244,s[52:55],s21 offen offset:1024 sc1\n"
B_"v[16:19],v244,s[52:55],s22 offen offset:1024 sc1\n"
B_"v[20:23],v244,s[52:55],s23 offen offset:1024 sc1\n"
B_"v[24:27],v244,s[52:55],s21 offen offset:2048 sc1\n"
B_"v[28:31],v244,s[52:55],s22 offen offset:2048 sc1\n"
B_"v[64:67],v244,s[52:55],s23 offen offset:2048 sc1\n"
B_"v[68:71],v244,s[52:55],s21 offen offset:3072 sc1\n"
B_"v[72:75],v244,s[52:55],s22 offen offset:3072 sc1\n"
B_"v[76:79],v244,s[52:55],s23 offen offset:3072 sc1\n"
"s_xor_b32 s21,s21,4096\n"
"s_xor_b32 s22,s22,4096\n"
"s_xor_b32 s23,s23,4096\n"
B_"v[80:83],v244,s[52:55],s21 offen offset:0 sc1\n"
B_"v[84:87],v244,s[52:55],s22 offen offset:0 sc1\n"
B_"v[88:91],v244,s[52:55],s23 offen offset:0 sc1\n"
B_"v[92:95],v244,s[52:55],s21 offen offset:1024 sc1\n"
W_"vmcnt(7)\n"
"s_branch L_p3_go\n"
"L_p3_2nd:\n"
"v_cmp_ne_u32 vcc,s32,v236\n"
"v_cmp_eq_u32 s[10:11],s32,v237\n"
"s_or_b64 s[10:11],s[10:11],vcc\n"
"v_cmp_ne_u32 vcc,s32,v238\n"
"s_and_b64 vcc,vcc,s[10:11]\n"
"s_cbranch_scc1 L_p3_2nd_back\n"
"s_mov_b32 s24,2\n"
"s_xor_b32 s21,s21,4096\n"
"s_xor_b32 s22,s22,4096\n"
"s_xor_b32 s23,s23,4096\n"
B_"v[0:3],v244,s[52:55],s21 offen offset:0 sc1\n"
B_"v[4:7],v244,s[52:55],s22 offen offset:0 sc1\n"
B_"v[8:11],v244,s[52:55],s23 offen offset:0 sc1\n"
B_"v[12:15],v244,s[52:55],s21 offen offset:1024 sc1\n"
B_"v[16:19],v244,s[52:55],s22 offen offset:1024 sc1\n"
B_"v[20:23],v244,s[52:55],s23 offen offset:1024 sc1\n"
B_"v[24:27],v244,s[52:55],s21 offen offset:2048 sc1\n"
B_"v[28:31],v244,s[52:55],s22 offen offset:2048 sc1\n"
B_"v[64:67],v244,s[52:55],s23 offen offset:2048 sc1\n"
B_"v[68:71],v244,s[52:55],s21 offen offset:3072 sc1\n"
B_"v[72:75],v244,s[52:55],s22 offen offset:3072 sc1\n"
B_"v[76:79],v244,s[52:55],s23 offen offset:3072 sc1\n"
"s_xor_b32 s21,s21,4096\n"
"s_xor_b32 s22,s22,4096\n"
"s_xor_b32 s23,s23,4096\n"
B_"v[80:83],v244,s[52:55],s21 offen offset:0 sc1\n"
B_"v[84:87],v244,s[52:55],s22 offen offset:0 sc1\n"
B_"v[88:91],v244,s[52:55],s23 offen offset:0 sc1\n"
B_"v[92:95],v244,s[52:55],s21 offen offset:1024 sc1\n"
"s_branch L_p3_2nd_back\n"
"L_k2_a7:\n"
M_"v[96:99],a[192:195],a[160:163],v[200:203]\n"
"s_barrier\n"
E_"v0,v0\n"
M_"v[96:99],a[196:199],a[164:167],v[96:99]\n"
"v_xor_b32 v245,0x10000,v245\n"
"v_xor_b32 v192,0,v255\n"
"v_xor_b32 v193,32,v255\n"
M_"v[96:99],a[200:203],a[168:171],v[96:99]\n"
E_"v1,v1\n"
M_"v[96:99],a[204:207],a[172:175],v[96:99]\n"
"v_xor_b32 v194,64,v255\n"
"v_xor_b32 v195,96,v255\n"
M_"v[112:115],a[192:195],a[176:179],v[204:207]\n"
"v_bfe_u32 v255,v244,4,5\n"
"v_lshlrev_b32 v255,7,v255\n"
M_"v[112:115],a[196:199],a[180:183],v[112:115]\n"
T_"a[128:129],v192 offset:0\n"
E_"v2,v2\n"
M_"v[112:115],a[200:203],a[184:187],v[112:115]\n"
T_"a[130:131],v192 offset:2048\n"
T_"a[132:133],v193 offset:0\n"
M_"v[112:115],a[204:207],a[188:191],v[112:115]\n"
T_"a[134:135],v193 offset:2048\n"
C_"v128,v0,v1\n"
M_"v[80:83],a[192:195],a[144:147],v[224:227]\n"
E_"v3,v3\n"
M_"v[80:83],a[196:199],a[148:151],v[80:83]\n"
T_"a[136:137],v194 offset:0\n"
T_"a[138:139],v194 offset:2048\n"
T_"a[140:141],v195 offset:0\n"
M_"v[80:83],a[200:203],a[152:155],v[80:83]\n"
T_"a[142:143],v195 offset:2048\n"
C_"v129,v2,v3\n"
M_"v[80:83],a[204:207],a[156:159],v[80:83]\n"
"s_xor_b32 s57,s57,0x10000\n"
D_"a[192:195],v245 offset:16384\n"
D_"a[196:199],v245 offset:24576\n"
M_"v[100:103],a[208:211],a[160:163],v[200:203]\n"
A_"m0,s57,16384\n"
D_"a[200:203],v245 offset:17408\n"
D_"a[204:207],v245 offset:25600\n"
M_"v[100:103],a[212:215],a[164:167],v[100:103]\n"
B_"v244,s[44:47],s56 offen offset:0 lds\n"
M_"v[100:103],a[216:219],a[168:171],v[100:103]\n"
E_"v4,v4\n"
M_"v[100:103],a[220:223],a[172:175],v[100:103]\n"
E_"v5,v5\n"
M_"v[116:119],a[208:211],a[176:179],v[204:207]\n"
B_"v244,s[44:47],s56 offen offset:1024 lds\n"
M_"v[116:119],a[212:215],a[180:183],v[116:119]\n"
C_"v130,v4,v5\n"
E_"v6,v6\n"
M_"v[116:119],a[216:219],a[184:187],v[116:119]\n"
E_"v7,v7\n"
M_"v[116:119],a[220:223],a[188:191],v[116:119]\n"
E_"v8,v8\n"
M_"v[84:87],a[208:211],a[144:147],v[228:231]\n"
B_"v244,s[44:47],s56 offen offset:2048 lds\n"
M_"v[84:87],a[212:215],a[148:151],v[84:87]\n"
C_"v131,v6,v7\n"
E_"v9,v9\n"
M_"v[84:87],a[216:219],a[152:155],v[84:87]\n"
E_"v10,v10\n"
M_"v[84:87],a[220:223],a[156:159],v[84:87]\n"
W_"lgkmcnt(12)\n"
D_"a[208:211],v245 offset:18432\n"
D_"a[212:215],v245 offset:26624\n"
M_"v[104:107],a[224:227],a[160:163],v[200:203]\n"
B_"v244,s[44:47],s56 offen offset:3072 lds\n"
M_"v[104:107],a[228:231],a[164:167],v[104:107]\n"
D_"a[216:219],v245 offset:19456\n"
D_"a[220:223],v245 offset:27648\n"
C_"v132,v8,v9\n"
M_"v[104:107],a[232:235],a[168:171],v[104:107]\n"
E_"v11,v11\n"
M_"v[104:107],a[236:239],a[172:175],v[104:107]\n"
A_"s56,s56,16384\n"
E_"v12,v12\n"
M_"v[120:123],a[224:227],a[176:179],v[204:207]\n"
"s_xor_b32 s15,s14,131072\n"
E_"v13,v13\n"
M_"v[120:123],a[228:231],a[180:183],v[120:123]\n"
"s_xor_b32 m0,s16,0\n"
C_"v133,v10,v11\n"
E_"v14,v14\n"
M_"v[120:123],a[232:235],a[184:187],v[120:123]\n"
B_"v249,s[48:51],s15 offen offset:0  lds\n"
M_"v[120:123],a[236:239],a[188:191],v[120:123]\n"
E_"v15,v15\n"
M_"v[88:91],a[224:227],a[144:147],v[232:235]\n"
C_"v134,v12,v13\n"
E_"v16,v16\n"
M_"v[88:91],a[228:231],a[148:151],v[88:91]\n"
B_"v250,s[48:51],s15 offen offset:1024  lds\n"
M_"v[88:91],a[232:235],a[152:155],v[88:91]\n"
E_"v17,v17\n"
M_"v[88:91],a[236:239],a[156:159],v[88:91]\n"
D_"a[224:227],v245 offset:20480\n"
D_"a[228:231],v245 offset:28672\n"
M_"v[108:111],a[240:243],a[160:163],v[200:203]\n"
B_"v251,s[48:51],s15 offen offset:2048  lds\n"
D_"a[232:235],v245 offset:21504\n"
M_"v[108:111],a[244:247],a[164:167],v[108:111]\n"
D_"a[236:239],v245 offset:29696\n"
C_"v135,v14,v15\n"
M_"v[108:111],a[248:251],a[168:171],v[108:111]\n"
E_"v18,v18\n"
M_"v[108:111],a[252:255],a[172:175],v[108:111]\n"
E_"v19,v19\n"
M_"v[124:127],a[240:243],a[176:179],v[204:207]\n"
B_"v253,s[48:51],s15 offen offset:3072  lds\n"
C_"v136,v16,v17\n"
M_"v[124:127],a[244:247],a[180:183],v[124:127]\n"
E_"v20,v20\n"
M_"v[124:127],a[248:251],a[184:187],v[124:127]\n"
E_"v21,v21\n"
M_"v[124:127],a[252:255],a[188:191],v[124:127]\n"
C_"v137,v18,v19\n"
C_"v138,v20,v21\n"
M_"v[92:95],a[240:243],a[144:147],v[236:239]\n"
W_"lgkmcnt(12)\n"
"s_xor_b32 s15,s14,32768\n"
E_"v22,v22\n"
M_"v[92:95],a[244:247],a[148:151],v[92:95]\n"
"s_and_b32 s13,s16,0xffff\n"
E_"v23,v23\n"
M_"v[92:95],a[248:251],a[152:155],v[92:95]\n"
A_"m0,s13,131072\n"
E_"v24,v24\n"
M_"v[92:95],a[252:255],a[156:159],v[92:95]\n"
W_"lgkmcnt(4)\n"
D_"a[240:243],v245 offset:22528\n"
D_"a[244:247],v245 offset:30720\n"
M_"a[0:3],a[192:195],v[160:163],a[0:3]\n"
B_"v249,s[48:51],s15 offen offset:0  lds\n"
D_"a[248:251],v245 offset:23552\n"
M_"a[0:3],a[196:199],v[164:167],a[0:3]\n"
D_"a[252:255],v245 offset:31744\n"
C_"v139,v22,v23\n"
M_"a[4:7],a[192:195],v[168:171],a[4:7]\n"
B_"v250,s[48:51],s15 offen offset:1024  lds\n"
M_"a[4:7],a[196:199],v[172:175],a[4:7]\n"
E_"v25,v25\n"
M_"a[8:11],a[192:195],v[176:179],a[8:11]\n"
C_"v140,v24,v25\n"
E_"v26,v26\n"
M_"a[8:11],a[196:199],v[180:183],a[8:11]\n"
E_"v27,v27\n"
M_"a[12:15],a[192:195],v[184:187],a[12:15]\n"
B_"v251,s[48:51],s15 offen offset:2048  lds\n"
M_"a[12:15],a[196:199],v[188:191],a[12:15]\n"
D_"a[192:195],v245 offset:0\n"
D_"a[196:199],v245 offset:1024\n"
M_"a[16:19],a[200:203],v[160:163],a[16:19]\n"
C_"v141,v26,v27\n"
E_"v28,v28\n"
M_"a[16:19],a[204:207],v[164:167],a[16:19]\n"
E_"v29,v29\n"
M_"a[20:23],a[200:203],v[168:171],a[20:23]\n"
B_"v253,s[48:51],s15 offen offset:3072  lds\n"
M_"a[20:23],a[204:207],v[172:175],a[20:23]\n"
C_"v142,v28,v29\n"
E_"v30,v30\n"
M_"a[24:27],a[200:203],v[176:179],a[24:27]\n"
E_"v31,v31\n"
M_"a[24:27],a[204:207],v[180:183],a[24:27]\n"
"v_mov_b32 v64,-1\n"
C_"v143,v30,v31\n"
M_"a[28:31],a[200:203],v[184:187],a[28:31]\n"
E_"v32,v32\n"
M_"a[28:31],a[204:207],v[188:191],a[28:31]\n"
D_"a[200:203],v245 offset:2048\n"
D_"a[204:207],v245 offset:3072\n"
"v_mov_b32 v65,-1\n"
M_"a[32:35],a[208:211],v[160:163],a[32:35]\n"
"v_mov_b32 v66,-1\n"
"v_mov_b32 v67,-1\n"
M_"a[32:35],a[212:215],v[164:167],a[32:35]\n"
"s_lshl3_add_u32 s13,s30,s31\n"
E_"v33,v33\n"
M_"a[36:39],a[208:211],v[168:171],a[36:39]\n"
"s_lshl_b32 s13,s13,13\n"
E_"v34,v34\n"
M_"a[36:39],a[212:215],v[172:175],a[36:39]\n"
A_"s13,s13,0x5000000\n"
C_"v144,v32,v33\n"
E_"v35,v35\n"
M_"a[40:43],a[208:211],v[176:179],a[40:43]\n"
S_"v[64:67],v244,s[52:55],s13 offen offset:0 sc1\n"
M_"a[40:43],a[212:215],v[180:183],a[40:43]\n"
E_"v36,v36\n"
M_"a[44:47],a[208:211],v[184:187],a[44:47]\n"
S_"v[64:67],v244,s[52:55],s13 offen offset:1024 sc1\n"
M_"a[44:47],a[212:215],v[188:191],a[44:47]\n"
D_"a[208:211],v245 offset:4096\n"
D_"a[212:215],v245 offset:5120\n"
C_"v145,v34,v35\n"
M_"a[48:51],a[216:219],v[160:163],a[48:51]\n"
E_"v37,v37\n"
M_"a[48:51],a[220:223],v[164:167],a[48:51]\n"
E_"v38,v38\n"
M_"a[52:55],a[216:219],v[168:171],a[52:55]\n"
S_"v[64:67],v244,s[52:55],s13 offen offset:2048 sc1\n"
M_"a[52:55],a[220:223],v[172:175],a[52:55]\n"
C_"v146,v36,v37\n"
E_"v39,v39\n"
M_"a[56:59],a[216:219],v[176:179],a[56:59]\n"
E_"v40,v40\n"
M_"a[56:59],a[220:223],v[180:183],a[56:59]\n"
A_"s13,s13,4096\n"
E_"v41,v41\n"
M_"a[60:63],a[216:219],v[184:187],a[60:63]\n"
S_"v[64:67],v244,s[52:55],s13 offen offset:0 sc1\n"
M_"a[60:63],a[220:223],v[188:191],a[60:63]\n"
W_"lgkmcnt(6)\n"
D_"a[216:219],v245 offset:6144\n"
D_"a[220:223],v245 offset:7168\n"
C_"v147,v38,v39\n"
M_"a[64:67],a[224:227],v[160:163],a[64:67]\n"
E_"v42,v42\n"
M_"a[64:67],a[228:231],v[164:167],a[64:67]\n"
E_"v43,v43\n"
M_"a[68:71],a[224:227],v[168:171],a[68:71]\n"
S_"v[64:67],v244,s[52:55],s13 offen offset:1024 sc1\n"
M_"a[68:71],a[228:231],v[172:175],a[68:71]\n"
C_"v148,v40,v41\n"
E_"v44,v44\n"
M_"a[72:75],a[224:227],v[176:179],a[72:75]\n"
E_"v45,v45\n"
M_"a[72:75],a[228:231],v[180:183],a[72:75]\n"
S_"v[64:67],v244,s[52:55],s13 offen offset:2048 sc1\n"
M_"a[76:79],a[224:227],v[184:187],a[76:79]\n"
C_"v149,v42,v43\n"
C_"v150,v44,v45\n"
M_"a[76:79],a[228:231],v[188:191],a[76:79]\n"
D_"a[224:227],v245 offset:8192\n"
E_"v46,v46\n"
M_"a[80:83],a[232:235],v[160:163],a[80:83]\n"
E_"v47,v47\n"
M_"a[80:83],a[236:239],v[164:167],a[80:83]\n"
"s_xor_b32 s15,s14,131200\n"
D_"a[228:231],v245 offset:9216\n"
C_"v151,v46,v47\n"
M_"a[84:87],a[232:235],v[168:171],a[84:87]\n"
"s_xor_b32 m0,s16,32768\n"
E_"v48,v48\n"
M_"a[84:87],a[236:239],v[172:175],a[84:87]\n"
B_"v249,s[48:51],s15 offen offset:0  lds\n"
M_"a[88:91],a[232:235],v[176:179],a[88:91]\n"
E_"v49,v49\n"
M_"a[88:91],a[236:239],v[180:183],a[88:91]\n"
E_"v50,v50\n"
M_"a[92:95],a[232:235],v[184:187],a[92:95]\n"
B_"v250,s[48:51],s15 offen offset:1024  lds\n"
C_"v152,v48,v49\n"
M_"a[92:95],a[236:239],v[188:191],a[92:95]\n"
D_"a[232:235],v245 offset:10240\n"
D_"a[236:239],v245 offset:11264\n"
M_"a[96:99],a[240:243],v[160:163],a[96:99]\n"
E_"v51,v51\n"
M_"a[96:99],a[244:247],v[164:167],a[96:99]\n"
E_"v52,v52\n"
M_"a[100:103],a[240:243],v[168:171],a[100:103]\n"
B_"v251,s[48:51],s15 offen offset:2048  lds\n"
C_"v153,v50,v51\n"
M_"a[100:103],a[244:247],v[172:175],a[100:103]\n"
E_"v53,v53\n"
M_"a[104:107],a[240:243],v[176:179],a[104:107]\n"
E_"v54,v54\n"
M_"a[104:107],a[244:247],v[180:183],a[104:107]\n"
B_"v253,s[48:51],s15 offen offset:3072  lds\n"
M_"a[108:111],a[240:243],v[184:187],a[108:111]\n"
C_"v154,v52,v53\n"
E_"v55,v55\n"
M_"a[108:111],a[244:247],v[188:191],a[108:111]\n"
D_"a[240:243],v245 offset:12288\n"
D_"a[244:247],v245 offset:13312\n"
M_"a[112:115],a[248:251],v[160:163],a[112:115]\n"
E_"v56,v56\n"
M_"a[112:115],a[252:255],v[164:167],a[112:115]\n"
E_"v57,v57\n"
M_"a[116:119],a[248:251],v[168:171],a[116:119]\n"
C_"v155,v54,v55\n"
E_"v58,v58\n"
M_"a[116:119],a[252:255],v[172:175],a[116:119]\n"
E_"v59,v59\n"
M_"a[120:123],a[248:251],v[176:179],a[120:123]\n"
C_"v156,v56,v57\n"
C_"v157,v58,v59\n"
M_"a[120:123],a[252:255],v[180:183],a[120:123]\n"
E_"v60,v60\n"
M_"a[124:127],a[248:251],v[184:187],a[124:127]\n"
E_"v61,v61\n"
M_"a[124:127],a[252:255],v[188:191],a[124:127]\n"
D_"a[248:251],v245 offset:14336\n"
D_"a[252:255],v245 offset:15360\n"
"v_smfmac_f32_16x16x64_bf16 v[208:211],v[240:243],v[160:167],v246\n"
E_"v62,v62\n"
"v_smfmac_f32_16x16x64_bf16 v[212:215],v[240:243],v[168:175],v246\n"
E_"v63,v63\n"
"v_smfmac_f32_16x16x64_bf16 v[216:219],v[240:243],v[176:183],v246\n"
C_"v158,v60,v61\n"
C_"v159,v62,v63\n"
"v_smfmac_f32_16x16x64_bf16 v[220:223],v[240:243],v[184:191],v246\n"
W_"vmcnt(22) lgkmcnt(8)\n"
M_"v[48:51],a[192:195],a[176:179],v[204:207]\n"
"s_barrier\n"
"v_mov_b32 v248,0xff800000\n"
K_"v224,v248,v200,s[64:65]\n"
K_"v225,v248,v200,s[66:67]\n"
M_"v[48:51],a[196:199],a[180:183],v[48:51]\n"
K_"v226,v248,v200,s[68:69]\n"
K_"v227,v248,v200,s[70:71]\n"
K_"v228,v248,v200,s[72:73]\n"
K_"v229,v248,v200,s[74:75]\n"
M_"v[48:51],a[200:203],a[184:187],v[48:51]\n"
"buffer_load_dword v196,v255,s[52:55],s20 offen offset:24 sc1\n"
T_"a[144:145],v192 offset:16384\n"
M_"v[48:51],a[204:207],a[188:191],v[48:51]\n"
K_"v230,v248,v200,s[76:77]\n"
K_"v231,v248,v200,s[78:79]\n"
T_"a[146:147],v192 offset:18432\n"
M_"v[32:35],a[192:195],a[160:163],v[224:227]\n"
T_"a[148:149],v193 offset:16384\n"
T_"a[150:151],v193 offset:18432\n"
K_"v232,v248,v200,s[80:81]\n"
M_"v[32:35],a[196:199],a[164:167],v[32:35]\n"
T_"a[152:153],v194 offset:16384\n"
K_"v233,v248,v200,s[82:83]\n"
K_"v234,v248,v200,s[84:85]\n"
K_"v235,v248,v200,s[86:87]\n"
M_"v[32:35],a[200:203],a[168:171],v[32:35]\n"
T_"a[154:155],v194 offset:18432\n"
K_"v236,v248,v200,s[88:89]\n"
K_"v237,v248,v200,s[90:91]\n"
M_"v[32:35],a[204:207],a[172:175],v[32:35]\n"
D_"a[192:195],v245 offset:49152\n"
D_"a[196:199],v245 offset:57344\n"
D_"a[200:203],v245 offset:50176\n"
M_"v[52:55],a[208:211],a[176:179],v[204:207]\n"
D_"a[204:207],v245 offset:58368\n"
T_"a[156:157],v195 offset:16384\n"
T_"a[158:159],v195 offset:18432\n"
K_"v238,v248,v200,s[92:93]\n"
M_"v[52:55],a[212:215],a[180:183],v[52:55]\n"
A_"m0,s57,49152\n"
K_"v239,v248,v200,s[94:95]\n"
E_"v80,v80\n"
M_"v[52:55],a[216:219],a[184:187],v[52:55]\n"
B_"v244,s[44:47],s56 offen offset:0 lds\n"
M_"v[52:55],a[220:223],a[188:191],v[52:55]\n"
E_"v81,v81\n"
M_"v[36:39],a[208:211],a[160:163],v[228:231]\n"
E_"v82,v82\n"
E_"v83,v83\n"
M_"v[36:39],a[212:215],a[164:167],v[36:39]\n"
C_"v168,v80,v81\n"
E_"v84,v84\n"
M_"v[36:39],a[216:219],a[168:171],v[36:39]\n"
B_"v244,s[44:47],s56 offen offset:1024 lds\n"
C_"v169,v82,v83\n"
M_"v[36:39],a[220:223],a[172:175],v[36:39]\n"
W_"lgkmcnt(12)\n"
D_"a[208:211],v245 offset:51200\n"
D_"a[212:215],v245 offset:59392\n"
D_"a[216:219],v245 offset:52224\n"
M_"v[56:59],a[224:227],a[176:179],v[204:207]\n"
E_"v85,v85\n"
E_"v86,v86\n"
M_"v[56:59],a[228:231],a[180:183],v[56:59]\n"
D_"a[220:223],v245 offset:60416\n"
E_"v87,v87\n"
M_"v[56:59],a[232:235],a[184:187],v[56:59]\n"
C_"v170,v84,v85\n"
E_"v88,v88\n"
M_"v[56:59],a[236:239],a[188:191],v[56:59]\n"
B_"v244,s[44:47],s56 offen offset:2048 lds\n"
E_"v89,v89\n"
M_"v[40:43],a[224:227],a[160:163],v[232:235]\n"
C_"v171,v86,v87\n"
E_"v90,v90\n"
M_"v[40:43],a[228:231],a[164:167],v[40:43]\n"
C_"v172,v88,v89\n"
E_"v91,v91\n"
M_"v[40:43],a[232:235],a[168:171],v[40:43]\n"
C_"v173,v90,v91\n"
E_"v92,v92\n"
M_"v[40:43],a[236:239],a[172:175],v[40:43]\n"
B_"v244,s[44:47],s56 offen offset:3072 lds\n"
D_"a[224:227],v245 offset:53248\n"
D_"a[228:231],v245 offset:61440\n"
M_"v[60:63],a[240:243],a[176:179],v[204:207]\n"
D_"a[232:235],v245 offset:54272\n"
E_"v93,v93\n"
M_"v[60:63],a[244:247],a[180:183],v[60:63]\n"
D_"a[236:239],v245 offset:62464\n"
E_"v94,v94\n"
M_"v[60:63],a[248:251],a[184:187],v[60:63]\n"
E_"v95,v95\n"
E_"v96,v96\n"
M_"v[60:63],a[252:255],a[188:191],v[60:63]\n"
C_"v174,v92,v93\n"
E_"v97,v97\n"
M_"v[44:47],a[240:243],a[160:163],v[236:239]\n"
A_"s56,s56,16384\n"
C_"v175,v94,v95\n"
E_"v98,v98\n"
M_"v[44:47],a[244:247],a[164:167],v[44:47]\n"
W_"lgkmcnt(8)\n"
C_"v176,v96,v97\n"
E_"v99,v99\n"
M_"v[44:47],a[248:251],a[168:171],v[44:47]\n"
"s_xor_b32 s15,s14,32896\n"
E_"v100,v100\n"
E_"v101,v101\n"
M_"v[44:47],a[252:255],a[172:175],v[44:47]\n"
"s_and_b32 s13,s16,0xffff\n"
D_"a[240:243],v245 offset:55296\n"
D_"a[244:247],v245 offset:63488\n"
D_"a[248:251],v245 offset:56320\n"
"v_smfmac_f32_16x16x64_bf16 v[208:211],v[240:243],v[128:135],v246\n"
W_"lgkmcnt(7)\n"
D_"a[252:255],v245 offset:64512\n"
C_"v177,v98,v99\n"
C_"v178,v100,v101\n"
M_"a[0:3],a[192:195],v[128:131],a[0:3]\n"
A_"m0,s13,147456\n"
E_"v102,v102\n"
E_"v103,v103\n"
M_"a[0:3],a[196:199],v[132:135],a[0:3]\n"
B_"v249,s[48:51],s15 offen offset:0  lds\n"
K_"v224,v248,v204,s[64:65]\n"
M_"a[16:19],a[200:203],v[128:131],a[16:19]\n"
K_"v225,v248,v204,s[66:67]\n"
K_"v226,v248,v204,s[68:69]\n"
K_"v227,v248,v204,s[70:71]\n"
M_"a[16:19],a[204:207],v[132:135],a[16:19]\n"
"v_add_f32 v208,v208,v211\n"
"v_rcp_f32 v247,v208\n"
M_"a[32:35],a[208:211],v[128:131],a[32:35]\n"
B_"v250,s[48:51],s15 offen offset:1024  lds\n"
"v_cmp_nge_f32 vcc,s60,v208\n"
K_"v228,v248,v204,s[72:73]\n"
M_"a[32:35],a[212:215],v[132:135],a[32:35]\n"
"s_or_b64 s[62:63],s[62:63],vcc\n"
R_"v0,a0\n"
R_"v1,a1\n"
R_"v2,a2\n"
M_"a[48:51],a[216:219],v[128:131],a[48:51]\n"
R_"v3,a3\n"
K_"v229,v248,v204,s[74:75]\n"
K_"v230,v248,v204,s[76:77]\n"
M_"a[48:51],a[220:223],v[132:135],a[48:51]\n"
W_"lgkmcnt(4)\n"
R_"v4,a16\n"
R_"v5,a17\n"
R_"v6,a18\n"
M_"a[64:67],a[224:227],v[128:131],a[64:67]\n"
R_"v7,a19\n"
U_"v0,v247,v0\n"
U_"v1,v247,v1\n"
U_"v2,v247,v2\n"
M_"a[64:67],a[228:231],v[132:135],a[64:67]\n"
B_"v251,s[48:51],s15 offen offset:2048  lds\n"
U_"v3,v247,v3\n"
M_"a[80:83],a[232:235],v[128:131],a[80:83]\n"
U_"v4,v247,v4\n"
U_"v5,v247,v5\n"
U_"v6,v247,v6\n"
M_"a[80:83],a[236:239],v[132:135],a[80:83]\n"
W_"lgkmcnt(0)\n"
U_"v7,v247,v7\n"
C_"v0,v0,v1\n"
C_"v1,v2,v3\n"
C_"v2,v4,v5\n"
M_"a[96:99],a[240:243],v[128:131],a[96:99]\n"
C_"v3,v6,v7\n"
K_"v231,v248,v204,s[78:79]\n"
K_"v232,v248,v204,s[80:81]\n"
M_"a[96:99],a[244:247],v[132:135],a[96:99]\n"
B_"v253,s[48:51],s15 offen offset:3072  lds\n"
K_"v233,v248,v204,s[82:83]\n"
M_"a[112:115],a[248:251],v[128:131],a[112:115]\n"
S_"v[0:3],v244,s[52:55],s96 offen offset:0 sc1\n"
R_"v8,a32\n"
M_"a[112:115],a[252:255],v[132:135],a[112:115]\n"
R_"v9,a33\n"
R_"v10,a34\n"
R_"v11,a35\n"
R_"v12,a48\n"
M_"a[4:7],a[192:195],v[136:139],a[4:7]\n"
R_"v13,a49\n"
R_"v14,a50\n"
R_"v15,a51\n"
M_"a[4:7],a[196:199],v[140:143],a[4:7]\n"
U_"v8,v247,v8\n"
U_"v9,v247,v9\n"
U_"v10,v247,v10\n"
M_"a[8:11],a[192:195],v[144:147],a[8:11]\n"
"s_xor_b32 m0,s57,0x14000\n"
U_"v11,v247,v11\n"
U_"v12,v247,v12\n"
U_"v13,v247,v13\n"
U_"v14,v247,v14\n"
M_"a[8:11],a[196:199],v[148:151],a[8:11]\n"
B_"v244,s[44:47],s56 offen offset:0 lds\n"
U_"v15,v247,v15\n"
M_"a[12:15],a[192:195],v[152:155],a[12:15]\n"
C_"v8,v8,v9\n"
C_"v9,v10,v11\n"
C_"v10,v12,v13\n"
M_"a[12:15],a[196:199],v[156:159],a[12:15]\n"
D_"a[192:195],v245 offset:32768\n"
D_"a[196:199],v245 offset:33792\n"
C_"v11,v14,v15\n"
M_"a[20:23],a[200:203],v[136:139],a[20:23]\n"
S_"v[8:11],v244,s[52:55],s96 offen offset:1024 sc1\n"
R_"v0,a64\n"
R_"v1,a65\n"
M_"a[20:23],a[204:207],v[140:143],a[20:23]\n"
B_"v244,s[44:47],s56 offen offset:1024 lds\n"
R_"v2,a66\n"
M_"a[24:27],a[200:203],v[144:147],a[24:27]\n"
R_"v3,a67\n"
R_"v4,a80\n"
R_"v5,a81\n"
M_"a[24:27],a[204:207],v[148:151],a[24:27]\n"
R_"v6,a82\n"
R_"v7,a83\n"
U_"v0,v247,v0\n"
U_"v1,v247,v1\n"
M_"a[28:31],a[200:203],v[152:155],a[28:31]\n"
U_"v2,v247,v2\n"
U_"v3,v247,v3\n"
U_"v4,v247,v4\n"
M_"a[28:31],a[204:207],v[156:159],a[28:31]\n"
D_"a[200:203],v245 offset:34816\n"
D_"a[204:207],v245 offset:35840\n"
U_"v5,v247,v5\n"
M_"a[36:39],a[208:211],v[136:139],a[36:39]\n"
B_"v244,s[44:47],s56 offen offset:2048 lds\n"
U_"v6,v247,v6\n"
M_"a[36:39],a[212:215],v[140:143],a[36:39]\n"
U_"v7,v247,v7\n"
C_"v0,v0,v1\n"
C_"v1,v2,v3\n"
C_"v2,v4,v5\n"
M_"a[40:43],a[208:211],v[144:147],a[40:43]\n"
C_"v3,v6,v7\n"
K_"v234,v248,v204,s[84:85]\n"
K_"v235,v248,v204,s[86:87]\n"
M_"a[40:43],a[212:215],v[148:151],a[40:43]\n"
S_"v[0:3],v244,s[52:55],s96 offen offset:2048 sc1\n"
R_"v8,a96\n"
M_"a[44:47],a[208:211],v[152:155],a[44:47]\n"
B_"v244,s[44:47],s56 offen offset:3072 lds\n"
R_"v9,a97\n"
M_"a[44:47],a[212:215],v[156:159],a[44:47]\n"
D_"a[208:211],v245 offset:36864\n"
D_"a[212:215],v245 offset:37888\n"
R_"v10,a98\n"
R_"v11,a99\n"
M_"a[52:55],a[216:219],v[136:139],a[52:55]\n"
R_"v12,a112\n"
R_"v13,a113\n"
R_"v14,a114\n"
M_"a[52:55],a[220:223],v[140:143],a[52:55]\n"
R_"v15,a115\n"
U_"v8,v247,v8\n"
U_"v9,v247,v9\n"
M_"a[56:59],a[216:219],v[144:147],a[56:59]\n"
U_"v10,v247,v10\n"
U_"v11,v247,v11\n"
U_"v12,v247,v12\n"
U_"v13,v247,v13\n"
M_"a[56:59],a[220:223],v[148:151],a[56:59]\n"
"s_xor_b32 s15,s14,163840\n"
U_"v14,v247,v14\n"
U_"v15,v247,v15\n"
C_"v8,v8,v9\n"
M_"a[60:63],a[216:219],v[152:155],a[60:63]\n"
"s_xor_b32 m0,s16,65536\n"
K_"v236,v248,v204,s[88:89]\n"
K_"v237,v248,v204,s[90:91]\n"
K_"v238,v248,v204,s[92:93]\n"
M_"a[60:63],a[220:223],v[156:159],a[60:63]\n"
B_"v249,s[48:51],s15 offen offset:0  lds\n"
D_"a[216:219],v245 offset:38912\n"
M_"a[68:71],a[224:227],v[136:139],a[68:71]\n"
D_"a[220:223],v245 offset:39936\n"
K_"v239,v248,v204,s[94:95]\n"
C_"v9,v10,v11\n"
C_"v10,v12,v13\n"
M_"a[68:71],a[228:231],v[140:143],a[68:71]\n"
C_"v11,v14,v15\n"
E_"v104,v104\n"
M_"a[72:75],a[224:227],v[144:147],a[72:75]\n"
S_"v[8:11],v244,s[52:55],s96 offen offset:3072 sc1\n"
C_"v179,v102,v103\n"
M_"a[72:75],a[228:231],v[148:151],a[72:75]\n"
B_"v250,s[48:51],s15 offen offset:1024  lds\n"
E_"v105,v105\n"
M_"a[76:79],a[224:227],v[152:155],a[76:79]\n"
C_"v180,v104,v105\n"
E_"v106,v106\n"
M_"a[76:79],a[228:231],v[156:159],a[76:79]\n"
D_"a[224:227],v245 offset:40960\n"
E_"v107,v107\n"
M_"a[84:87],a[232:235],v[136:139],a[84:87]\n"
D_"a[228:231],v245 offset:41984\n"
E_"v108,v108\n"
M_"a[84:87],a[236:239],v[140:143],a[84:87]\n"
E_"v109,v109\n"
E_"v110,v110\n"
M_"a[88:91],a[232:235],v[144:147],a[88:91]\n"
B_"v251,s[48:51],s15 offen offset:2048  lds\n"
C_"v181,v106,v107\n"
M_"a[88:91],a[236:239],v[148:151],a[88:91]\n"
C_"v182,v108,v109\n"
E_"v111,v111\n"
M_"a[92:95],a[232:235],v[152:155],a[92:95]\n"
E_"v112,v112\n"
E_"v113,v113\n"
M_"a[92:95],a[236:239],v[156:159],a[92:95]\n"
D_"a[232:235],v245 offset:43008\n"
D_"a[236:239],v245 offset:44032\n"
C_"v183,v110,v111\n"
M_"a[100:103],a[240:243],v[136:139],a[100:103]\n"
B_"v253,s[48:51],s15 offen offset:3072  lds\n"
C_"v184,v112,v113\n"
M_"a[100:103],a[244:247],v[140:143],a[100:103]\n"
E_"v114,v114\n"
M_"a[104:107],a[240:243],v[144:147],a[104:107]\n"
E_"v115,v115\n"
E_"v116,v116\n"
M_"a[104:107],a[244:247],v[148:151],a[104:107]\n"
C_"v185,v114,v115\n"
E_"v117,v117\n"
M_"a[108:111],a[240:243],v[152:155],a[108:111]\n"
C_"v186,v116,v117\n"
E_"v118,v118\n"
M_"a[108:111],a[244:247],v[156:159],a[108:111]\n"
D_"a[240:243],v245 offset:45056\n"
E_"v119,v119\n"
M_"a[116:119],a[248:251],v[136:139],a[116:119]\n"
D_"a[244:247],v245 offset:46080\n"
C_"v187,v118,v119\n"
E_"v120,v120\n"
M_"a[116:119],a[252:255],v[140:143],a[116:119]\n"
E_"v121,v121\n"
M_"a[120:123],a[248:251],v[144:147],a[120:123]\n"
C_"v188,v120,v121\n"
E_"v122,v122\n"
M_"a[120:123],a[252:255],v[148:151],a[120:123]\n"
E_"v123,v123\n"
E_"v124,v124\n"
M_"a[124:127],a[248:251],v[152:155],a[124:127]\n"
C_"v189,v122,v123\n"
E_"v125,v125\n"
M_"a[124:127],a[252:255],v[156:159],a[124:127]\n"
D_"a[248:251],v245 offset:47104\n"
D_"a[252:255],v245 offset:48128\n"
C_"v190,v124,v125\n"
"v_smfmac_f32_16x16x64_bf16 v[212:215],v[240:243],v[136:143],v246\n"
E_"v126,v126\n"
"v_smfmac_f32_16x16x64_bf16 v[216:219],v[240:243],v[144:151],v246\n"
E_"v127,v127\n"
"v_smfmac_f32_16x16x64_bf16 v[220:223],v[240:243],v[152:159],v246\n"
C_"v191,v126,v127\n"
W_"vmcnt(21) lgkmcnt(8)\n"
M_"v[112:115],a[192:195],a[176:179],v[224:227]\n"
"s_barrier\n"
E_"v32,v32\n"
M_"v[112:115],a[196:199],a[180:183],v[112:115]\n"
"s_xor_b32 s57,s57,0x10000\n"
"v_xor_b32 v245,0x10000,v245\n"
W_"vmcnt(12)\n"
E_"v33,v33\n"
M_"v[112:115],a[200:203],a[184:187],v[112:115]\n"
T_"a[0:1],v192 offset:0\n"
T_"a[2:3],v192 offset:2048\n"
C_"v144,v32,v33\n"
M_"v[112:115],a[204:207],a[188:191],v[112:115]\n"
D_"a[192:195],v245 offset:16384\n"
D_"a[196:199],v245 offset:24576\n"
D_"a[200:203],v245 offset:17408\n"
D_"a[204:207],v245 offset:25600\n"
M_"v[116:119],a[208:211],a[176:179],v[228:231]\n"
T_"a[16:17],v193 offset:0\n"
T_"a[18:19],v193 offset:2048\n"
T_"a[32:33],v194 offset:0\n"
M_"v[116:119],a[212:215],a[180:183],v[116:119]\n"
T_"a[34:35],v194 offset:2048\n"
T_"a[48:49],v195 offset:0\n"
E_"v34,v34\n"
M_"v[116:119],a[216:219],a[184:187],v[116:119]\n"
T_"a[50:51],v195 offset:2048\n"
T_"a[64:65],v192 offset:16384\n"
T_"a[66:67],v192 offset:18432\n"
M_"v[116:119],a[220:223],a[188:191],v[116:119]\n"
W_"lgkmcnt(14)\n"
D_"a[208:211],v245 offset:18432\n"
D_"a[212:215],v245 offset:26624\n"
D_"a[216:219],v245 offset:19456\n"
D_"a[220:223],v245 offset:27648\n"
M_"v[120:123],a[224:227],a[176:179],v[232:235]\n"
T_"a[80:81],v193 offset:16384\n"
T_"a[82:83],v193 offset:18432\n"
T_"a[96:97],v194 offset:16384\n"
M_"v[120:123],a[228:231],a[180:183],v[120:123]\n"
T_"a[98:99],v194 offset:18432\n"
T_"a[112:113],v195 offset:16384\n"
T_"a[114:115],v195 offset:18432\n"
M_"v[120:123],a[232:235],a[184:187],v[120:123]\n"
W_"lgkmcnt(0)\n"
E_"v35,v35\n"
E_"v36,v36\n"
M_"v[120:123],a[236:239],a[188:191],v[120:123]\n"
"s_xor_b32 s15,s14,65536\n"
D_"a[224:227],v245 offset:20480\n"
D_"a[228:231],v245 offset:28672\n"
D_"a[232:235],v245 offset:21504\n"
M_"v[124:127],a[240:243],a[176:179],v[236:239]\n"
"s_and_b32 s13,s16,0xffff\n"
D_"a[236:239],v245 offset:29696\n"
C_"v145,v34,v35\n"
E_"v37,v37\n"
M_"v[124:127],a[244:247],a[180:183],v[124:127]\n"
A_"m0,s13,131072\n"
C_"v146,v36,v37\n"
E_"v38,v38\n"
M_"v[124:127],a[248:251],a[184:187],v[124:127]\n"
B_"v249,s[48:51],s15 offen offset:0  lds\n"
E_"v39,v39\n"
M_"v[124:127],a[252:255],a[188:191],v[124:127]\n"
B_"v250,s[48:51],s15 offen offset:1024  lds\n"
D_"a[240:243],v245 offset:22528\n"
"v_smfmac_f32_16x16x64_bf16 v[212:215],v[240:243],v[168:175],v246\n"
D_"a[244:247],v245 offset:30720\n"
D_"a[248:251],v245 offset:23552\n"
D_"a[252:255],v245 offset:31744\n"
C_"v147,v38,v39\n"
M_"a[4:7],a[192:195],v[168:171],a[4:7]\n"
B_"v251,s[48:51],s15 offen offset:2048  lds\n"
M_"a[4:7],a[196:199],v[172:175],a[4:7]\n"
E_"v40,v40\n"
E_"v41,v41\n"
M_"a[20:23],a[200:203],v[168:171],a[20:23]\n"
B_"v253,s[48:51],s15 offen offset:3072  lds\n"
C_"v148,v40,v41\n"
M_"a[20:23],a[204:207],v[172:175],a[20:23]\n"
"v_add_f32 v212,v212,v215\n"
"v_rcp_f32 v248,v212\n"
M_"a[36:39],a[208:211],v[168:171],a[36:39]\n"
"v_cmp_nge_f32 vcc,s60,v212\n"
"s_or_b64 s[62:63],s[62:63],vcc\n"
E_"v42,v42\n"
M_"a[36:39],a[212:215],v[172:175],a[36:39]\n"
"s_or_b64 s[62:63],s[62:63],%8\n"
"s_cselect_b32 s25,1,0\n"
"s_cselect_b32 s12,s12,0x7f000000\n"
"s_mov_b64 s[62:63],0\n"
"buffer_store_dword v254,off,s[52:55],s12 sc1\n"
R_"v16,a4\n"
M_"a[52:55],a[216:219],v[168:171],a[52:55]\n"
"s_xor_b32 s15,s14,65664\n"
R_"v17,a5\n"
R_"v18,a6\n"
R_"v19,a7\n"
"s_and_b32 s13,s16,0xffff\n"
M_"a[52:55],a[220:223],v[172:175],a[52:55]\n"
W_"lgkmcnt(4)\n"
R_"v20,a20\n"
R_"v21,a21\n"
R_"v22,a22\n"
R_"v23,a23\n"
M_"a[68:71],a[224:227],v[168:171],a[68:71]\n"
A_"m0,s13,147456\n"
U_"v16,v248,v16\n"
U_"v17,v248,v17\n"
U_"v18,v248,v18\n"
U_"v19,v248,v19\n"
M_"a[68:71],a[228:231],v[172:175],a[68:71]\n"
U_"v20,v248,v20\n"
U_"v21,v248,v21\n"
U_"v22,v248,v22\n"
M_"a[84:87],a[232:235],v[168:171],a[84:87]\n"
U_"v23,v248,v23\n"
C_"v16,v16,v17\n"
C_"v17,v18,v19\n"
C_"v18,v20,v21\n"
M_"a[84:87],a[236:239],v[172:175],a[84:87]\n"
W_"lgkmcnt(0)\n"
C_"v19,v22,v23\n"
S_"v[16:19],v244,s[52:55],s97 offen offset:0 sc1\n"
R_"v24,a36\n"
M_"a[100:103],a[240:243],v[168:171],a[100:103]\n"
R_"v25,a37\n"
R_"v26,a38\n"
R_"v27,a39\n"
R_"v28,a52\n"
M_"a[100:103],a[244:247],v[172:175],a[100:103]\n"
R_"v29,a53\n"
R_"v30,a54\n"
R_"v31,a55\n"
U_"v24,v248,v24\n"
M_"a[116:119],a[248:251],v[168:171],a[116:119]\n"
B_"v249,s[48:51],s15 offen offset:0  lds\n"
U_"v25,v248,v25\n"
U_"v26,v248,v26\n"
U_"v27,v248,v27\n"
M_"a[116:119],a[252:255],v[172:175],a[116:119]\n"
U_"v28,v248,v28\n"
U_"v29,v248,v29\n"
U_"v30,v248,v30\n"
U_"v31,v248,v31\n"
M_"a[8:11],a[192:195],v[176:179],a[8:11]\n"
C_"v24,v24,v25\n"
C_"v25,v26,v27\n"
C_"v26,v28,v29\n"
M_"a[8:11],a[196:199],v[180:183],a[8:11]\n"
B_"v250,s[48:51],s15 offen offset:1024  lds\n"
C_"v27,v30,v31\n"
S_"v[24:27],v244,s[52:55],s97 offen offset:1024 sc1\n"
R_"v16,a68\n"
R_"v17,a69\n"
M_"a[12:15],a[192:195],v[184:187],a[12:15]\n"
R_"v18,a70\n"
R_"v19,a71\n"
R_"v20,a84\n"
M_"a[12:15],a[196:199],v[188:191],a[12:15]\n"
B_"v251,s[48:51],s15 offen offset:2048  lds\n"
R_"v21,a85\n"
R_"v22,a86\n"
R_"v23,a87\n"
U_"v16,v248,v16\n"
M_"a[24:27],a[200:203],v[176:179],a[24:27]\n"
U_"v17,v248,v17\n"
U_"v18,v248,v18\n"
U_"v19,v248,v19\n"
U_"v20,v248,v20\n"
M_"a[24:27],a[204:207],v[180:183],a[24:27]\n"
U_"v21,v248,v21\n"
U_"v22,v248,v22\n"
U_"v23,v248,v23\n"
M_"a[28:31],a[200:203],v[184:187],a[28:31]\n"
B_"v253,s[48:51],s15 offen offset:3072  lds\n"
C_"v16,v16,v17\n"
C_"v17,v18,v19\n"
C_"v18,v20,v21\n"
C_"v19,v22,v23\n"
M_"a[28:31],a[204:207],v[188:191],a[28:31]\n"
S_"v[16:19],v244,s[52:55],s97 offen offset:2048 sc1\n"
R_"v24,a100\n"
R_"v25,a101\n"
M_"a[40:43],a[208:211],v[176:179],a[40:43]\n"
"s_xor_b32 s15,s14,163968\n"
R_"v26,a102\n"
R_"v27,a103\n"
R_"v28,a116\n"
R_"v29,a117\n"
M_"a[40:43],a[212:215],v[180:183],a[40:43]\n"
"s_xor_b32 m0,s16,98304\n"
R_"v30,a118\n"
R_"v31,a119\n"
U_"v24,v248,v24\n"
U_"v25,v248,v25\n"
M_"a[44:47],a[208:211],v[184:187],a[44:47]\n"
B_"v249,s[48:51],s15 offen offset:0  lds\n"
U_"v26,v248,v26\n"
U_"v27,v248,v27\n"
U_"v28,v248,v28\n"
M_"a[44:47],a[212:215],v[188:191],a[44:47]\n"
U_"v29,v248,v29\n"
U_"v30,v248,v30\n"
U_"v31,v248,v31\n"
C_"v24,v24,v25\n"
M_"a[56:59],a[216:219],v[176:179],a[56:59]\n"
B_"v250,s[48:51],s15 offen offset:1024  lds\n"
C_"v25,v26,v27\n"
C_"v26,v28,v29\n"
C_"v27,v30,v31\n"
M_"a[56:59],a[220:223],v[180:183],a[56:59]\n"
S_"v[24:27],v244,s[52:55],s97 offen offset:3072 sc1\n"
M_"a[60:63],a[216:219],v[184:187],a[60:63]\n"
B_"v251,s[48:51],s15 offen offset:2048  lds\n"
M_"a[60:63],a[220:223],v[188:191],a[60:63]\n"
B_"v253,s[48:51],s15 offen offset:3072  lds\n"
E_"v43,v43\n"
M_"a[72:75],a[224:227],v[176:179],a[72:75]\n"
"v_cmp_ne_u32 s[10:11],s32,v196\n"
E_"v44,v44\n"
M_"a[72:75],a[228:231],v[180:183],a[72:75]\n"
"s_cmp_lg_u64 s[10:11],0\n"
"s_cbranch_scc1 L_slow_PF7\n"
"L_back_PF7:\n"
E_"v45,v45\n"
M_"a[76:79],a[224:227],v[184:187],a[76:79]\n"
"s_mov_b32 s13,s18\n"
C_"v149,v42,v43\n"
E_"v46,v46\n"
M_"a[76:79],a[228:231],v[188:191],a[76:79]\n"
B_"v[128:131],v244,s[52:55],s13 offen sc1\n"
E_"v47,v47\n"
M_"a[88:91],a[232:235],v[176:179],a[88:91]\n"
A_"s13,s13,0x200000\n"
C_"v150,v44,v45\n"
E_"v48,v48\n"
M_"a[88:91],a[236:239],v[180:183],a[88:91]\n"
B_"v[132:135],v244,s[52:55],s13 offen sc1\n"
E_"v49,v49\n"
M_"a[92:95],a[232:235],v[184:187],a[92:95]\n"
A_"s13,s13,0x200000\n"
C_"v151,v46,v47\n"
E_"v50,v50\n"
M_"a[92:95],a[236:239],v[188:191],a[92:95]\n"
B_"v[136:139],v244,s[52:55],s13 offen sc1\n"
E_"v51,v51\n"
M_"a[104:107],a[240:243],v[176:179],a[104:107]\n"
A_"s13,s13,0x200000\n"
C_"v152,v48,v49\n"
E_"v52,v52\n"
M_"a[104:107],a[244:247],v[180:183],a[104:107]\n"
B_"v[140:143],v244,s[52:55],s13 offen sc1\n"
C_"v153,v50,v51\n"
M_"a[108:111],a[240:243],v[184:187],a[108:111]\n"
E_"v53,v53\n"
E_"v54,v54\n"
M_"a[108:111],a[244:247],v[188:191],a[108:111]\n"
C_"v154,v52,v53\n"
E_"v55,v55\n"
M_"a[120:123],a[248:251],v[176:179],a[120:123]\n"
E_"v56,v56\n"
E_"v57,v57\n"
M_"a[120:123],a[252:255],v[180:183],a[120:123]\n"
C_"v155,v54,v55\n"
E_"v58,v58\n"
M_"a[124:127],a[248:251],v[184:187],a[124:127]\n"
E_"v59,v59\n"
E_"v60,v60\n"
M_"a[124:127],a[252:255],v[188:191],a[124:127]\n"
C_"v156,v56,v57\n"
E_"v61,v61\n"
"v_smfmac_f32_16x16x64_bf16 v[216:219],v[240:243],v[176:183],v246\n"
C_"v157,v58,v59\n"
C_"v158,v60,v61\n"
E_"v62,v62\n"
"v_smfmac_f32_16x16x64_bf16 v[220:223],v[240:243],v[184:191],v246\n"
W_"vmcnt(6)\n"
E_"v63,v63\n"
"s_nop 0\n"
C_"v159,v62,v63\n"
"v_smfmac_f32_16x16x64_bf16 v[216:219],v[240:243],v[144:151],v246\n"
"s_barrier\n"
D_"a[192:195],v245 offset:49152\n"
D_"a[196:199],v245 offset:57344\n"
D_"a[200:203],v245 offset:50176\n"
"v_smfmac_f32_16x16x64_bf16 v[220:223],v[240:243],v[152:159],v246\n"
W_"lgkmcnt(1)\n"
D_"a[204:207],v245 offset:58368\n"
D_"a[208:211],v245 offset:51200\n"
D_"a[212:215],v245 offset:59392\n"
D_"a[216:219],v245 offset:52224\n"
M_"a[8:11],a[192:195],v[144:147],a[8:11]\n"
"buffer_store_dword v254,off,s[52:55],s17 sc1\n"
D_"a[220:223],v245 offset:60416\n"
M_"a[8:11],a[196:199],v[148:151],a[8:11]\n"
A_"s13,s18,s19\n"
D_"a[224:227],v245 offset:53248\n"
D_"a[228:231],v245 offset:61440\n"
D_"a[232:235],v245 offset:54272\n"
D_"a[236:239],v245 offset:62464\n"
M_"a[12:15],a[192:195],v[152:155],a[12:15]\n"
B_"v[160:163],v244,s[52:55],s13 offen sc1\n"
D_"a[240:243],v245 offset:55296\n"
M_"a[12:15],a[196:199],v[156:159],a[12:15]\n"
W_"lgkmcnt(9)\n"
D_"a[244:247],v245 offset:63488\n"
D_"a[248:251],v245 offset:56320\n"
D_"a[252:255],v245 offset:64512\n"
"v_bfe_u32 v212,v244,6,2\n"
M_"a[24:27],a[200:203],v[144:147],a[24:27]\n"
A_"s13,s13,0x200000\n"
"v_lshrrev_b32 v213,8,v244\n"
"v_cmp_eq_u32 s[10:11],v212,v213\n"
"v_mov_b32 v208,0x3f80\n"
M_"a[24:27],a[204:207],v[148:151],a[24:27]\n"
B_"v[164:167],v244,s[52:55],s13 offen sc1\n"
"v_mov_b32 v209,0\n"
"v_and_b32 v212,48,v244\n"
M_"a[28:31],a[200:203],v[152:155],a[28:31]\n"
A_"s13,s13,0x200000\n"
K_"v208,0,v208,s[10:11]\n"
"v_lshlrev_b64 v[210:211],v212,v[208:209]\n"
"v_mov_b32 v208,0\n"
M_"a[28:31],a[204:207],v[156:159],a[28:31]\n"
W_"lgkmcnt(10)\n"
"v_mov_b32 v212,0\n"
"v_mov_b32 v213,0\n"
E_"v112,v112\n"
M_"a[40:43],a[208:211],v[144:147],a[40:43]\n"
B_"v[168:171],v244,s[52:55],s13 offen sc1\n"
A_"s13,s13,0x200000\n"
M_"a[40:43],a[212:215],v[148:151],a[40:43]\n"
B_"v[172:175],v244,s[52:55],s13 offen sc1\n"
E_"v113,v113\n"
M_"a[44:47],a[208:211],v[152:155],a[44:47]\n"
T_"a[4:5],v192 offset:0\n"
T_"a[6:7],v192 offset:2048\n"
T_"a[20:21],v193 offset:0\n"
M_"a[44:47],a[212:215],v[156:159],a[44:47]\n"
W_"lgkmcnt(11)\n"
T_"a[22:23],v193 offset:2048\n"
T_"a[36:37],v194 offset:0\n"
T_"a[38:39],v194 offset:2048\n"
T_"a[52:53],v195 offset:0\n"
M_"a[56:59],a[216:219],v[144:147],a[56:59]\n"
T_"a[54:55],v195 offset:2048\n"
T_"a[68:69],v192 offset:16384\n"
T_"a[70:71],v192 offset:18432\n"
M_"a[56:59],a[220:223],v[148:151],a[56:59]\n"
T_"a[84:85],v193 offset:16384\n"
T_"a[86:87],v193 offset:18432\n"
T_"a[100:101],v194 offset:16384\n"
T_"a[102:103],v194 offset:18432\n"
M_"a[60:63],a[216:219],v[152:155],a[60:63]\n"
T_"a[116:117],v195 offset:16384\n"
T_"a[118:119],v195 offset:18432\n"
C_"v184,v112,v113\n"
M_"a[60:63],a[220:223],v[156:159],a[60:63]\n"
W_"lgkmcnt(15)\n"
E_"v114,v114\n"
E_"v115,v115\n"
M_"a[72:75],a[224:227],v[144:147],a[72:75]\n"
W_"lgkmcnt(0)\n"
C_"v185,v114,v115\n"
E_"v116,v116\n"
M_"a[72:75],a[228:231],v[148:151],a[72:75]\n"
"s_xor_b32 s15,s14,98304\n"
E_"v117,v117\n"
E_"v118,v118\n"
M_"a[76:79],a[224:227],v[152:155],a[76:79]\n"
"s_and_b32 s13,s16,0xffff\n"
A_"m0,s13,131072\n"
E_"v119,v119\n"
M_"a[76:79],a[228:231],v[156:159],a[76:79]\n"
B_"v249,s[48:51],s15 offen offset:0  lds\n"
C_"v186,v116,v117\n"
M_"a[88:91],a[232:235],v[144:147],a[88:91]\n"
B_"v250,s[48:51],s15 offen offset:1024  lds\n"
C_"v187,v118,v119\n"
M_"a[88:91],a[236:239],v[148:151],a[88:91]\n"
B_"v251,s[48:51],s15 offen offset:2048  lds\n"
E_"v120,v120\n"
M_"a[92:95],a[232:235],v[152:155],a[92:95]\n"
B_"v253,s[48:51],s15 offen offset:3072  lds\n"
"s_xor_b32 s15,s14,98432\n"
M_"a[92:95],a[236:239],v[156:159],a[92:95]\n"
E_"v121,v121\n"
E_"v122,v122\n"
M_"a[104:107],a[240:243],v[144:147],a[104:107]\n"
"s_and_b32 s13,s16,0xffff\n"
C_"v188,v120,v121\n"
E_"v123,v123\n"
M_"a[104:107],a[244:247],v[148:151],a[104:107]\n"
A_"m0,s13,147456\n"
E_"v124,v124\n"
E_"v125,v125\n"
M_"a[108:111],a[240:243],v[152:155],a[108:111]\n"
B_"v249,s[48:51],s15 offen offset:0  lds\n"
C_"v189,v122,v123\n"
M_"a[108:111],a[244:247],v[156:159],a[108:111]\n"
B_"v250,s[48:51],s15 offen offset:1024  lds\n"
C_"v190,v124,v125\n"
M_"a[120:123],a[248:251],v[144:147],a[120:123]\n"
B_"v251,s[48:51],s15 offen offset:2048  lds\n"
M_"a[120:123],a[252:255],v[148:151],a[120:123]\n"
B_"v253,s[48:51],s15 offen offset:3072  lds\n"
E_"v126,v126\n"
M_"a[124:127],a[248:251],v[152:155],a[124:127]\n"
B_"v[236:239],v255,s[52:55],s20 offen sc1\n"
M_"a[124:127],a[252:255],v[156:159],a[124:127]\n"
E_"v127,v127\n"
"s_nop 0\n"
C_"v191,v126,v127\n"
W_"vmcnt(63)\n"
"s_nop 1\n"
"v_smfmac_f32_16x16x64_bf16 v[220:223],v[240:243],v[184:191],v246\n"
"s_barrier\n"
"v_xor_b32 v245,0x10000,v245\n"
D_"a[192:195],v245 offset:16384\n"
D_"a[196:199],v245 offset:24576\n"
D_"a[200:203],v245 offset:17408\n"
D_"a[204:207],v245 offset:25600\n"
D_"a[208:211],v245 offset:18432\n"
D_"a[212:215],v245 offset:26624\n"
D_"a[216:219],v245 offset:19456\n"
D_"a[220:223],v245 offset:27648\n"
W_"lgkmcnt(6)\n"
M_"a[12:15],a[192:195],v[184:187],a[12:15]\n"
"s_xor_b32 s57,s57,0x10000\n"
D_"a[224:227],v245 offset:20480\n"
D_"a[228:231],v245 offset:28672\n"
D_"a[232:235],v245 offset:21504\n"
D_"a[236:239],v245 offset:29696\n"
D_"a[240:243],v245 offset:22528\n"
D_"a[244:247],v245 offset:30720\n"
D_"a[248:251],v245 offset:23552\n"
D_"a[252:255],v245 offset:31744\n"
"v_add_f32 v216,v216,v219\n"
"v_rcp_f32 v247,v216\n"
M_"a[12:15],a[196:199],v[188:191],a[12:15]\n"
W_"lgkmcnt(12)\n"
"v_cmp_nge_f32 vcc,s60,v216\n"
"s_mov_b64 s[40:41],s[52:53]\n"
"s_or_b64 s[62:63],s[62:63],vcc\n"
R_"v32,a8\n"
R_"v33,a9\n"
R_"v34,a10\n"
R_"v35,a11\n"
R_"v36,a24\n"
R_"v37,a25\n"
R_"v38,a26\n"
M_"a[28:31],a[200:203],v[184:187],a[28:31]\n"
R_"v39,a27\n"
U_"v32,v247,v32\n"
U_"v33,v247,v33\n"
U_"v34,v247,v34\n"
U_"v35,v247,v35\n"
U_"v36,v247,v36\n"
U_"v37,v247,v37\n"
U_"v38,v247,v38\n"
U_"v39,v247,v39\n"
C_"v32,v32,v33\n"
M_"a[28:31],a[204:207],v[188:191],a[28:31]\n"
W_"lgkmcnt(10)\n"
C_"v33,v34,v35\n"
C_"v34,v36,v37\n"
C_"v35,v38,v39\n"
S_"v[32:35],v244,s[52:55],s98 offen offset:0 sc1\n"
R_"v40,a40\n"
R_"v41,a41\n"
R_"v42,a42\n"
R_"v43,a43\n"
M_"a[44:47],a[208:211],v[184:187],a[44:47]\n"
R_"v44,a56\n"
R_"v45,a57\n"
R_"v46,a58\n"
R_"v47,a59\n"
U_"v40,v247,v40\n"
U_"v41,v247,v41\n"
U_"v42,v247,v42\n"
U_"v43,v247,v43\n"
U_"v44,v247,v44\n"
U_"v45,v247,v45\n"
U_"v46,v247,v46\n"
M_"a[44:47],a[212:215],v[188:191],a[44:47]\n"
W_"lgkmcnt(8)\n"
U_"v47,v247,v47\n"
C_"v40,v40,v41\n"
C_"v41,v42,v43\n"
C_"v42,v44,v45\n"
C_"v43,v46,v47\n"
S_"v[40:43],v244,s[52:55],s98 offen offset:1024 sc1\n"
R_"v32,a72\n"
R_"v33,a73\n"
M_"a[60:63],a[216:219],v[184:187],a[60:63]\n"
R_"v34,a74\n"
R_"v35,a75\n"
R_"v36,a88\n"
R_"v37,a89\n"
R_"v38,a90\n"
R_"v39,a91\n"
U_"v32,v247,v32\n"
U_"v33,v247,v33\n"
U_"v34,v247,v34\n"
U_"v35,v247,v35\n"
M_"a[60:63],a[220:223],v[188:191],a[60:63]\n"
W_"lgkmcnt(6)\n"
U_"v36,v247,v36\n"
U_"v37,v247,v37\n"
U_"v38,v247,v38\n"
U_"v39,v247,v39\n"
C_"v32,v32,v33\n"
C_"v33,v34,v35\n"
C_"v34,v36,v37\n"
C_"v35,v38,v39\n"
S_"v[32:35],v244,s[52:55],s98 offen offset:2048 sc1\n"
M_"a[76:79],a[224:227],v[184:187],a[76:79]\n"
W_"vmcnt(3)\n"
"v_cmp_ne_u32 vcc,s32,v236\n"
"v_cmp_eq_u32 s[10:11],s32,v237\n"
"s_or_b64 s[10:11],s[10:11],vcc\n"
"v_cmp_ne_u32 vcc,s32,v238\n"
"s_and_b64 vcc,vcc,s[10:11]\n"
"s_cselect_b32 s24,1,2\n"
"s_cselect_b32 s42,0,0x6000000\n"
B_"v[0:3],v244,s[40:43],s21 offen offset:0 sc1\n"
M_"a[76:79],a[228:231],v[188:191],a[76:79]\n"
W_"lgkmcnt(4)\n"
B_"v[4:7],v244,s[40:43],s22 offen offset:0 sc1\n"
B_"v[8:11],v244,s[40:43],s23 offen offset:0 sc1\n"
B_"v[12:15],v244,s[40:43],s21 offen offset:1024 sc1\n"
R_"v40,a104\n"
M_"a[92:95],a[232:235],v[184:187],a[92:95]\n"
B_"v[16:19],v244,s[40:43],s22 offen offset:1024 sc1\n"
B_"v[20:23],v244,s[40:43],s23 offen offset:1024 sc1\n"
B_"v[24:27],v244,s[40:43],s21 offen offset:2048 sc1\n"
B_"v[28:31],v244,s[40:43],s22 offen offset:2048 sc1\n"
M_"a[92:95],a[236:239],v[188:191],a[92:95]\n"
W_"lgkmcnt(2)\n"
B_"v[64:67],v244,s[40:43],s23 offen offset:2048 sc1\n"
R_"v41,a105\n"
R_"v42,a106\n"
R_"v43,a107\n"
R_"v44,a120\n"
R_"v45,a121\n"
R_"v46,a122\n"
R_"v47,a123\n"
M_"a[108:111],a[240:243],v[184:187],a[108:111]\n"
B_"v[68:71],v244,s[40:43],s21 offen offset:3072 sc1\n"
B_"v[72:75],v244,s[40:43],s22 offen offset:3072 sc1\n"
B_"v[76:79],v244,s[40:43],s23 offen offset:3072 sc1\n"
U_"v40,v247,v40\n"
U_"v41,v247,v41\n"
M_"a[108:111],a[244:247],v[188:191],a[108:111]\n"
W_"lgkmcnt(0)\n"
"s_xor_b32 s21,s21,4096\n"
"s_xor_b32 s22,s22,4096\n"
"s_xor_b32 s23,s23,4096\n"
B_"v[80:83],v244,s[40:43],s21 offen offset:0 sc1\n"
B_"v[84:87],v244,s[40:43],s22 offen offset:0 sc1\n"
U_"v42,v247,v42\n"
M_"a[124:127],a[248:251],v[184:187],a[124:127]\n"
B_"v[88:91],v244,s[40:43],s23 offen offset:0 sc1\n"
B_"v[92:95],v244,s[40:43],s21 offen offset:1024 sc1\n"
U_"v43,v247,v43\n"
U_"v44,v247,v44\n"
U_"v45,v247,v45\n"
U_"v46,v247,v46\n"
U_"v47,v247,v47\n"
C_"v40,v40,v41\n"
M_"a[124:127],a[252:255],v[188:191],a[124:127]\n"
C_"v41,v42,v43\n"
C_"v42,v44,v45\n"
C_"v43,v46,v47\n"
S_"v[40:43],v244,s[52:55],s98 offen offset:3072 sc1\n"
W_"vmcnt(63)\n"
"s_mov_b32 s35,12\n"
"s_cmp_eq_u32 s24,2\n"
"s_cbranch_scc1 L_p3_2nd_back\n"
B_"v[236:239],v255,s[52:55],s20 offen sc1\n"
W_"vmcnt(0)\n"
"L_p3_try:\n"
"v_cmp_ne_u32 vcc,s32,v236\n"
"v_cmp_eq_u32 s[10:11],s32,v237\n"
"s_or_b64 s[10:11],s[10:11],vcc\n"
"v_cmp_ne_u32 vcc,s32,v238\n"
"s_and_b64 vcc,vcc,s[10:11]\n"
"s_cbranch_scc0 L_p3_try1\n"
B_"v[236:239],v255,s[52:55],s20 offen sc1\n"
W_"vmcnt(0)\n"
"s_sub_u32 s35,s35,1\n"
"s_cbranch_scc0 L_p3_try\n"
"s_branch L_p3_2nd_back\n"
"L_p3_try1:\n"
"s_mov_b32 s24,2\n"
"s_xor_b32 s21,s21,4096\n"
"s_xor_b32 s22,s22,4096\n"
"s_xor_b32 s23,s23,4096\n"
B_"v[0:3],v244,s[52:55],s21 offen offset:0 sc1\n"
B_"v[4:7],v244,s[52:55],s22 offen offset:0 sc1\n"
B_"v[8:11],v244,s[52:55],s23 offen offset:0 sc1\n"
B_"v[12:15],v244,s[52:55],s21 offen offset:1024 sc1\n"
B_"v[16:19],v244,s[52:55],s22 offen offset:1024 sc1\n"
B_"v[20:23],v244,s[52:55],s23 offen offset:1024 sc1\n"
B_"v[24:27],v244,s[52:55],s21 offen offset:2048 sc1\n"
B_"v[28:31],v244,s[52:55],s22 offen offset:2048 sc1\n"
B_"v[64:67],v244,s[52:55],s23 offen offset:2048 sc1\n"
B_"v[68:71],v244,s[52:55],s21 offen offset:3072 sc1\n"
B_"v[72:75],v244,s[52:55],s22 offen offset:3072 sc1\n"
B_"v[76:79],v244,s[52:55],s23 offen offset:3072 sc1\n"
"s_xor_b32 s21,s21,4096\n"
"s_xor_b32 s22,s22,4096\n"
"s_xor_b32 s23,s23,4096\n"
B_"v[80:83],v244,s[52:55],s21 offen offset:0 sc1\n"
B_"v[84:87],v244,s[52:55],s22 offen offset:0 sc1\n"
B_"v[88:91],v244,s[52:55],s23 offen offset:0 sc1\n"
B_"v[92:95],v244,s[52:55],s21 offen offset:1024 sc1\n"
"s_branch L_p3_2nd_back\n"
"L_m_V:\n"
"v_xor_b32 v205,8192,v204\n"
"s_lshr_b32 s85,s70,1\n"
"s_lshl_b32 s85,s85,19\n"
"s_and_b32 s70,s70,1\n"
"s_lshl_b32 s70,s70,12\n"
A_"s70,s70,s85\n"
A_"s70,s70,s91\n"
"s_lshr_b32 s85,s71,1\n"
"s_lshl_b32 s85,s85,19\n"
"s_and_b32 s71,s71,1\n"
"s_lshl_b32 s71,s71,12\n"
A_"s71,s71,s85\n"
A_"s71,s71,s91\n"
"s_lshr_b32 s85,s72,1\n"
"s_lshl_b32 s85,s85,19\n"
"s_and_b32 s72,s72,1\n"
"s_lshl_b32 s72,s72,12\n"
A_"s72,s72,s85\n"
A_"s72,s72,s91\n"
"s_lshr_b32 s85,s73,1\n"
"s_lshl_b32 s85,s85,19\n"
"s_and_b32 s73,s73,1\n"
"s_lshl_b32 s73,s73,12\n"
A_"s73,s73,s85\n"
A_"s73,s73,s91\n"
"s_xor_b32 s75,s36,16384\n"
A_"m0,s56,114688\n"
"s_nop 0\n"
B_"v208,s[40:43],s75 offen offset:0  lds\n"
B_"v209,s[40:43],s75 offen offset:1024  lds\n"
B_"v210,s[40:43],s75 offen offset:2048  lds\n"
B_"v211,s[40:43],s75 offen offset:3072  lds\n"
W_"vmcnt(13)\n"
"s_barrier\n"
T_"v[0:1],v192 offset:16384\n"
T_"v[2:3],v192 offset:16896\n"
T_"v[4:5],v192 offset:20480\n"
T_"v[6:7],v192 offset:20992\n"
T_"v[8:9],v192 offset:24576\n"
T_"v[10:11],v192 offset:25088\n"
T_"v[12:13],v192 offset:28672\n"
T_"v[14:15],v192 offset:29184\n"
T_"v[16:17],v193 offset:16384\n"
T_"v[18:19],v193 offset:16896\n"
T_"v[20:21],v193 offset:20480\n"
T_"v[22:23],v193 offset:20992\n"
T_"v[24:25],v193 offset:24576\n"
T_"v[26:27],v193 offset:25088\n"
T_"v[28:29],v193 offset:28672\n"
T_"v[30:31],v193 offset:29184\n"
T_"v[32:33],v194 offset:16384\n"
T_"v[34:35],v194 offset:16896\n"
T_"v[36:37],v194 offset:20480\n"
T_"v[38:39],v194 offset:20992\n"
T_"v[40:41],v194 offset:24576\n"
T_"v[42:43],v194 offset:25088\n"
T_"v[44:45],v194 offset:28672\n"
T_"v[46:47],v194 offset:29184\n"
T_"v[48:49],v195 offset:16384\n"
T_"v[50:51],v195 offset:16896\n"
T_"v[52:53],v195 offset:20480\n"
T_"v[54:55],v195 offset:20992\n"
T_"v[56:57],v195 offset:24576\n"
T_"v[58:59],v195 offset:25088\n"
T_"v[60:61],v195 offset:28672\n"
T_"v[62:63],v195 offset:29184\n"
"s_xor_b32 s75,s36,32768\n"
A_"m0,s56,147456\n"
"s_nop 0\n"
B_"v208,s[40:43],s75 offen offset:0  lds\n"
B_"v209,s[40:43],s75 offen offset:1024  lds\n"
B_"v210,s[40:43],s75 offen offset:2048  lds\n"
B_"v211,s[40:43],s75 offen offset:3072  lds\n"
"s_xor_b32 s75,s36,49152\n"
A_"m0,s56,0\n"
"s_nop 0\n"
B_"v208,s[40:43],s75 offen offset:0  lds\n"
B_"v209,s[40:43],s75 offen offset:1024  lds\n"
B_"v210,s[40:43],s75 offen offset:2048  lds\n"
B_"v211,s[40:43],s75 offen offset:3072  lds\n"
B_"v[240:243],v244,s[44:47],s84 offen offset:0\n"
B_"v[248:251],v244,s[44:47],s84 offen offset:1024\n"
W_"vmcnt(19)\n"
D_"a[192:195],v212 offset:32768\n"
D_"a[196:199],v213 offset:32768\n"
D_"a[200:203],v214 offset:32768\n"
D_"a[204:207],v215 offset:32768\n"
W_"lgkmcnt(0)\n"
M_"v[128:131],a[192:195],v[0:3],0\n"
M_"v[128:131],a[196:199],v[4:7],v[128:131]\n"
M_"v[128:131],a[200:203],v[8:11],v[128:131]\n"
M_"v[128:131],a[204:207],v[12:15],v[128:131]\n"
M_"v[144:147],a[192:195],v[16:19],0\n"
W_"vmcnt(10)\n"
D_"a[208:211],v212 offset:49152\n"
M_"v[144:147],a[196:199],v[20:23],v[144:147]\n"
D_"a[212:215],v213 offset:49152\n"
D_"a[216:219],v214 offset:49152\n"
M_"v[144:147],a[200:203],v[24:27],v[144:147]\n"
D_"a[220:223],v215 offset:49152\n"
M_"v[144:147],a[204:207],v[28:31],v[144:147]\n"
M_"v[160:163],a[192:195],v[32:35],0\n"
"s_bfe_u32 s75,s37,0x20008\n"
M_"v[160:163],a[196:199],v[36:39],v[160:163]\n"
"s_lshl_b32 s75,s75,20\n"
M_"v[160:163],a[200:203],v[40:43],v[160:163]\n"
A_"s75,s75,s64\n"
M_"v[160:163],a[204:207],v[44:47],v[160:163]\n"
A_"m0,s56,16384\n"
M_"v[176:179],a[192:195],v[48:51],0\n"
M_"v[176:179],a[196:199],v[52:55],v[176:179]\n"
B_"v196,s[48:51],s75 offen offset:0  lds\n"
M_"v[176:179],a[200:203],v[56:59],v[176:179]\n"
M_"v[176:179],a[204:207],v[60:63],v[176:179]\n"
W_"lgkmcnt(0)\n"
M_"v[132:135],a[208:211],v[0:3],0\n"
M_"v[132:135],a[212:215],v[4:7],v[132:135]\n"
B_"v197,s[48:51],s75 offen offset:1024  lds\n"
M_"v[132:135],a[216:219],v[8:11],v[132:135]\n"
M_"v[132:135],a[220:223],v[12:15],v[132:135]\n"
M_"v[148:151],a[208:211],v[16:19],0\n"
W_"vmcnt(8)\n"
"v_xor_b32 v212,0x30000,v212\n"
"v_xor_b32 v213,0x30000,v213\n"
M_"v[148:151],a[212:215],v[20:23],v[148:151]\n"
B_"v198,s[48:51],s75 offen offset:2048  lds\n"
M_"v[148:151],a[216:219],v[24:27],v[148:151]\n"
"v_xor_b32 v214,0x30000,v214\n"
"v_xor_b32 v215,0x30000,v215\n"
M_"v[148:151],a[220:223],v[28:31],v[148:151]\n"
D_"a[224:227],v212 offset:16384\n"
D_"a[228:231],v213 offset:16384\n"
M_"v[164:167],a[208:211],v[32:35],0\n"
D_"a[232:235],v214 offset:16384\n"
D_"a[236:239],v215 offset:16384\n"
M_"v[164:167],a[212:215],v[36:39],v[164:167]\n"
W_"vmcnt(5)\n"
"v_xor_b32 v212,0x20000,v212\n"
"v_xor_b32 v213,0x20000,v213\n"
M_"v[164:167],a[216:219],v[40:43],v[164:167]\n"
B_"v199,s[48:51],s75 offen offset:3072  lds\n"
M_"v[164:167],a[220:223],v[44:47],v[164:167]\n"
"s_xor_b32 s75,s37,0\n"
"v_xor_b32 v214,0x20000,v214\n"
"v_xor_b32 v215,0x20000,v215\n"
D_"a[240:243],v212 offset:0\n"
D_"a[244:247],v213 offset:0\n"
D_"a[248:251],v214 offset:0\n"
D_"a[252:255],v215 offset:0\n"
M_"v[180:183],a[208:211],v[48:51],0\n"
A_"m0,s56,32768\n"
M_"v[180:183],a[212:215],v[52:55],v[180:183]\n"
M_"v[180:183],a[216:219],v[56:59],v[180:183]\n"
B_"v208,s[40:43],s75 offen offset:0  lds\n"
M_"v[180:183],a[220:223],v[60:63],v[180:183]\n"
W_"lgkmcnt(4)\n"
"s_nop 3\n"
M_"v[136:139],a[224:227],v[0:3],0\n"
M_"v[136:139],a[228:231],v[4:7],v[136:139]\n"
M_"v[136:139],a[232:235],v[8:11],v[136:139]\n"
B_"v209,s[40:43],s75 offen offset:1024  lds\n"
M_"v[136:139],a[236:239],v[12:15],v[136:139]\n"
W_"lgkmcnt(0)\n"
"s_nop 3\n"
M_"v[140:143],a[240:243],v[0:3],0\n"
W_" lgkmcnt(0)\n"
"s_barrier\n"
M_"v[140:143],a[244:247],v[4:7],v[140:143]\n"
M_"v[140:143],a[248:251],v[8:11],v[140:143]\n"
B_"v210,s[40:43],s75 offen offset:2048  lds\n"
M_"v[140:143],a[252:255],v[12:15],v[140:143]\n"
"s_nop 3\n"
M_"v[152:155],a[224:227],v[16:19],0\n"
"v_xor_b32 v192,0x30000,v192\n"
M_"v[152:155],a[228:231],v[20:23],v[152:155]\n"
"v_xor_b32 v193,0x30000,v193\n"
M_"v[152:155],a[232:235],v[24:27],v[152:155]\n"
B_"v211,s[40:43],s75 offen offset:3072  lds\n"
M_"v[152:155],a[236:239],v[28:31],v[152:155]\n"
"s_xor_b32 s75,s37,16384\n"
"v_xor_b32 v194,0x30000,v194\n"
"v_xor_b32 v195,0x30000,v195\n"
"s_nop 3\n"
M_"v[156:159],a[240:243],v[16:19],0\n"
A_"m0,s56,49152\n"
T_"v[0:1],v192 offset:0\n"
W_"vmcnt(8)\n"
M_"v[156:159],a[244:247],v[20:23],v[156:159]\n"
T_"v[2:3],v192 offset:512\n"
"v_lshlrev_b32 v224,16,v240\n"
M_"v[156:159],a[248:251],v[24:27],v[156:159]\n"
B_"v208,s[40:43],s75 offen offset:0  lds\n"
M_"v[156:159],a[252:255],v[28:31],v[156:159]\n"
T_"v[4:5],v192 offset:4096\n"
T_"v[6:7],v192 offset:4608\n"
"s_nop 3\n"
M_"v[168:171],a[224:227],v[32:35],0\n"
T_"v[8:9],v192 offset:8192\n"
"v_lshlrev_b32 v232,16,v248\n"
M_"v[168:171],a[228:231],v[36:39],v[168:171]\n"
T_"v[10:11],v192 offset:8704\n"
"v_and_b32 v225,0xffff0000,v240\n"
M_"v[168:171],a[232:235],v[40:43],v[168:171]\n"
B_"v209,s[40:43],s75 offen offset:1024  lds\n"
M_"v[168:171],a[236:239],v[44:47],v[168:171]\n"
T_"v[12:13],v192 offset:12288\n"
T_"v[14:15],v192 offset:12800\n"
"s_nop 3\n"
M_"v[172:175],a[240:243],v[32:35],0\n"
T_"v[16:17],v193 offset:0\n"
T_"v[18:19],v193 offset:512\n"
M_"v[172:175],a[244:247],v[36:39],v[172:175]\n"
T_"v[20:21],v193 offset:4096\n"
T_"v[22:23],v193 offset:4608\n"
M_"v[172:175],a[248:251],v[40:43],v[172:175]\n"
B_"v210,s[40:43],s75 offen offset:2048  lds\n"
M_"v[172:175],a[252:255],v[44:47],v[172:175]\n"
T_"v[24:25],v193 offset:8192\n"
"v_and_b32 v233,0xffff0000,v248\n"
"s_nop 3\n"
M_"v[184:187],a[224:227],v[48:51],0\n"
T_"v[26:27],v193 offset:8704\n"
"v_lshlrev_b32 v226,16,v241\n"
M_"v[184:187],a[228:231],v[52:55],v[184:187]\n"
"v_lshlrev_b32 v234,16,v249\n"
"v_and_b32 v227,0xffff0000,v241\n"
M_"v[184:187],a[232:235],v[56:59],v[184:187]\n"
B_"v211,s[40:43],s75 offen offset:3072  lds\n"
M_"v[184:187],a[236:239],v[60:63],v[184:187]\n"
"s_bfe_u32 s75,s37,0x20008\n"
T_"v[28:29],v193 offset:12288\n"
"v_and_b32 v235,0xffff0000,v249\n"
"s_nop 3\n"
M_"v[188:191],a[240:243],v[48:51],0\n"
"s_lshl_b32 s75,s75,20\n"
T_"v[30:31],v193 offset:12800\n"
T_"v[32:33],v194 offset:0\n"
M_"v[188:191],a[244:247],v[52:55],v[188:191]\n"
A_"s75,s75,s65\n"
T_"v[34:35],v194 offset:512\n"
T_"v[36:37],v194 offset:4096\n"
M_"v[188:191],a[248:251],v[56:59],v[188:191]\n"
A_"m0,s56,65536\n"
T_"v[38:39],v194 offset:4608\n"
"v_lshlrev_b32 v228,16,v242\n"
M_"v[188:191],a[252:255],v[60:63],v[188:191]\n"
W_"lgkmcnt(12)\n"
T_"v[40:41],v194 offset:8192\n"
"v_lshlrev_b32 v236,16,v250\n"
"s_nop 3\n"
M_"v[64:67],a[192:195],v[0:3],0\n"
B_"v196,s[48:51],s75 offen offset:0  lds\n"
M_"v[64:67],a[196:199],v[4:7],v[64:67]\n"
T_"v[42:43],v194 offset:8704\n"
"v_and_b32 v229,0xffff0000,v242\n"
M_"v[64:67],a[200:203],v[8:11],v[64:67]\n"
T_"v[44:45],v194 offset:12288\n"
"v_and_b32 v237,0xffff0000,v250\n"
M_"v[64:67],a[204:207],v[12:15],v[64:67]\n"
"v_lshlrev_b32 v230,16,v243\n"
"v_lshlrev_b32 v238,16,v251\n"
"s_nop 3\n"
M_"v[68:71],a[208:211],v[0:3],0\n"
B_"v197,s[48:51],s75 offen offset:1024  lds\n"
M_"v[68:71],a[212:215],v[4:7],v[68:71]\n"
T_"v[46:47],v194 offset:12800\n"
T_"v[48:49],v195 offset:0\n"
M_"v[68:71],a[216:219],v[8:11],v[68:71]\n"
T_"v[50:51],v195 offset:512\n"
T_"v[52:53],v195 offset:4096\n"
M_"v[68:71],a[220:223],v[12:15],v[68:71]\n"
T_"v[54:55],v195 offset:4608\n"
T_"v[56:57],v195 offset:8192\n"
"s_nop 3\n"
M_"v[72:75],a[224:227],v[0:3],0\n"
B_"v198,s[48:51],s75 offen offset:2048  lds\n"
M_"v[72:75],a[228:231],v[4:7],v[72:75]\n"
T_"v[58:59],v195 offset:8704\n"
T_"v[60:61],v195 offset:12288\n"
M_"v[72:75],a[232:235],v[8:11],v[72:75]\n"
T_"v[62:63],v195 offset:12800\n"
"v_and_b32 v231,0xffff0000,v243\n"
M_"v[72:75],a[236:239],v[12:15],v[72:75]\n"
"v_and_b32 v239,0xffff0000,v251\n"
"v_add_f32 v240,v224,v225\n"
"s_nop 3\n"
M_"v[76:79],a[240:243],v[0:3],0\n"
B_"v199,s[48:51],s75 offen offset:3072  lds\n"
M_"v[76:79],a[244:247],v[4:7],v[76:79]\n"
"s_xor_b32 s75,s37,32768\n"
"v_add_f32 v248,v232,v233\n"
"v_add_f32 v240,v240,v226\n"
M_"v[76:79],a[248:251],v[8:11],v[76:79]\n"
A_"m0,s56,81920\n"
"v_add_f32 v248,v248,v234\n"
"v_add_f32 v240,v240,v227\n"
M_"v[76:79],a[252:255],v[12:15],v[76:79]\n"
W_"lgkmcnt(15)\n"
"v_add_f32 v248,v248,v235\n"
"v_add_f32 v240,v240,v228\n"
"s_nop 3\n"
M_"v[80:83],a[192:195],v[16:19],0\n"
B_"v208,s[40:43],s75 offen offset:0  lds\n"
M_"v[80:83],a[196:199],v[20:23],v[80:83]\n"
"v_add_f32 v248,v248,v236\n"
M_"v[80:83],a[200:203],v[24:27],v[80:83]\n"
M_"v[80:83],a[204:207],v[28:31],v[80:83]\n"
"v_add_f32 v240,v240,v229\n"
"s_nop 3\n"
M_"v[84:87],a[208:211],v[16:19],0\n"
W_"vmcnt(13) lgkmcnt(0)\n"
"s_barrier\n"
"v_add_f32 v248,v248,v237\n"
M_"v[84:87],a[212:215],v[20:23],v[84:87]\n"
B_"v209,s[40:43],s75 offen offset:1024  lds\n"
M_"v[84:87],a[216:219],v[24:27],v[84:87]\n"
"v_xor_b32 v192,0x20000,v192\n"
"v_xor_b32 v193,0x20000,v193\n"
M_"v[84:87],a[220:223],v[28:31],v[84:87]\n"
"v_xor_b32 v194,0x20000,v194\n"
"v_add_f32 v240,v240,v230\n"
"s_nop 3\n"
M_"v[88:91],a[224:227],v[16:19],0\n"
"v_xor_b32 v195,0x20000,v195\n"
"v_add_f32 v248,v248,v238\n"
M_"v[88:91],a[228:231],v[20:23],v[88:91]\n"
B_"v210,s[40:43],s75 offen offset:2048  lds\n"
M_"v[88:91],a[232:235],v[24:27],v[88:91]\n"
T_"v[0:1],v192 offset:16384\n"
T_"v[2:3],v192 offset:16896\n"
M_"v[88:91],a[236:239],v[28:31],v[88:91]\n"
"v_add_f32 v240,v240,v231\n"
"v_add_f32 v248,v248,v239\n"
"s_nop 3\n"
M_"v[92:95],a[240:243],v[16:19],0\n"
T_"v[4:5],v192 offset:20480\n"
"v_add_f32_dpp v240,v240,v240 row_ror:8 row_mask:0xf bank_mask:0xf\n"
M_"v[92:95],a[244:247],v[20:23],v[92:95]\n"
B_"v211,s[40:43],s75 offen offset:3072  lds\n"
M_"v[92:95],a[248:251],v[24:27],v[92:95]\n"
"s_xor_b32 s75,s37,49152\n"
T_"v[6:7],v192 offset:20992\n"
T_"v[8:9],v192 offset:24576\n"
M_"v[92:95],a[252:255],v[28:31],v[92:95]\n"
T_"v[10:11],v192 offset:25088\n"
"v_add_f32_dpp v248,v248,v248 row_ror:8 row_mask:0xf bank_mask:0xf\n"
"s_nop 3\n"
M_"v[96:99],a[192:195],v[32:35],0\n"
A_"m0,s56,98304\n"
T_"v[12:13],v192 offset:28672\n"
"v_add_f32_dpp v240,v240,v240 row_ror:4 row_mask:0xf bank_mask:0xf\n"
M_"v[96:99],a[196:199],v[36:39],v[96:99]\n"
B_"v208,s[40:43],s75 offen offset:0  lds\n"
M_"v[96:99],a[200:203],v[40:43],v[96:99]\n"
T_"v[14:15],v192 offset:29184\n"
T_"v[16:17],v193 offset:16384\n"
M_"v[96:99],a[204:207],v[44:47],v[96:99]\n"
T_"v[18:19],v193 offset:16896\n"
T_"v[20:21],v193 offset:20480\n"
"s_nop 3\n"
M_"v[100:103],a[208:211],v[32:35],0\n"
"v_add_f32_dpp v248,v248,v248 row_ror:4 row_mask:0xf bank_mask:0xf\n"
"v_add_f32_dpp v240,v240,v240 row_ror:2 row_mask:0xf bank_mask:0xf\n"
M_"v[100:103],a[212:215],v[36:39],v[100:103]\n"
B_"v209,s[40:43],s75 offen offset:1024  lds\n"
M_"v[100:103],a[216:219],v[40:43],v[100:103]\n"
T_"v[22:23],v193 offset:20992\n"
"v_add_f32_dpp v248,v248,v248 row_ror:2 row_mask:0xf bank_mask:0xf\n"
M_"v[100:103],a[220:223],v[44:47],v[100:103]\n"
T_"v[24:25],v193 offset:24576\n"
"v_add_f32_dpp v240,v240,v240 row_ror:1 row_mask:0xf bank_mask:0xf\n"
"s_nop 3\n"
M_"v[104:107],a[224:227],v[32:35],0\n"
T_"v[26:27],v193 offset:25088\n"
"v_add_f32_dpp v248,v248,v248 row_ror:1 row_mask:0xf bank_mask:0xf\n"
M_"v[104:107],a[228:231],v[36:39],v[104:107]\n"
B_"v210,s[40:43],s75 offen offset:2048  lds\n"
M_"v[104:107],a[232:235],v[40:43],v[104:107]\n"
T_"v[28:29],v193 offset:28672\n"
"v_mov_b32 v241,v240\n"
M_"v[104:107],a[236:239],v[44:47],v[104:107]\n"
"v_mov_b32 v249,v248\n"
"s_nop 3\n"
M_"v[108:111],a[240:243],v[32:35],0\n"
T_"v[30:31],v193 offset:29184\n"
"v_permlane16_swap_b32 v240,v241\n"
M_"v[108:111],a[244:247],v[36:39],v[108:111]\n"
B_"v211,s[40:43],s75 offen offset:3072  lds\n"
M_"v[108:111],a[248:251],v[40:43],v[108:111]\n"
"s_bfe_u32 s75,s38,0x20008\n"
"v_permlane16_swap_b32 v248,v249\n"
"s_lshl_b32 s75,s75,20\n"
M_"v[108:111],a[252:255],v[44:47],v[108:111]\n"
"v_add_f32 v240,v240,v241\n"
A_"s75,s75,s64\n"
"s_nop 3\n"
M_"v[112:115],a[192:195],v[48:51],0\n"
A_"m0,s56,114688\n"
T_"v[32:33],v194 offset:16384\n"
"v_add_f32 v248,v248,v249\n"
M_"v[112:115],a[196:199],v[52:55],v[112:115]\n"
B_"v196,s[48:51],s75 offen offset:0  lds\n"
M_"v[112:115],a[200:203],v[56:59],v[112:115]\n"
T_"v[34:35],v194 offset:16896\n"
"v_mov_b32 v241,v240\n"
M_"v[112:115],a[204:207],v[60:63],v[112:115]\n"
T_"v[36:37],v194 offset:20480\n"
"v_mov_b32 v249,v248\n"
"s_nop 3\n"
M_"v[116:119],a[208:211],v[48:51],0\n"
W_"vmcnt(17)\n"
D_"a[64:67],v212 offset:32768\n"
M_"v[116:119],a[212:215],v[52:55],v[116:119]\n"
B_"v197,s[48:51],s75 offen offset:1024  lds\n"
M_"v[116:119],a[216:219],v[56:59],v[116:119]\n"
D_"a[68:71],v213 offset:32768\n"
D_"a[72:75],v214 offset:32768\n"
M_"v[116:119],a[220:223],v[60:63],v[116:119]\n"
D_"a[76:79],v215 offset:32768\n"
T_"v[38:39],v194 offset:20992\n"
"s_nop 3\n"
M_"v[120:123],a[224:227],v[48:51],0\n"
W_"vmcnt(14)\n"
D_"a[80:83],v212 offset:49152\n"
D_"a[84:87],v213 offset:49152\n"
M_"v[120:123],a[228:231],v[52:55],v[120:123]\n"
B_"v198,s[48:51],s75 offen offset:2048  lds\n"
M_"v[120:123],a[232:235],v[56:59],v[120:123]\n"
D_"a[88:91],v214 offset:49152\n"
D_"a[92:95],v215 offset:49152\n"
M_"v[120:123],a[236:239],v[60:63],v[120:123]\n"
T_"v[40:41],v194 offset:24576\n"
T_"v[42:43],v194 offset:25088\n"
"s_nop 3\n"
M_"v[124:127],a[240:243],v[48:51],0\n"
T_"v[44:45],v194 offset:28672\n"
T_"v[46:47],v194 offset:29184\n"
M_"v[124:127],a[244:247],v[52:55],v[124:127]\n"
B_"v199,s[48:51],s75 offen offset:3072  lds\n"
M_"v[124:127],a[248:251],v[56:59],v[124:127]\n"
"s_xor_b32 s75,s38,0\n"
"v_permlane32_swap_b32 v240,v241\n"
"v_permlane32_swap_b32 v248,v249\n"
M_"v[124:127],a[252:255],v[60:63],v[124:127]\n"
W_"lgkmcnt(15)\n"
"v_add_f32 v240,v240,v241\n"
W_"lgkmcnt(9)\n"
"s_nop 3\n"
M_"v[128:131],a[64:67],v[0:3],v[128:131]\n"
A_"m0,s56,131072\n"
T_"v[48:49],v195 offset:16384\n"
"v_add_f32 v248,v248,v249\n"
M_"v[128:131],a[68:71],v[4:7],v[128:131]\n"
B_"v208,s[40:43],s75 offen offset:0  lds\n"
M_"v[128:131],a[72:75],v[8:11],v[128:131]\n"
T_"v[50:51],v195 offset:16896\n"
U_"v240,0xbb000000,v240\n"
M_"v[128:131],a[76:79],v[12:15],v[128:131]\n"
W_"lgkmcnt(6)\n"
T_"v[52:53],v195 offset:20480\n"
U_"v248,0xbb000000,v248\n"
"s_nop 3\n"
M_"v[132:135],a[80:83],v[0:3],v[132:135]\n"
T_"v[54:55],v195 offset:20992\n"
"v_add_f32 v224,v240,v224\n"
M_"v[132:135],a[84:87],v[4:7],v[132:135]\n"
B_"v209,s[40:43],s75 offen offset:1024  lds\n"
M_"v[132:135],a[88:91],v[8:11],v[132:135]\n"
T_"v[56:57],v195 offset:24576\n"
"v_add_f32 v232,v248,v232\n"
M_"v[132:135],a[92:95],v[12:15],v[132:135]\n"
"v_add_f32 v225,v240,v225\n"
"v_add_f32 v233,v248,v233\n"
"s_nop 3\n"
M_"v[144:147],a[64:67],v[16:19],v[144:147]\n"
T_"v[58:59],v195 offset:25088\n"
"v_add_f32 v226,v240,v226\n"
M_"v[144:147],a[68:71],v[20:23],v[144:147]\n"
B_"v210,s[40:43],s75 offen offset:2048  lds\n"
M_"v[144:147],a[72:75],v[24:27],v[144:147]\n"
T_"v[60:61],v195 offset:28672\n"
"v_add_f32 v234,v248,v234\n"
M_"v[144:147],a[76:79],v[28:31],v[144:147]\n"
T_"v[62:63],v195 offset:29184\n"
"v_add_f32 v227,v240,v227\n"
"s_nop 3\n"
M_"v[148:151],a[80:83],v[16:19],v[148:151]\n"
"v_add_f32 v235,v248,v235\n"
"v_add_f32 v228,v240,v228\n"
M_"v[148:151],a[84:87],v[20:23],v[148:151]\n"
B_"v211,s[40:43],s75 offen offset:3072  lds\n"
M_"v[148:151],a[88:91],v[24:27],v[148:151]\n"
"s_xor_b32 s75,s38,16384\n"
"v_add_f32 v236,v248,v236\n"
"v_add_f32 v229,v240,v229\n"
M_"v[148:151],a[92:95],v[28:31],v[148:151]\n"
W_"lgkmcnt(8)\n"
"v_add_f32 v237,v248,v237\n"
A_"m0,s56,147456\n"
"s_nop 3\n"
M_"v[160:163],a[64:67],v[32:35],v[160:163]\n"
M_"v[160:163],a[68:71],v[36:39],v[160:163]\n"
B_"v208,s[40:43],s75 offen offset:0  lds\n"
M_"v[160:163],a[72:75],v[40:43],v[160:163]\n"
"v_add_f32 v230,v240,v230\n"
M_"v[160:163],a[76:79],v[44:47],v[160:163]\n"
"v_add_f32 v238,v248,v238\n"
"s_nop 3\n"
M_"v[164:167],a[80:83],v[32:35],v[164:167]\n"
W_"vmcnt(13)\n"
"v_xor_b32 v212,0x10000,v212\n"
"v_xor_b32 v213,0x10000,v213\n"
M_"v[164:167],a[84:87],v[36:39],v[164:167]\n"
B_"v209,s[40:43],s75 offen offset:1024  lds\n"
M_"v[164:167],a[88:91],v[40:43],v[164:167]\n"
"v_xor_b32 v214,0x10000,v214\n"
"v_xor_b32 v215,0x10000,v215\n"
M_"v[164:167],a[92:95],v[44:47],v[164:167]\n"
W_"lgkmcnt(0)\n"
D_"a[96:99],v212 offset:16384\n"
D_"a[100:103],v213 offset:16384\n"
"s_nop 3\n"
M_"v[176:179],a[64:67],v[48:51],v[176:179]\n"
D_"a[104:107],v214 offset:16384\n"
D_"a[108:111],v215 offset:16384\n"
M_"v[176:179],a[68:71],v[52:55],v[176:179]\n"
W_"vmcnt(10)\n"
D_"a[112:115],v212 offset:32768\n"
D_"a[116:119],v213 offset:32768\n"
M_"v[176:179],a[72:75],v[56:59],v[176:179]\n"
B_"v210,s[40:43],s75 offen offset:2048  lds\n"
M_"v[176:179],a[76:79],v[60:63],v[176:179]\n"
D_"a[120:123],v214 offset:32768\n"
D_"a[124:127],v215 offset:32768\n"
"s_nop 3\n"
M_"v[180:183],a[80:83],v[48:51],v[180:183]\n"
"v_add_f32 v231,v240,v231\n"
"v_add_f32 v239,v248,v239\n"
M_"v[180:183],a[84:87],v[52:55],v[180:183]\n"
U_"v240,v224,v224\n"
U_"v248,v232,v232\n"
M_"v[180:183],a[88:91],v[56:59],v[180:183]\n"
B_"v211,s[40:43],s75 offen offset:3072  lds\n"
M_"v[180:183],a[92:95],v[60:63],v[180:183]\n"
W_"lgkmcnt(4)\n"
"v_fmac_f32 v240,v225,v225\n"
"v_fmac_f32 v248,v233,v233\n"
"s_nop 3\n"
M_"v[136:139],a[96:99],v[0:3],v[136:139]\n"
"s_bfe_u32 s75,s38,0x20008\n"
"v_fmac_f32 v240,v226,v226\n"
"v_fmac_f32 v248,v234,v234\n"
M_"v[136:139],a[100:103],v[4:7],v[136:139]\n"
"s_lshl_b32 s75,s75,20\n"
"v_fmac_f32 v240,v227,v227\n"
A_"s75,s75,s65\n"
M_"v[136:139],a[104:107],v[8:11],v[136:139]\n"
A_"m0,s56,0\n"
"v_fmac_f32 v248,v235,v235\n"
M_"v[136:139],a[108:111],v[12:15],v[136:139]\n"
W_"lgkmcnt(0)\n"
"s_nop 3\n"
M_"v[140:143],a[112:115],v[0:3],v[140:143]\n"
W_" lgkmcnt(0)\n"
"s_barrier\n"
"v_fmac_f32 v240,v228,v228\n"
M_"v[140:143],a[116:119],v[4:7],v[140:143]\n"
B_"v196,s[48:51],s75 offen offset:0  lds\n"
M_"v[140:143],a[120:123],v[8:11],v[140:143]\n"
"v_fmac_f32 v248,v236,v236\n"
M_"v[140:143],a[124:127],v[12:15],v[140:143]\n"
"v_fmac_f32 v240,v229,v229\n"
"s_nop 3\n"
M_"v[152:155],a[96:99],v[16:19],v[152:155]\n"
"v_xor_b32 v192,0x10000,v192\n"
"v_fmac_f32 v248,v237,v237\n"
M_"v[152:155],a[100:103],v[20:23],v[152:155]\n"
B_"v197,s[48:51],s75 offen offset:1024  lds\n"
M_"v[152:155],a[104:107],v[24:27],v[152:155]\n"
"v_xor_b32 v193,0x10000,v193\n"
"v_xor_b32 v194,0x10000,v194\n"
M_"v[152:155],a[108:111],v[28:31],v[152:155]\n"
"v_xor_b32 v195,0x10000,v195\n"
"v_fmac_f32 v240,v230,v230\n"
"s_nop 3\n"
M_"v[156:159],a[112:115],v[16:19],v[156:159]\n"
T_"v[0:1],v192 offset:0\n"
"v_fmac_f32 v248,v238,v238\n"
M_"v[156:159],a[116:119],v[20:23],v[156:159]\n"
B_"v198,s[48:51],s75 offen offset:2048  lds\n"
M_"v[156:159],a[120:123],v[24:27],v[156:159]\n"
T_"v[2:3],v192 offset:512\n"
T_"v[4:5],v192 offset:4096\n"
M_"v[156:159],a[124:127],v[28:31],v[156:159]\n"
T_"v[6:7],v192 offset:4608\n"
"v_fmac_f32 v240,v231,v231\n"
"s_nop 3\n"
M_"v[168:171],a[96:99],v[32:35],v[168:171]\n"
T_"v[8:9],v192 offset:8192\n"
"v_fmac_f32 v248,v239,v239\n"
M_"v[168:171],a[100:103],v[36:39],v[168:171]\n"
B_"v199,s[48:51],s75 offen offset:3072  lds\n"
M_"v[168:171],a[104:107],v[40:43],v[168:171]\n"
"s_xor_b32 s75,s38,32768\n"
T_"v[10:11],v192 offset:8704\n"
T_"v[12:13],v192 offset:12288\n"
M_"v[168:171],a[108:111],v[44:47],v[168:171]\n"
A_"m0,s56,16384\n"
T_"v[14:15],v192 offset:12800\n"
T_"v[16:17],v193 offset:0\n"
"s_nop 3\n"
M_"v[172:175],a[112:115],v[32:35],v[172:175]\n"
T_"v[18:19],v193 offset:512\n"
T_"v[20:21],v193 offset:4096\n"
M_"v[172:175],a[116:119],v[36:39],v[172:175]\n"
B_"v208,s[40:43],s75 offen offset:0  lds\n"
M_"v[172:175],a[120:123],v[40:43],v[172:175]\n"
T_"v[22:23],v193 offset:4608\n"
"v_add_f32_dpp v240,v240,v240 row_ror:8 row_mask:0xf bank_mask:0xf\n"
M_"v[172:175],a[124:127],v[44:47],v[172:175]\n"
T_"v[24:25],v193 offset:8192\n"
"v_add_f32_dpp v248,v248,v248 row_ror:8 row_mask:0xf bank_mask:0xf\n"
"s_nop 3\n"
M_"v[184:187],a[96:99],v[48:51],v[184:187]\n"
T_"v[26:27],v193 offset:8704\n"
"v_add_f32_dpp v240,v240,v240 row_ror:4 row_mask:0xf bank_mask:0xf\n"
M_"v[184:187],a[100:103],v[52:55],v[184:187]\n"
B_"v209,s[40:43],s75 offen offset:1024  lds\n"
M_"v[184:187],a[104:107],v[56:59],v[184:187]\n"
T_"v[28:29],v193 offset:12288\n"
"v_add_f32_dpp v248,v248,v248 row_ror:4 row_mask:0xf bank_mask:0xf\n"
M_"v[184:187],a[108:111],v[60:63],v[184:187]\n"
"v_add_f32_dpp v240,v240,v240 row_ror:2 row_mask:0xf bank_mask:0xf\n"
"v_add_f32_dpp v248,v248,v248 row_ror:2 row_mask:0xf bank_mask:0xf\n"
"s_nop 3\n"
M_"v[188:191],a[112:115],v[48:51],v[188:191]\n"
T_"v[30:31],v193 offset:12800\n"
T_"v[32:33],v194 offset:0\n"
M_"v[188:191],a[116:119],v[52:55],v[188:191]\n"
B_"v210,s[40:43],s75 offen offset:2048  lds\n"
M_"v[188:191],a[120:123],v[56:59],v[188:191]\n"
T_"v[34:35],v194 offset:512\n"
T_"v[36:37],v194 offset:4096\n"
M_"v[188:191],a[124:127],v[60:63],v[188:191]\n"
W_"lgkmcnt(11)\n"
T_"v[38:39],v194 offset:4608\n"
T_"v[40:41],v194 offset:8192\n"
"s_nop 3\n"
M_"v[64:67],a[64:67],v[0:3],v[64:67]\n"
T_"v[42:43],v194 offset:8704\n"
"v_add_f32_dpp v240,v240,v240 row_ror:1 row_mask:0xf bank_mask:0xf\n"
M_"v[64:67],a[68:71],v[4:7],v[64:67]\n"
B_"v211,s[40:43],s75 offen offset:3072  lds\n"
M_"v[64:67],a[72:75],v[8:11],v[64:67]\n"
"s_xor_b32 s75,s38,49152\n"
T_"v[44:45],v194 offset:12288\n"
"v_add_f32_dpp v248,v248,v248 row_ror:1 row_mask:0xf bank_mask:0xf\n"
M_"v[64:67],a[76:79],v[12:15],v[64:67]\n"
A_"m0,s56,32768\n"
"v_mov_b32 v241,v240\n"
"v_mov_b32 v249,v248\n"
"s_nop 3\n"
M_"v[68:71],a[80:83],v[0:3],v[68:71]\n"
T_"v[46:47],v194 offset:12800\n"
T_"v[48:49],v195 offset:0\n"
M_"v[68:71],a[84:87],v[4:7],v[68:71]\n"
B_"v208,s[40:43],s75 offen offset:0  lds\n"
M_"v[68:71],a[88:91],v[8:11],v[68:71]\n"
T_"v[50:51],v195 offset:512\n"
T_"v[52:53],v195 offset:4096\n"
M_"v[68:71],a[92:95],v[12:15],v[68:71]\n"
T_"v[54:55],v195 offset:4608\n"
T_"v[56:57],v195 offset:8192\n"
"s_nop 3\n"
M_"v[72:75],a[96:99],v[0:3],v[72:75]\n"
T_"v[58:59],v195 offset:8704\n"
T_"v[60:61],v195 offset:12288\n"
M_"v[72:75],a[100:103],v[4:7],v[72:75]\n"
B_"v209,s[40:43],s75 offen offset:1024  lds\n"
M_"v[72:75],a[104:107],v[8:11],v[72:75]\n"
T_"v[62:63],v195 offset:12800\n"
"v_permlane16_swap_b32 v240,v241\n"
M_"v[72:75],a[108:111],v[12:15],v[72:75]\n"
"v_permlane16_swap_b32 v248,v249\n"
"v_add_f32 v240,v240,v241\n"
"s_nop 3\n"
M_"v[76:79],a[112:115],v[0:3],v[76:79]\n"
"v_add_f32 v248,v248,v249\n"
"v_mov_b32 v241,v240\n"
M_"v[76:79],a[116:119],v[4:7],v[76:79]\n"
B_"v210,s[40:43],s75 offen offset:2048  lds\n"
M_"v[76:79],a[120:123],v[8:11],v[76:79]\n"
"v_mov_b32 v249,v248\n"
"v_permlane32_swap_b32 v240,v241\n"
M_"v[76:79],a[124:127],v[12:15],v[76:79]\n"
W_"lgkmcnt(15)\n"
"v_permlane32_swap_b32 v248,v249\n"
"v_add_f32 v240,v240,v241\n"
"s_nop 3\n"
M_"v[80:83],a[64:67],v[16:19],v[80:83]\n"
"v_add_f32 v248,v248,v249\n"
U_"v240,0x3b000000,v240\n"
M_"v[80:83],a[68:71],v[20:23],v[80:83]\n"
B_"v211,s[40:43],s75 offen offset:3072  lds\n"
M_"v[80:83],a[72:75],v[24:27],v[80:83]\n"
U_"v248,0x3b000000,v248\n"
M_"v[80:83],a[76:79],v[28:31],v[80:83]\n"
"v_add_f32 v240,0x3727c5ac,v240\n"
"s_nop 3\n"
M_"v[84:87],a[80:83],v[16:19],v[84:87]\n"
W_"vmcnt(20) lgkmcnt(0)\n"
"s_barrier\n"
"v_add_f32 v248,0x3727c5ac,v248\n"
M_"v[84:87],a[84:87],v[20:23],v[84:87]\n"
"s_bfe_u32 s75,s39,0x20008\n"
T_"v[0:1],v192 offset:49152\n"
M_"v[84:87],a[88:91],v[24:27],v[84:87]\n"
"s_lshl_b32 s75,s75,20\n"
"v_rsq_f32 v240,v240\n"
M_"v[84:87],a[92:95],v[28:31],v[84:87]\n"
A_"s75,s75,s64\n"
T_"v[2:3],v192 offset:49664\n"
"s_nop 3\n"
M_"v[88:91],a[96:99],v[16:19],v[88:91]\n"
A_"m0,s56,65536\n"
T_"v[4:5],v192 offset:53248\n"
M_"v[88:91],a[100:103],v[20:23],v[88:91]\n"
"v_rsq_f32 v248,v248\n"
M_"v[88:91],a[104:107],v[24:27],v[88:91]\n"
B_"v196,s[48:51],s75 offen offset:0  lds\n"
M_"v[88:91],a[108:111],v[28:31],v[88:91]\n"
T_"v[6:7],v192 offset:53760\n"
"s_nop 3\n"
M_"v[92:95],a[112:115],v[16:19],v[92:95]\n"
T_"v[8:9],v192 offset:57344\n"
U_"v224,v240,v224\n"
M_"v[92:95],a[116:119],v[20:23],v[92:95]\n"
T_"v[10:11],v192 offset:57856\n"
M_"v[92:95],a[120:123],v[24:27],v[92:95]\n"
B_"v197,s[48:51],s75 offen offset:1024  lds\n"
M_"v[92:95],a[124:127],v[28:31],v[92:95]\n"
T_"v[12:13],v192 offset:61440\n"
U_"v232,v248,v232\n"
"s_nop 3\n"
M_"v[96:99],a[64:67],v[32:35],v[96:99]\n"
U_"v225,v240,v225\n"
M_"v[96:99],a[68:71],v[36:39],v[96:99]\n"
T_"v[14:15],v192 offset:61952\n"
T_"v[16:17],v193 offset:49152\n"
M_"v[96:99],a[72:75],v[40:43],v[96:99]\n"
B_"v198,s[48:51],s75 offen offset:2048  lds\n"
M_"v[96:99],a[76:79],v[44:47],v[96:99]\n"
T_"v[18:19],v193 offset:49664\n"
T_"v[20:21],v193 offset:53248\n"
"s_nop 3\n"
M_"v[100:103],a[80:83],v[32:35],v[100:103]\n"
U_"v233,v248,v233\n"
U_"v226,v240,v226\n"
M_"v[100:103],a[84:87],v[36:39],v[100:103]\n"
T_"v[22:23],v193 offset:53760\n"
U_"v234,v248,v234\n"
M_"v[100:103],a[88:91],v[40:43],v[100:103]\n"
B_"v199,s[48:51],s75 offen offset:3072  lds\n"
M_"v[100:103],a[92:95],v[44:47],v[100:103]\n"
T_"v[24:25],v193 offset:57344\n"
U_"v227,v240,v227\n"
"s_nop 3\n"
M_"v[104:107],a[96:99],v[32:35],v[104:107]\n"
T_"v[26:27],v193 offset:57856\n"
U_"v235,v248,v235\n"
M_"v[104:107],a[100:103],v[36:39],v[104:107]\n"
U_"v228,v240,v228\n"
M_"v[104:107],a[104:107],v[40:43],v[104:107]\n"
T_"v[28:29],v193 offset:61440\n"
U_"v236,v248,v236\n"
M_"v[104:107],a[108:111],v[44:47],v[104:107]\n"
"s_nop 3\n"
M_"v[108:111],a[112:115],v[32:35],v[108:111]\n"
T_"v[30:31],v193 offset:61952\n"
U_"v229,v240,v229\n"
M_"v[108:111],a[116:119],v[36:39],v[108:111]\n"
U_"v237,v248,v237\n"
M_"v[108:111],a[120:123],v[40:43],v[108:111]\n"
M_"v[108:111],a[124:127],v[44:47],v[108:111]\n"
U_"v230,v240,v230\n"
"s_nop 3\n"
M_"v[112:115],a[64:67],v[48:51],v[112:115]\n"
T_"v[32:33],v194 offset:49152\n"
U_"v238,v248,v238\n"
M_"v[112:115],a[68:71],v[52:55],v[112:115]\n"
T_"v[34:35],v194 offset:49664\n"
M_"v[112:115],a[72:75],v[56:59],v[112:115]\n"
U_"v231,v240,v231\n"
M_"v[112:115],a[76:79],v[60:63],v[112:115]\n"
T_"v[36:37],v194 offset:53248\n"
U_"v239,v248,v239\n"
"s_nop 3\n"
M_"v[116:119],a[80:83],v[48:51],v[116:119]\n"
W_"vmcnt(20)\n"
"v_xor_b32 v212,0x30000,v212\n"
"v_xor_b32 v213,0x30000,v213\n"
M_"v[116:119],a[84:87],v[52:55],v[116:119]\n"
"v_xor_b32 v214,0x30000,v214\n"
"v_xor_b32 v215,0x30000,v215\n"
M_"v[116:119],a[88:91],v[56:59],v[116:119]\n"
D_"a[0:3],v212 offset:0\n"
D_"a[4:7],v213 offset:0\n"
M_"v[116:119],a[92:95],v[60:63],v[116:119]\n"
D_"a[8:11],v214 offset:0\n"
D_"a[12:15],v215 offset:0\n"
"s_nop 3\n"
M_"v[120:123],a[96:99],v[48:51],v[120:123]\n"
W_"vmcnt(16)\n"
T_"v[38:39],v194 offset:53760\n"
D_"a[16:19],v212 offset:16384\n"
M_"v[120:123],a[100:103],v[52:55],v[120:123]\n"
D_"a[20:23],v213 offset:16384\n"
D_"a[24:27],v214 offset:16384\n"
M_"v[120:123],a[104:107],v[56:59],v[120:123]\n"
D_"a[28:31],v215 offset:16384\n"
T_"v[40:41],v194 offset:57344\n"
M_"v[120:123],a[108:111],v[60:63],v[120:123]\n"
T_"v[42:43],v194 offset:57856\n"
T_"v[44:45],v194 offset:61440\n"
"s_nop 3\n"
M_"v[124:127],a[112:115],v[48:51],v[124:127]\n"
"s_xor_b32 s75,s39,0\n"
T_"v[46:47],v194 offset:61952\n"
C_"v240,v224,v225\n"
M_"v[124:127],a[116:119],v[52:55],v[124:127]\n"
A_"m0,s56,81920\n"
C_"v248,v232,v233\n"
C_"v241,v226,v227\n"
M_"v[124:127],a[120:123],v[56:59],v[124:127]\n"
C_"v249,v234,v235\n"
C_"v242,v228,v229\n"
M_"v[124:127],a[124:127],v[60:63],v[124:127]\n"
W_"lgkmcnt(15)\n"
C_"v250,v236,v237\n"
W_"lgkmcnt(9)\n"
"s_nop 3\n"
M_"v[128:131],a[0:3],v[0:3],v[128:131]\n"
B_"v208,s[40:43],s75 offen offset:0  lds\n"
M_"v[128:131],a[4:7],v[4:7],v[128:131]\n"
T_"v[48:49],v195 offset:49152\n"
T_"v[50:51],v195 offset:49664\n"
M_"v[128:131],a[8:11],v[8:11],v[128:131]\n"
C_"v243,v230,v231\n"
C_"v251,v238,v239\n"
M_"v[128:131],a[12:15],v[12:15],v[128:131]\n"
W_"lgkmcnt(6)\n"
T_"v[52:53],v195 offset:53248\n"
"s_nop 3\n"
M_"v[132:135],a[16:19],v[0:3],v[132:135]\n"
B_"v209,s[40:43],s75 offen offset:1024  lds\n"
M_"v[132:135],a[20:23],v[4:7],v[132:135]\n"
T_"v[54:55],v195 offset:53760\n"
M_"v[132:135],a[24:27],v[8:11],v[132:135]\n"
T_"v[56:57],v195 offset:57344\n"
M_"v[132:135],a[28:31],v[12:15],v[132:135]\n"
"s_nop 3\n"
M_"v[144:147],a[0:3],v[16:19],v[144:147]\n"
B_"v210,s[40:43],s75 offen offset:2048  lds\n"
M_"v[144:147],a[4:7],v[20:23],v[144:147]\n"
T_"v[58:59],v195 offset:57856\n"
T_"v[60:61],v195 offset:61440\n"
M_"v[144:147],a[8:11],v[24:27],v[144:147]\n"
M_"v[144:147],a[12:15],v[28:31],v[144:147]\n"
T_"v[62:63],v195 offset:61952\n"
"s_nop 3\n"
M_"v[148:151],a[16:19],v[16:19],v[148:151]\n"
B_"v211,s[40:43],s75 offen offset:3072  lds\n"
M_"v[148:151],a[20:23],v[20:23],v[148:151]\n"
"s_xor_b32 s75,s39,16384\n"
A_"m0,s56,98304\n"
M_"v[148:151],a[24:27],v[24:27],v[148:151]\n"
M_"v[148:151],a[28:31],v[28:31],v[148:151]\n"
W_"lgkmcnt(8)\n"
"s_nop 3\n"
M_"v[160:163],a[0:3],v[32:35],v[160:163]\n"
B_"v208,s[40:43],s75 offen offset:0  lds\n"
M_"v[160:163],a[4:7],v[36:39],v[160:163]\n"
M_"v[160:163],a[8:11],v[40:43],v[160:163]\n"
M_"v[160:163],a[12:15],v[44:47],v[160:163]\n"
"s_nop 3\n"
M_"v[164:167],a[16:19],v[32:35],v[164:167]\n"
W_"vmcnt(13)\n"
"v_xor_b32 v212,0x20000,v212\n"
"v_xor_b32 v213,0x20000,v213\n"
M_"v[164:167],a[20:23],v[36:39],v[164:167]\n"
B_"v209,s[40:43],s75 offen offset:1024  lds\n"
M_"v[164:167],a[24:27],v[40:43],v[164:167]\n"
"v_xor_b32 v214,0x20000,v214\n"
"v_xor_b32 v215,0x20000,v215\n"
M_"v[164:167],a[28:31],v[44:47],v[164:167]\n"
W_"lgkmcnt(0)\n"
D_"a[32:35],v212 offset:16384\n"
D_"a[36:39],v213 offset:16384\n"
"s_nop 3\n"
M_"v[176:179],a[0:3],v[48:51],v[176:179]\n"
D_"a[40:43],v214 offset:16384\n"
D_"a[44:47],v215 offset:16384\n"
M_"v[176:179],a[4:7],v[52:55],v[176:179]\n"
W_"vmcnt(10)\n"
D_"a[48:51],v212 offset:32768\n"
D_"a[52:55],v213 offset:32768\n"
M_"v[176:179],a[8:11],v[56:59],v[176:179]\n"
B_"v210,s[40:43],s75 offen offset:2048  lds\n"
M_"v[176:179],a[12:15],v[60:63],v[176:179]\n"
D_"a[56:59],v214 offset:32768\n"
D_"a[60:63],v215 offset:32768\n"
"ds_write_b128 v220,v[240:243] offset:0\n"
"ds_write_b128 v220,v[248:251] offset:16\n"
"s_nop 3\n"
M_"v[180:183],a[16:19],v[48:51],v[180:183]\n"
M_"v[180:183],a[20:23],v[52:55],v[180:183]\n"
M_"v[180:183],a[24:27],v[56:59],v[180:183]\n"
B_"v211,s[40:43],s75 offen offset:3072  lds\n"
M_"v[180:183],a[28:31],v[60:63],v[180:183]\n"
W_"lgkmcnt(6)\n"
"s_nop 3\n"
M_"v[136:139],a[32:35],v[0:3],v[136:139]\n"
M_"v[136:139],a[36:39],v[4:7],v[136:139]\n"
M_"v[136:139],a[40:43],v[8:11],v[136:139]\n"
M_"v[136:139],a[44:47],v[12:15],v[136:139]\n"
W_"lgkmcnt(2)\n"
"s_nop 3\n"
M_"v[140:143],a[48:51],v[0:3],v[140:143]\n"
W_" lgkmcnt(0)\n"
"s_barrier\n"
M_"v[140:143],a[52:55],v[4:7],v[140:143]\n"
"s_bfe_u32 s75,s39,0x20008\n"
D_"v[240:243],v221 offset:0\n"
D_"v[248:251],v221 offset:256\n"
M_"v[140:143],a[56:59],v[8:11],v[140:143]\n"
"s_lshl_b32 s75,s75,20\n"
M_"v[140:143],a[60:63],v[12:15],v[140:143]\n"
A_"s75,s75,s65\n"
"s_nop 3\n"
M_"v[152:155],a[32:35],v[16:19],v[152:155]\n"
A_"m0,s56,114688\n"
"v_xor_b32 v192,0x10000,v192\n"
M_"v[152:155],a[36:39],v[20:23],v[152:155]\n"
"v_xor_b32 v193,0x10000,v193\n"
M_"v[152:155],a[40:43],v[24:27],v[152:155]\n"
B_"v196,s[48:51],s75 offen offset:0  lds\n"
M_"v[152:155],a[44:47],v[28:31],v[152:155]\n"
"v_xor_b32 v194,0x10000,v194\n"
"v_xor_b32 v195,0x10000,v195\n"
M_"v[156:159],a[48:51],v[16:19],v[156:159]\n"
T_"v[0:1],v192 offset:0\n"
M_"v[156:159],a[52:55],v[20:23],v[156:159]\n"
T_"v[2:3],v192 offset:512\n"
M_"v[156:159],a[56:59],v[24:27],v[156:159]\n"
B_"v197,s[48:51],s75 offen offset:1024  lds\n"
M_"v[156:159],a[60:63],v[28:31],v[156:159]\n"
T_"v[4:5],v192 offset:4096\n"
T_"v[6:7],v192 offset:4608\n"
M_"v[168:171],a[32:35],v[32:35],v[168:171]\n"
T_"v[8:9],v192 offset:8192\n"
M_"v[168:171],a[36:39],v[36:39],v[168:171]\n"
W_"lgkmcnt(5)\n"
T_"v[10:11],v192 offset:8704\n"
M_"v[168:171],a[40:43],v[40:43],v[168:171]\n"
S_"v[240:243],v222,s[52:55],s79 offen offset:0 sc1\n"
M_"v[168:171],a[44:47],v[44:47],v[168:171]\n"
T_"v[12:13],v192 offset:12288\n"
T_"v[14:15],v192 offset:12800\n"
M_"v[172:175],a[48:51],v[32:35],v[172:175]\n"
T_"v[16:17],v193 offset:0\n"
T_"v[18:19],v193 offset:512\n"
M_"v[172:175],a[52:55],v[36:39],v[172:175]\n"
S_"v[248:251],v222,s[52:55],s79 offen offset:2048 sc1\n"
M_"v[172:175],a[56:59],v[40:43],v[172:175]\n"
T_"v[20:21],v193 offset:4096\n"
T_"v[22:23],v193 offset:4608\n"
M_"v[172:175],a[60:63],v[44:47],v[172:175]\n"
T_"v[24:25],v193 offset:8192\n"
M_"v[184:187],a[32:35],v[48:51],v[184:187]\n"
T_"v[26:27],v193 offset:8704\n"
M_"v[184:187],a[36:39],v[52:55],v[184:187]\n"
B_"v198,s[48:51],s75 offen offset:2048  lds\n"
M_"v[184:187],a[40:43],v[56:59],v[184:187]\n"
T_"v[28:29],v193 offset:12288\n"
M_"v[184:187],a[44:47],v[60:63],v[184:187]\n"
B_"v199,s[48:51],s75 offen offset:3072  lds\n"
M_"v[188:191],a[48:51],v[48:51],v[188:191]\n"
"s_xor_b32 s75,s39,32768\n"
T_"v[30:31],v193 offset:12800\n"
T_"v[32:33],v194 offset:0\n"
M_"v[188:191],a[52:55],v[52:55],v[188:191]\n"
A_"m0,s56,131072\n"
T_"v[34:35],v194 offset:512\n"
T_"v[36:37],v194 offset:4096\n"
M_"v[188:191],a[56:59],v[56:59],v[188:191]\n"
T_"v[38:39],v194 offset:4608\n"
M_"v[188:191],a[60:63],v[60:63],v[188:191]\n"
W_"lgkmcnt(12)\n"
T_"v[40:41],v194 offset:8192\n"
M_"v[64:67],a[0:3],v[0:3],v[64:67]\n"
B_"v208,s[40:43],s75 offen offset:0  lds\n"
M_"v[64:67],a[4:7],v[4:7],v[64:67]\n"
T_"v[42:43],v194 offset:8704\n"
M_"v[64:67],a[8:11],v[8:11],v[64:67]\n"
T_"v[44:45],v194 offset:12288\n"
M_"v[64:67],a[12:15],v[12:15],v[64:67]\n"
M_"v[68:71],a[16:19],v[0:3],v[68:71]\n"
B_"v209,s[40:43],s75 offen offset:1024  lds\n"
M_"v[68:71],a[20:23],v[4:7],v[68:71]\n"
T_"v[46:47],v194 offset:12800\n"
T_"v[48:49],v195 offset:0\n"
M_"v[68:71],a[24:27],v[8:11],v[68:71]\n"
T_"v[50:51],v195 offset:512\n"
T_"v[52:53],v195 offset:4096\n"
M_"v[68:71],a[28:31],v[12:15],v[68:71]\n"
T_"v[54:55],v195 offset:4608\n"
T_"v[56:57],v195 offset:8192\n"
M_"v[72:75],a[32:35],v[0:3],v[72:75]\n"
B_"v210,s[40:43],s75 offen offset:2048  lds\n"
M_"v[72:75],a[36:39],v[4:7],v[72:75]\n"
T_"v[58:59],v195 offset:8704\n"
T_"v[60:61],v195 offset:12288\n"
M_"v[72:75],a[40:43],v[8:11],v[72:75]\n"
T_"v[62:63],v195 offset:12800\n"
M_"v[72:75],a[44:47],v[12:15],v[72:75]\n"
B_"v211,s[40:43],s75 offen offset:3072  lds\n"
M_"v[76:79],a[48:51],v[0:3],v[76:79]\n"
"s_xor_b32 s75,s39,49152\n"
A_"m0,s56,147456\n"
M_"v[76:79],a[52:55],v[4:7],v[76:79]\n"
M_"v[76:79],a[56:59],v[8:11],v[76:79]\n"
M_"v[76:79],a[60:63],v[12:15],v[76:79]\n"
W_"lgkmcnt(15)\n"
M_"v[80:83],a[0:3],v[16:19],v[80:83]\n"
B_"v208,s[40:43],s75 offen offset:0  lds\n"
M_"v[80:83],a[4:7],v[20:23],v[80:83]\n"
M_"v[80:83],a[8:11],v[24:27],v[80:83]\n"
M_"v[80:83],a[12:15],v[28:31],v[80:83]\n"
M_"v[84:87],a[16:19],v[16:19],v[84:87]\n"
W_"vmcnt(19) lgkmcnt(0)\n"
"s_barrier\n"
M_"v[84:87],a[20:23],v[20:23],v[84:87]\n"
B_"v209,s[40:43],s75 offen offset:1024  lds\n"
M_"v[84:87],a[24:27],v[24:27],v[84:87]\n"
"v_xor_b32 v192,0x10000,v192\n"
"v_xor_b32 v193,0x10000,v193\n"
M_"v[84:87],a[28:31],v[28:31],v[84:87]\n"
"v_xor_b32 v194,0x10000,v194\n"
M_"v[88:91],a[32:35],v[16:19],v[88:91]\n"
B_"v210,s[40:43],s75 offen offset:2048  lds\n"
M_"v[88:91],a[36:39],v[20:23],v[88:91]\n"
"v_xor_b32 v195,0x10000,v195\n"
T_"v[0:1],v192 offset:0\n"
M_"v[88:91],a[40:43],v[24:27],v[88:91]\n"
T_"v[2:3],v192 offset:512\n"
M_"v[88:91],a[44:47],v[28:31],v[88:91]\n"
B_"v211,s[40:43],s75 offen offset:3072  lds\n"
M_"v[92:95],a[48:51],v[16:19],v[92:95]\n"
"s_bfe_u32 s75,s36,0x20008\n"
T_"v[4:5],v192 offset:4096\n"
"s_lshl_b32 s75,s75,20\n"
M_"v[92:95],a[52:55],v[20:23],v[92:95]\n"
A_"s75,s75,s66\n"
T_"v[6:7],v192 offset:4608\n"
A_"m0,s56,0\n"
M_"v[92:95],a[56:59],v[24:27],v[92:95]\n"
T_"v[8:9],v192 offset:8192\n"
M_"v[92:95],a[60:63],v[28:31],v[92:95]\n"
T_"v[10:11],v192 offset:8704\n"
M_"v[96:99],a[0:3],v[32:35],v[96:99]\n"
B_"v196,s[48:51],s75 offen offset:0  lds\n"
M_"v[96:99],a[4:7],v[36:39],v[96:99]\n"
T_"v[12:13],v192 offset:12288\n"
T_"v[14:15],v192 offset:12800\n"
M_"v[96:99],a[8:11],v[40:43],v[96:99]\n"
T_"v[16:17],v193 offset:0\n"
T_"v[18:19],v193 offset:512\n"
M_"v[96:99],a[12:15],v[44:47],v[96:99]\n"
T_"v[20:21],v193 offset:4096\n"
M_"v[100:103],a[16:19],v[32:35],v[100:103]\n"
B_"v197,s[48:51],s75 offen offset:1024  lds\n"
M_"v[100:103],a[20:23],v[36:39],v[100:103]\n"
T_"v[22:23],v193 offset:4608\n"
M_"v[100:103],a[24:27],v[40:43],v[100:103]\n"
M_"v[100:103],a[28:31],v[44:47],v[100:103]\n"
T_"v[24:25],v193 offset:8192\n"
M_"v[104:107],a[32:35],v[32:35],v[104:107]\n"
B_"v198,s[48:51],s75 offen offset:2048  lds\n"
M_"v[104:107],a[36:39],v[36:39],v[104:107]\n"
T_"v[26:27],v193 offset:8704\n"
M_"v[104:107],a[40:43],v[40:43],v[104:107]\n"
T_"v[28:29],v193 offset:12288\n"
M_"v[104:107],a[44:47],v[44:47],v[104:107]\n"
B_"v199,s[48:51],s75 offen offset:3072  lds\n"
M_"v[108:111],a[48:51],v[32:35],v[108:111]\n"
"s_bfe_u32 s75,s37,0x20008\n"
T_"v[30:31],v193 offset:12800\n"
"s_lshl_b32 s75,s75,20\n"
M_"v[108:111],a[52:55],v[36:39],v[108:111]\n"
A_"s75,s75,s66\n"
A_"m0,s56,16384\n"
M_"v[108:111],a[56:59],v[40:43],v[108:111]\n"
M_"v[108:111],a[60:63],v[44:47],v[108:111]\n"
M_"v[112:115],a[0:3],v[48:51],v[112:115]\n"
B_"v196,s[48:51],s75 offen offset:0  lds\n"
M_"v[112:115],a[4:7],v[52:55],v[112:115]\n"
T_"v[32:33],v194 offset:0\n"
T_"v[34:35],v194 offset:512\n"
M_"v[112:115],a[8:11],v[56:59],v[112:115]\n"
M_"v[112:115],a[12:15],v[60:63],v[112:115]\n"
T_"v[36:37],v194 offset:4096\n"
M_"v[116:119],a[16:19],v[48:51],v[116:119]\n"
B_"v197,s[48:51],s75 offen offset:1024  lds\n"
M_"v[116:119],a[20:23],v[52:55],v[116:119]\n"
W_"vmcnt(24)\n"
"v_xor_b32 v212,0x10000,v212\n"
"v_xor_b32 v213,0x10000,v213\n"
M_"v[116:119],a[24:27],v[56:59],v[116:119]\n"
"v_xor_b32 v214,0x10000,v214\n"
"v_xor_b32 v215,0x10000,v215\n"
M_"v[116:119],a[28:31],v[60:63],v[116:119]\n"
D_"a[128:131],v212 offset:16384\n"
D_"a[132:135],v213 offset:16384\n"
M_"v[120:123],a[32:35],v[48:51],v[120:123]\n"
B_"v198,s[48:51],s75 offen offset:2048  lds\n"
M_"v[120:123],a[36:39],v[52:55],v[120:123]\n"
D_"a[136:139],v214 offset:16384\n"
D_"a[140:143],v215 offset:16384\n"
M_"v[120:123],a[40:43],v[56:59],v[120:123]\n"
"s_getpc_b64 s[44:45]\n"
"L_crV:\n"
"s_sub_u32 s44,s44,L_crV-L_m_org\n"
"s_subb_u32 s45,s45,0\n"
M_"v[120:123],a[44:47],v[60:63],v[120:123]\n"
B_"v199,s[48:51],s75 offen offset:3072  lds\n"
T_"v[38:39],v194 offset:4608\n"
W_"vmcnt(22)\n"
D_"a[144:147],v212 offset:32768\n"
D_"a[148:151],v213 offset:32768\n"
D_"a[152:155],v214 offset:32768\n"
D_"a[156:159],v215 offset:32768\n"
M_"v[124:127],a[48:51],v[48:51],v[124:127]\n"
"s_bfe_u32 s75,s38,0x20008\n"
T_"v[40:41],v194 offset:8192\n"
T_"v[42:43],v194 offset:8704\n"
M_"v[124:127],a[52:55],v[52:55],v[124:127]\n"
"s_lshl_b32 s75,s75,20\n"
T_"v[44:45],v194 offset:12288\n"
T_"v[46:47],v194 offset:12800\n"
M_"v[124:127],a[56:59],v[56:59],v[124:127]\n"
A_"s75,s75,s66\n"
A_"m0,s56,32768\n"
"s_mov_b32 s46,L_m_end-L_m_org\n"
M_"v[124:127],a[60:63],v[60:63],v[124:127]\n"
W_"lgkmcnt(15)\n"
"s_lshr_b32 s57,s30,3\n"
W_"lgkmcnt(9)\n"
M_"v[128:131],a[128:131],v[0:3],v[128:131]\n"
B_"v196,s[48:51],s75 offen offset:0  lds\n"
M_"v[128:131],a[132:135],v[4:7],v[128:131]\n"
"s_lshl2_add_u32 s57,s57,s31\n"
T_"v[48:49],v195 offset:0\n"
T_"v[50:51],v195 offset:512\n"
M_"v[128:131],a[136:139],v[8:11],v[128:131]\n"
M_"v[128:131],a[140:143],v[12:15],v[128:131]\n"
W_"lgkmcnt(6)\n"
T_"v[52:53],v195 offset:4096\n"
"s_lshl_b32 s57,s57,11\n"
M_"v[132:135],a[144:147],v[0:3],v[132:135]\n"
B_"v197,s[48:51],s75 offen offset:1024  lds\n"
M_"v[132:135],a[148:151],v[4:7],v[132:135]\n"
T_"v[54:55],v195 offset:4608\n"
"v_lshlrev_b32 v253,1,v244\n"
M_"v[132:135],a[152:155],v[8:11],v[132:135]\n"
T_"v[56:57],v195 offset:8192\n"
M_"v[132:135],a[156:159],v[12:15],v[132:135]\n"
"v_and_b32 v253,0x780,v253\n"
M_"v[144:147],a[128:131],v[16:19],v[144:147]\n"
B_"v198,s[48:51],s75 offen offset:2048  lds\n"
M_"v[144:147],a[132:135],v[20:23],v[144:147]\n"
"v_add_u32 v243,s57,v253\n"
T_"v[58:59],v195 offset:8704\n"
M_"v[144:147],a[136:139],v[24:27],v[144:147]\n"
T_"v[60:61],v195 offset:12288\n"
C_"v128,v128,v129\n"
M_"v[144:147],a[140:143],v[28:31],v[144:147]\n"
B_"v199,s[48:51],s75 offen offset:3072  lds\n"
M_"v[148:151],a[144:147],v[16:19],v[148:151]\n"
"s_bfe_u32 s75,s39,0x20008\n"
T_"v[62:63],v195 offset:12800\n"
"s_lshl_b32 s75,s75,20\n"
M_"v[148:151],a[148:151],v[20:23],v[148:151]\n"
A_"s75,s75,s66\n"
C_"v129,v130,v131\n"
A_"m0,s56,49152\n"
M_"v[148:151],a[152:155],v[24:27],v[148:151]\n"
M_"v[148:151],a[156:159],v[28:31],v[148:151]\n"
W_"lgkmcnt(8)\n"
M_"v[160:163],a[128:131],v[32:35],v[160:163]\n"
B_"v196,s[48:51],s75 offen offset:0  lds\n"
M_"v[160:163],a[132:135],v[36:39],v[160:163]\n"
"v_lshrrev_b32 v228,6,v244\n"
C_"v130,v132,v133\n"
M_"v[160:163],a[136:139],v[40:43],v[160:163]\n"
M_"v[160:163],a[140:143],v[44:47],v[160:163]\n"
M_"v[164:167],a[144:147],v[32:35],v[164:167]\n"
B_"v197,s[48:51],s75 offen offset:1024  lds\n"
M_"v[164:167],a[148:151],v[36:39],v[164:167]\n"
W_"vmcnt(18)\n"
"v_xor_b32 v212,0x30000,v212\n"
"v_xor_b32 v213,0x30000,v213\n"
M_"v[164:167],a[152:155],v[40:43],v[164:167]\n"
"v_xor_b32 v214,0x30000,v214\n"
"v_xor_b32 v215,0x30000,v215\n"
M_"v[164:167],a[156:159],v[44:47],v[164:167]\n"
W_"lgkmcnt(0)\n"
D_"a[160:163],v212 offset:0\n"
D_"a[164:167],v213 offset:0\n"
M_"v[176:179],a[128:131],v[48:51],v[176:179]\n"
B_"v198,s[48:51],s75 offen offset:2048  lds\n"
M_"v[176:179],a[132:135],v[52:55],v[176:179]\n"
D_"a[168:171],v214 offset:0\n"
D_"a[172:175],v215 offset:0\n"
M_"v[176:179],a[136:139],v[56:59],v[176:179]\n"
W_"vmcnt(15)\n"
D_"a[176:179],v212 offset:16384\n"
D_"a[180:183],v213 offset:16384\n"
M_"v[176:179],a[140:143],v[60:63],v[176:179]\n"
B_"v199,s[48:51],s75 offen offset:3072  lds\n"
D_"a[184:187],v214 offset:16384\n"
D_"a[188:191],v215 offset:16384\n"
M_"v[180:183],a[144:147],v[48:51],v[180:183]\n"
C_"v131,v134,v135\n"
M_"v[180:183],a[148:151],v[52:55],v[180:183]\n"
M_"v[180:183],a[152:155],v[56:59],v[180:183]\n"
M_"v[180:183],a[156:159],v[60:63],v[180:183]\n"
W_"lgkmcnt(4)\n"
M_"v[136:139],a[160:163],v[0:3],v[136:139]\n"
S_"v[128:131],v204,s[52:55],s70 offen offset:0 sc1\n"
M_"v[136:139],a[164:167],v[4:7],v[136:139]\n"
M_"v[136:139],a[168:171],v[8:11],v[136:139]\n"
M_"v[136:139],a[172:175],v[12:15],v[136:139]\n"
W_"lgkmcnt(0)\n"
M_"v[140:143],a[176:179],v[0:3],v[140:143]\n"
W_" lgkmcnt(0)\n"
"s_barrier\n"
M_"v[140:143],a[180:183],v[4:7],v[140:143]\n"
"s_bfe_u32 s75,s36,0x20008\n"
M_"v[140:143],a[184:187],v[8:11],v[140:143]\n"
"s_lshl_b32 s75,s75,20\n"
M_"v[140:143],a[188:191],v[12:15],v[140:143]\n"
A_"s75,s75,s67\n"
M_"v[152:155],a[160:163],v[16:19],v[152:155]\n"
A_"m0,s56,65536\n"
T_"v[0:1],v192 offset:49152\n"
M_"v[152:155],a[164:167],v[20:23],v[152:155]\n"
T_"v[2:3],v192 offset:49664\n"
M_"v[152:155],a[168:171],v[24:27],v[152:155]\n"
B_"v196,s[48:51],s75 offen offset:0  lds\n"
M_"v[152:155],a[172:175],v[28:31],v[152:155]\n"
T_"v[4:5],v192 offset:53248\n"
C_"v136,v136,v137\n"
M_"v[156:159],a[176:179],v[16:19],v[156:159]\n"
T_"v[6:7],v192 offset:53760\n"
M_"v[156:159],a[180:183],v[20:23],v[156:159]\n"
C_"v137,v138,v139\n"
M_"v[156:159],a[184:187],v[24:27],v[156:159]\n"
B_"v197,s[48:51],s75 offen offset:1024  lds\n"
M_"v[156:159],a[188:191],v[28:31],v[156:159]\n"
T_"v[8:9],v192 offset:57344\n"
M_"v[168:171],a[160:163],v[32:35],v[168:171]\n"
T_"v[10:11],v192 offset:57856\n"
M_"v[168:171],a[164:167],v[36:39],v[168:171]\n"
T_"v[12:13],v192 offset:61440\n"
C_"v138,v140,v141\n"
M_"v[168:171],a[168:171],v[40:43],v[168:171]\n"
B_"v198,s[48:51],s75 offen offset:2048  lds\n"
M_"v[168:171],a[172:175],v[44:47],v[168:171]\n"
T_"v[14:15],v192 offset:61952\n"
T_"v[16:17],v193 offset:49152\n"
M_"v[172:175],a[176:179],v[32:35],v[172:175]\n"
T_"v[18:19],v193 offset:49664\n"
T_"v[20:21],v193 offset:53248\n"
M_"v[172:175],a[180:183],v[36:39],v[172:175]\n"
T_"v[22:23],v193 offset:53760\n"
C_"v139,v142,v143\n"
M_"v[172:175],a[184:187],v[40:43],v[172:175]\n"
B_"v199,s[48:51],s75 offen offset:3072  lds\n"
M_"v[172:175],a[188:191],v[44:47],v[172:175]\n"
"s_bfe_u32 s75,s37,0x20008\n"
T_"v[24:25],v193 offset:57344\n"
"s_lshl_b32 s75,s75,20\n"
M_"v[184:187],a[160:163],v[48:51],v[184:187]\n"
A_"s75,s75,s67\n"
T_"v[26:27],v193 offset:57856\n"
A_"m0,s56,81920\n"
M_"v[184:187],a[164:167],v[52:55],v[184:187]\n"
M_"v[184:187],a[168:171],v[56:59],v[184:187]\n"
S_"v[136:139],v205,s[52:55],s70 offen offset:0 sc1\n"
M_"v[184:187],a[172:175],v[60:63],v[184:187]\n"
T_"v[28:29],v193 offset:61440\n"
M_"v[188:191],a[176:179],v[48:51],v[188:191]\n"
T_"v[30:31],v193 offset:61952\n"
T_"v[32:33],v194 offset:49152\n"
M_"v[188:191],a[180:183],v[52:55],v[188:191]\n"
T_"v[34:35],v194 offset:49664\n"
T_"v[36:37],v194 offset:53248\n"
M_"v[188:191],a[184:187],v[56:59],v[188:191]\n"
B_"v196,s[48:51],s75 offen offset:0  lds\n"
M_"v[188:191],a[188:191],v[60:63],v[188:191]\n"
W_"lgkmcnt(11)\n"
T_"v[38:39],v194 offset:53760\n"
T_"v[40:41],v194 offset:57344\n"
M_"v[64:67],a[128:131],v[0:3],v[64:67]\n"
T_"v[42:43],v194 offset:57856\n"
M_"v[64:67],a[132:135],v[4:7],v[64:67]\n"
M_"v[64:67],a[136:139],v[8:11],v[64:67]\n"
B_"v197,s[48:51],s75 offen offset:1024  lds\n"
M_"v[64:67],a[140:143],v[12:15],v[64:67]\n"
T_"v[44:45],v194 offset:61440\n"
C_"v144,v144,v145\n"
M_"v[68:71],a[144:147],v[0:3],v[68:71]\n"
T_"v[46:47],v194 offset:61952\n"
T_"v[48:49],v195 offset:49152\n"
M_"v[68:71],a[148:151],v[4:7],v[68:71]\n"
T_"v[50:51],v195 offset:49664\n"
T_"v[52:53],v195 offset:53248\n"
M_"v[68:71],a[152:155],v[8:11],v[68:71]\n"
B_"v198,s[48:51],s75 offen offset:2048  lds\n"
M_"v[68:71],a[156:159],v[12:15],v[68:71]\n"
T_"v[54:55],v195 offset:53760\n"
T_"v[56:57],v195 offset:57344\n"
M_"v[72:75],a[160:163],v[0:3],v[72:75]\n"
T_"v[58:59],v195 offset:57856\n"
T_"v[60:61],v195 offset:61440\n"
M_"v[72:75],a[164:167],v[4:7],v[72:75]\n"
T_"v[62:63],v195 offset:61952\n"
C_"v145,v146,v147\n"
M_"v[72:75],a[168:171],v[8:11],v[72:75]\n"
B_"v199,s[48:51],s75 offen offset:3072  lds\n"
M_"v[72:75],a[172:175],v[12:15],v[72:75]\n"
"s_bfe_u32 s75,s38,0x20008\n"
C_"v146,v148,v149\n"
"s_lshl_b32 s75,s75,20\n"
M_"v[76:79],a[176:179],v[0:3],v[76:79]\n"
"s_memtime s[60:61]\n"
"s_memrealtime s[62:63]\n"
C_"v147,v150,v151\n"
M_"v[76:79],a[180:183],v[4:7],v[76:79]\n"
A_"s75,s75,s67\n"
A_"m0,s56,98304\n"
M_"v[76:79],a[184:187],v[8:11],v[76:79]\n"
B_"v196,s[48:51],s75 offen offset:0  lds\n"
M_"v[76:79],a[188:191],v[12:15],v[76:79]\n"
W_"lgkmcnt(15)\n"
M_"v[80:83],a[128:131],v[16:19],v[80:83]\n"
M_"v[80:83],a[132:135],v[20:23],v[80:83]\n"
M_"v[80:83],a[136:139],v[24:27],v[80:83]\n"
B_"v197,s[48:51],s75 offen offset:1024  lds\n"
M_"v[80:83],a[140:143],v[28:31],v[80:83]\n"
M_"v[84:87],a[144:147],v[16:19],v[84:87]\n"
W_"vmcnt(12) lgkmcnt(0)\n"
"s_barrier\n"
M_"v[84:87],a[148:151],v[20:23],v[84:87]\n"
"v_xor_b32 v192,0x10000,v192\n"
M_"v[84:87],a[152:155],v[24:27],v[84:87]\n"
S_"v[144:147],v204,s[52:55],s70 offen offset:1024 sc1\n"
M_"v[84:87],a[156:159],v[28:31],v[84:87]\n"
"v_xor_b32 v193,0x10000,v193\n"
"v_xor_b32 v194,0x10000,v194\n"
M_"v[88:91],a[160:163],v[16:19],v[88:91]\n"
"v_xor_b32 v195,0x10000,v195\n"
M_"v[88:91],a[164:167],v[20:23],v[88:91]\n"
B_"v198,s[48:51],s75 offen offset:2048  lds\n"
M_"v[88:91],a[168:171],v[24:27],v[88:91]\n"
T_"v[0:1],v192 offset:0\n"
T_"v[2:3],v192 offset:512\n"
M_"v[88:91],a[172:175],v[28:31],v[88:91]\n"
B_"v199,s[48:51],s75 offen offset:3072  lds\n"
"buffer_store_dword v254,off,s[52:55],s78 sc1\n"
M_"v[92:95],a[176:179],v[16:19],v[92:95]\n"
"s_bfe_u32 s75,s39,0x20008\n"
C_"v152,v152,v153\n"
T_"v[4:5],v192 offset:4096\n"
M_"v[92:95],a[180:183],v[20:23],v[92:95]\n"
"s_lshl_b32 s75,s75,20\n"
T_"v[6:7],v192 offset:4608\n"
C_"v153,v154,v155\n"
M_"v[92:95],a[184:187],v[24:27],v[92:95]\n"
A_"s75,s75,s67\n"
T_"v[8:9],v192 offset:8192\n"
A_"m0,s56,114688\n"
M_"v[92:95],a[188:191],v[28:31],v[92:95]\n"
T_"v[10:11],v192 offset:8704\n"
M_"v[96:99],a[128:131],v[32:35],v[96:99]\n"
B_"v196,s[48:51],s75 offen offset:0  lds\n"
M_"v[96:99],a[132:135],v[36:39],v[96:99]\n"
T_"v[12:13],v192 offset:12288\n"
T_"v[14:15],v192 offset:12800\n"
M_"v[96:99],a[136:139],v[40:43],v[96:99]\n"
T_"v[16:17],v192 offset:16384\n"
T_"v[18:19],v192 offset:16896\n"
M_"v[96:99],a[140:143],v[44:47],v[96:99]\n"
C_"v154,v156,v157\n"
T_"v[20:21],v192 offset:20480\n"
M_"v[100:103],a[144:147],v[32:35],v[100:103]\n"
B_"v197,s[48:51],s75 offen offset:1024  lds\n"
M_"v[100:103],a[148:151],v[36:39],v[100:103]\n"
T_"v[22:23],v192 offset:20992\n"
C_"v155,v158,v159\n"
M_"v[100:103],a[152:155],v[40:43],v[100:103]\n"
M_"v[100:103],a[156:159],v[44:47],v[100:103]\n"
T_"v[24:25],v192 offset:24576\n"
M_"v[104:107],a[160:163],v[32:35],v[104:107]\n"
B_"v198,s[48:51],s75 offen offset:2048  lds\n"
M_"v[104:107],a[164:167],v[36:39],v[104:107]\n"
T_"v[26:27],v192 offset:25088\n"
M_"v[104:107],a[168:171],v[40:43],v[104:107]\n"
T_"v[28:29],v192 offset:28672\n"
M_"v[104:107],a[172:175],v[44:47],v[104:107]\n"
B_"v199,s[48:51],s75 offen offset:3072  lds\n"
M_"v[108:111],a[176:179],v[32:35],v[108:111]\n"
T_"v[30:31],v192 offset:29184\n"
M_"v[108:111],a[180:183],v[36:39],v[108:111]\n"
M_"v[108:111],a[184:187],v[40:43],v[108:111]\n"
M_"v[108:111],a[188:191],v[44:47],v[108:111]\n"
M_"v[112:115],a[128:131],v[48:51],v[112:115]\n"
S_"v[152:155],v205,s[52:55],s70 offen offset:1024 sc1\n"
M_"v[112:115],a[132:135],v[52:55],v[112:115]\n"
T_"v[32:33],v192 offset:32768\n"
T_"v[34:35],v192 offset:33280\n"
M_"v[112:115],a[136:139],v[56:59],v[112:115]\n"
C_"v160,v160,v161\n"
M_"v[112:115],a[140:143],v[60:63],v[112:115]\n"
T_"v[36:37],v192 offset:36864\n"
M_"v[116:119],a[144:147],v[48:51],v[116:119]\n"
M_"v[116:119],a[148:151],v[52:55],v[116:119]\n"
T_"v[38:39],v192 offset:37376\n"
C_"v161,v162,v163\n"
M_"v[116:119],a[152:155],v[56:59],v[116:119]\n"
M_"v[116:119],a[156:159],v[60:63],v[116:119]\n"
T_"v[40:41],v192 offset:40960\n"
M_"v[120:123],a[160:163],v[48:51],v[120:123]\n"
T_"v[42:43],v192 offset:41472\n"
M_"v[120:123],a[164:167],v[52:55],v[120:123]\n"
C_"v162,v164,v165\n"
M_"v[120:123],a[168:171],v[56:59],v[120:123]\n"
T_"v[44:45],v192 offset:45056\n"
M_"v[120:123],a[172:175],v[60:63],v[120:123]\n"
M_"v[124:127],a[176:179],v[48:51],v[124:127]\n"
T_"v[46:47],v192 offset:45568\n"
C_"v163,v166,v167\n"
M_"v[124:127],a[180:183],v[52:55],v[124:127]\n"
M_"v[124:127],a[184:187],v[56:59],v[124:127]\n"
M_"v[124:127],a[188:191],v[60:63],v[124:127]\n"
W_"lgkmcnt(15)\n"
M_"v[128:131],a[192:195],v[0:3],0\n"
S_"v[160:163],v204,s[52:55],s70 offen offset:2048 sc1\n"
M_"v[128:131],a[196:199],v[4:7],v[128:131]\n"
T_"v[48:49],v192 offset:49152\n"
T_"v[50:51],v192 offset:49664\n"
M_"v[128:131],a[200:203],v[8:11],v[128:131]\n"
M_"v[128:131],a[204:207],v[12:15],v[128:131]\n"
T_"v[52:53],v192 offset:53248\n"
M_"v[132:135],a[208:211],v[0:3],0\n"
M_"v[132:135],a[212:215],v[4:7],v[132:135]\n"
T_"v[54:55],v192 offset:53760\n"
M_"v[132:135],a[216:219],v[8:11],v[132:135]\n"
M_"v[132:135],a[220:223],v[12:15],v[132:135]\n"
T_"v[56:57],v192 offset:57344\n"
M_"v[136:139],a[224:227],v[0:3],0\n"
"s_mov_b64 s[40:41],s[20:21]\n"
T_"v[58:59],v192 offset:57856\n"
M_"v[136:139],a[228:231],v[4:7],v[136:139]\n"
M_"v[136:139],a[232:235],v[8:11],v[136:139]\n"
"s_mov_b32 s42,0x1c0000\n"
T_"v[60:61],v192 offset:61440\n"
C_"v168,v168,v169\n"
M_"v[136:139],a[236:239],v[12:15],v[136:139]\n"
M_"v[140:143],a[240:243],v[0:3],0\n"
"v_mul_u32_u24 v229,3584,v228\n"
T_"v[62:63],v192 offset:61952\n"
M_"v[140:143],a[244:247],v[4:7],v[140:143]\n"
C_"v169,v170,v171\n"
M_"v[140:143],a[248:251],v[8:11],v[140:143]\n"
"buffer_load_dword v229,v229,s[40:43],s88 offen\n"
M_"v[140:143],a[252:255],v[12:15],v[140:143]\n"
W_"lgkmcnt(15)\n"
M_"v[128:131],a[64:67],v[16:19],v[128:131]\n"
T_"v[0:1],v193 offset:0\n"
M_"v[128:131],a[68:71],v[20:23],v[128:131]\n"
T_"v[2:3],v193 offset:512\n"
C_"v170,v172,v173\n"
M_"v[128:131],a[72:75],v[24:27],v[128:131]\n"
M_"v[128:131],a[76:79],v[28:31],v[128:131]\n"
T_"v[4:5],v193 offset:4096\n"
M_"v[132:135],a[80:83],v[16:19],v[132:135]\n"
C_"v171,v174,v175\n"
M_"v[132:135],a[84:87],v[20:23],v[132:135]\n"
T_"v[6:7],v193 offset:4608\n"
M_"v[132:135],a[88:91],v[24:27],v[132:135]\n"
M_"v[132:135],a[92:95],v[28:31],v[132:135]\n"
T_"v[8:9],v193 offset:8192\n"
M_"v[136:139],a[96:99],v[16:19],v[136:139]\n"
S_"v[168:171],v205,s[52:55],s70 offen offset:2048 sc1\n"
M_"v[136:139],a[100:103],v[20:23],v[136:139]\n"
T_"v[10:11],v193 offset:8704\n"
M_"v[136:139],a[104:107],v[24:27],v[136:139]\n"
T_"v[12:13],v193 offset:12288\n"
M_"v[136:139],a[108:111],v[28:31],v[136:139]\n"
M_"v[140:143],a[112:115],v[16:19],v[140:143]\n"
T_"v[14:15],v193 offset:12800\n"
M_"v[140:143],a[116:119],v[20:23],v[140:143]\n"
M_"v[140:143],a[120:123],v[24:27],v[140:143]\n"
M_"v[140:143],a[124:127],v[28:31],v[140:143]\n"
W_"lgkmcnt(15)\n"
M_"v[128:131],a[0:3],v[32:35],v[128:131]\n"
T_"v[16:17],v193 offset:16384\n"
M_"v[128:131],a[4:7],v[36:39],v[128:131]\n"
T_"v[18:19],v193 offset:16896\n"
M_"v[128:131],a[8:11],v[40:43],v[128:131]\n"
C_"v176,v176,v177\n"
M_"v[128:131],a[12:15],v[44:47],v[128:131]\n"
T_"v[20:21],v193 offset:20480\n"
M_"v[132:135],a[16:19],v[32:35],v[132:135]\n"
M_"v[132:135],a[20:23],v[36:39],v[132:135]\n"
T_"v[22:23],v193 offset:20992\n"
C_"v177,v178,v179\n"
M_"v[132:135],a[24:27],v[40:43],v[132:135]\n"
M_"v[132:135],a[28:31],v[44:47],v[132:135]\n"
T_"v[24:25],v193 offset:24576\n"
M_"v[136:139],a[32:35],v[32:35],v[136:139]\n"
T_"v[26:27],v193 offset:25088\n"
M_"v[136:139],a[36:39],v[36:39],v[136:139]\n"
C_"v178,v180,v181\n"
M_"v[136:139],a[40:43],v[40:43],v[136:139]\n"
T_"v[28:29],v193 offset:28672\n"
M_"v[136:139],a[44:47],v[44:47],v[136:139]\n"
M_"v[140:143],a[48:51],v[32:35],v[140:143]\n"
"s_mov_b32 s94,0x7f000000\n"
T_"v[30:31],v193 offset:29184\n"
C_"v179,v182,v183\n"
M_"v[140:143],a[52:55],v[36:39],v[140:143]\n"
M_"v[140:143],a[56:59],v[40:43],v[140:143]\n"
M_"v[140:143],a[60:63],v[44:47],v[140:143]\n"
W_"lgkmcnt(15)\n"
M_"v[128:131],a[128:131],v[48:51],v[128:131]\n"
A_"s78,s78,4\n"
T_"v[32:33],v193 offset:32768\n"
M_"v[128:131],a[132:135],v[52:55],v[128:131]\n"
S_"v[176:179],v204,s[52:55],s70 offen offset:3072 sc1\n"
M_"v[128:131],a[136:139],v[56:59],v[128:131]\n"
T_"v[34:35],v193 offset:33280\n"
M_"v[128:131],a[140:143],v[60:63],v[128:131]\n"
T_"v[36:37],v193 offset:36864\n"
M_"v[132:135],a[144:147],v[48:51],v[132:135]\n"
M_"v[132:135],a[148:151],v[52:55],v[132:135]\n"
T_"v[38:39],v193 offset:37376\n"
M_"v[132:135],a[152:155],v[56:59],v[132:135]\n"
M_"v[132:135],a[156:159],v[60:63],v[132:135]\n"
T_"v[40:41],v193 offset:40960\n"
M_"v[136:139],a[160:163],v[48:51],v[136:139]\n"
T_"v[42:43],v193 offset:41472\n"
M_"v[136:139],a[164:167],v[52:55],v[136:139]\n"
M_"v[136:139],a[168:171],v[56:59],v[136:139]\n"
"s_mov_b64 s[40:41],s[22:23]\n"
T_"v[44:45],v193 offset:45056\n"
C_"v184,v184,v185\n"
M_"v[136:139],a[172:175],v[60:63],v[136:139]\n"
M_"v[140:143],a[176:179],v[48:51],v[140:143]\n"
"s_mov_b32 s42,0x1c0000\n"
T_"v[46:47],v193 offset:45568\n"
M_"v[140:143],a[180:183],v[52:55],v[140:143]\n"
C_"v185,v186,v187\n"
M_"v[140:143],a[184:187],v[56:59],v[140:143]\n"
"v_mul_u32_u24 v230,3584,v228\n"
M_"v[140:143],a[188:191],v[60:63],v[140:143]\n"
W_"lgkmcnt(15)\n"
M_"v[144:147],a[192:195],v[0:3],0\n"
"buffer_load_dword v230,v230,s[40:43],s88 offen\n"
M_"v[144:147],a[196:199],v[4:7],v[144:147]\n"
T_"v[48:49],v193 offset:49152\n"
T_"v[50:51],v193 offset:49664\n"
M_"v[144:147],a[200:203],v[8:11],v[144:147]\n"
C_"v186,v188,v189\n"
M_"v[144:147],a[204:207],v[12:15],v[144:147]\n"
T_"v[52:53],v193 offset:53248\n"
M_"v[148:151],a[208:211],v[0:3],0\n"
C_"v187,v190,v191\n"
M_"v[148:151],a[212:215],v[4:7],v[148:151]\n"
T_"v[54:55],v193 offset:53760\n"
M_"v[148:151],a[216:219],v[8:11],v[148:151]\n"
M_"v[148:151],a[220:223],v[12:15],v[148:151]\n"
T_"v[56:57],v193 offset:57344\n"
M_"v[152:155],a[224:227],v[0:3],0\n"
S_"v[184:187],v205,s[52:55],s70 offen offset:3072 sc1\n"
M_"v[152:155],a[228:231],v[4:7],v[152:155]\n"
T_"v[58:59],v193 offset:57856\n"
M_"v[152:155],a[232:235],v[8:11],v[152:155]\n"
T_"v[60:61],v193 offset:61440\n"
M_"v[152:155],a[236:239],v[12:15],v[152:155]\n"
M_"v[156:159],a[240:243],v[0:3],0\n"
T_"v[62:63],v193 offset:61952\n"
M_"v[156:159],a[244:247],v[4:7],v[156:159]\n"
M_"v[156:159],a[248:251],v[8:11],v[156:159]\n"
M_"v[156:159],a[252:255],v[12:15],v[156:159]\n"
W_"lgkmcnt(15)\n"
M_"v[144:147],a[64:67],v[16:19],v[144:147]\n"
T_"v[0:1],v194 offset:0\n"
M_"v[144:147],a[68:71],v[20:23],v[144:147]\n"
T_"v[2:3],v194 offset:512\n"
M_"v[144:147],a[72:75],v[24:27],v[144:147]\n"
C_"v64,v64,v65\n"
M_"v[144:147],a[76:79],v[28:31],v[144:147]\n"
T_"v[4:5],v194 offset:4096\n"
M_"v[148:151],a[80:83],v[16:19],v[148:151]\n"
M_"v[148:151],a[84:87],v[20:23],v[148:151]\n"
T_"v[6:7],v194 offset:4608\n"
C_"v65,v66,v67\n"
M_"v[148:151],a[88:91],v[24:27],v[148:151]\n"
M_"v[148:151],a[92:95],v[28:31],v[148:151]\n"
T_"v[8:9],v194 offset:8192\n"
M_"v[152:155],a[96:99],v[16:19],v[152:155]\n"
T_"v[10:11],v194 offset:8704\n"
M_"v[152:155],a[100:103],v[20:23],v[152:155]\n"
C_"v66,v68,v69\n"
M_"v[152:155],a[104:107],v[24:27],v[152:155]\n"
T_"v[12:13],v194 offset:12288\n"
M_"v[152:155],a[108:111],v[28:31],v[152:155]\n"
M_"v[156:159],a[112:115],v[16:19],v[156:159]\n"
T_"v[14:15],v194 offset:12800\n"
C_"v67,v70,v71\n"
M_"v[156:159],a[116:119],v[20:23],v[156:159]\n"
M_"v[156:159],a[120:123],v[24:27],v[156:159]\n"
M_"v[156:159],a[124:127],v[28:31],v[156:159]\n"
W_"lgkmcnt(15)\n"
M_"v[144:147],a[0:3],v[32:35],v[144:147]\n"
S_"v[64:67],v204,s[52:55],s71 offen offset:0 sc1\n"
M_"v[144:147],a[4:7],v[36:39],v[144:147]\n"
T_"v[16:17],v194 offset:16384\n"
T_"v[18:19],v194 offset:16896\n"
M_"v[144:147],a[8:11],v[40:43],v[144:147]\n"
M_"v[144:147],a[12:15],v[44:47],v[144:147]\n"
T_"v[20:21],v194 offset:20480\n"
M_"v[148:151],a[16:19],v[32:35],v[148:151]\n"
M_"v[148:151],a[20:23],v[36:39],v[148:151]\n"
T_"v[22:23],v194 offset:20992\n"
M_"v[148:151],a[24:27],v[40:43],v[148:151]\n"
M_"v[148:151],a[28:31],v[44:47],v[148:151]\n"
T_"v[24:25],v194 offset:24576\n"
M_"v[152:155],a[32:35],v[32:35],v[152:155]\n"
T_"v[26:27],v194 offset:25088\n"
M_"v[152:155],a[36:39],v[36:39],v[152:155]\n"
M_"v[152:155],a[40:43],v[40:43],v[152:155]\n"
T_"v[28:29],v194 offset:28672\n"
C_"v72,v72,v73\n"
M_"v[152:155],a[44:47],v[44:47],v[152:155]\n"
M_"v[156:159],a[48:51],v[32:35],v[156:159]\n"
"s_mov_b64 s[40:41],s[12:13]\n"
T_"v[30:31],v194 offset:29184\n"
M_"v[156:159],a[52:55],v[36:39],v[156:159]\n"
C_"v73,v74,v75\n"
M_"v[156:159],a[56:59],v[40:43],v[156:159]\n"
"s_mov_b32 s42,0x400000\n"
M_"v[156:159],a[60:63],v[44:47],v[156:159]\n"
W_"lgkmcnt(15)\n"
M_"v[144:147],a[128:131],v[48:51],v[144:147]\n"
"v_mul_u32_u24 v231,8192,v228\n"
T_"v[32:33],v194 offset:32768\n"
M_"v[144:147],a[132:135],v[52:55],v[144:147]\n"
T_"v[34:35],v194 offset:33280\n"
C_"v74,v76,v77\n"
M_"v[144:147],a[136:139],v[56:59],v[144:147]\n"
"buffer_load_dword v231,v231,s[40:43],s90 offen\n"
M_"v[144:147],a[140:143],v[60:63],v[144:147]\n"
T_"v[36:37],v194 offset:36864\n"
M_"v[148:151],a[144:147],v[48:51],v[148:151]\n"
C_"v75,v78,v79\n"
M_"v[148:151],a[148:151],v[52:55],v[148:151]\n"
T_"v[38:39],v194 offset:37376\n"
M_"v[148:151],a[152:155],v[56:59],v[148:151]\n"
M_"v[148:151],a[156:159],v[60:63],v[148:151]\n"
T_"v[40:41],v194 offset:40960\n"
M_"v[152:155],a[160:163],v[48:51],v[152:155]\n"
S_"v[72:75],v205,s[52:55],s71 offen offset:0 sc1\n"
M_"v[152:155],a[164:167],v[52:55],v[152:155]\n"
T_"v[42:43],v194 offset:41472\n"
M_"v[152:155],a[168:171],v[56:59],v[152:155]\n"
T_"v[44:45],v194 offset:45056\n"
M_"v[152:155],a[172:175],v[60:63],v[152:155]\n"
M_"v[156:159],a[176:179],v[48:51],v[156:159]\n"
T_"v[46:47],v194 offset:45568\n"
M_"v[156:159],a[180:183],v[52:55],v[156:159]\n"
M_"v[156:159],a[184:187],v[56:59],v[156:159]\n"
M_"v[156:159],a[188:191],v[60:63],v[156:159]\n"
W_"lgkmcnt(15)\n"
M_"v[160:163],a[192:195],v[0:3],0\n"
T_"v[48:49],v194 offset:49152\n"
M_"v[160:163],a[196:199],v[4:7],v[160:163]\n"
T_"v[50:51],v194 offset:49664\n"
M_"v[160:163],a[200:203],v[8:11],v[160:163]\n"
C_"v80,v80,v81\n"
M_"v[160:163],a[204:207],v[12:15],v[160:163]\n"
T_"v[52:53],v194 offset:53248\n"
M_"v[164:167],a[208:211],v[0:3],0\n"
M_"v[164:167],a[212:215],v[4:7],v[164:167]\n"
T_"v[54:55],v194 offset:53760\n"
C_"v81,v82,v83\n"
M_"v[164:167],a[216:219],v[8:11],v[164:167]\n"
M_"v[164:167],a[220:223],v[12:15],v[164:167]\n"
T_"v[56:57],v194 offset:57344\n"
M_"v[168:171],a[224:227],v[0:3],0\n"
T_"v[58:59],v194 offset:57856\n"
M_"v[168:171],a[228:231],v[4:7],v[168:171]\n"
C_"v82,v84,v85\n"
M_"v[168:171],a[232:235],v[8:11],v[168:171]\n"
"v_bfe_u32 v241,v244,4,5\n"
T_"v[60:61],v194 offset:61440\n"
M_"v[168:171],a[236:239],v[12:15],v[168:171]\n"
"v_lshlrev_b32 v241,7,v241\n"
M_"v[172:175],a[240:243],v[0:3],0\n"
"s_lshl_b32 s79,s34,12\n"
T_"v[62:63],v194 offset:61952\n"
C_"v83,v86,v87\n"
M_"v[172:175],a[244:247],v[4:7],v[172:175]\n"
M_"v[172:175],a[248:251],v[8:11],v[172:175]\n"
A_"s79,s79,0x4f10010\n"
M_"v[172:175],a[252:255],v[12:15],v[172:175]\n"
W_"lgkmcnt(15)\n"
M_"v[160:163],a[64:67],v[16:19],v[160:163]\n"
"buffer_load_dword v240,v241,s[52:55],s79 offen sc1\n"
M_"v[160:163],a[68:71],v[20:23],v[160:163]\n"
S_"v[80:83],v204,s[52:55],s71 offen offset:1024 sc1\n"
M_"v[160:163],a[72:75],v[24:27],v[160:163]\n"
T_"v[0:1],v195 offset:0\n"
T_"v[2:3],v195 offset:512\n"
M_"v[160:163],a[76:79],v[28:31],v[160:163]\n"
T_"v[4:5],v195 offset:4096\n"
M_"v[164:167],a[80:83],v[16:19],v[164:167]\n"
M_"v[164:167],a[84:87],v[20:23],v[164:167]\n"
T_"v[6:7],v195 offset:4608\n"
M_"v[164:167],a[88:91],v[24:27],v[164:167]\n"
M_"v[164:167],a[92:95],v[28:31],v[164:167]\n"
T_"v[8:9],v195 offset:8192\n"
M_"v[168:171],a[96:99],v[16:19],v[168:171]\n"
T_"v[10:11],v195 offset:8704\n"
M_"v[168:171],a[100:103],v[20:23],v[168:171]\n"
M_"v[168:171],a[104:107],v[24:27],v[168:171]\n"
T_"v[12:13],v195 offset:12288\n"
C_"v88,v88,v89\n"
M_"v[168:171],a[108:111],v[28:31],v[168:171]\n"
M_"v[172:175],a[112:115],v[16:19],v[172:175]\n"
T_"v[14:15],v195 offset:12800\n"
M_"v[172:175],a[116:119],v[20:23],v[172:175]\n"
C_"v89,v90,v91\n"
M_"v[172:175],a[120:123],v[24:27],v[172:175]\n"
M_"v[172:175],a[124:127],v[28:31],v[172:175]\n"
W_"lgkmcnt(15)\n"
M_"v[160:163],a[0:3],v[32:35],v[160:163]\n"
T_"v[16:17],v195 offset:16384\n"
M_"v[160:163],a[4:7],v[36:39],v[160:163]\n"
T_"v[18:19],v195 offset:16896\n"
C_"v90,v92,v93\n"
M_"v[160:163],a[8:11],v[40:43],v[160:163]\n"
M_"v[160:163],a[12:15],v[44:47],v[160:163]\n"
T_"v[20:21],v195 offset:20480\n"
M_"v[164:167],a[16:19],v[32:35],v[164:167]\n"
C_"v91,v94,v95\n"
M_"v[164:167],a[20:23],v[36:39],v[164:167]\n"
T_"v[22:23],v195 offset:20992\n"
M_"v[164:167],a[24:27],v[40:43],v[164:167]\n"
M_"v[164:167],a[28:31],v[44:47],v[164:167]\n"
T_"v[24:25],v195 offset:24576\n"
M_"v[168:171],a[32:35],v[32:35],v[168:171]\n"
S_"v[88:91],v205,s[52:55],s71 offen offset:1024 sc1\n"
M_"v[168:171],a[36:39],v[36:39],v[168:171]\n"
T_"v[26:27],v195 offset:25088\n"
M_"v[168:171],a[40:43],v[40:43],v[168:171]\n"
T_"v[28:29],v195 offset:28672\n"
M_"v[168:171],a[44:47],v[44:47],v[168:171]\n"
M_"v[172:175],a[48:51],v[32:35],v[172:175]\n"
T_"v[30:31],v195 offset:29184\n"
M_"v[172:175],a[52:55],v[36:39],v[172:175]\n"
M_"v[172:175],a[56:59],v[40:43],v[172:175]\n"
M_"v[172:175],a[60:63],v[44:47],v[172:175]\n"
W_"lgkmcnt(15)\n"
M_"v[160:163],a[128:131],v[48:51],v[160:163]\n"
T_"v[32:33],v195 offset:32768\n"
M_"v[160:163],a[132:135],v[52:55],v[160:163]\n"
T_"v[34:35],v195 offset:33280\n"
M_"v[160:163],a[136:139],v[56:59],v[160:163]\n"
C_"v96,v96,v97\n"
M_"v[160:163],a[140:143],v[60:63],v[160:163]\n"
T_"v[36:37],v195 offset:36864\n"
M_"v[164:167],a[144:147],v[48:51],v[164:167]\n"
M_"v[164:167],a[148:151],v[52:55],v[164:167]\n"
T_"v[38:39],v195 offset:37376\n"
C_"v97,v98,v99\n"
M_"v[164:167],a[152:155],v[56:59],v[164:167]\n"
M_"v[164:167],a[156:159],v[60:63],v[164:167]\n"
T_"v[40:41],v195 offset:40960\n"
M_"v[168:171],a[160:163],v[48:51],v[168:171]\n"
T_"v[42:43],v195 offset:41472\n"
M_"v[168:171],a[164:167],v[52:55],v[168:171]\n"
C_"v98,v100,v101\n"
M_"v[168:171],a[168:171],v[56:59],v[168:171]\n"
T_"v[44:45],v195 offset:45056\n"
M_"v[168:171],a[172:175],v[60:63],v[168:171]\n"
M_"v[172:175],a[176:179],v[48:51],v[172:175]\n"
T_"v[46:47],v195 offset:45568\n"
C_"v99,v102,v103\n"
M_"v[172:175],a[180:183],v[52:55],v[172:175]\n"
M_"v[172:175],a[184:187],v[56:59],v[172:175]\n"
M_"v[172:175],a[188:191],v[60:63],v[172:175]\n"
W_"lgkmcnt(15)\n"
M_"v[176:179],a[192:195],v[0:3],0\n"
S_"v[96:99],v204,s[52:55],s71 offen offset:2048 sc1\n"
M_"v[176:179],a[196:199],v[4:7],v[176:179]\n"
T_"v[48:49],v195 offset:49152\n"
T_"v[50:51],v195 offset:49664\n"
M_"v[176:179],a[200:203],v[8:11],v[176:179]\n"
T_"v[52:53],v195 offset:53248\n"
M_"v[176:179],a[204:207],v[12:15],v[176:179]\n"
M_"v[180:183],a[208:211],v[0:3],0\n"
T_"v[54:55],v195 offset:53760\n"
M_"v[180:183],a[212:215],v[4:7],v[180:183]\n"
T_"v[56:57],v195 offset:57344\n"
M_"v[180:183],a[216:219],v[8:11],v[180:183]\n"
T_"v[58:59],v195 offset:57856\n"
M_"v[180:183],a[220:223],v[12:15],v[180:183]\n"
M_"v[184:187],a[224:227],v[0:3],0\n"
T_"v[60:61],v195 offset:61440\n"
M_"v[184:187],a[228:231],v[4:7],v[184:187]\n"
T_"v[62:63],v195 offset:61952\n"
M_"v[184:187],a[232:235],v[8:11],v[184:187]\n"
C_"v104,v104,v105\n"
M_"v[184:187],a[236:239],v[12:15],v[184:187]\n"
M_"v[188:191],a[240:243],v[0:3],0\n"
M_"v[188:191],a[244:247],v[4:7],v[188:191]\n"
C_"v105,v106,v107\n"
M_"v[188:191],a[248:251],v[8:11],v[188:191]\n"
M_"v[188:191],a[252:255],v[12:15],v[188:191]\n"
W_"lgkmcnt(15)\n"
M_"v[176:179],a[64:67],v[16:19],v[176:179]\n"
M_"v[176:179],a[68:71],v[20:23],v[176:179]\n"
C_"v106,v108,v109\n"
M_"v[176:179],a[72:75],v[24:27],v[176:179]\n"
M_"v[176:179],a[76:79],v[28:31],v[176:179]\n"
M_"v[180:183],a[80:83],v[16:19],v[180:183]\n"
W_"vmcnt(14) lgkmcnt(0)\n"
"s_barrier\n"
C_"v107,v110,v111\n"
M_"v[180:183],a[84:87],v[20:23],v[180:183]\n"
"v_xor_b32 v192,0x10000,v192\n"
M_"v[180:183],a[88:91],v[24:27],v[180:183]\n"
"s_mov_b64 s[48:49],s[20:21]\n"
"v_xor_b32 v193,0x10000,v193\n"
M_"v[180:183],a[92:95],v[28:31],v[180:183]\n"
"s_mov_b32 s50,0x1c0000\n"
"v_xor_b32 v194,0x10000,v194\n"
M_"v[184:187],a[96:99],v[16:19],v[184:187]\n"
S_"v[104:107],v205,s[52:55],s71 offen offset:2048 sc1\n"
M_"v[184:187],a[100:103],v[20:23],v[184:187]\n"
"s_mul_i32 s75,s80,112\n"
"v_xor_b32 v195,0x10000,v195\n"
T_"v[0:1],v192 offset:0\n"
M_"v[184:187],a[104:107],v[24:27],v[184:187]\n"
A_"s75,s75,s68\n"
T_"v[2:3],v192 offset:512\n"
M_"v[184:187],a[108:111],v[28:31],v[184:187]\n"
A_"m0,s56,0\n"
M_"v[188:191],a[112:115],v[16:19],v[188:191]\n"
B_"v200,s[48:51],s75 offen offset:0  lds\n"
M_"v[188:191],a[116:119],v[20:23],v[188:191]\n"
T_"v[4:5],v192 offset:4096\n"
T_"v[6:7],v192 offset:4608\n"
M_"v[188:191],a[120:123],v[24:27],v[188:191]\n"
T_"v[8:9],v192 offset:8192\n"
M_"v[188:191],a[124:127],v[28:31],v[188:191]\n"
T_"v[10:11],v192 offset:8704\n"
M_"v[176:179],a[0:3],v[32:35],v[176:179]\n"
B_"v201,s[48:51],s75 offen offset:1024  lds\n"
M_"v[176:179],a[4:7],v[36:39],v[176:179]\n"
T_"v[12:13],v192 offset:12288\n"
T_"v[14:15],v192 offset:12800\n"
M_"v[176:179],a[8:11],v[40:43],v[176:179]\n"
T_"v[16:17],v193 offset:0\n"
T_"v[18:19],v193 offset:512\n"
M_"v[176:179],a[12:15],v[44:47],v[176:179]\n"
T_"v[20:21],v193 offset:4096\n"
C_"v112,v112,v113\n"
M_"v[180:183],a[16:19],v[32:35],v[180:183]\n"
B_"v202,s[48:51],s75 offen offset:2048  lds\n"
M_"v[180:183],a[20:23],v[36:39],v[180:183]\n"
T_"v[22:23],v193 offset:4608\n"
C_"v113,v114,v115\n"
M_"v[180:183],a[24:27],v[40:43],v[180:183]\n"
M_"v[180:183],a[28:31],v[44:47],v[180:183]\n"
T_"v[24:25],v193 offset:8192\n"
M_"v[184:187],a[32:35],v[32:35],v[184:187]\n"
B_"v203,s[48:51],s75 offen offset:3072  lds\n"
M_"v[184:187],a[36:39],v[36:39],v[184:187]\n"
"s_mul_i32 s75,s81,112\n"
T_"v[26:27],v193 offset:8704\n"
C_"v114,v116,v117\n"
M_"v[184:187],a[40:43],v[40:43],v[184:187]\n"
A_"s75,s75,s68\n"
T_"v[28:29],v193 offset:12288\n"
A_"m0,s56,16384\n"
M_"v[184:187],a[44:47],v[44:47],v[184:187]\n"
M_"v[188:191],a[48:51],v[32:35],v[188:191]\n"
B_"v200,s[48:51],s75 offen offset:0  lds\n"
M_"v[188:191],a[52:55],v[36:39],v[188:191]\n"
T_"v[30:31],v193 offset:12800\n"
C_"v115,v118,v119\n"
M_"v[188:191],a[56:59],v[40:43],v[188:191]\n"
M_"v[188:191],a[60:63],v[44:47],v[188:191]\n"
M_"v[176:179],a[128:131],v[48:51],v[176:179]\n"
B_"v201,s[48:51],s75 offen offset:1024  lds\n"
M_"v[176:179],a[132:135],v[52:55],v[176:179]\n"
T_"v[32:33],v194 offset:0\n"
T_"v[34:35],v194 offset:512\n"
M_"v[176:179],a[136:139],v[56:59],v[176:179]\n"
M_"v[176:179],a[140:143],v[60:63],v[176:179]\n"
T_"v[36:37],v194 offset:4096\n"
M_"v[180:183],a[144:147],v[48:51],v[180:183]\n"
B_"v202,s[48:51],s75 offen offset:2048  lds\n"
M_"v[180:183],a[148:151],v[52:55],v[180:183]\n"
T_"v[38:39],v194 offset:4608\n"
M_"v[180:183],a[152:155],v[56:59],v[180:183]\n"
M_"v[180:183],a[156:159],v[60:63],v[180:183]\n"
T_"v[40:41],v194 offset:8192\n"
M_"v[184:187],a[160:163],v[48:51],v[184:187]\n"
S_"v[112:115],v204,s[52:55],s71 offen offset:3072 sc1\n"
B_"v203,s[48:51],s75 offen offset:3072  lds\n"
M_"v[184:187],a[164:167],v[52:55],v[184:187]\n"
"s_mul_i32 s75,s82,112\n"
T_"v[42:43],v194 offset:8704\n"
A_"s75,s75,s68\n"
M_"v[184:187],a[168:171],v[56:59],v[184:187]\n"
A_"m0,s56,32768\n"
T_"v[44:45],v194 offset:12288\n"
C_"v120,v120,v121\n"
M_"v[184:187],a[172:175],v[60:63],v[184:187]\n"
M_"v[188:191],a[176:179],v[48:51],v[188:191]\n"
B_"v200,s[48:51],s75 offen offset:0  lds\n"
M_"v[188:191],a[180:183],v[52:55],v[188:191]\n"
T_"v[46:47],v194 offset:12800\n"
C_"v121,v122,v123\n"
M_"v[188:191],a[184:187],v[56:59],v[188:191]\n"
M_"v[188:191],a[188:191],v[60:63],v[188:191]\n"
W_"lgkmcnt(15)\n"
M_"v[64:67],a[192:195],v[0:3],0\n"
B_"v201,s[48:51],s75 offen offset:1024  lds\n"
M_"v[64:67],a[196:199],v[4:7],v[64:67]\n"
T_"v[48:49],v195 offset:0\n"
T_"v[50:51],v195 offset:512\n"
M_"v[64:67],a[200:203],v[8:11],v[64:67]\n"
C_"v122,v124,v125\n"
M_"v[64:67],a[204:207],v[12:15],v[64:67]\n"
T_"v[52:53],v195 offset:4096\n"
M_"v[68:71],a[208:211],v[0:3],0\n"
B_"v202,s[48:51],s75 offen offset:2048  lds\n"
M_"v[68:71],a[212:215],v[4:7],v[68:71]\n"
T_"v[54:55],v195 offset:4608\n"
C_"v123,v126,v127\n"
M_"v[68:71],a[216:219],v[8:11],v[68:71]\n"
T_"v[56:57],v195 offset:8192\n"
M_"v[68:71],a[220:223],v[12:15],v[68:71]\n"
W_"lgkmcnt(13)\n"
B_"v203,s[48:51],s75 offen offset:3072  lds\n"
M_"v[80:83],a[192:195],v[16:19],0\n"
"s_mul_i32 s75,s83,112\n"
T_"v[58:59],v195 offset:8704\n"
A_"s75,s75,s68\n"
M_"v[80:83],a[196:199],v[20:23],v[80:83]\n"
A_"m0,s56,49152\n"
T_"v[60:61],v195 offset:12288\n"
M_"v[80:83],a[200:203],v[24:27],v[80:83]\n"
M_"v[80:83],a[204:207],v[28:31],v[80:83]\n"
B_"v200,s[48:51],s75 offen offset:0  lds\n"
M_"v[84:87],a[208:211],v[16:19],0\n"
W_"vmcnt(18)\n"
T_"v[62:63],v195 offset:12800\n"
M_"v[84:87],a[212:215],v[20:23],v[84:87]\n"
"v_cmp_ne_u32 vcc,s32,v240\n"
M_"v[84:87],a[216:219],v[24:27],v[84:87]\n"
"s_cmp_lg_u64 vcc,0\n"
"s_cbranch_scc1 L_slow_PLV\n"
"L_back_PLV:\n"
M_"v[84:87],a[220:223],v[28:31],v[84:87]\n"
W_"lgkmcnt(8)\n"
M_"v[96:99],a[192:195],v[32:35],0\n"
S_"v[120:123],v205,s[52:55],s71 offen offset:3072 sc1\n"
B_"v201,s[48:51],s75 offen offset:1024  lds\n"
M_"v[96:99],a[196:199],v[36:39],v[96:99]\n"
M_"v[96:99],a[200:203],v[40:43],v[96:99]\n"
B_"v202,s[48:51],s75 offen offset:2048  lds\n"
B_"v203,s[48:51],s75 offen offset:3072  lds\n"
M_"v[96:99],a[204:207],v[44:47],v[96:99]\n"
M_"v[100:103],a[208:211],v[32:35],0\n"
M_"v[100:103],a[212:215],v[36:39],v[100:103]\n"
M_"v[100:103],a[216:219],v[40:43],v[100:103]\n"
M_"v[100:103],a[220:223],v[44:47],v[100:103]\n"
W_"lgkmcnt(0)\n"
M_"v[112:115],a[192:195],v[48:51],0\n"
C_"v128,v128,v129\n"
M_"v[112:115],a[196:199],v[52:55],v[112:115]\n"
M_"v[112:115],a[200:203],v[56:59],v[112:115]\n"
M_"v[112:115],a[204:207],v[60:63],v[112:115]\n"
M_"v[116:119],a[208:211],v[48:51],0\n"
A_"s10,s96,s80\n"
C_"v129,v130,v131\n"
M_"v[116:119],a[212:215],v[52:55],v[116:119]\n"
B_"a[192:195],v244,s[52:55],s10 offen offset:0 sc1\n"
M_"v[116:119],a[216:219],v[56:59],v[116:119]\n"
M_"v[116:119],a[220:223],v[60:63],v[116:119]\n"
M_"v[72:75],a[224:227],v[0:3],0\n"
M_"v[72:75],a[228:231],v[4:7],v[72:75]\n"
B_"a[196:199],v244,s[52:55],s10 offen offset:1024 sc1\n"
M_"v[72:75],a[232:235],v[8:11],v[72:75]\n"
C_"v130,v132,v133\n"
M_"v[72:75],a[236:239],v[12:15],v[72:75]\n"
M_"v[76:79],a[240:243],v[0:3],0\n"
M_"v[76:79],a[244:247],v[4:7],v[76:79]\n"
B_"a[200:203],v244,s[52:55],s10 offen offset:2048 sc1\n"
M_"v[76:79],a[248:251],v[8:11],v[76:79]\n"
C_"v131,v134,v135\n"
M_"v[76:79],a[252:255],v[12:15],v[76:79]\n"
M_"v[88:91],a[224:227],v[16:19],0\n"
T_"v[0:1],v192 offset:16384\n"
M_"v[88:91],a[228:231],v[20:23],v[88:91]\n"
B_"a[204:207],v244,s[52:55],s10 offen offset:3072 sc1\n"
M_"v[88:91],a[232:235],v[24:27],v[88:91]\n"
T_"v[2:3],v192 offset:16896\n"
M_"v[88:91],a[236:239],v[28:31],v[88:91]\n"
T_"v[4:5],v192 offset:20480\n"
M_"v[92:95],a[240:243],v[16:19],0\n"
T_"v[6:7],v192 offset:20992\n"
M_"v[92:95],a[244:247],v[20:23],v[92:95]\n"
S_"v[128:131],v204,s[52:55],s72 offen offset:0 sc1\n"
M_"v[92:95],a[248:251],v[24:27],v[92:95]\n"
T_"v[8:9],v192 offset:24576\n"
M_"v[92:95],a[252:255],v[28:31],v[92:95]\n"
M_"v[104:107],a[224:227],v[32:35],0\n"
T_"v[10:11],v192 offset:25088\n"
M_"v[104:107],a[228:231],v[36:39],v[104:107]\n"
T_"v[12:13],v192 offset:28672\n"
M_"v[104:107],a[232:235],v[40:43],v[104:107]\n"
M_"v[104:107],a[236:239],v[44:47],v[104:107]\n"
T_"v[14:15],v192 offset:29184\n"
T_"v[16:17],v193 offset:16384\n"
M_"v[108:111],a[240:243],v[32:35],0\n"
T_"v[18:19],v193 offset:16896\n"
T_"v[20:21],v193 offset:20480\n"
M_"v[108:111],a[244:247],v[36:39],v[108:111]\n"
A_"s10,s97,s80\n"
T_"v[22:23],v193 offset:20992\n"
M_"v[108:111],a[248:251],v[40:43],v[108:111]\n"
B_"a[208:211],v244,s[52:55],s10 offen offset:0 sc1\n"
M_"v[108:111],a[252:255],v[44:47],v[108:111]\n"
T_"v[24:25],v193 offset:24576\n"
C_"v136,v136,v137\n"
M_"v[120:123],a[224:227],v[48:51],0\n"
T_"v[26:27],v193 offset:25088\n"
M_"v[120:123],a[228:231],v[52:55],v[120:123]\n"
T_"v[28:29],v193 offset:28672\n"
M_"v[120:123],a[232:235],v[56:59],v[120:123]\n"
B_"a[212:215],v244,s[52:55],s10 offen offset:1024 sc1\n"
M_"v[120:123],a[236:239],v[60:63],v[120:123]\n"
T_"v[30:31],v193 offset:29184\n"
T_"v[32:33],v194 offset:16384\n"
M_"v[124:127],a[240:243],v[48:51],0\n"
T_"v[34:35],v194 offset:16896\n"
T_"v[36:37],v194 offset:20480\n"
M_"v[124:127],a[244:247],v[52:55],v[124:127]\n"
C_"v137,v138,v139\n"
T_"v[38:39],v194 offset:20992\n"
M_"v[124:127],a[248:251],v[56:59],v[124:127]\n"
B_"a[216:219],v244,s[52:55],s10 offen offset:2048 sc1\n"
M_"v[124:127],a[252:255],v[60:63],v[124:127]\n"
W_"lgkmcnt(12)\n"
T_"v[40:41],v194 offset:24576\n"
C_"v138,v140,v141\n"
M_"v[64:67],a[64:67],v[0:3],v[64:67]\n"
T_"v[42:43],v194 offset:25088\n"
M_"v[64:67],a[68:71],v[4:7],v[64:67]\n"
T_"v[44:45],v194 offset:28672\n"
M_"v[64:67],a[72:75],v[8:11],v[64:67]\n"
B_"a[220:223],v244,s[52:55],s10 offen offset:3072 sc1\n"
M_"v[64:67],a[76:79],v[12:15],v[64:67]\n"
T_"v[46:47],v194 offset:29184\n"
T_"v[48:49],v195 offset:16384\n"
M_"v[68:71],a[80:83],v[0:3],v[68:71]\n"
T_"v[50:51],v195 offset:16896\n"
T_"v[52:53],v195 offset:20480\n"
M_"v[68:71],a[84:87],v[4:7],v[68:71]\n"
T_"v[54:55],v195 offset:20992\n"
C_"v139,v142,v143\n"
M_"v[68:71],a[88:91],v[8:11],v[68:71]\n"
T_"v[56:57],v195 offset:24576\n"
M_"v[68:71],a[92:95],v[12:15],v[68:71]\n"
W_"lgkmcnt(13)\n"
M_"v[80:83],a[64:67],v[16:19],v[80:83]\n"
S_"v[136:139],v205,s[52:55],s72 offen offset:0 sc1\n"
M_"v[80:83],a[68:71],v[20:23],v[80:83]\n"
T_"v[58:59],v195 offset:25088\n"
T_"v[60:61],v195 offset:28672\n"
M_"v[80:83],a[72:75],v[24:27],v[80:83]\n"
M_"v[80:83],a[76:79],v[28:31],v[80:83]\n"
T_"v[62:63],v195 offset:29184\n"
M_"v[84:87],a[80:83],v[16:19],v[84:87]\n"
M_"v[84:87],a[84:87],v[20:23],v[84:87]\n"
M_"v[84:87],a[88:91],v[24:27],v[84:87]\n"
A_"s10,s98,s80\n"
M_"v[84:87],a[92:95],v[28:31],v[84:87]\n"
W_"lgkmcnt(8)\n"
M_"v[96:99],a[64:67],v[32:35],v[96:99]\n"
B_"a[224:227],v244,s[52:55],s10 offen offset:0 sc1\n"
M_"v[96:99],a[68:71],v[36:39],v[96:99]\n"
M_"v[96:99],a[72:75],v[40:43],v[96:99]\n"
M_"v[96:99],a[76:79],v[44:47],v[96:99]\n"
M_"v[100:103],a[80:83],v[32:35],v[100:103]\n"
B_"a[228:231],v244,s[52:55],s10 offen offset:1024 sc1\n"
M_"v[100:103],a[84:87],v[36:39],v[100:103]\n"
C_"v144,v144,v145\n"
M_"v[100:103],a[88:91],v[40:43],v[100:103]\n"
M_"v[100:103],a[92:95],v[44:47],v[100:103]\n"
W_"lgkmcnt(0)\n"
M_"v[112:115],a[64:67],v[48:51],v[112:115]\n"
B_"a[232:235],v244,s[52:55],s10 offen offset:2048 sc1\n"
M_"v[112:115],a[68:71],v[52:55],v[112:115]\n"
C_"v145,v146,v147\n"
M_"v[112:115],a[72:75],v[56:59],v[112:115]\n"
M_"v[112:115],a[76:79],v[60:63],v[112:115]\n"
M_"v[116:119],a[80:83],v[48:51],v[116:119]\n"
B_"a[236:239],v244,s[52:55],s10 offen offset:3072 sc1\n"
M_"v[116:119],a[84:87],v[52:55],v[116:119]\n"
C_"v146,v148,v149\n"
M_"v[116:119],a[88:91],v[56:59],v[116:119]\n"
M_"v[116:119],a[92:95],v[60:63],v[116:119]\n"
M_"v[72:75],a[96:99],v[0:3],v[72:75]\n"
M_"v[72:75],a[100:103],v[4:7],v[72:75]\n"
C_"v147,v150,v151\n"
M_"v[72:75],a[104:107],v[8:11],v[72:75]\n"
M_"v[72:75],a[108:111],v[12:15],v[72:75]\n"
M_"v[76:79],a[112:115],v[0:3],v[76:79]\n"
M_"v[76:79],a[116:119],v[4:7],v[76:79]\n"
M_"v[76:79],a[120:123],v[8:11],v[76:79]\n"
S_"v[144:147],v204,s[52:55],s72 offen offset:1024 sc1\n"
M_"v[76:79],a[124:127],v[12:15],v[76:79]\n"
A_"s10,s99,s80\n"
M_"v[88:91],a[96:99],v[16:19],v[88:91]\n"
T_"v[0:1],v192 offset:32768\n"
M_"v[88:91],a[100:103],v[20:23],v[88:91]\n"
T_"v[2:3],v192 offset:33280\n"
M_"v[88:91],a[104:107],v[24:27],v[88:91]\n"
B_"a[240:243],v244,s[52:55],s10 offen offset:0 sc1\n"
M_"v[88:91],a[108:111],v[28:31],v[88:91]\n"
T_"v[4:5],v192 offset:36864\n"
M_"v[92:95],a[112:115],v[16:19],v[92:95]\n"
T_"v[6:7],v192 offset:37376\n"
M_"v[92:95],a[116:119],v[20:23],v[92:95]\n"
M_"v[92:95],a[120:123],v[24:27],v[92:95]\n"
B_"a[244:247],v244,s[52:55],s10 offen offset:1024 sc1\n"
M_"v[92:95],a[124:127],v[28:31],v[92:95]\n"
T_"v[8:9],v192 offset:40960\n"
M_"v[104:107],a[96:99],v[32:35],v[104:107]\n"
T_"v[10:11],v192 offset:41472\n"
M_"v[104:107],a[100:103],v[36:39],v[104:107]\n"
T_"v[12:13],v192 offset:45056\n"
M_"v[104:107],a[104:107],v[40:43],v[104:107]\n"
B_"a[248:251],v244,s[52:55],s10 offen offset:2048 sc1\n"
M_"v[104:107],a[108:111],v[44:47],v[104:107]\n"
T_"v[14:15],v192 offset:45568\n"
T_"v[16:17],v193 offset:32768\n"
M_"v[108:111],a[112:115],v[32:35],v[108:111]\n"
T_"v[18:19],v193 offset:33280\n"
T_"v[20:21],v193 offset:36864\n"
M_"v[108:111],a[116:119],v[36:39],v[108:111]\n"
C_"v152,v152,v153\n"
T_"v[22:23],v193 offset:37376\n"
M_"v[108:111],a[120:123],v[40:43],v[108:111]\n"
B_"a[252:255],v244,s[52:55],s10 offen offset:3072 sc1\n"
M_"v[108:111],a[124:127],v[44:47],v[108:111]\n"
T_"v[24:25],v193 offset:40960\n"
C_"v153,v154,v155\n"
M_"v[120:123],a[96:99],v[48:51],v[120:123]\n"
T_"v[26:27],v193 offset:41472\n"
M_"v[120:123],a[100:103],v[52:55],v[120:123]\n"
T_"v[28:29],v193 offset:45056\n"
M_"v[120:123],a[104:107],v[56:59],v[120:123]\n"
M_"v[120:123],a[108:111],v[60:63],v[120:123]\n"
T_"v[30:31],v193 offset:45568\n"
T_"v[32:33],v194 offset:32768\n"
M_"v[124:127],a[112:115],v[48:51],v[124:127]\n"
T_"v[34:35],v194 offset:33280\n"
T_"v[36:37],v194 offset:36864\n"
M_"v[124:127],a[116:119],v[52:55],v[124:127]\n"
T_"v[38:39],v194 offset:37376\n"
C_"v154,v156,v157\n"
M_"v[124:127],a[120:123],v[56:59],v[124:127]\n"
T_"v[40:41],v194 offset:40960\n"
M_"v[124:127],a[124:127],v[60:63],v[124:127]\n"
W_"lgkmcnt(13)\n"
C_"v155,v158,v159\n"
M_"v[64:67],a[0:3],v[0:3],v[64:67]\n"
A_"s10,s96,s81\n"
T_"v[42:43],v194 offset:41472\n"
M_"v[64:67],a[4:7],v[4:7],v[64:67]\n"
B_"a[64:67],v244,s[52:55],s10 offen offset:0 sc1\n"
M_"v[64:67],a[8:11],v[8:11],v[64:67]\n"
T_"v[44:45],v194 offset:45056\n"
M_"v[64:67],a[12:15],v[12:15],v[64:67]\n"
T_"v[46:47],v194 offset:45568\n"
T_"v[48:49],v195 offset:32768\n"
M_"v[68:71],a[16:19],v[0:3],v[68:71]\n"
T_"v[50:51],v195 offset:33280\n"
T_"v[52:53],v195 offset:36864\n"
M_"v[68:71],a[20:23],v[4:7],v[68:71]\n"
S_"v[152:155],v205,s[52:55],s72 offen offset:1024 sc1\n"
M_"v[68:71],a[24:27],v[8:11],v[68:71]\n"
T_"v[54:55],v195 offset:37376\n"
T_"v[56:57],v195 offset:40960\n"
M_"v[68:71],a[28:31],v[12:15],v[68:71]\n"
W_"lgkmcnt(13)\n"
M_"v[80:83],a[0:3],v[16:19],v[80:83]\n"
T_"v[58:59],v195 offset:41472\n"
M_"v[80:83],a[4:7],v[20:23],v[80:83]\n"
B_"a[68:71],v244,s[52:55],s10 offen offset:1024 sc1\n"
M_"v[80:83],a[8:11],v[24:27],v[80:83]\n"
T_"v[60:61],v195 offset:45056\n"
M_"v[80:83],a[12:15],v[28:31],v[80:83]\n"
T_"v[62:63],v195 offset:45568\n"
M_"v[84:87],a[16:19],v[16:19],v[84:87]\n"
M_"v[84:87],a[20:23],v[20:23],v[84:87]\n"
B_"a[72:75],v244,s[52:55],s10 offen offset:2048 sc1\n"
M_"v[84:87],a[24:27],v[24:27],v[84:87]\n"
M_"v[84:87],a[28:31],v[28:31],v[84:87]\n"
W_"lgkmcnt(8)\n"
M_"v[96:99],a[0:3],v[32:35],v[96:99]\n"
C_"v160,v160,v161\n"
M_"v[96:99],a[4:7],v[36:39],v[96:99]\n"
B_"a[76:79],v244,s[52:55],s10 offen offset:3072 sc1\n"
M_"v[96:99],a[8:11],v[40:43],v[96:99]\n"
M_"v[96:99],a[12:15],v[44:47],v[96:99]\n"
M_"v[100:103],a[16:19],v[32:35],v[100:103]\n"
C_"v161,v162,v163\n"
M_"v[100:103],a[20:23],v[36:39],v[100:103]\n"
M_"v[100:103],a[24:27],v[40:43],v[100:103]\n"
M_"v[100:103],a[28:31],v[44:47],v[100:103]\n"
W_"lgkmcnt(0)\n"
M_"v[112:115],a[0:3],v[48:51],v[112:115]\n"
M_"v[112:115],a[4:7],v[52:55],v[112:115]\n"
A_"s10,s97,s81\n"
C_"v162,v164,v165\n"
M_"v[112:115],a[8:11],v[56:59],v[112:115]\n"
B_"a[80:83],v244,s[52:55],s10 offen offset:0 sc1\n"
M_"v[112:115],a[12:15],v[60:63],v[112:115]\n"
M_"v[116:119],a[16:19],v[48:51],v[116:119]\n"
M_"v[116:119],a[20:23],v[52:55],v[116:119]\n"
C_"v163,v166,v167\n"
M_"v[116:119],a[24:27],v[56:59],v[116:119]\n"
B_"a[84:87],v244,s[52:55],s10 offen offset:1024 sc1\n"
M_"v[116:119],a[28:31],v[60:63],v[116:119]\n"
M_"v[72:75],a[32:35],v[0:3],v[72:75]\n"
M_"v[72:75],a[36:39],v[4:7],v[72:75]\n"
M_"v[72:75],a[40:43],v[8:11],v[72:75]\n"
S_"v[160:163],v204,s[52:55],s72 offen offset:2048 sc1\n"
M_"v[72:75],a[44:47],v[12:15],v[72:75]\n"
M_"v[76:79],a[48:51],v[0:3],v[76:79]\n"
M_"v[76:79],a[52:55],v[4:7],v[76:79]\n"
M_"v[76:79],a[56:59],v[8:11],v[76:79]\n"
B_"a[88:91],v244,s[52:55],s10 offen offset:2048 sc1\n"
M_"v[76:79],a[60:63],v[12:15],v[76:79]\n"
M_"v[88:91],a[32:35],v[16:19],v[88:91]\n"
T_"v[0:1],v192 offset:49152\n"
M_"v[88:91],a[36:39],v[20:23],v[88:91]\n"
T_"v[2:3],v192 offset:49664\n"
M_"v[88:91],a[40:43],v[24:27],v[88:91]\n"
B_"a[92:95],v244,s[52:55],s10 offen offset:3072 sc1\n"
M_"v[88:91],a[44:47],v[28:31],v[88:91]\n"
T_"v[4:5],v192 offset:53248\n"
M_"v[92:95],a[48:51],v[16:19],v[92:95]\n"
T_"v[6:7],v192 offset:53760\n"
M_"v[92:95],a[52:55],v[20:23],v[92:95]\n"
M_"v[92:95],a[56:59],v[24:27],v[92:95]\n"
T_"v[8:9],v192 offset:57344\n"
C_"v168,v168,v169\n"
M_"v[92:95],a[60:63],v[28:31],v[92:95]\n"
M_"v[104:107],a[32:35],v[32:35],v[104:107]\n"
T_"v[10:11],v192 offset:57856\n"
M_"v[104:107],a[36:39],v[36:39],v[104:107]\n"
T_"v[12:13],v192 offset:61440\n"
M_"v[104:107],a[40:43],v[40:43],v[104:107]\n"
A_"s10,s98,s81\n"
C_"v169,v170,v171\n"
M_"v[104:107],a[44:47],v[44:47],v[104:107]\n"
B_"a[96:99],v244,s[52:55],s10 offen offset:0 sc1\n"
M_"v[108:111],a[48:51],v[32:35],v[108:111]\n"
T_"v[14:15],v192 offset:61952\n"
T_"v[16:17],v193 offset:49152\n"
M_"v[108:111],a[52:55],v[36:39],v[108:111]\n"
T_"v[18:19],v193 offset:49664\n"
T_"v[20:21],v193 offset:53248\n"
M_"v[108:111],a[56:59],v[40:43],v[108:111]\n"
T_"v[22:23],v193 offset:53760\n"
T_"v[24:25],v193 offset:57344\n"
M_"v[108:111],a[60:63],v[44:47],v[108:111]\n"
B_"a[100:103],v244,s[52:55],s10 offen offset:1024 sc1\n"
M_"v[120:123],a[32:35],v[48:51],v[120:123]\n"
T_"v[26:27],v193 offset:57856\n"
C_"v170,v172,v173\n"
M_"v[120:123],a[36:39],v[52:55],v[120:123]\n"
T_"v[28:29],v193 offset:61440\n"
M_"v[120:123],a[40:43],v[56:59],v[120:123]\n"
M_"v[120:123],a[44:47],v[60:63],v[120:123]\n"
B_"a[104:107],v244,s[52:55],s10 offen offset:2048 sc1\n"
M_"v[124:127],a[48:51],v[48:51],v[124:127]\n"
T_"v[30:31],v193 offset:61952\n"
T_"v[32:33],v194 offset:49152\n"
M_"v[124:127],a[52:55],v[52:55],v[124:127]\n"
T_"v[34:35],v194 offset:49664\n"
T_"v[36:37],v194 offset:53248\n"
M_"v[124:127],a[56:59],v[56:59],v[124:127]\n"
T_"v[38:39],v194 offset:53760\n"
C_"v171,v174,v175\n"
M_"v[124:127],a[60:63],v[60:63],v[124:127]\n"
W_"lgkmcnt(12)\n"
T_"v[40:41],v194 offset:57344\n"
M_"v[64:67],a[128:131],v[0:3],v[64:67]\n"
B_"a[108:111],v244,s[52:55],s10 offen offset:3072 sc1\n"
M_"v[64:67],a[132:135],v[4:7],v[64:67]\n"
T_"v[42:43],v194 offset:57856\n"
T_"v[44:45],v194 offset:61440\n"
M_"v[64:67],a[136:139],v[8:11],v[64:67]\n"
M_"v[64:67],a[140:143],v[12:15],v[64:67]\n"
T_"v[46:47],v194 offset:61952\n"
T_"v[48:49],v195 offset:49152\n"
M_"v[68:71],a[144:147],v[0:3],v[68:71]\n"
S_"v[168:171],v205,s[52:55],s72 offen offset:2048 sc1\n"
M_"v[68:71],a[148:151],v[4:7],v[68:71]\n"
T_"v[50:51],v195 offset:49664\n"
T_"v[52:53],v195 offset:53248\n"
M_"v[68:71],a[152:155],v[8:11],v[68:71]\n"
T_"v[54:55],v195 offset:53760\n"
T_"v[56:57],v195 offset:57344\n"
M_"v[68:71],a[156:159],v[12:15],v[68:71]\n"
W_"lgkmcnt(13)\n"
M_"v[80:83],a[128:131],v[16:19],v[80:83]\n"
T_"v[58:59],v195 offset:57856\n"
M_"v[80:83],a[132:135],v[20:23],v[80:83]\n"
T_"v[60:61],v195 offset:61440\n"
M_"v[80:83],a[136:139],v[24:27],v[80:83]\n"
M_"v[80:83],a[140:143],v[28:31],v[80:83]\n"
A_"s10,s99,s81\n"
T_"v[62:63],v195 offset:61952\n"
M_"v[84:87],a[144:147],v[16:19],v[84:87]\n"
B_"a[112:115],v244,s[52:55],s10 offen offset:0 sc1\n"
M_"v[84:87],a[148:151],v[20:23],v[84:87]\n"
C_"v176,v176,v177\n"
M_"v[84:87],a[152:155],v[24:27],v[84:87]\n"
M_"v[84:87],a[156:159],v[28:31],v[84:87]\n"
W_"lgkmcnt(8)\n"
M_"v[96:99],a[128:131],v[32:35],v[96:99]\n"
B_"a[116:119],v244,s[52:55],s10 offen offset:1024 sc1\n"
M_"v[96:99],a[132:135],v[36:39],v[96:99]\n"
C_"v177,v178,v179\n"
M_"v[96:99],a[136:139],v[40:43],v[96:99]\n"
M_"v[96:99],a[140:143],v[44:47],v[96:99]\n"
M_"v[100:103],a[144:147],v[32:35],v[100:103]\n"
B_"a[120:123],v244,s[52:55],s10 offen offset:2048 sc1\n"
M_"v[100:103],a[148:151],v[36:39],v[100:103]\n"
C_"v178,v180,v181\n"
M_"v[100:103],a[152:155],v[40:43],v[100:103]\n"
M_"v[100:103],a[156:159],v[44:47],v[100:103]\n"
W_"lgkmcnt(0)\n"
M_"v[112:115],a[128:131],v[48:51],v[112:115]\n"
B_"a[124:127],v244,s[52:55],s10 offen offset:3072 sc1\n"
M_"v[112:115],a[132:135],v[52:55],v[112:115]\n"
C_"v179,v182,v183\n"
M_"v[112:115],a[136:139],v[56:59],v[112:115]\n"
M_"v[112:115],a[140:143],v[60:63],v[112:115]\n"
M_"v[116:119],a[144:147],v[48:51],v[116:119]\n"
M_"v[116:119],a[148:151],v[52:55],v[116:119]\n"
M_"v[116:119],a[152:155],v[56:59],v[116:119]\n"
S_"v[176:179],v204,s[52:55],s72 offen offset:3072 sc1\n"
M_"v[116:119],a[156:159],v[60:63],v[116:119]\n"
M_"v[72:75],a[160:163],v[0:3],v[72:75]\n"
M_"v[72:75],a[164:167],v[4:7],v[72:75]\n"
M_"v[72:75],a[168:171],v[8:11],v[72:75]\n"
M_"v[72:75],a[172:175],v[12:15],v[72:75]\n"
M_"v[76:79],a[176:179],v[0:3],v[76:79]\n"
W_"vmcnt(39) lgkmcnt(0)\n"
"s_barrier\n"
A_"s10,s96,s82\n"
M_"v[76:79],a[180:183],v[4:7],v[76:79]\n"
B_"a[0:3],v244,s[52:55],s10 offen offset:0 sc1\n"
M_"v[76:79],a[184:187],v[8:11],v[76:79]\n"
"s_mov_b64 s[48:49],s[22:23]\n"
M_"v[76:79],a[188:191],v[12:15],v[76:79]\n"
M_"v[88:91],a[160:163],v[16:19],v[88:91]\n"
"s_mov_b32 s50,0x1c0000\n"
"v_xor_b32 v192,0x10000,v192\n"
M_"v[88:91],a[164:167],v[20:23],v[88:91]\n"
B_"a[4:7],v244,s[52:55],s10 offen offset:1024 sc1\n"
M_"v[88:91],a[168:171],v[24:27],v[88:91]\n"
"s_mul_i32 s75,s80,112\n"
"v_xor_b32 v193,0x10000,v193\n"
"v_xor_b32 v194,0x10000,v194\n"
M_"v[88:91],a[172:175],v[28:31],v[88:91]\n"
"v_xor_b32 v195,0x10000,v195\n"
C_"v184,v184,v185\n"
M_"v[92:95],a[176:179],v[16:19],v[92:95]\n"
T_"v[0:1],v192 offset:0\n"
M_"v[92:95],a[180:183],v[20:23],v[92:95]\n"
A_"s75,s75,s68\n"
T_"v[2:3],v192 offset:512\n"
M_"v[92:95],a[184:187],v[24:27],v[92:95]\n"
B_"a[8:11],v244,s[52:55],s10 offen offset:2048 sc1\n"
M_"v[92:95],a[188:191],v[28:31],v[92:95]\n"
A_"m0,s56,65536\n"
T_"v[4:5],v192 offset:4096\n"
T_"v[6:7],v192 offset:4608\n"
M_"v[104:107],a[160:163],v[32:35],v[104:107]\n"
T_"v[8:9],v192 offset:8192\n"
C_"v185,v186,v187\n"
M_"v[104:107],a[164:167],v[36:39],v[104:107]\n"
T_"v[10:11],v192 offset:8704\n"
M_"v[104:107],a[168:171],v[40:43],v[104:107]\n"
B_"v200,s[48:51],s75 offen offset:0  lds\n"
M_"v[104:107],a[172:175],v[44:47],v[104:107]\n"
T_"v[12:13],v192 offset:12288\n"
T_"v[14:15],v192 offset:12800\n"
M_"v[108:111],a[176:179],v[32:35],v[108:111]\n"
T_"v[16:17],v193 offset:0\n"
T_"v[18:19],v193 offset:512\n"
M_"v[108:111],a[180:183],v[36:39],v[108:111]\n"
T_"v[20:21],v193 offset:4096\n"
T_"v[22:23],v193 offset:4608\n"
M_"v[108:111],a[184:187],v[40:43],v[108:111]\n"
B_"a[12:15],v244,s[52:55],s10 offen offset:3072 sc1\n"
M_"v[108:111],a[188:191],v[44:47],v[108:111]\n"
C_"v186,v188,v189\n"
T_"v[24:25],v193 offset:8192\n"
M_"v[120:123],a[160:163],v[48:51],v[120:123]\n"
T_"v[26:27],v193 offset:8704\n"
C_"v187,v190,v191\n"
M_"v[120:123],a[164:167],v[52:55],v[120:123]\n"
T_"v[28:29],v193 offset:12288\n"
M_"v[120:123],a[168:171],v[56:59],v[120:123]\n"
B_"v201,s[48:51],s75 offen offset:1024  lds\n"
M_"v[120:123],a[172:175],v[60:63],v[120:123]\n"
T_"v[30:31],v193 offset:12800\n"
T_"v[32:33],v194 offset:0\n"
M_"v[124:127],a[176:179],v[48:51],v[124:127]\n"
T_"v[34:35],v194 offset:512\n"
T_"v[36:37],v194 offset:4096\n"
M_"v[124:127],a[180:183],v[52:55],v[124:127]\n"
A_"s10,s97,s82\n"
T_"v[38:39],v194 offset:4608\n"
M_"v[124:127],a[184:187],v[56:59],v[124:127]\n"
B_"v202,s[48:51],s75 offen offset:2048  lds\n"
M_"v[124:127],a[188:191],v[60:63],v[124:127]\n"
W_"lgkmcnt(12)\n"
T_"v[40:41],v194 offset:8192\n"
W_"vmcnt(42)\n"
M_"v[128:131],v[0:3],a[192:195],0\n"
T_"v[42:43],v194 offset:8704\n"
M_"v[128:131],v[4:7],a[196:199],v[128:131]\n"
T_"v[44:45],v194 offset:12288\n"
M_"v[128:131],v[8:11],a[200:203],v[128:131]\n"
S_"v[184:187],v205,s[52:55],s72 offen offset:3072 sc1\n"
M_"v[128:131],v[12:15],a[204:207],v[128:131]\n"
W_"vmcnt(38)\n"
T_"v[46:47],v194 offset:12800\n"
T_"v[48:49],v195 offset:0\n"
M_"v[132:135],v[0:3],a[208:211],0\n"
T_"v[50:51],v195 offset:512\n"
T_"v[52:53],v195 offset:4096\n"
M_"v[132:135],v[4:7],a[212:215],v[132:135]\n"
T_"v[54:55],v195 offset:4608\n"
M_"v[132:135],v[8:11],a[216:219],v[132:135]\n"
B_"v203,s[48:51],s75 offen offset:3072  lds\n"
M_"v[132:135],v[12:15],a[220:223],v[132:135]\n"
W_"lgkmcnt(12)\n"
T_"v[56:57],v195 offset:8192\n"
"s_mul_i32 s75,s81,112\n"
M_"v[144:147],v[16:19],a[192:195],0\n"
B_"a[16:19],v244,s[52:55],s10 offen offset:0 sc1\n"
M_"v[144:147],v[20:23],a[196:199],v[144:147]\n"
A_"s75,s75,s68\n"
T_"v[58:59],v195 offset:8704\n"
T_"v[60:61],v195 offset:12288\n"
M_"v[144:147],v[24:27],a[200:203],v[144:147]\n"
A_"m0,s56,81920\n"
M_"v[144:147],v[28:31],a[204:207],v[144:147]\n"
T_"v[62:63],v195 offset:12800\n"
M_"v[148:151],v[16:19],a[208:211],0\n"
B_"a[20:23],v244,s[52:55],s10 offen offset:1024 sc1\n"
M_"v[148:151],v[20:23],a[212:215],v[148:151]\n"
C_"v64,v64,v65\n"
M_"v[148:151],v[24:27],a[216:219],v[148:151]\n"
C_"v65,v66,v67\n"
M_"v[148:151],v[28:31],a[220:223],v[148:151]\n"
W_"lgkmcnt(8)\n"
B_"a[24:27],v244,s[52:55],s10 offen offset:2048 sc1\n"
M_"v[160:163],v[32:35],a[192:195],0\n"
M_"v[160:163],v[36:39],a[196:199],v[160:163]\n"
C_"v66,v68,v69\n"
M_"v[160:163],v[40:43],a[200:203],v[160:163]\n"
B_"v200,s[48:51],s75 offen offset:0  lds\n"
M_"v[160:163],v[44:47],a[204:207],v[160:163]\n"
B_"a[28:31],v244,s[52:55],s10 offen offset:3072 sc1\n"
M_"v[164:167],v[32:35],a[208:211],0\n"
C_"v67,v70,v71\n"
M_"v[164:167],v[36:39],a[212:215],v[164:167]\n"
M_"v[164:167],v[40:43],a[216:219],v[164:167]\n"
A_"s10,s98,s82\n"
M_"v[164:167],v[44:47],a[220:223],v[164:167]\n"
W_"lgkmcnt(0)\n"
B_"v201,s[48:51],s75 offen offset:1024  lds\n"
M_"v[176:179],v[48:51],a[192:195],0\n"
M_"v[176:179],v[52:55],a[196:199],v[176:179]\n"
M_"v[176:179],v[56:59],a[200:203],v[176:179]\n"
M_"v[176:179],v[60:63],a[204:207],v[176:179]\n"
B_"v202,s[48:51],s75 offen offset:2048  lds\n"
M_"v[180:183],v[48:51],a[208:211],0\n"
M_"v[180:183],v[52:55],a[212:215],v[180:183]\n"
M_"v[180:183],v[56:59],a[216:219],v[180:183]\n"
S_"v[64:67],v204,s[52:55],s73 offen offset:0 sc1\n"
M_"v[180:183],v[60:63],a[220:223],v[180:183]\n"
W_"vmcnt(42)\n"
"v_mov_b32 v208,0xc0135761\n"
C_"v80,v80,v81\n"
M_"v[136:139],v[0:3],a[224:227],0\n"
B_"v203,s[48:51],s75 offen offset:3072  lds\n"
M_"v[136:139],v[4:7],a[228:231],v[136:139]\n"
B_"a[32:35],v244,s[52:55],s10 offen offset:0 sc1\n"
M_"v[136:139],v[8:11],a[232:235],v[136:139]\n"
"s_mul_i32 s75,s82,112\n"
C_"v81,v82,v83\n"
A_"s75,s75,s68\n"
M_"v[136:139],v[12:15],a[236:239],v[136:139]\n"
W_"vmcnt(39)\n"
C_"v82,v84,v85\n"
"s_mov_b32 s76,0xbdd2d3e8\n"
M_"v[140:143],v[0:3],a[240:243],0\n"
A_"m0,s56,98304\n"
M_"v[140:143],v[4:7],a[244:247],v[140:143]\n"
B_"a[36:39],v244,s[52:55],s10 offen offset:1024 sc1\n"
M_"v[140:143],v[8:11],a[248:251],v[140:143]\n"
"s_lshl_b32 s85,s92,11\n"
C_"v83,v86,v87\n"
M_"v[140:143],v[12:15],a[252:255],v[140:143]\n"
M_"v[152:155],v[16:19],a[224:227],0\n"
B_"a[40:43],v244,s[52:55],s10 offen offset:2048 sc1\n"
M_"v[152:155],v[20:23],a[228:231],v[152:155]\n"
A_"s85,s85,0x3200000\n"
T_"v[0:1],v192 offset:16384\n"
T_"v[2:3],v192 offset:16896\n"
M_"v[152:155],v[24:27],a[232:235],v[152:155]\n"
M_"v[152:155],v[28:31],a[236:239],v[152:155]\n"
T_"v[4:5],v192 offset:20480\n"
M_"v[156:159],v[16:19],a[240:243],0\n"
B_"a[44:47],v244,s[52:55],s10 offen offset:3072 sc1\n"
B_"v200,s[48:51],s75 offen offset:0  lds\n"
M_"v[156:159],v[20:23],a[244:247],v[156:159]\n"
"s_lshr_b32 s36,s96,11\n"
T_"v[6:7],v192 offset:20992\n"
M_"v[156:159],v[24:27],a[248:251],v[156:159]\n"
T_"v[8:9],v192 offset:24576\n"
M_"v[156:159],v[28:31],a[252:255],v[156:159]\n"
"s_mul_i32 s36,s36,7168\n"
A_"s10,s99,s82\n"
M_"v[168:171],v[32:35],a[224:227],0\n"
S_"v[80:83],v204,s[52:55],s73 offen offset:1024 sc1\n"
M_"v[168:171],v[36:39],a[228:231],v[168:171]\n"
B_"v201,s[48:51],s75 offen offset:1024  lds\n"
M_"v[168:171],v[40:43],a[232:235],v[168:171]\n"
T_"v[10:11],v192 offset:25088\n"
T_"v[12:13],v192 offset:28672\n"
M_"v[168:171],v[44:47],a[236:239],v[168:171]\n"
A_"s36,s36,s85\n"
T_"v[14:15],v192 offset:29184\n"
T_"v[16:17],v193 offset:16384\n"
M_"v[172:175],v[32:35],a[240:243],0\n"
T_"v[18:19],v193 offset:16896\n"
C_"v96,v96,v97\n"
M_"v[172:175],v[36:39],a[244:247],v[172:175]\n"
B_"v202,s[48:51],s75 offen offset:2048  lds\n"
M_"v[172:175],v[40:43],a[248:251],v[172:175]\n"
"s_lshr_b32 s37,s97,11\n"
T_"v[20:21],v193 offset:20480\n"
C_"v97,v98,v99\n"
M_"v[172:175],v[44:47],a[252:255],v[172:175]\n"
T_"v[22:23],v193 offset:20992\n"
T_"v[24:25],v193 offset:24576\n"
M_"v[184:187],v[48:51],a[224:227],0\n"
C_"v98,v100,v101\n"
T_"v[26:27],v193 offset:25088\n"
M_"v[184:187],v[52:55],a[228:231],v[184:187]\n"
B_"a[48:51],v244,s[52:55],s10 offen offset:0 sc1\n"
M_"v[184:187],v[56:59],a[232:235],v[184:187]\n"
B_"v203,s[48:51],s75 offen offset:3072  lds\n"
M_"v[184:187],v[60:63],a[236:239],v[184:187]\n"
"s_mul_i32 s75,s83,112\n"
T_"v[28:29],v193 offset:28672\n"
T_"v[30:31],v193 offset:29184\n"
M_"v[188:191],v[48:51],a[240:243],0\n"
"s_mul_i32 s37,s37,7168\n"
T_"v[32:33],v194 offset:16384\n"
C_"v99,v102,v103\n"
M_"v[188:191],v[52:55],a[244:247],v[188:191]\n"
A_"s75,s75,s68\n"
T_"v[34:35],v194 offset:16896\n"
T_"v[36:37],v194 offset:20480\n"
M_"v[188:191],v[56:59],a[248:251],v[188:191]\n"
B_"a[52:55],v244,s[52:55],s10 offen offset:1024 sc1\n"
M_"v[188:191],v[60:63],a[252:255],v[188:191]\n"
W_"lgkmcnt(11)\n"
T_"v[38:39],v194 offset:20992\n"
W_"vmcnt(44)\n"
M_"v[128:131],v[0:3],a[64:67],v[128:131]\n"
A_"s37,s37,s85\n"
T_"v[40:41],v194 offset:24576\n"
T_"v[42:43],v194 offset:25088\n"
M_"v[128:131],v[4:7],a[68:71],v[128:131]\n"
B_"a[56:59],v244,s[52:55],s10 offen offset:2048 sc1\n"
M_"v[128:131],v[8:11],a[72:75],v[128:131]\n"
S_"v[96:99],v204,s[52:55],s73 offen offset:2048 sc1\n"
M_"v[128:131],v[12:15],a[76:79],v[128:131]\n"
W_"vmcnt(41)\n"
A_"m0,s56,114688\n"
T_"v[44:45],v194 offset:28672\n"
M_"v[132:135],v[0:3],a[80:83],v[132:135]\n"
"s_lshr_b32 s38,s98,11\n"
T_"v[46:47],v194 offset:29184\n"
T_"v[48:49],v195 offset:16384\n"
M_"v[132:135],v[4:7],a[84:87],v[132:135]\n"
B_"a[60:63],v244,s[52:55],s10 offen offset:3072 sc1\n"
M_"v[132:135],v[8:11],a[88:91],v[132:135]\n"
B_"v200,s[48:51],s75 offen offset:0  lds\n"
M_"v[132:135],v[12:15],a[92:95],v[132:135]\n"
W_"lgkmcnt(9)\n"
T_"v[50:51],v195 offset:16896\n"
T_"v[52:53],v195 offset:20480\n"
M_"v[144:147],v[16:19],a[64:67],v[144:147]\n"
"s_mul_i32 s38,s38,7168\n"
C_"v112,v112,v113\n"
T_"v[54:55],v195 offset:20992\n"
M_"v[144:147],v[20:23],a[68:71],v[144:147]\n"
A_"s38,s38,s85\n"
C_"v113,v114,v115\n"
T_"v[56:57],v195 offset:24576\n"
M_"v[144:147],v[24:27],a[72:75],v[144:147]\n"
B_"v201,s[48:51],s75 offen offset:1024  lds\n"
M_"v[144:147],v[28:31],a[76:79],v[144:147]\n"
"s_lshr_b32 s39,s99,11\n"
T_"v[58:59],v195 offset:25088\n"
T_"v[60:61],v195 offset:28672\n"
M_"v[148:151],v[16:19],a[80:83],v[148:151]\n"
A_"s10,s96,s83\n"
T_"v[62:63],v195 offset:29184\n"
C_"v114,v116,v117\n"
M_"v[148:151],v[20:23],a[84:87],v[148:151]\n"
C_"v115,v118,v119\n"
M_"v[148:151],v[24:27],a[88:91],v[148:151]\n"
B_"v202,s[48:51],s75 offen offset:2048  lds\n"
M_"v[148:151],v[28:31],a[92:95],v[148:151]\n"
W_"lgkmcnt(8)\n"
"s_mul_i32 s39,s39,7168\n"
M_"v[160:163],v[32:35],a[64:67],v[160:163]\n"
M_"v[160:163],v[36:39],a[68:71],v[160:163]\n"
M_"v[160:163],v[40:43],a[72:75],v[160:163]\n"
B_"a[128:131],v244,s[52:55],s10 offen offset:0 sc1\n"
M_"v[160:163],v[44:47],a[76:79],v[160:163]\n"
A_"s39,s39,s85\n"
M_"v[164:167],v[32:35],a[80:83],v[164:167]\n"
S_"v[112:115],v204,s[52:55],s73 offen offset:3072 sc1\n"
B_"v203,s[48:51],s75 offen offset:3072  lds\n"
M_"v[164:167],v[36:39],a[84:87],v[164:167]\n"
C_"v72,v72,v73\n"
M_"v[164:167],v[40:43],a[88:91],v[164:167]\n"
C_"v73,v74,v75\n"
M_"v[164:167],v[44:47],a[92:95],v[164:167]\n"
W_"lgkmcnt(0)\n"
B_"a[132:135],v244,s[52:55],s10 offen offset:1024 sc1\n"
M_"v[176:179],v[48:51],a[64:67],v[176:179]\n"
M_"v[176:179],v[52:55],a[68:71],v[176:179]\n"
C_"v74,v76,v77\n"
M_"v[176:179],v[56:59],a[72:75],v[176:179]\n"
B_"a[136:139],v244,s[52:55],s10 offen offset:2048 sc1\n"
M_"v[176:179],v[60:63],a[76:79],v[176:179]\n"
C_"v75,v78,v79\n"
M_"v[180:183],v[48:51],a[80:83],v[180:183]\n"
"s_bfe_u32 s85,s30,0x20001\n"
M_"v[180:183],v[52:55],a[84:87],v[180:183]\n"
"s_mul_i32 s91,s85,14\n"
M_"v[180:183],v[56:59],a[88:91],v[180:183]\n"
B_"a[140:143],v244,s[52:55],s10 offen offset:3072 sc1\n"
M_"v[180:183],v[60:63],a[92:95],v[180:183]\n"
W_"vmcnt(47)\n"
A_"s68,s91,s31\n"
M_"v[136:139],v[0:3],a[96:99],v[136:139]\n"
"s_lshl_b32 s68,s68,15\n"
M_"v[136:139],v[4:7],a[100:103],v[136:139]\n"
A_"s10,s97,s83\n"
M_"v[136:139],v[8:11],a[104:107],v[136:139]\n"
S_"v[72:75],v205,s[52:55],s73 offen offset:0 sc1\n"
M_"v[136:139],v[12:15],a[108:111],v[136:139]\n"
W_"vmcnt(43)\n"
"s_lshl_b32 s91,s91,10\n"
M_"v[140:143],v[0:3],a[112:115],v[140:143]\n"
A_"s91,s91,0x3200000\n"
M_"v[140:143],v[4:7],a[116:119],v[140:143]\n"
"s_and_b32 s95,s30,1\n"
M_"v[140:143],v[8:11],a[120:123],v[140:143]\n"
B_"a[144:147],v244,s[52:55],s10 offen offset:0 sc1\n"
M_"v[140:143],v[12:15],a[124:127],v[140:143]\n"
"s_mul_i32 s14,s95,229376\n"
C_"v88,v88,v89\n"
M_"v[152:155],v[16:19],a[96:99],v[152:155]\n"
A_"s91,s91,s14\n"
T_"v[0:1],v192 offset:32768\n"
C_"v89,v90,v91\n"
M_"v[152:155],v[20:23],a[100:103],v[152:155]\n"
T_"v[2:3],v192 offset:33280\n"
M_"v[152:155],v[24:27],a[104:107],v[152:155]\n"
B_"a[148:151],v244,s[52:55],s10 offen offset:1024 sc1\n"
M_"v[152:155],v[28:31],a[108:111],v[152:155]\n"
"s_lshr_b32 s14,s96,11\n"
T_"v[4:5],v192 offset:36864\n"
C_"v90,v92,v93\n"
M_"v[156:159],v[16:19],a[112:115],v[156:159]\n"
"s_mul_i32 s14,s14,7168\n"
T_"v[6:7],v192 offset:37376\n"
M_"v[156:159],v[20:23],a[116:119],v[156:159]\n"
A_"s14,s14,s91\n"
C_"v91,v94,v95\n"
M_"v[156:159],v[24:27],a[120:123],v[156:159]\n"
B_"a[152:155],v244,s[52:55],s10 offen offset:2048 sc1\n"
M_"v[156:159],v[28:31],a[124:127],v[156:159]\n"
"s_lshr_b32 s15,s98,11\n"
T_"v[8:9],v192 offset:40960\n"
M_"v[168:171],v[32:35],a[96:99],v[168:171]\n"
"s_mul_i32 s15,s15,7168\n"
T_"v[10:11],v192 offset:41472\n"
M_"v[168:171],v[36:39],a[100:103],v[168:171]\n"
T_"v[12:13],v192 offset:45056\n"
M_"v[168:171],v[40:43],a[104:107],v[168:171]\n"
B_"a[156:159],v244,s[52:55],s10 offen offset:3072 sc1\n"
M_"v[168:171],v[44:47],a[108:111],v[168:171]\n"
A_"s15,s15,s91\n"
T_"v[14:15],v192 offset:45568\n"
T_"v[16:17],v193 offset:32768\n"
M_"v[172:175],v[32:35],a[112:115],v[172:175]\n"
"s_and_b32 s91,s33,3\n"
T_"v[18:19],v193 offset:33280\n"
T_"v[20:21],v193 offset:36864\n"
M_"v[172:175],v[36:39],a[116:119],v[172:175]\n"
"s_lshl_b32 s85,s85,5\n"
T_"v[22:23],v193 offset:37376\n"
M_"v[172:175],v[40:43],a[120:123],v[172:175]\n"
S_"v[88:91],v205,s[52:55],s73 offen offset:1024 sc1\n"
M_"v[172:175],v[44:47],a[124:127],v[172:175]\n"
A_"s85,s85,s95\n"
T_"v[24:25],v193 offset:40960\n"
M_"v[184:187],v[48:51],a[96:99],v[184:187]\n"
"s_lshl4_add_u32 s85,s85,s91\n"
T_"v[26:27],v193 offset:41472\n"
C_"v104,v104,v105\n"
M_"v[184:187],v[52:55],a[100:103],v[184:187]\n"
T_"v[28:29],v193 offset:45056\n"
M_"v[184:187],v[56:59],a[104:107],v[184:187]\n"
"s_lshl_b32 s85,s85,12\n"
C_"v105,v106,v107\n"
A_"s10,s98,s83\n"
M_"v[184:187],v[60:63],a[108:111],v[184:187]\n"
B_"a[160:163],v244,s[52:55],s10 offen offset:0 sc1\n"
M_"v[188:191],v[48:51],a[112:115],v[188:191]\n"
A_"s87,s85,0x200000\n"
T_"v[30:31],v193 offset:45568\n"
T_"v[32:33],v194 offset:32768\n"
M_"v[188:191],v[52:55],a[116:119],v[188:191]\n"
"s_lshl_b32 s91,s91,8\n"
T_"v[34:35],v194 offset:33280\n"
T_"v[36:37],v194 offset:36864\n"
M_"v[188:191],v[56:59],a[120:123],v[188:191]\n"
T_"v[38:39],v194 offset:37376\n"
T_"v[40:41],v194 offset:40960\n"
M_"v[188:191],v[60:63],a[124:127],v[188:191]\n"
W_"lgkmcnt(13)\n"
C_"v106,v108,v109\n"
W_"vmcnt(43)\n"
M_"v[128:131],v[0:3],a[0:3],v[128:131]\n"
A_"s68,s68,s91\n"
T_"v[42:43],v194 offset:41472\n"
C_"v107,v110,v111\n"
M_"v[128:131],v[4:7],a[4:7],v[128:131]\n"
"s_lshr_b32 s91,s31,1\n"
T_"v[44:45],v194 offset:45056\n"
M_"v[128:131],v[8:11],a[8:11],v[128:131]\n"
B_"a[164:167],v244,s[52:55],s10 offen offset:1024 sc1\n"
M_"v[128:131],v[12:15],a[12:15],v[128:131]\n"
W_"vmcnt(35)\n"
T_"v[46:47],v194 offset:45568\n"
T_"v[48:49],v195 offset:32768\n"
M_"v[132:135],v[0:3],a[16:19],v[132:135]\n"
"s_mul_i32 s91,s91,65408\n"
T_"v[50:51],v195 offset:33280\n"
T_"v[52:53],v195 offset:36864\n"
M_"v[132:135],v[4:7],a[20:23],v[132:135]\n"
"s_sub_u32 s16,s68,s91\n"
T_"v[54:55],v195 offset:37376\n"
"s_mul_i32 s91,s95,458752\n"
M_"v[132:135],v[8:11],a[24:27],v[132:135]\n"
S_"v[104:107],v205,s[52:55],s73 offen offset:2048 sc1\n"
M_"v[132:135],v[12:15],a[28:31],v[132:135]\n"
W_"lgkmcnt(12)\n"
T_"v[56:57],v195 offset:40960\n"
"s_sub_u32 s17,229376,s91\n"
M_"v[144:147],v[16:19],a[0:3],v[144:147]\n"
T_"v[58:59],v195 offset:41472\n"
"v_bfe_u32 v201,v244,7,2\n"
M_"v[144:147],v[20:23],a[4:7],v[144:147]\n"
T_"v[60:61],v195 offset:45056\n"
M_"v[144:147],v[24:27],a[8:11],v[144:147]\n"
B_"a[168:171],v244,s[52:55],s10 offen offset:2048 sc1\n"
M_"v[144:147],v[28:31],a[12:15],v[144:147]\n"
T_"v[62:63],v195 offset:45568\n"
"v_lshrrev_b32 v202,9,v244\n"
M_"v[148:151],v[16:19],a[16:19],v[148:151]\n"
C_"v120,v120,v121\n"
"v_lshl_add_u32 v200,v202,4,v201\n"
M_"v[148:151],v[20:23],a[20:23],v[148:151]\n"
"v_lshlrev_b32 v200,10,v200\n"
C_"v121,v122,v123\n"
M_"v[148:151],v[24:27],a[24:27],v[148:151]\n"
B_"a[172:175],v244,s[52:55],s10 offen offset:3072 sc1\n"
M_"v[148:151],v[28:31],a[28:31],v[148:151]\n"
W_"lgkmcnt(8)\n"
"v_and_b32 v201,2,v201\n"
C_"v122,v124,v125\n"
M_"v[160:163],v[32:35],a[0:3],v[160:163]\n"
"v_bfe_u32 v202,v244,4,3\n"
M_"v[160:163],v[36:39],a[4:7],v[160:163]\n"
C_"v123,v126,v127\n"
M_"v[160:163],v[40:43],a[8:11],v[160:163]\n"
"v_xor_b32 v201,v201,v202\n"
M_"v[160:163],v[44:47],a[12:15],v[160:163]\n"
M_"v[164:167],v[32:35],a[16:19],v[164:167]\n"
S_"v[120:123],v205,s[52:55],s73 offen offset:3072 sc1\n"
M_"v[164:167],v[36:39],a[20:23],v[164:167]\n"
"v_lshl_add_u32 v200,v201,4,v200\n"
"v_xor_b32 v203,64,v200\n"
M_"v[164:167],v[40:43],a[24:27],v[164:167]\n"
M_"v[164:167],v[44:47],a[28:31],v[164:167]\n"
W_"lgkmcnt(0)\n"
"v_add_u32 v201,3072,v203\n"
M_"v[176:179],v[48:51],a[0:3],v[176:179]\n"
"v_add_u32 v202,6144,v200\n"
M_"v[176:179],v[52:55],a[4:7],v[176:179]\n"
M_"v[176:179],v[56:59],a[8:11],v[176:179]\n"
"v_add_u32 v203,9216,v203\n"
M_"v[176:179],v[60:63],a[12:15],v[176:179]\n"
M_"v[180:183],v[48:51],a[16:19],v[180:183]\n"
M_"v[180:183],v[52:55],a[20:23],v[180:183]\n"
M_"v[180:183],v[56:59],a[24:27],v[180:183]\n"
M_"v[180:183],v[60:63],a[28:31],v[180:183]\n"
W_"vmcnt(31)\n"
W_"lgkmcnt(0)\n"
"s_branch L_m_join\n"
"L_slow_PLK:\n"
"s_mov_b32 s35,0x40000\n"
"L_spin_PLK:\n"
"s_sleep 4\n"
"buffer_load_dword v240,v241,s[52:55],s79 offen sc1\n"
W_"vmcnt(0)\n"
"v_cmp_ne_u32 vcc,s32,v240\n"
"s_cmp_eq_u64 vcc,0\n"
"s_cbranch_scc1 L_back_PLK\n"
"s_sub_u32 s35,s35,1\n"
"s_cbranch_scc0 L_spin_PLK\n"
"s_branch L_back_PLK\n"
"L_slow_PK:\n"
"s_mov_b32 s35,0x40000\n"
"L_spin_PK:\n"
"s_sleep 4\n"
"buffer_load_dword v204,v205,s[52:55],s79 offen sc1\n"
W_"vmcnt(0)\n"
"v_cmp_ne_u32 s[58:59],s32,v204\n"
"s_cmp_eq_u64 s[58:59],0\n"
"s_cbranch_scc1 L_got_PK\n"
"s_sub_u32 s35,s35,1\n"
"s_cbranch_scc0 L_spin_PK\n"
"L_got_PK:\n"
"buffer_inv sc1\n"
"s_branch L_back_PK\n"
"L_slow_PT:\n"
"s_mov_b32 s35,0x40000\n"
"L_spin_PT:\n"
"s_sleep 4\n"
"buffer_load_dwordx2 v[206:207],v245,s[52:55],0 offen sc1\n"
W_"vmcnt(0)\n"
"v_xor_b32 v206,v206,v207\n"
"v_cmp_ne_u32 s[60:61],s32,v206\n"
"s_cmp_eq_u64 s[60:61],0\n"
"s_cbranch_scc1 L_back_PT\n"
"s_sub_u32 s35,s35,1\n"
"s_cbranch_scc0 L_spin_PT\n"
"s_branch L_back_PT\n"
"L_slow_PH:\n"
"s_mov_b32 s35,0x40000\n"
"L_spin_PH:\n"
"s_sleep 4\n"
"buffer_load_dword v206,v207,s[52:55],0 offen sc1\n"
W_"vmcnt(0)\n"
"v_cmp_ne_u32 s[12:13],s32,v206\n"
"s_cmp_eq_u64 s[12:13],0\n"
"s_cbranch_scc1 L_back_PH\n"
"s_sub_u32 s35,s35,1\n"
"s_cbranch_scc0 L_spin_PH\n"
"s_branch L_back_PH\n"
"L_slow_PKg:\n"
"s_mov_b32 s35,0x40000\n"
"L_spin_PKg:\n"
"s_sleep 4\n"
"buffer_load_dword v204,v205,s[52:55],s79 offen sc1\n"
W_"vmcnt(0)\n"
"v_cmp_ne_u32 s[58:59],s32,v204\n"
"s_cmp_eq_u64 s[58:59],0\n"
"s_cbranch_scc1 L_got_PKg\n"
"s_sub_u32 s35,s35,1\n"
"s_cbranch_scc0 L_spin_PKg\n"
"L_got_PKg:\n"
"buffer_inv sc1\n"
"s_branch L_back_PKg\n"
"L_slow_PHg:\n"
"s_mov_b32 s35,0x40000\n"
"L_spin_PHg:\n"
"s_sleep 4\n"
"buffer_load_dword v206,v207,s[52:55],0 offen sc1\n"
W_"vmcnt(0)\n"
"v_cmp_ne_u32 s[12:13],s32,v206\n"
"s_cmp_eq_u64 s[12:13],0\n"
"s_cbranch_scc1 L_back_PHg\n"
"s_sub_u32 s35,s35,1\n"
"s_cbranch_scc0 L_spin_PHg\n"
"s_branch L_back_PHg\n"
"L_slow_PKt:\n"
"s_mov_b32 s35,0x40000\n"
"L_spin_PKt:\n"
"s_sleep 4\n"
"buffer_load_dword v204,v205,s[52:55],s79 offen sc1\n"
W_"vmcnt(0)\n"
"v_cmp_ne_u32 s[58:59],s32,v204\n"
"s_cmp_eq_u64 s[58:59],0\n"
"s_cbranch_scc1 L_got_PKt\n"
"s_sub_u32 s35,s35,1\n"
"s_cbranch_scc0 L_spin_PKt\n"
"L_got_PKt:\n"
"buffer_inv sc1\n"
"s_branch L_back_PKt\n"
"L_slow_PHt:\n"
"s_mov_b32 s35,0x40000\n"
"L_spin_PHt:\n"
"s_sleep 4\n"
"buffer_load_dword v206,v207,s[52:55],0 offen sc1\n"
W_"vmcnt(0)\n"
"v_cmp_ne_u32 s[12:13],s32,v206\n"
"s_cmp_eq_u64 s[12:13],0\n"
"s_cbranch_scc1 L_back_PHt\n"
"s_sub_u32 s35,s35,1\n"
"s_cbranch_scc0 L_spin_PHt\n"
"s_branch L_back_PHt\n"
"L_slow_PLV:\n"
"s_mov_b32 s35,0x40000\n"
"L_spin_PLV:\n"
"s_sleep 4\n"
"buffer_load_dword v240,v241,s[52:55],s79 offen sc1\n"
W_"vmcnt(0)\n"
"v_cmp_ne_u32 vcc,s32,v240\n"
"s_cmp_eq_u64 vcc,0\n"
"s_cbranch_scc1 L_back_PLV\n"
"s_sub_u32 s35,s35,1\n"
"s_cbranch_scc0 L_spin_PLV\n"
"s_branch L_back_PLV\n"
"L_slow_PF:\n"
"s_mov_b32 s35,0x40000\n"
"L_spin_PF:\n"
"s_sleep 4\n"
"buffer_load_dword v196,v255,s[52:55],s20 offen offset:24 sc1\n"
W_"vmcnt(0)\n"
"v_cmp_ne_u32 s[10:11],s32,v196\n"
"s_cmp_eq_u64 s[10:11],0\n"
"s_cbranch_scc1 L_back_PF\n"
"s_sub_u32 s35,s35,1\n"
"s_cbranch_scc0 L_spin_PF\n"
"s_branch L_back_PF\n"
"L_slow_PF7:\n"
"s_mov_b32 s35,0x40000\n"
"L_spin_PF7:\n"
"s_sleep 4\n"
"buffer_load_dword v196,v255,s[52:55],s20 offen offset:24 sc1\n"
W_"vmcnt(0)\n"
"v_cmp_ne_u32 s[10:11],s32,v196\n"
"s_cmp_eq_u64 s[10:11],0\n"
"s_cbranch_scc1 L_back_PF7\n"
"s_sub_u32 s35,s35,1\n"
"s_cbranch_scc0 L_spin_PF7\n"
"s_branch L_back_PF7\n"
"L_slow_PCn:\n"
W_"vmcnt(0)\n"
"v_cmp_ne_u32 vcc,s32,v214\n"
"s_cmp_eq_u64 vcc,0\n"
"s_cbranch_scc1 L_back_PCn\n"
"s_mov_b32 s35,0x40000\n"
"L_spin_PCn:\n"
"s_sleep 0\n"
"buffer_load_dword v255,v215,s[52:55],s20 offen offset:8 sc1\n"
W_"vmcnt(0)\n"
"v_cmp_ne_u32 vcc,s32,v255\n"
"s_cmp_eq_u64 vcc,0\n"
"s_cbranch_scc1 L_back_PCn\n"
"s_sub_u32 s35,s35,1\n"
"s_cbranch_scc0 L_spin_PCn\n"
"s_branch L_back_PCn\n"
"L_slow_XEn:\n"
"s_mov_b32 s35,0x40000\n"
"L_spin_XEn:\n"
B_"v[216:219],v244,s[52:55],s66 offen offset:0 sc1\n"
B_"v[220:223],v244,s[52:55],s66 offen offset:1024 sc1\n"
B_"v[224:227],v244,s[52:55],s66 offen offset:2048 sc1\n"
W_"vmcnt(0)\n"
"v_max3_u32 v247,v216,v217,v218\n"
"v_max3_u32 v248,v219,v220,v221\n"
"v_max3_u32 v247,v247,v222,v223\n"
"v_max3_u32 v248,v248,v224,v225\n"
"v_max3_u32 v247,v247,v226,v227\n"
"v_max_u32 v247,v247,v248\n"
"v_cmp_eq_u32 vcc,-1,v247\n"
"s_cmp_eq_u64 vcc,0\n"
"s_cbranch_scc1 L_back_XEn\n"
"s_sleep 1\n"
"s_sub_u32 s35,s35,1\n"
"s_cbranch_scc0 L_spin_XEn\n"
"s_branch L_back_XEn\n"
"L_slow_XLn:\n"
W_"vmcnt(0)\n"
"v_mov_b64 v[236:237],v[216:217]\n"
"v_mov_b64 v[238:239],v[218:219]\n"
"v_max3_u32 v247,v228,v229,v230\n"
"v_max3_u32 v248,v231,v232,v233\n"
"v_max3_u32 v247,v247,v234,v235\n"
"v_max3_u32 v248,v248,v236,v237\n"
"v_max3_u32 v247,v247,v238,v239\n"
"v_max_u32 v247,v247,v248\n"
"v_cmp_eq_u32 vcc,-1,v247\n"
"s_cmp_eq_u64 vcc,0\n"
"s_cbranch_scc1 L_back_XLn\n"
"s_mov_b32 s35,0x40000\n"
"L_spin_XLn:\n"
B_"v[228:231],v244,s[52:55],s72 offen offset:0 sc1\n"
B_"v[232:235],v244,s[52:55],s72 offen offset:1024 sc1\n"
B_"v[236:239],v244,s[52:55],s72 offen offset:2048 sc1\n"
W_"vmcnt(0)\n"
"v_max3_u32 v247,v228,v229,v230\n"
"v_max3_u32 v248,v231,v232,v233\n"
"v_max3_u32 v247,v247,v234,v235\n"
"v_max3_u32 v248,v248,v236,v237\n"
"v_max3_u32 v247,v247,v238,v239\n"
"v_max_u32 v247,v247,v248\n"
"v_cmp_eq_u32 vcc,-1,v247\n"
"s_cmp_eq_u64 vcc,0\n"
"s_cbranch_scc1 L_back_XLn\n"
"s_sleep 1\n"
"s_sub_u32 s35,s35,1\n"
"s_cbranch_scc0 L_spin_XLn\n"
"s_branch L_back_XLn\n"
".p2align 12\n"
"L_m_g:\n"
"L_pt0_2:\n"
"s_cbranch_execz L_pr0\n"
M_"v[116:119],v[24:27],a[88:91],v[116:119]\n"
"v_xor_b32 v192,0x10000,v192\n"
M_"v[116:119],v[28:31],a[92:95],v[116:119]\n"
"v_xor_b32 v193,0x10000,v193\n"
M_"v[120:123],v[16:19],a[96:99],v[120:123]\n"
"v_xor_b32 v194,0x10000,v194\n"
M_"v[120:123],v[20:23],a[100:103],v[120:123]\n"
"v_xor_b32 v195,0x10000,v195\n"
M_"v[120:123],v[24:27],a[104:107],v[120:123]\n"
"s_mov_b64 s[48:49],s[24:25]\n"
T_"v[0:1],v192 offset:49152\n"
M_"v[120:123],v[28:31],a[108:111],v[120:123]\n"
T_"v[2:3],v192 offset:49664\n"
M_"v[124:127],v[16:19],a[112:115],v[124:127]\n"
"s_mov_b32 s50,0x1c0000\n"
M_"v[124:127],v[20:23],a[116:119],v[124:127]\n"
T_"v[4:5],v192 offset:53248\n"
M_"v[124:127],v[24:27],a[120:123],v[124:127]\n"
T_"v[6:7],v192 offset:53760\n"
M_"v[124:127],v[28:31],a[124:127],v[124:127]\n"
T_"v[8:9],v192 offset:57344\n"
A_"s75,s68,128\n"
M_"v[112:115],v[32:35],a[0:3],v[112:115]\n"
T_"v[10:11],v192 offset:57856\n"
M_"v[112:115],v[36:39],a[4:7],v[112:115]\n"
A_"m0,s56,16384\n"
T_"v[12:13],v192 offset:61440\n"
M_"v[112:115],v[40:43],a[8:11],v[112:115]\n"
T_"v[14:15],v192 offset:61952\n"
T_"v[16:17],v193 offset:49152\n"
M_"v[112:115],v[44:47],a[12:15],v[112:115]\n"
T_"v[18:19],v193 offset:49664\n"
T_"v[20:21],v193 offset:53248\n"
M_"v[116:119],v[32:35],a[16:19],v[116:119]\n"
B_"v200,s[48:51],s75 offen offset:0  lds\n"
M_"v[116:119],v[36:39],a[20:23],v[116:119]\n"
T_"v[22:23],v193 offset:53760\n"
M_"v[116:119],v[40:43],a[24:27],v[116:119]\n"
M_"v[116:119],v[44:47],a[28:31],v[116:119]\n"
T_"v[24:25],v193 offset:57344\n"
M_"v[120:123],v[32:35],a[32:35],v[120:123]\n"
T_"v[26:27],v193 offset:57856\n"
M_"v[120:123],v[36:39],a[36:39],v[120:123]\n"
B_"v201,s[48:51],s75 offen offset:1024  lds\n"
M_"v[120:123],v[40:43],a[40:43],v[120:123]\n"
T_"v[28:29],v193 offset:61440\n"
M_"v[120:123],v[44:47],a[44:47],v[120:123]\n"
M_"v[124:127],v[32:35],a[48:51],v[124:127]\n"
T_"v[30:31],v193 offset:61952\n"
M_"v[124:127],v[36:39],a[52:55],v[124:127]\n"
M_"v[124:127],v[40:43],a[56:59],v[124:127]\n"
B_"v202,s[48:51],s75 offen offset:2048  lds\n"
M_"v[124:127],v[44:47],a[60:63],v[124:127]\n"
M_"v[112:115],v[48:51],a[128:131],v[112:115]\n"
T_"v[32:33],v194 offset:49152\n"
M_"v[112:115],v[52:55],a[132:135],v[112:115]\n"
T_"v[34:35],v194 offset:49664\n"
M_"v[112:115],v[56:59],a[136:139],v[112:115]\n"
"s_cmp_lg_u32 s11,0\n"
"s_cbranch_scc1 L_m_ni204\n"
"buffer_inv sc1\n"
"L_m_ni204:\n"
M_"v[112:115],v[60:63],a[140:143],v[112:115]\n"
B_"v203,s[48:51],s75 offen offset:3072  lds\n"
M_"v[116:119],v[48:51],a[144:147],v[116:119]\n"
T_"v[36:37],v194 offset:53248\n"
M_"v[116:119],v[52:55],a[148:151],v[116:119]\n"
T_"v[38:39],v194 offset:53760\n"
M_"v[116:119],v[56:59],a[152:155],v[116:119]\n"
C_"v208,v96,v97\n"
M_"v[116:119],v[60:63],a[156:159],v[116:119]\n"
T_"v[40:41],v194 offset:57344\n"
M_"v[120:123],v[48:51],a[160:163],v[120:123]\n"
A_"s75,s16,393216\n"
T_"v[42:43],v194 offset:57856\n"
C_"v209,v98,v99\n"
M_"v[120:123],v[52:55],a[164:167],v[120:123]\n"
M_"v[120:123],v[56:59],a[168:171],v[120:123]\n"
T_"v[44:45],v194 offset:61440\n"
M_"v[120:123],v[60:63],a[172:175],v[120:123]\n"
A_"m0,s56,147456\n"
C_"v210,v112,v113\n"
M_"v[124:127],v[48:51],a[176:179],v[124:127]\n"
T_"v[46:47],v194 offset:61952\n"
M_"v[124:127],v[52:55],a[180:183],v[124:127]\n"
B_"v200,s[48:51],s75 offen offset:0  lds\n"
M_"v[124:127],v[56:59],a[184:187],v[124:127]\n"
C_"v211,v114,v115\n"
C_"v212,v100,v101\n"
M_"v[124:127],v[60:63],a[188:191],v[124:127]\n"
W_"lgkmcnt(15)\n"
C_"v213,v102,v103\n"
C_"v214,v116,v117\n"
M_"v[128:131],v[0:3],a[128:131],0\n"
T_"v[48:49],v195 offset:49152\n"
M_"v[128:131],v[4:7],a[132:135],v[128:131]\n"
T_"v[50:51],v195 offset:49664\n"
C_"v215,v118,v119\n"
M_"v[128:131],v[8:11],a[136:139],v[128:131]\n"
B_"v201,s[48:51],s75 offen offset:1024  lds\n"
M_"v[128:131],v[12:15],a[140:143],v[128:131]\n"
T_"v[52:53],v195 offset:53248\n"
C_"v216,v104,v105\n"
M_"v[132:135],v[0:3],a[144:147],0\n"
T_"v[54:55],v195 offset:53760\n"
C_"v217,v106,v107\n"
M_"v[132:135],v[4:7],a[148:151],v[132:135]\n"
C_"v218,v120,v121\n"
C_"v219,v122,v123\n"
M_"v[132:135],v[8:11],a[152:155],v[132:135]\n"
T_"v[56:57],v195 offset:57344\n"
C_"v220,v108,v109\n"
M_"v[132:135],v[12:15],a[156:159],v[132:135]\n"
W_"lgkmcnt(13)\n"
C_"v221,v110,v111\n"
C_"v222,v124,v125\n"
M_"v[144:147],v[16:19],a[128:131],0\n"
B_"v202,s[48:51],s75 offen offset:2048  lds\n"
M_"v[144:147],v[20:23],a[132:135],v[144:147]\n"
T_"v[58:59],v195 offset:57856\n"
T_"v[60:61],v195 offset:61440\n"
M_"v[144:147],v[24:27],a[136:139],v[144:147]\n"
C_"v223,v126,v127\n"
M_"v[144:147],v[28:31],a[140:143],v[144:147]\n"
T_"v[62:63],v195 offset:61952\n"
M_"v[148:151],v[16:19],a[144:147],0\n"
B_"v203,s[48:51],s75 offen offset:3072  lds\n"
M_"v[148:151],v[20:23],a[148:151],v[148:151]\n"
M_"v[148:151],v[24:27],a[152:155],v[148:151]\n"
M_"v[148:151],v[28:31],a[156:159],v[148:151]\n"
W_"lgkmcnt(8)\n"
M_"v[160:163],v[32:35],a[128:131],0\n"
M_"v[160:163],v[36:39],a[132:135],v[160:163]\n"
M_"v[160:163],v[40:43],a[136:139],v[160:163]\n"
M_"v[160:163],v[44:47],a[140:143],v[160:163]\n"
M_"v[164:167],v[32:35],a[144:147],0\n"
M_"v[164:167],v[36:39],a[148:151],v[164:167]\n"
M_"v[164:167],v[40:43],a[152:155],v[164:167]\n"
M_"v[164:167],v[44:47],a[156:159],v[164:167]\n"
W_"lgkmcnt(0)\n"
M_"v[176:179],v[48:51],a[128:131],0\n"
M_"v[176:179],v[52:55],a[132:135],v[176:179]\n"
M_"v[176:179],v[56:59],a[136:139],v[176:179]\n"
M_"v[176:179],v[60:63],a[140:143],v[176:179]\n"
M_"v[180:183],v[48:51],a[144:147],0\n"
M_"v[180:183],v[52:55],a[148:151],v[180:183]\n"
M_"v[180:183],v[56:59],a[152:155],v[180:183]\n"
M_"v[180:183],v[60:63],a[156:159],v[180:183]\n"
M_"v[136:139],v[0:3],a[160:163],0\n"
"s_cmp_lg_u32 s11,1\n"
"s_cbranch_scc1 L_m_ni205\n"
"buffer_inv sc1\n"
"L_m_ni205:\n"
M_"v[136:139],v[4:7],a[164:167],v[136:139]\n"
M_"v[136:139],v[8:11],a[168:171],v[136:139]\n"
M_"v[136:139],v[12:15],a[172:175],v[136:139]\n"
M_"v[140:143],v[0:3],a[176:179],0\n"
M_"v[140:143],v[4:7],a[180:183],v[140:143]\n"
M_"v[140:143],v[8:11],a[184:187],v[140:143]\n"
M_"v[140:143],v[12:15],a[188:191],v[140:143]\n"
M_"v[152:155],v[16:19],a[160:163],0\n"
T_"v[0:1],v192 offset:16384\n"
M_"v[152:155],v[20:23],a[164:167],v[152:155]\n"
T_"v[2:3],v192 offset:16896\n"
M_"v[152:155],v[24:27],a[168:171],v[152:155]\n"
M_"v[152:155],v[28:31],a[172:175],v[152:155]\n"
T_"v[4:5],v192 offset:20480\n"
M_"v[156:159],v[16:19],a[176:179],0\n"
T_"v[6:7],v192 offset:20992\n"
M_"v[156:159],v[20:23],a[180:183],v[156:159]\n"
M_"v[156:159],v[24:27],a[184:187],v[156:159]\n"
T_"v[8:9],v192 offset:24576\n"
M_"v[156:159],v[28:31],a[188:191],v[156:159]\n"
M_"v[168:171],v[32:35],a[160:163],0\n"
T_"v[10:11],v192 offset:25088\n"
M_"v[168:171],v[36:39],a[164:167],v[168:171]\n"
T_"v[12:13],v192 offset:28672\n"
M_"v[168:171],v[40:43],a[168:171],v[168:171]\n"
M_"v[168:171],v[44:47],a[172:175],v[168:171]\n"
T_"v[14:15],v192 offset:29184\n"
T_"v[16:17],v193 offset:16384\n"
M_"v[172:175],v[32:35],a[176:179],0\n"
T_"v[18:19],v193 offset:16896\n"
T_"v[20:21],v193 offset:20480\n"
M_"v[172:175],v[36:39],a[180:183],v[172:175]\n"
T_"v[22:23],v193 offset:20992\n"
M_"v[172:175],v[40:43],a[184:187],v[172:175]\n"
T_"v[24:25],v193 offset:24576\n"
M_"v[172:175],v[44:47],a[188:191],v[172:175]\n"
M_"v[184:187],v[48:51],a[160:163],0\n"
T_"v[26:27],v193 offset:25088\n"
M_"v[184:187],v[52:55],a[164:167],v[184:187]\n"
T_"v[28:29],v193 offset:28672\n"
M_"v[184:187],v[56:59],a[168:171],v[184:187]\n"
M_"v[184:187],v[60:63],a[172:175],v[184:187]\n"
T_"v[30:31],v193 offset:29184\n"
T_"v[32:33],v194 offset:16384\n"
M_"v[188:191],v[48:51],a[176:179],0\n"
"s_lshr_b32 s85,s11,3\n"
T_"v[34:35],v194 offset:16896\n"
T_"v[36:37],v194 offset:20480\n"
M_"v[188:191],v[52:55],a[180:183],v[188:191]\n"
"s_cmp_eq_u32 s85,8\n"
"s_cbranch_scc1 L_slow_PKg\n"
"L_back_PKg:\n"
M_"v[188:191],v[56:59],a[184:187],v[188:191]\n"
T_"v[38:39],v194 offset:20992\n"
T_"v[40:41],v194 offset:24576\n"
M_"v[188:191],v[60:63],a[188:191],v[188:191]\n"
W_"lgkmcnt(13)\n"
"s_lshl_b32 s85,s31,8\n"
M_"v[128:131],v[0:3],a[64:67],v[128:131]\n"
A_"s85,s85,s30\n"
T_"v[42:43],v194 offset:25088\n"
M_"v[128:131],v[4:7],a[68:71],v[128:131]\n"
"s_cmp_eq_u32 s85,0\n"
"s_cselect_b32 s79,0x4f00000,s94\n"
T_"v[44:45],v194 offset:28672\n"
M_"v[128:131],v[8:11],a[72:75],v[128:131]\n"
"buffer_store_dword v254,off,s[52:55],s79 sc1\n"
M_"v[128:131],v[12:15],a[76:79],v[128:131]\n"
T_"v[46:47],v194 offset:29184\n"
T_"v[48:49],v195 offset:16384\n"
M_"v[132:135],v[0:3],a[80:83],v[132:135]\n"
"v_mov_b32 v205,v243\n"
T_"v[50:51],v195 offset:16896\n"
M_"v[132:135],v[4:7],a[84:87],v[132:135]\n"
"v_accvgpr_write_b32 a128,v224\n"
T_"v[52:53],v195 offset:20480\n"
M_"v[132:135],v[8:11],a[88:91],v[132:135]\n"
"v_and_b32 v206,96,v244\n"
"v_and_b32 v207,16,v244\n"
M_"v[132:135],v[12:15],a[92:95],v[132:135]\n"
W_"lgkmcnt(11)\n"
"v_accvgpr_write_b32 a129,v225\n"
T_"v[54:55],v195 offset:20992\n"
M_"v[144:147],v[16:19],a[64:67],v[144:147]\n"
"s_and_b32 s10,s30,0xe6\n"
"v_lshl_add_u32 v207,v206,2,v207\n"
T_"v[56:57],v195 offset:24576\n"
M_"v[144:147],v[20:23],a[68:71],v[144:147]\n"
"s_lshl4_add_u32 s10,s10,0x9e2000\n"
"v_add_lshl_u32 v207,s10,v207,3\n"
"v_add_u32 v207,20,v207\n"
M_"v[144:147],v[24:27],a[72:75],v[144:147]\n"
"buffer_load_dword v206,v207,s[52:55],0 offen sc1\n"
M_"v[144:147],v[28:31],a[76:79],v[144:147]\n"
A_"s36,s96,s87\n"
"v_accvgpr_write_b32 a130,v226\n"
T_"v[58:59],v195 offset:25088\n"
M_"v[148:151],v[16:19],a[80:83],v[148:151]\n"
A_"s37,s98,s87\n"
T_"v[60:61],v195 offset:28672\n"
T_"v[62:63],v195 offset:29184\n"
M_"v[148:151],v[20:23],a[84:87],v[148:151]\n"
A_"s57,s56,0x0\n"
"v_accvgpr_write_b32 a131,v227\n"
"v_accvgpr_write_b32 a132,v208\n"
M_"v[148:151],v[24:27],a[88:91],v[148:151]\n"
"s_cmp_lg_u32 s11,2\n"
"s_cbranch_scc1 L_m_ni206\n"
"buffer_inv sc1\n"
"L_m_ni206:\n"
M_"v[148:151],v[28:31],a[92:95],v[148:151]\n"
W_"lgkmcnt(8)\n"
"v_accvgpr_write_b32 a133,v209\n"
"s_sub_u32 s58,s34,1\n"
M_"v[160:163],v[32:35],a[64:67],v[160:163]\n"
"s_lshl_b32 s59,s34,1\n"
M_"v[160:163],v[36:39],a[68:71],v[160:163]\n"
"v_accvgpr_write_b32 a134,v210\n"
M_"v[160:163],v[40:43],a[72:75],v[160:163]\n"
"s_sub_u32 s59,13,s59\n"
M_"v[160:163],v[44:47],a[76:79],v[160:163]\n"
M_"v[164:167],v[32:35],a[80:83],v[164:167]\n"
"v_accvgpr_write_b32 a135,v211\n"
M_"v[164:167],v[36:39],a[84:87],v[164:167]\n"
"s_mov_b64 s[62:63],0\n"
M_"v[164:167],v[40:43],a[88:91],v[164:167]\n"
M_"v[164:167],v[44:47],a[92:95],v[164:167]\n"
W_"lgkmcnt(0)\n"
"v_add_u32 v245,0x10000,v244\n"
"v_accvgpr_write_b32 a144,v228\n"
M_"v[176:179],v[48:51],a[64:67],v[176:179]\n"
M_"v[176:179],v[52:55],a[68:71],v[176:179]\n"
"v_bfe_u32 v251,v244,4,4\n"
"v_accvgpr_write_b32 a145,v229\n"
M_"v[176:179],v[56:59],a[72:75],v[176:179]\n"
M_"v[176:179],v[60:63],a[76:79],v[176:179]\n"
"v_lshrrev_b32 v246,8,v244\n"
M_"v[180:183],v[48:51],a[80:83],v[180:183]\n"
"v_accvgpr_write_b32 a146,v230\n"
M_"v[180:183],v[52:55],a[84:87],v[180:183]\n"
"v_lshlrev_b32 v246,2,v246\n"
M_"v[180:183],v[56:59],a[88:91],v[180:183]\n"
"v_accvgpr_write_b32 a147,v231\n"
M_"v[180:183],v[60:63],a[92:95],v[180:183]\n"
M_"v[136:139],v[0:3],a[96:99],v[136:139]\n"
"v_sub_u32 v251,v251,v246\n"
M_"v[136:139],v[4:7],a[100:103],v[136:139]\n"
"v_accvgpr_write_b32 a148,v212\n"
M_"v[136:139],v[8:11],a[104:107],v[136:139]\n"
"s_lshl_b32 s60,s31,4\n"
M_"v[136:139],v[12:15],a[108:111],v[136:139]\n"
M_"v[140:143],v[0:3],a[112:115],v[140:143]\n"
"s_barrier\n"
M_"v[140:143],v[4:7],a[116:119],v[140:143]\n"
"v_add_u32 v251,s60,v251\n"
"v_accvgpr_write_b32 a149,v213\n"
M_"v[140:143],v[8:11],a[120:123],v[140:143]\n"
"s_cmp_eq_u32 s34,0\n"
"s_cselect_b32 s60,0xff800000,0\n"
"v_accvgpr_write_b32 a150,v214\n"
M_"v[140:143],v[12:15],a[124:127],v[140:143]\n"
A_"s75,s68,131200\n"
M_"v[152:155],v[16:19],a[96:99],v[152:155]\n"
T_"v[0:1],v192 offset:32768\n"
M_"v[152:155],v[20:23],a[100:103],v[152:155]\n"
A_"m0,s56,114688\n"
"v_accvgpr_write_b32 a151,v215\n"
T_"v[2:3],v192 offset:33280\n"
M_"v[152:155],v[24:27],a[104:107],v[152:155]\n"
"v_mov_b32 v253,s60\n"
M_"v[152:155],v[28:31],a[108:111],v[152:155]\n"
B_"v200,s[48:51],s75 offen offset:0  lds\n"
M_"v[156:159],v[16:19],a[112:115],v[156:159]\n"
T_"v[4:5],v192 offset:36864\n"
"v_accvgpr_write_b32 a160,v232\n"
M_"v[156:159],v[20:23],a[116:119],v[156:159]\n"
T_"v[6:7],v192 offset:37376\n"
"v_bfe_i32 v246,v244,5,1\n"
M_"v[156:159],v[24:27],a[120:123],v[156:159]\n"
"v_accvgpr_write_b32 a161,v233\n"
T_"v[8:9],v192 offset:40960\n"
M_"v[156:159],v[28:31],a[124:127],v[156:159]\n"
B_"v201,s[48:51],s75 offen offset:1024  lds\n"
M_"v[168:171],v[32:35],a[96:99],v[168:171]\n"
"v_and_b32 v246,0xaaaaaaaa,v246\n"
T_"v[10:11],v192 offset:41472\n"
M_"v[168:171],v[36:39],a[100:103],v[168:171]\n"
T_"v[12:13],v192 offset:45056\n"
"v_or_b32 v246,0x44444444,v246\n"
M_"v[168:171],v[40:43],a[104:107],v[168:171]\n"
"v_accvgpr_write_b32 a162,v234\n"
M_"v[168:171],v[44:47],a[108:111],v[168:171]\n"
B_"v202,s[48:51],s75 offen offset:2048  lds\n"
M_"v[172:175],v[32:35],a[112:115],v[172:175]\n"
T_"v[14:15],v192 offset:45568\n"
T_"v[16:17],v193 offset:32768\n"
M_"v[172:175],v[36:39],a[116:119],v[172:175]\n"
T_"v[18:19],v193 offset:33280\n"
"v_add_u32 v240,48,v244\n"
M_"v[172:175],v[40:43],a[120:123],v[172:175]\n"
"v_accvgpr_write_b32 a163,v235\n"
T_"v[20:21],v193 offset:36864\n"
M_"v[172:175],v[44:47],a[124:127],v[172:175]\n"
B_"v203,s[48:51],s75 offen offset:3072  lds\n"
M_"v[184:187],v[48:51],a[96:99],v[184:187]\n"
T_"v[22:23],v193 offset:37376\n"
"v_bfe_i32 v240,v240,5,1\n"
M_"v[184:187],v[52:55],a[100:103],v[184:187]\n"
"v_accvgpr_write_b32 a164,v216\n"
T_"v[24:25],v193 offset:40960\n"
M_"v[184:187],v[56:59],a[104:107],v[184:187]\n"
T_"v[26:27],v193 offset:41472\n"
T_"v[28:29],v193 offset:45056\n"
M_"v[184:187],v[60:63],a[108:111],v[184:187]\n"
A_"s75,s68,262272\n"
T_"v[30:31],v193 offset:45568\n"
"v_and_b32 v240,0x3f803f80,v240\n"
M_"v[188:191],v[48:51],a[112:115],v[188:191]\n"
"s_cmp_lg_u32 s11,3\n"
"s_cbranch_scc1 L_m_ni207\n"
"buffer_inv sc1\n"
"L_m_ni207:\n"
M_"v[188:191],v[52:55],a[116:119],v[188:191]\n"
A_"m0,s56,81920\n"
T_"v[32:33],v194 offset:32768\n"
"v_accvgpr_write_b32 a165,v217\n"
M_"v[188:191],v[56:59],a[120:123],v[188:191]\n"
T_"v[34:35],v194 offset:33280\n"
"v_mov_b32 v241,v240\n"
M_"v[188:191],v[60:63],a[124:127],v[188:191]\n"
W_"lgkmcnt(10)\n"
"v_accvgpr_write_b32 a166,v218\n"
T_"v[36:37],v194 offset:36864\n"
M_"v[128:131],v[0:3],a[0:3],v[128:131]\n"
B_"v200,s[48:51],s75 offen offset:0  lds\n"
M_"v[128:131],v[4:7],a[4:7],v[128:131]\n"
"v_mov_b32 v242,v240\n"
T_"v[38:39],v194 offset:37376\n"
M_"v[128:131],v[8:11],a[8:11],v[128:131]\n"
"v_mov_b32 v243,v240\n"
"v_accvgpr_write_b32 a167,v219\n"
M_"v[128:131],v[12:15],a[12:15],v[128:131]\n"
"s_lshl_b32 s60,s33,12\n"
T_"v[40:41],v194 offset:40960\n"
T_"v[42:43],v194 offset:41472\n"
M_"v[132:135],v[0:3],a[16:19],v[132:135]\n"
B_"v201,s[48:51],s75 offen offset:1024  lds\n"
M_"v[132:135],v[4:7],a[20:23],v[132:135]\n"
A_"s60,s60,0x3a00000\n"
T_"v[44:45],v194 offset:45056\n"
T_"v[46:47],v194 offset:45568\n"
M_"v[132:135],v[8:11],a[24:27],v[132:135]\n"
"s_lshl3_add_u32 s96,s96,s60\n"
T_"v[48:49],v195 offset:32768\n"
"v_accvgpr_write_b32 a176,v236\n"
M_"v[132:135],v[12:15],a[28:31],v[132:135]\n"
W_"lgkmcnt(9)\n"
T_"v[50:51],v195 offset:33280\n"
"v_accvgpr_write_b32 a177,v237\n"
M_"v[144:147],v[16:19],a[0:3],v[144:147]\n"
B_"v202,s[48:51],s75 offen offset:2048  lds\n"
M_"v[144:147],v[20:23],a[4:7],v[144:147]\n"
W_"vmcnt(7)\n"
T_"v[52:53],v195 offset:36864\n"
T_"v[54:55],v195 offset:37376\n"
M_"v[144:147],v[24:27],a[8:11],v[144:147]\n"
"s_lshl3_add_u32 s97,s97,s60\n"
"v_cmp_ne_u32 s[12:13],s32,v206\n"
"v_accvgpr_write_b32 a178,v238\n"
M_"v[144:147],v[28:31],a[12:15],v[144:147]\n"
"s_cmp_lg_u64 s[12:13],0\n"
"s_cbranch_scc1 L_slow_PHg\n"
"L_back_PHg:\n"
M_"v[148:151],v[16:19],a[16:19],v[148:151]\n"
T_"v[56:57],v195 offset:40960\n"
T_"v[58:59],v195 offset:41472\n"
M_"v[148:151],v[20:23],a[20:23],v[148:151]\n"
T_"v[60:61],v195 offset:45056\n"
T_"v[62:63],v195 offset:45568\n"
M_"v[148:151],v[24:27],a[24:27],v[148:151]\n"
"v_accvgpr_write_b32 a179,v239\n"
"s_lshl3_add_u32 s98,s98,s60\n"
M_"v[148:151],v[28:31],a[28:31],v[148:151]\n"
W_"lgkmcnt(8)\n"
B_"v203,s[48:51],s75 offen offset:3072  lds\n"
M_"v[160:163],v[32:35],a[0:3],v[160:163]\n"
"s_lshl3_add_u32 s99,s99,s60\n"
"s_mov_b32 s60,0x5d800000\n"
"v_accvgpr_write_b32 a180,v220\n"
M_"v[160:163],v[36:39],a[4:7],v[160:163]\n"
A_"s10,s14,0\n"
"v_accvgpr_write_b32 a181,v221\n"
"v_accvgpr_write_b32 a182,v222\n"
M_"v[160:163],v[40:43],a[8:11],v[160:163]\n"
"v_cmp_le_i32 s[64:65],0,v251\n"
"v_cmp_le_i32 s[66:67],1,v251\n"
M_"v[160:163],v[44:47],a[12:15],v[160:163]\n"
B_"a[80:83],v244,s[52:55],s10 offen offset:0 sc1\n"
M_"v[164:167],v[32:35],a[16:19],v[164:167]\n"
"v_accvgpr_write_b32 a183,v223\n"
"v_cmp_le_i32 s[68:69],2,v251\n"
M_"v[164:167],v[36:39],a[20:23],v[164:167]\n"
"v_cmp_le_i32 s[70:71],3,v251\n"
M_"v[164:167],v[40:43],a[24:27],v[164:167]\n"
"v_cmp_le_i32 s[72:73],16,v251\n"
M_"v[164:167],v[44:47],a[28:31],v[164:167]\n"
W_"lgkmcnt(0)\n"
M_"v[176:179],v[48:51],a[0:3],v[176:179]\n"
B_"a[84:87],v244,s[52:55],s10 offen offset:1024 sc1\n"
M_"v[176:179],v[52:55],a[4:7],v[176:179]\n"
"v_cmp_le_i32 s[74:75],17,v251\n"
"v_cmp_le_i32 s[76:77],18,v251\n"
M_"v[176:179],v[56:59],a[8:11],v[176:179]\n"
M_"v[176:179],v[60:63],a[12:15],v[176:179]\n"
B_"a[88:91],v244,s[52:55],s10 offen offset:2048 sc1\n"
M_"v[180:183],v[48:51],a[16:19],v[180:183]\n"
"v_cmp_le_i32 s[78:79],19,v251\n"
"v_cmp_le_i32 s[80:81],32,v251\n"
M_"v[180:183],v[52:55],a[20:23],v[180:183]\n"
"v_cmp_le_i32 s[82:83],33,v251\n"
M_"v[180:183],v[56:59],a[24:27],v[180:183]\n"
B_"a[92:95],v244,s[52:55],s10 offen offset:3072 sc1\n"
M_"v[180:183],v[60:63],a[28:31],v[180:183]\n"
A_"s10,s15,0\n"
"v_cmp_le_i32 s[84:85],34,v251\n"
M_"v[136:139],v[0:3],a[32:35],v[136:139]\n"
"v_cmp_le_i32 s[86:87],35,v251\n"
M_"v[136:139],v[4:7],a[36:39],v[136:139]\n"
M_"v[136:139],v[8:11],a[40:43],v[136:139]\n"
B_"a[96:99],v244,s[52:55],s10 offen offset:0 sc1\n"
M_"v[136:139],v[12:15],a[44:47],v[136:139]\n"
"v_cmp_le_i32 s[88:89],48,v251\n"
"v_cmp_le_i32 s[90:91],49,v251\n"
M_"v[140:143],v[0:3],a[48:51],v[140:143]\n"
M_"v[140:143],v[4:7],a[52:55],v[140:143]\n"
"v_cmp_le_i32 s[92:93],50,v251\n"
M_"v[140:143],v[8:11],a[56:59],v[140:143]\n"
B_"a[100:103],v244,s[52:55],s10 offen offset:1024 sc1\n"
M_"v[140:143],v[12:15],a[60:63],v[140:143]\n"
"v_cmp_le_i32 s[94:95],51,v251\n"
M_"v[152:155],v[16:19],a[32:35],v[152:155]\n"
T_"v[0:1],v192 offset:0\n"
M_"v[152:155],v[20:23],a[36:39],v[152:155]\n"
T_"v[2:3],v192 offset:512\n"
K_"v224,v253,0,s[64:65]\n"
M_"v[152:155],v[24:27],a[40:43],v[152:155]\n"
B_"a[104:107],v244,s[52:55],s10 offen offset:2048 sc1\n"
M_"v[152:155],v[28:31],a[44:47],v[152:155]\n"
T_"v[4:5],v192 offset:4096\n"
K_"v225,v253,0,s[66:67]\n"
M_"v[156:159],v[16:19],a[48:51],v[156:159]\n"
T_"v[6:7],v192 offset:4608\n"
K_"v226,v253,0,s[68:69]\n"
M_"v[156:159],v[20:23],a[52:55],v[156:159]\n"
K_"v227,v253,0,s[70:71]\n"
M_"v[156:159],v[24:27],a[56:59],v[156:159]\n"
B_"a[108:111],v244,s[52:55],s10 offen offset:3072 sc1\n"
M_"v[156:159],v[28:31],a[60:63],v[156:159]\n"
A_"s10,s14,12288\n"
T_"v[8:9],v192 offset:8192\n"
K_"v228,v253,0,s[72:73]\n"
M_"v[168:171],v[32:35],a[32:35],v[168:171]\n"
T_"v[10:11],v192 offset:8704\n"
K_"v229,v253,0,s[74:75]\n"
M_"v[168:171],v[36:39],a[36:39],v[168:171]\n"
T_"v[12:13],v192 offset:12288\n"
M_"v[168:171],v[40:43],a[40:43],v[168:171]\n"
B_"a[64:67],v244,s[52:55],s10 offen offset:0 sc1\n"
M_"v[168:171],v[44:47],a[44:47],v[168:171]\n"
T_"v[14:15],v192 offset:12800\n"
T_"v[16:17],v193 offset:0\n"
M_"v[172:175],v[32:35],a[48:51],v[172:175]\n"
T_"v[18:19],v193 offset:512\n"
T_"v[20:21],v193 offset:4096\n"
M_"v[172:175],v[36:39],a[52:55],v[172:175]\n"
T_"v[22:23],v193 offset:4608\n"
K_"v230,v253,0,s[76:77]\n"
M_"v[172:175],v[40:43],a[56:59],v[172:175]\n"
B_"a[68:71],v244,s[52:55],s10 offen offset:1024 sc1\n"
M_"v[172:175],v[44:47],a[60:63],v[172:175]\n"
A_"s10,s15,12288\n"
T_"v[24:25],v193 offset:8192\n"
K_"v231,v253,0,s[78:79]\n"
M_"v[184:187],v[48:51],a[32:35],v[184:187]\n"
T_"v[26:27],v193 offset:8704\n"
K_"v232,v253,0,s[80:81]\n"
M_"v[184:187],v[52:55],a[36:39],v[184:187]\n"
T_"v[28:29],v193 offset:12288\n"
K_"v233,v253,0,s[82:83]\n"
M_"v[184:187],v[56:59],a[40:43],v[184:187]\n"
B_"a[72:75],v244,s[52:55],s10 offen offset:0 sc1\n"
M_"v[184:187],v[60:63],a[44:47],v[184:187]\n"
T_"v[30:31],v193 offset:12800\n"
T_"v[32:33],v194 offset:0\n"
M_"v[188:191],v[48:51],a[48:51],v[188:191]\n"
T_"v[34:35],v194 offset:512\n"
T_"v[36:37],v194 offset:4096\n"
M_"v[188:191],v[52:55],a[52:55],v[188:191]\n"
T_"v[38:39],v194 offset:4608\n"
K_"v234,v253,0,s[84:85]\n"
M_"v[188:191],v[56:59],a[56:59],v[188:191]\n"
B_"a[76:79],v244,s[52:55],s10 offen offset:1024 sc1\n"
M_"v[188:191],v[60:63],a[60:63],v[188:191]\n"
W_"lgkmcnt(12)\n"
T_"v[40:41],v194 offset:8192\n"
K_"v235,v253,0,s[86:87]\n"
M_"v[128:131],v[0:3],a[192:195],v[128:131]\n"
A_"s10,s14,4096\n"
T_"v[42:43],v194 offset:8704\n"
K_"v236,v253,0,s[88:89]\n"
M_"v[128:131],v[4:7],a[196:199],v[128:131]\n"
T_"v[44:45],v194 offset:12288\n"
K_"v237,v253,0,s[90:91]\n"
M_"v[128:131],v[8:11],a[200:203],v[128:131]\n"
B_"a[112:115],v244,s[52:55],s10 offen offset:0 sc1\n"
M_"v[128:131],v[12:15],a[204:207],v[128:131]\n"
T_"v[46:47],v194 offset:12800\n"
T_"v[48:49],v195 offset:0\n"
M_"v[132:135],v[0:3],a[208:211],v[132:135]\n"
T_"v[50:51],v195 offset:512\n"
T_"v[52:53],v195 offset:4096\n"
M_"v[132:135],v[4:7],a[212:215],v[132:135]\n"
T_"v[54:55],v195 offset:4608\n"
K_"v238,v253,0,s[92:93]\n"
M_"v[132:135],v[8:11],a[216:219],v[132:135]\n"
B_"a[116:119],v244,s[52:55],s10 offen offset:1024 sc1\n"
M_"v[132:135],v[12:15],a[220:223],v[132:135]\n"
W_"lgkmcnt(12)\n"
T_"v[56:57],v195 offset:8192\n"
K_"v239,v253,0,s[94:95]\n"
M_"v[144:147],v[16:19],a[192:195],v[144:147]\n"
"buffer_load_dword v204,v205,s[44:47],0 offen\n"
M_"v[144:147],v[20:23],a[196:199],v[144:147]\n"
T_"v[58:59],v195 offset:8704\n"
T_"v[60:61],v195 offset:12288\n"
M_"v[144:147],v[24:27],a[200:203],v[144:147]\n"
B_"a[120:123],v244,s[52:55],s10 offen offset:2048 sc1\n"
M_"v[144:147],v[28:31],a[204:207],v[144:147]\n"
T_"v[62:63],v195 offset:12800\n"
"v_mov_b64 v[208:209],0\n"
M_"v[148:151],v[16:19],a[208:211],v[148:151]\n"
"v_mov_b64 v[210:211],0\n"
"v_mov_b64 v[212:213],0\n"
M_"v[148:151],v[20:23],a[212:215],v[148:151]\n"
"v_mov_b64 v[214:215],0\n"
"v_mov_b64 v[216:217],0\n"
M_"v[148:151],v[24:27],a[216:219],v[148:151]\n"
B_"a[124:127],v244,s[52:55],s10 offen offset:3072 sc1\n"
M_"v[148:151],v[28:31],a[220:223],v[148:151]\n"
W_"lgkmcnt(8)\n"
"v_mov_b64 v[218:219],0\n"
"v_mov_b64 v[220:221],0\n"
M_"v[160:163],v[32:35],a[192:195],v[160:163]\n"
A_"s10,s15,4096\n"
"v_mov_b64 v[222:223],0\n"
M_"v[160:163],v[36:39],a[196:199],v[160:163]\n"
M_"v[160:163],v[40:43],a[200:203],v[160:163]\n"
B_"a[0:3],v244,s[52:55],s10 offen offset:0 sc1\n"
M_"v[160:163],v[44:47],a[204:207],v[160:163]\n"
M_"v[164:167],v[32:35],a[208:211],v[164:167]\n"
M_"v[164:167],v[36:39],a[212:215],v[164:167]\n"
M_"v[164:167],v[40:43],a[216:219],v[164:167]\n"
B_"a[4:7],v244,s[52:55],s10 offen offset:1024 sc1\n"
M_"v[164:167],v[44:47],a[220:223],v[164:167]\n"
W_"lgkmcnt(0)\n"
M_"v[176:179],v[48:51],a[192:195],v[176:179]\n"
M_"v[176:179],v[52:55],a[196:199],v[176:179]\n"
M_"v[176:179],v[56:59],a[200:203],v[176:179]\n"
B_"a[8:11],v244,s[52:55],s10 offen offset:2048 sc1\n"
M_"v[176:179],v[60:63],a[204:207],v[176:179]\n"
M_"v[180:183],v[48:51],a[208:211],v[180:183]\n"
M_"v[180:183],v[52:55],a[212:215],v[180:183]\n"
M_"v[180:183],v[56:59],a[216:219],v[180:183]\n"
B_"a[12:15],v244,s[52:55],s10 offen offset:3072 sc1\n"
M_"v[180:183],v[60:63],a[220:223],v[180:183]\n"
A_"s10,s14,8192\n"
C_"v128,v128,v129\n"
C_"v129,v130,v131\n"
M_"v[136:139],v[0:3],a[224:227],v[136:139]\n"
C_"v130,v144,v145\n"
M_"v[136:139],v[4:7],a[228:231],v[136:139]\n"
C_"v131,v146,v147\n"
M_"v[136:139],v[8:11],a[232:235],v[136:139]\n"
B_"a[16:19],v244,s[52:55],s10 offen offset:0 sc1\n"
M_"v[136:139],v[12:15],a[236:239],v[136:139]\n"
C_"v160,v160,v161\n"
C_"v161,v162,v163\n"
M_"v[140:143],v[0:3],a[240:243],v[140:143]\n"
W_"vmcnt(22) lgkmcnt(0)\n"
"s_barrier\n"
C_"v162,v176,v177\n"
M_"v[140:143],v[4:7],a[244:247],v[140:143]\n"
C_"v163,v178,v179\n"
M_"v[140:143],v[8:11],a[248:251],v[140:143]\n"
B_"a[20:23],v244,s[52:55],s10 offen offset:1024 sc1\n"
M_"v[140:143],v[12:15],a[252:255],v[140:143]\n"
"v_accvgpr_write_b32 a136,v128\n"
"v_accvgpr_write_b32 a137,v129\n"
M_"v[152:155],v[16:19],a[224:227],v[152:155]\n"
"v_xor_b32 v192,0x10000,v192\n"
"v_accvgpr_write_b32 a138,v130\n"
M_"v[152:155],v[20:23],a[228:231],v[152:155]\n"
"v_xor_b32 v193,0x10000,v193\n"
"v_accvgpr_write_b32 a139,v131\n"
M_"v[152:155],v[24:27],a[232:235],v[152:155]\n"
B_"a[24:27],v244,s[52:55],s10 offen offset:2048 sc1\n"
M_"v[152:155],v[28:31],a[236:239],v[152:155]\n"
"v_xor_b32 v194,0x10000,v194\n"
"v_xor_b32 v195,0x10000,v195\n"
M_"v[156:159],v[16:19],a[240:243],v[156:159]\n"
T_"v[0:1],v192 offset:16384\n"
"v_accvgpr_write_b32 a140,v160\n"
"v_accvgpr_write_b32 a141,v161\n"
M_"v[156:159],v[20:23],a[244:247],v[156:159]\n"
T_"v[2:3],v192 offset:16896\n"
"v_accvgpr_write_b32 a142,v162\n"
M_"v[156:159],v[24:27],a[248:251],v[156:159]\n"
B_"a[28:31],v244,s[52:55],s10 offen offset:3072 sc1\n"
A_"s10,s56,0\n"
M_"v[156:159],v[28:31],a[252:255],v[156:159]\n"
A_"m0,s56,65536\n"
T_"v[4:5],v192 offset:20480\n"
T_"v[6:7],v192 offset:20992\n"
"v_accvgpr_write_b32 a143,v163\n"
M_"v[168:171],v[32:35],a[224:227],v[168:171]\n"
B_"v244,s[40:43],s10 offen offset:0 lds\n"
T_"v[8:9],v192 offset:24576\n"
M_"v[168:171],v[36:39],a[228:231],v[168:171]\n"
T_"v[10:11],v192 offset:25088\n"
C_"v132,v132,v133\n"
M_"v[168:171],v[40:43],a[232:235],v[168:171]\n"
T_"v[12:13],v192 offset:28672\n"
C_"v133,v134,v135\n"
C_"v134,v148,v149\n"
M_"v[168:171],v[44:47],a[236:239],v[168:171]\n"
B_"v244,s[40:43],s10 offen offset:1024 lds\n"
T_"v[14:15],v192 offset:29184\n"
M_"v[172:175],v[32:35],a[240:243],v[172:175]\n"
T_"v[16:17],v193 offset:16384\n"
T_"v[18:19],v193 offset:16896\n"
T_"v[20:21],v193 offset:20480\n"
M_"v[172:175],v[36:39],a[244:247],v[172:175]\n"
T_"v[22:23],v193 offset:20992\n"
C_"v135,v150,v151\n"
M_"v[172:175],v[40:43],a[248:251],v[172:175]\n"
B_"v244,s[40:43],s10 offen offset:2048 lds\n"
T_"v[24:25],v193 offset:24576\n"
M_"v[172:175],v[44:47],a[252:255],v[172:175]\n"
C_"v164,v164,v165\n"
C_"v165,v166,v167\n"
C_"v166,v180,v181\n"
M_"v[184:187],v[48:51],a[224:227],v[184:187]\n"
T_"v[26:27],v193 offset:25088\n"
C_"v167,v182,v183\n"
"v_accvgpr_write_b32 a152,v132\n"
M_"v[184:187],v[52:55],a[228:231],v[184:187]\n"
B_"v244,s[40:43],s10 offen offset:3072 lds\n"
M_"v[184:187],v[56:59],a[232:235],v[184:187]\n"
A_"s10,s56,16384\n"
T_"v[28:29],v193 offset:28672\n"
"v_accvgpr_write_b32 a153,v133\n"
"v_accvgpr_write_b32 a154,v134\n"
M_"v[184:187],v[60:63],a[236:239],v[184:187]\n"
A_"m0,s56,98304\n"
T_"v[30:31],v193 offset:29184\n"
T_"v[32:33],v194 offset:16384\n"
T_"v[34:35],v194 offset:16896\n"
M_"v[188:191],v[48:51],a[240:243],v[188:191]\n"
T_"v[36:37],v194 offset:20480\n"
T_"v[38:39],v194 offset:20992\n"
M_"v[188:191],v[52:55],a[244:247],v[188:191]\n"
B_"v244,s[40:43],s10 offen offset:0 lds\n"
"v_accvgpr_write_b32 a155,v135\n"
M_"v[188:191],v[56:59],a[248:251],v[188:191]\n"
T_"v[40:41],v194 offset:24576\n"
"v_accvgpr_write_b32 a156,v164\n"
"v_accvgpr_write_b32 a157,v165\n"
M_"v[188:191],v[60:63],a[252:255],v[188:191]\n"
W_"lgkmcnt(13)\n"
"v_accvgpr_write_b32 a158,v166\n"
"v_accvgpr_write_b32 a159,v167\n"
W_"vmcnt(26)\n"
M_"v[96:99],v[0:3],a[80:83],0\n"
A_"s44,s40,0x1000000\n"
"s_addc_u32 s45,s41,0\n"
T_"v[42:43],v194 offset:25088\n"
M_"v[96:99],v[4:7],a[84:87],v[96:99]\n"
B_"v244,s[40:43],s10 offen offset:1024 lds\n"
T_"v[44:45],v194 offset:28672\n"
M_"v[96:99],v[8:11],a[88:91],v[96:99]\n"
C_"v136,v136,v137\n"
C_"v137,v138,v139\n"
C_"v138,v152,v153\n"
M_"v[96:99],v[12:15],a[92:95],v[96:99]\n"
W_"vmcnt(23)\n"
T_"v[46:47],v194 offset:29184\n"
T_"v[48:49],v195 offset:16384\n"
T_"v[50:51],v195 offset:16896\n"
M_"v[100:103],v[0:3],a[96:99],0\n"
B_"v244,s[40:43],s10 offen offset:2048 lds\n"
M_"v[100:103],v[4:7],a[100:103],v[100:103]\n"
T_"v[52:53],v195 offset:20480\n"
T_"v[54:55],v195 offset:20992\n"
C_"v139,v154,v155\n"
M_"v[100:103],v[8:11],a[104:107],v[100:103]\n"
T_"v[56:57],v195 offset:24576\n"
C_"v168,v168,v169\n"
C_"v169,v170,v171\n"
M_"v[100:103],v[12:15],a[108:111],v[100:103]\n"
W_"lgkmcnt(13)\n"
B_"v244,s[40:43],s10 offen offset:3072 lds\n"
M_"v[104:107],v[16:19],a[80:83],0\n"
A_"s10,s15,8192\n"
T_"v[58:59],v195 offset:25088\n"
C_"v170,v184,v185\n"
M_"v[104:107],v[20:23],a[84:87],v[104:107]\n"
T_"v[60:61],v195 offset:28672\n"
C_"v171,v186,v187\n"
"v_accvgpr_write_b32 a168,v136\n"
M_"v[104:107],v[24:27],a[88:91],v[104:107]\n"
"v_accvgpr_write_b32 a169,v137\n"
"v_accvgpr_write_b32 a170,v138\n"
"v_accvgpr_write_b32 a171,v139\n"
M_"v[104:107],v[28:31],a[92:95],v[104:107]\n"
B_"a[32:35],v244,s[52:55],s10 offen offset:0 sc1\n"
T_"v[62:63],v195 offset:29184\n"
M_"v[108:111],v[16:19],a[96:99],0\n"
"v_xor_b32 v192,0x20000,v192\n"
"v_xor_b32 v193,0x20000,v193\n"
M_"v[108:111],v[20:23],a[100:103],v[108:111]\n"
"s_mov_b32 s46,0x80000\n"
"v_xor_b32 v194,0x20000,v194\n"
"v_xor_b32 v195,0x20000,v195\n"
T_"v[0:1],v192 offset:24576\n"
M_"v[108:111],v[24:27],a[104:107],v[108:111]\n"
B_"a[36:39],v244,s[52:55],s10 offen offset:1024 sc1\n"
T_"v[2:3],v192 offset:25088\n"
M_"v[108:111],v[28:31],a[108:111],v[108:111]\n"
W_"lgkmcnt(10)\n"
T_"v[4:5],v192 offset:28672\n"
T_"v[6:7],v192 offset:29184\n"
M_"v[112:115],v[32:35],a[80:83],0\n"
T_"v[8:9],v193 offset:24576\n"
"v_accvgpr_write_b32 a172,v168\n"
"v_accvgpr_write_b32 a173,v169\n"
M_"v[112:115],v[36:39],a[84:87],v[112:115]\n"
B_"a[40:43],v244,s[52:55],s10 offen offset:2048 sc1\n"
T_"v[10:11],v193 offset:25088\n"
M_"v[112:115],v[40:43],a[88:91],v[112:115]\n"
T_"v[12:13],v193 offset:28672\n"
"v_accvgpr_write_b32 a174,v170\n"
"v_accvgpr_write_b32 a175,v171\n"
M_"v[112:115],v[44:47],a[92:95],v[112:115]\n"
T_"v[14:15],v193 offset:29184\n"
T_"v[16:17],v194 offset:24576\n"
M_"v[116:119],v[32:35],a[96:99],0\n"
B_"a[44:47],v244,s[52:55],s10 offen offset:3072 sc1\n"
T_"v[18:19],v194 offset:25088\n"
M_"v[116:119],v[36:39],a[100:103],v[116:119]\n"
T_"v[20:21],v194 offset:28672\n"
T_"v[22:23],v194 offset:29184\n"
C_"v140,v140,v141\n"
M_"v[116:119],v[40:43],a[104:107],v[116:119]\n"
T_"v[24:25],v195 offset:24576\n"
C_"v141,v142,v143\n"
C_"v142,v156,v157\n"
M_"v[116:119],v[44:47],a[108:111],v[116:119]\n"
W_"lgkmcnt(13)\n"
C_"v143,v158,v159\n"
C_"v172,v172,v173\n"
M_"v[120:123],v[48:51],a[80:83],0\n"
T_"v[26:27],v195 offset:25088\n"
C_"v173,v174,v175\n"
C_"v174,v188,v189\n"
M_"v[120:123],v[52:55],a[84:87],v[120:123]\n"
T_"v[28:29],v195 offset:28672\n"
C_"v175,v190,v191\n"
"v_accvgpr_write_b32 a184,v140\n"
M_"v[120:123],v[56:59],a[88:91],v[120:123]\n"
"v_accvgpr_write_b32 a185,v141\n"
"v_accvgpr_write_b32 a186,v142\n"
"v_accvgpr_write_b32 a187,v143\n"
M_"v[120:123],v[60:63],a[92:95],v[120:123]\n"
T_"v[30:31],v195 offset:29184\n"
"v_xor_b32 v192,0x30000,v192\n"
M_"v[124:127],v[48:51],a[96:99],0\n"
"v_xor_b32 v193,0x30000,v193\n"
"v_xor_b32 v194,0x30000,v194\n"
"v_xor_b32 v195,0x30000,v195\n"
M_"v[124:127],v[52:55],a[100:103],v[124:127]\n"
T_"v[32:33],v192 offset:49152\n"
T_"v[34:35],v192 offset:49664\n"
"v_accvgpr_write_b32 a188,v172\n"
M_"v[124:127],v[56:59],a[104:107],v[124:127]\n"
"s_mov_b64 s[48:49],s[18:19]\n"
T_"v[36:37],v192 offset:53248\n"
"v_accvgpr_write_b32 a189,v173\n"
M_"v[124:127],v[60:63],a[108:111],v[124:127]\n"
W_"lgkmcnt(11)\n"
T_"v[38:39],v192 offset:53760\n"
"v_accvgpr_write_b32 a190,v174\n"
W_"vmcnt(27)\n"
M_"v[96:99],v[0:3],a[64:67],v[96:99]\n"
"s_mov_b32 s50,0x400000\n"
T_"v[40:41],v192 offset:57344\n"
"v_accvgpr_write_b32 a191,v175\n"
"s_mov_b32 s51,0x20000\n"
M_"v[96:99],v[4:7],a[68:71],v[96:99]\n"
W_"vmcnt(25)\n"
T_"v[42:43],v192 offset:57856\n"
A_"s10,s56,32768\n"
"s_and_b32 s13,s30,6\n"
M_"v[100:103],v[0:3],a[72:75],v[100:103]\n"
"s_lshl1_add_u32 s13,s13,s31\n"
T_"v[44:45],v192 offset:61440\n"
A_"m0,s56,0\n"
M_"v[100:103],v[4:7],a[76:79],v[100:103]\n"
B_"v244,s[40:43],s10 offen offset:0 lds\n"
T_"v[46:47],v192 offset:61952\n"
M_"v[104:107],v[8:11],a[64:67],v[104:107]\n"
"s_and_b32 s15,s34,1\n"
T_"v[48:49],v193 offset:49152\n"
T_"v[50:51],v193 offset:49664\n"
T_"v[52:53],v193 offset:53248\n"
M_"v[104:107],v[12:15],a[68:71],v[104:107]\n"
"s_lshl1_add_u32 s13,s13,s15\n"
T_"v[54:55],v193 offset:53760\n"
"s_lshl_b32 s14,s13,17\n"
"s_lshl_b32 s21,s13,12\n"
M_"v[108:111],v[8:11],a[72:75],v[108:111]\n"
"s_bfe_u32 s13,s30,0x20003\n"
T_"v[56:57],v193 offset:57344\n"
"s_lshl2_add_u32 s18,s13,s31\n"
M_"v[108:111],v[12:15],a[76:79],v[108:111]\n"
W_"lgkmcnt(13)\n"
B_"v244,s[40:43],s10 offen offset:1024 lds\n"
M_"v[112:115],v[16:19],a[64:67],v[112:115]\n"
"s_lshr_b32 s13,s18,1\n"
T_"v[58:59],v193 offset:57856\n"
"s_lshl_b32 s13,s13,7\n"
A_"s14,s14,s13\n"
M_"v[112:115],v[20:23],a[68:71],v[112:115]\n"
"s_andn2_b32 s13,1,s34\n"
T_"v[60:61],v193 offset:61440\n"
"s_lshl4_add_u32 s13,s13,s31\n"
"s_lshl_b32 s16,s13,12\n"
M_"v[116:119],v[16:19],a[72:75],v[116:119]\n"
"s_lshl_b32 s13,s30,7\n"
A_"s17,s13,0x4f10000\n"
A_"s12,s17,4\n"
M_"v[116:119],v[20:23],a[76:79],v[116:119]\n"
B_"v244,s[40:43],s10 offen offset:2048 lds\n"
T_"v[62:63],v193 offset:61952\n"
M_"v[120:123],v[24:27],a[64:67],v[120:123]\n"
"s_andn2_b32 s20,s17,0xfff\n"
T_"v[0:1],v194 offset:49152\n"
T_"v[2:3],v194 offset:49664\n"
T_"v[4:5],v194 offset:53248\n"
M_"v[120:123],v[28:31],a[68:71],v[120:123]\n"
"s_cmp_eq_u32 s31,0\n"
"s_cselect_b32 s17,s17,0x7f000000\n"
T_"v[6:7],v194 offset:53760\n"
"s_mov_b32 s61,s17\n"
M_"v[124:127],v[24:27],a[72:75],v[124:127]\n"
"s_and_b32 s13,s30,1\n"
T_"v[8:9],v194 offset:57344\n"
"s_lshl1_add_u32 s13,s34,s13\n"
M_"v[124:127],v[28:31],a[76:79],v[124:127]\n"
W_"lgkmcnt(13)\n"
"s_bfe_u32 s15,s30,0x20001\n"
W_"vmcnt(23)\n"
M_"v[96:99],v[32:35],a[112:115],v[96:99]\n"
B_"v244,s[40:43],s10 offen offset:3072 lds\n"
T_"v[10:11],v194 offset:57856\n"
M_"v[96:99],v[36:39],a[116:119],v[96:99]\n"
A_"s10,s56,0\n"
T_"v[12:13],v194 offset:61440\n"
"s_lshl2_add_u32 s13,s13,s15\n"
M_"v[96:99],v[40:43],a[120:123],v[96:99]\n"
"s_lshl4_add_u32 s18,s13,s18\n"
A_"m0,s56,49152\n"
"s_lshl_b32 s18,s18,10\n"
A_"s18,s18,0x200000\n"
M_"v[96:99],v[44:47],a[124:127],v[96:99]\n"
W_"vmcnt(20)\n"
T_"v[14:15],v194 offset:61952\n"
T_"v[16:17],v195 offset:49152\n"
T_"v[18:19],v195 offset:49664\n"
M_"v[100:103],v[32:35],a[0:3],v[100:103]\n"
B_"v244,s[44:47],s10 offen offset:0 lds\n"
T_"v[20:21],v195 offset:53248\n"
M_"v[100:103],v[36:39],a[4:7],v[100:103]\n"
"s_lshl_b32 s19,s34,18\n"
T_"v[22:23],v195 offset:53760\n"
"s_sub_u32 s19,0x1e0000,s19\n"
M_"v[100:103],v[40:43],a[8:11],v[100:103]\n"
"s_lshl_b32 s13,s13,17\n"
T_"v[24:25],v195 offset:57344\n"
A_"s21,s21,s13\n"
A_"s21,s21,0x3a00000\n"
M_"v[100:103],v[44:47],a[12:15],v[100:103]\n"
W_"lgkmcnt(13)\n"
"s_xor_b32 s22,s21,0x40000\n"
"s_xor_b32 s23,s21,0x60000\n"
"s_xor_b32 s21,s21,0x20000\n"
M_"v[104:107],v[48:51],a[112:115],v[104:107]\n"
T_"v[26:27],v195 offset:57856\n"
W_"vmcnt(9)\n"
"s_barrier\n"
M_"v[104:107],v[52:55],a[116:119],v[104:107]\n"
B_"v244,s[44:47],s10 offen offset:1024 lds\n"
M_"v[104:107],v[56:59],a[120:123],v[104:107]\n"
"s_mov_b32 s24,1\n"
T_"v[28:29],v195 offset:61440\n"
"v_lshrrev_b32 v197,6,v244\n"
"v_lshlrev_b32 v255,7,v197\n"
M_"v[104:107],v[60:63],a[124:127],v[104:107]\n"
"s_lshl_b32 s13,s31,1\n"
T_"v[30:31],v195 offset:61952\n"
T_"v[32:33],v192 offset:16384\n"
T_"v[34:35],v192 offset:16896\n"
M_"v[108:111],v[48:51],a[0:3],v[108:111]\n"
"v_lshrrev_b32 v197,7,v244\n"
"v_xor_b32 v198,s13,v197\n"
T_"v[36:37],v192 offset:20480\n"
M_"v[108:111],v[52:55],a[4:7],v[108:111]\n"
B_"v244,s[44:47],s10 offen offset:2048 lds\n"
M_"v[108:111],v[56:59],a[8:11],v[108:111]\n"
"v_and_b32 v198,3,v198\n"
"v_lshl_add_u32 v255,v198,5,v255\n"
T_"v[38:39],v192 offset:20992\n"
M_"v[108:111],v[60:63],a[12:15],v[108:111]\n"
W_"lgkmcnt(12)\n"
"v_bfe_u32 v198,v244,4,2\n"
D_"a[192:195],v245 offset:0\n"
"v_lshl_add_u32 v255,v198,3,v255\n"
M_"v[112:115],v[0:3],a[112:115],v[112:115]\n"
A_"s13,s31,32\n"
T_"v[40:41],v192 offset:24576\n"
T_"v[42:43],v192 offset:25088\n"
M_"v[112:115],v[4:7],a[116:119],v[112:115]\n"
B_"v244,s[44:47],s10 offen offset:3072 lds\n"
T_"v[44:45],v192 offset:28672\n"
M_"v[112:115],v[8:11],a[120:123],v[112:115]\n"
"s_lshl_b32 s13,s13,12\n"
D_"a[196:199],v245 offset:1024\n"
"v_add_u32 v255,s13,v255\n"
"v_lshlrev_b32 v196,10,v197\n"
M_"v[112:115],v[12:15],a[124:127],v[112:115]\n"
A_"s10,s56,49152\n"
T_"v[46:47],v192 offset:29184\n"
T_"v[48:49],v193 offset:16384\n"
"v_and_b32 v197,6,v197\n"
M_"v[116:119],v[0:3],a[0:3],v[116:119]\n"
A_"m0,s56,32768\n"
"v_bfe_u32 v198,v244,4,3\n"
"v_xor_b32 v197,v197,v198\n"
M_"v[116:119],v[4:7],a[4:7],v[116:119]\n"
B_"v244,s[40:43],s10 offen offset:0 lds\n"
"v_lshl_add_u32 v196,v197,4,v196\n"
M_"v[116:119],v[8:11],a[8:11],v[116:119]\n"
"v_add_u32 v197,7168,v196\n"
"v_add_u32 v198,14336,v196\n"
"v_add_u32 v199,21504,v196\n"
M_"v[116:119],v[12:15],a[12:15],v[116:119]\n"
W_"lgkmcnt(11)\n"
T_"v[50:51],v193 offset:16896\n"
D_"a[200:203],v245 offset:2048\n"
T_"v[52:53],v193 offset:20480\n"
M_"v[120:123],v[16:19],a[112:115],v[120:123]\n"
T_"v[54:55],v193 offset:20992\n"
D_"a[204:207],v245 offset:3072\n"
M_"v[120:123],v[20:23],a[116:119],v[120:123]\n"
B_"v244,s[40:43],s10 offen offset:1024 lds\n"
T_"v[56:57],v193 offset:24576\n"
M_"v[120:123],v[24:27],a[120:123],v[120:123]\n"
T_"v[58:59],v193 offset:25088\n"
T_"v[60:61],v193 offset:28672\n"
D_"a[208:211],v245 offset:4096\n"
M_"v[120:123],v[28:31],a[124:127],v[120:123]\n"
B_"v244,s[40:43],s10 offen offset:2048 lds\n"
T_"v[62:63],v193 offset:29184\n"
M_"v[124:127],v[16:19],a[0:3],v[124:127]\n"
T_"v[0:1],v194 offset:16384\n"
T_"v[2:3],v194 offset:16896\n"
M_"v[124:127],v[20:23],a[4:7],v[124:127]\n"
B_"v244,s[40:43],s10 offen offset:3072 lds\n"
D_"a[212:215],v245 offset:5120\n"
M_"v[124:127],v[24:27],a[8:11],v[124:127]\n"
A_"s40,s40,0x10000\n"
"s_addc_u32 s41,s41,0\n"
T_"v[4:5],v194 offset:20480\n"
T_"v[6:7],v194 offset:20992\n"
M_"v[124:127],v[28:31],a[12:15],v[124:127]\n"
W_"lgkmcnt(15)\n"
"s_mov_b32 s42,0x70000\n"
D_"a[216:219],v245 offset:6144\n"
M_"v[96:99],v[32:35],a[16:19],v[96:99]\n"
A_"s44,s44,0x4000\n"
"s_addc_u32 s45,s45,0\n"
T_"v[8:9],v194 offset:24576\n"
M_"v[96:99],v[36:39],a[20:23],v[96:99]\n"
"s_mov_b32 s46,0x7c000\n"
D_"a[220:223],v245 offset:7168\n"
T_"v[10:11],v194 offset:25088\n"
T_"v[12:13],v194 offset:28672\n"
M_"v[96:99],v[40:43],a[24:27],v[96:99]\n"
"s_xor_b32 s15,s14,0\n"
D_"a[224:227],v245 offset:8192\n"
D_"a[228:231],v245 offset:9216\n"
"s_and_b32 s13,s16,0xffff\n"
M_"v[96:99],v[44:47],a[28:31],v[96:99]\n"
W_"vmcnt(12)\n"
T_"v[14:15],v194 offset:29184\n"
T_"v[16:17],v195 offset:16384\n"
M_"v[100:103],v[32:35],a[32:35],v[100:103]\n"
A_"m0,s13,131072\n"
T_"v[18:19],v195 offset:16896\n"
T_"v[20:21],v195 offset:20480\n"
T_"v[22:23],v195 offset:20992\n"
M_"v[100:103],v[36:39],a[36:39],v[100:103]\n"
B_"v196,s[48:51],s15 offen offset:0  lds\n"
D_"a[232:235],v245 offset:10240\n"
M_"v[100:103],v[40:43],a[40:43],v[100:103]\n"
T_"v[24:25],v195 offset:24576\n"
D_"a[236:239],v245 offset:11264\n"
M_"v[100:103],v[44:47],a[44:47],v[100:103]\n"
W_"lgkmcnt(15)\n"
M_"v[104:107],v[48:51],a[16:19],v[104:107]\n"
T_"v[26:27],v195 offset:25088\n"
D_"a[240:243],v245 offset:12288\n"
M_"v[104:107],v[52:55],a[20:23],v[104:107]\n"
B_"v197,s[48:51],s15 offen offset:1024  lds\n"
T_"v[28:29],v195 offset:28672\n"
M_"v[104:107],v[56:59],a[24:27],v[104:107]\n"
D_"a[244:247],v245 offset:13312\n"
M_"v[104:107],v[60:63],a[28:31],v[104:107]\n"
T_"v[30:31],v195 offset:29184\n"
M_"v[108:111],v[48:51],a[32:35],v[108:111]\n"
M_"v[108:111],v[52:55],a[36:39],v[108:111]\n"
B_"v198,s[48:51],s15 offen offset:2048  lds\n"
D_"a[248:251],v245 offset:14336\n"
M_"v[108:111],v[56:59],a[40:43],v[108:111]\n"
C_"v96,v96,v97\n"
C_"v97,v98,v99\n"
M_"v[108:111],v[60:63],a[44:47],v[108:111]\n"
W_"lgkmcnt(13)\n"
C_"v98,v104,v105\n"
D_"a[252:255],v245 offset:15360\n"
M_"v[112:115],v[0:3],a[16:19],v[112:115]\n"
C_"v99,v106,v107\n"
M_"v[112:115],v[4:7],a[20:23],v[112:115]\n"
B_"v199,s[48:51],s15 offen offset:3072  lds\n"
"s_xor_b32 s15,s14,128\n"
M_"v[112:115],v[8:11],a[24:27],v[112:115]\n"
"s_and_b32 s13,s16,0xffff\n"
A_"m0,s13,147456\n"
M_"v[112:115],v[12:15],a[28:31],v[112:115]\n"
M_"v[116:119],v[0:3],a[32:35],v[116:119]\n"
"v_mov_b32 v249,v196\n"
M_"v[116:119],v[4:7],a[36:39],v[116:119]\n"
B_"v196,s[48:51],s15 offen offset:0  lds\n"
M_"v[116:119],v[8:11],a[40:43],v[116:119]\n"
M_"v[116:119],v[12:15],a[44:47],v[116:119]\n"
W_"lgkmcnt(2)\n"
S_"v[96:99],v244,s[52:55],s36 offen offset:2048 sc1\n"
M_"v[120:123],v[16:19],a[16:19],v[120:123]\n"
"v_mov_b32 v250,v197\n"
M_"v[120:123],v[20:23],a[20:23],v[120:123]\n"
M_"v[120:123],v[24:27],a[24:27],v[120:123]\n"
B_"v197,s[48:51],s15 offen offset:1024  lds\n"
M_"v[120:123],v[28:31],a[28:31],v[120:123]\n"
"v_mov_b32 v251,v198\n"
M_"v[124:127],v[16:19],a[32:35],v[124:127]\n"
B_"v198,s[48:51],s15 offen offset:2048  lds\n"
M_"v[124:127],v[20:23],a[36:39],v[124:127]\n"
M_"v[124:127],v[24:27],a[40:43],v[124:127]\n"
"v_mov_b32 v253,v199\n"
C_"v100,v100,v101\n"
M_"v[124:127],v[28:31],a[44:47],v[124:127]\n"
B_"v199,s[48:51],s15 offen offset:3072  lds\n"
C_"v101,v102,v103\n"
W_"lgkmcnt(0)\n"
M_"v[32:35],a[192:195],a[160:163],0\n"
C_"v102,v108,v109\n"
C_"v103,v110,v111\n"
M_"v[32:35],a[196:199],a[164:167],v[32:35]\n"
M_"v[32:35],a[200:203],a[168:171],v[32:35]\n"
M_"v[32:35],a[204:207],a[172:175],v[32:35]\n"
S_"v[100:103],v244,s[52:55],s37 offen offset:2048 sc1\n"
M_"v[48:51],a[192:195],a[176:179],0\n"
M_"v[48:51],a[196:199],a[180:183],v[48:51]\n"
M_"v[48:51],a[200:203],a[184:187],v[48:51]\n"
M_"v[48:51],a[204:207],a[188:191],v[48:51]\n"
"s_nop 2\n"
"v_max3_f32 v200,v32,v33,v34\n"
"v_max_f32 v200,v200,v35\n"
M_"v[16:19],a[192:195],a[144:147],0\n"
"v_mov_b32 v201,v200\n"
"s_nop 0\n"
M_"v[16:19],a[196:199],a[148:151],v[16:19]\n"
"v_permlane32_swap_b32 v200,v201\n"
"v_max_f32 v200,v200,v201\n"
M_"v[16:19],a[200:203],a[152:155],v[16:19]\n"
"v_mov_b32 v201,v200\n"
"s_nop 0\n"
M_"v[16:19],a[204:207],a[156:159],v[16:19]\n"
"v_permlane16_swap_b32 v200,v201\n"
"v_min_f32_e64 v202,-v200,-v201\n"
M_"v[0:3],a[192:195],a[128:131],v[224:227]\n"
"v_min_f32_e64 v203,-v200,-v201\n"
"v_mov_b32 v200,v202\n"
M_"v[0:3],a[196:199],a[132:135],v[0:3]\n"
"v_mov_b32 v201,v202\n"
"v_max3_f32 v204,v48,v49,v50\n"
M_"v[0:3],a[200:203],a[136:139],v[0:3]\n"
"v_max_f32 v204,v204,v51\n"
"v_mov_b32 v205,v204\n"
M_"v[0:3],a[204:207],a[140:143],v[0:3]\n"
"s_nop 0\n"
"v_permlane32_swap_b32 v204,v205\n"
"v_max_f32 v204,v204,v205\n"
M_"v[4:7],a[208:211],a[128:131],v[228:231]\n"
"v_mov_b32 v205,v204\n"
D_"a[192:195],v245 offset:32768\n"
M_"v[4:7],a[212:215],a[132:135],v[4:7]\n"
"s_nop 0\n"
"v_permlane16_swap_b32 v204,v205\n"
"v_min_f32_e64 v206,-v204,-v205\n"
M_"v[4:7],a[216:219],a[136:139],v[4:7]\n"
"v_min_f32_e64 v207,-v204,-v205\n"
"v_mov_b32 v204,v206\n"
M_"v[4:7],a[220:223],a[140:143],v[4:7]\n"
"v_mov_b32 v205,v206\n"
"v_max3_f32 v196,v16,v17,v18\n"
M_"v[8:11],a[224:227],a[128:131],v[232:235]\n"
"v_max_f32 v196,v196,v19\n"
"v_mov_b32 v197,v196\n"
M_"v[8:11],a[228:231],a[132:135],v[8:11]\n"
"s_nop 0\n"
D_"a[196:199],v245 offset:33792\n"
D_"a[200:203],v245 offset:34816\n"
M_"v[8:11],a[232:235],a[136:139],v[8:11]\n"
D_"a[204:207],v245 offset:35840\n"
"v_permlane32_swap_b32 v196,v197\n"
M_"v[8:11],a[236:239],a[140:143],v[8:11]\n"
C_"v112,v112,v113\n"
C_"v113,v114,v115\n"
M_"v[36:39],a[208:211],a[160:163],v[200:203]\n"
"v_max_f32 v196,v196,v197\n"
"v_mov_b32 v197,v196\n"
M_"v[36:39],a[212:215],a[164:167],v[36:39]\n"
"s_nop 0\n"
C_"v114,v120,v121\n"
"v_permlane16_swap_b32 v196,v197\n"
M_"v[36:39],a[216:219],a[168:171],v[36:39]\n"
C_"v115,v122,v123\n"
"v_min_f32_e64 v198,-v196,-v197\n"
M_"v[36:39],a[220:223],a[172:175],v[36:39]\n"
S_"v[112:115],v244,s[52:55],s36 offen offset:3072 sc1\n"
M_"v[52:55],a[208:211],a[176:179],v[204:207]\n"
"v_min_f32_e64 v199,-v196,-v197\n"
"v_mov_b32 v196,v198\n"
M_"v[52:55],a[212:215],a[180:183],v[52:55]\n"
"v_mov_b32 v197,v198\n"
"v_max3_f32 v192,v0,v1,v2\n"
M_"v[52:55],a[216:219],a[184:187],v[52:55]\n"
"v_max_f32 v192,v192,v3\n"
"v_mov_b32 v193,v192\n"
M_"v[52:55],a[220:223],a[188:191],v[52:55]\n"
"s_nop 0\n"
"v_permlane32_swap_b32 v192,v193\n"
"v_max_f32 v192,v192,v193\n"
M_"v[40:43],a[224:227],a[160:163],v[200:203]\n"
"v_mov_b32 v193,v192\n"
"v_add_f32 v32,v32,v202\n"
M_"v[40:43],a[228:231],a[164:167],v[40:43]\n"
"s_nop 0\n"
"v_permlane16_swap_b32 v192,v193\n"
"v_min_f32_e64 v194,-v192,-v193\n"
M_"v[40:43],a[232:235],a[168:171],v[40:43]\n"
"v_min_f32_e64 v195,-v192,-v193\n"
"v_mov_b32 v192,v194\n"
M_"v[40:43],a[236:239],a[172:175],v[40:43]\n"
"v_mov_b32 v193,v194\n"
"v_add_f32 v33,v33,v202\n"
M_"v[20:23],a[208:211],a[144:147],v[196:199]\n"
"v_add_f32 v34,v34,v202\n"
"v_add_f32 v35,v35,v202\n"
M_"v[20:23],a[212:215],a[148:151],v[20:23]\n"
"v_add_f32 v48,v48,v206\n"
"v_add_f32 v49,v49,v206\n"
M_"v[20:23],a[216:219],a[152:155],v[20:23]\n"
C_"v116,v116,v117\n"
C_"v117,v118,v119\n"
M_"v[20:23],a[220:223],a[156:159],v[20:23]\n"
C_"v118,v124,v125\n"
"v_add_f32 v50,v50,v206\n"
M_"v[56:59],a[224:227],a[176:179],v[204:207]\n"
C_"v119,v126,v127\n"
"v_add_f32 v51,v51,v206\n"
M_"v[56:59],a[228:231],a[180:183],v[56:59]\n"
S_"v[116:119],v244,s[52:55],s37 offen offset:3072 sc1\n"
M_"v[56:59],a[232:235],a[184:187],v[56:59]\n"
"v_add_f32 v16,v16,v198\n"
"v_add_f32 v17,v17,v198\n"
M_"v[56:59],a[236:239],a[188:191],v[56:59]\n"
"v_add_f32 v18,v18,v198\n"
"v_add_f32 v19,v19,v198\n"
M_"v[24:27],a[224:227],a[144:147],v[196:199]\n"
D_"a[208:211],v245 offset:36864\n"
D_"a[212:215],v245 offset:37888\n"
M_"v[24:27],a[228:231],a[148:151],v[24:27]\n"
D_"a[216:219],v245 offset:38912\n"
D_"a[220:223],v245 offset:39936\n"
M_"v[24:27],a[232:235],a[152:155],v[24:27]\n"
"v_add_f32 v0,v0,v194\n"
"v_add_f32 v1,v1,v194\n"
M_"v[24:27],a[236:239],a[156:159],v[24:27]\n"
"v_add_f32 v2,v2,v194\n"
"v_add_f32 v3,v3,v194\n"
M_"v[12:15],a[240:243],a[128:131],v[236:239]\n"
"v_add_f32 v4,v4,v194\n"
"v_add_f32 v5,v5,v194\n"
M_"v[12:15],a[244:247],a[132:135],v[12:15]\n"
"v_add_f32 v6,v6,v194\n"
"v_add_f32 v7,v7,v194\n"
M_"v[12:15],a[248:251],a[136:139],v[12:15]\n"
D_"a[224:227],v245 offset:40960\n"
D_"a[228:231],v245 offset:41984\n"
M_"v[12:15],a[252:255],a[140:143],v[12:15]\n"
D_"a[232:235],v245 offset:43008\n"
D_"a[236:239],v245 offset:44032\n"
M_"v[28:31],a[240:243],a[144:147],v[196:199]\n"
"v_add_f32 v8,v8,v194\n"
"v_add_f32 v9,v9,v194\n"
M_"v[28:31],a[244:247],a[148:151],v[28:31]\n"
"v_add_f32 v10,v10,v194\n"
"v_add_f32 v11,v11,v194\n"
M_"v[28:31],a[248:251],a[152:155],v[28:31]\n"
E_"v36,v36\n"
M_"v[28:31],a[252:255],a[156:159],v[28:31]\n"
E_"v37,v37\n"
M_"v[44:47],a[240:243],a[160:163],v[200:203]\n"
E_"v38,v38\n"
M_"v[44:47],a[244:247],a[164:167],v[44:47]\n"
E_"v39,v39\n"
M_"v[44:47],a[248:251],a[168:171],v[44:47]\n"
"v_add_f32 v12,v12,v194\n"
"v_add_f32 v13,v13,v194\n"
M_"v[44:47],a[252:255],a[172:175],v[44:47]\n"
"v_add_f32 v14,v14,v194\n"
"v_add_f32 v15,v15,v194\n"
M_"v[60:63],a[240:243],a[176:179],v[204:207]\n"
C_"v146,v36,v37\n"
C_"v147,v38,v39\n"
M_"v[60:63],a[244:247],a[180:183],v[60:63]\n"
M_"v[60:63],a[248:251],a[184:187],v[60:63]\n"
M_"v[60:63],a[252:255],a[188:191],v[60:63]\n"
D_"a[240:243],v245 offset:45056\n"
D_"a[244:247],v245 offset:46080\n"
D_"a[248:251],v245 offset:47104\n"
D_"a[252:255],v245 offset:48128\n"
W_"vmcnt(20) lgkmcnt(8)\n"
"s_branch L_m_att\n"
".p2align 12\n"
"L_m_t:\n"
"L_pt0_3:\n"
"s_cbranch_execz L_pr0\n"
M_"v[116:119],v[24:27],a[88:91],v[116:119]\n"
"v_xor_b32 v192,0x10000,v192\n"
M_"v[116:119],v[28:31],a[92:95],v[116:119]\n"
"v_xor_b32 v193,0x10000,v193\n"
M_"v[120:123],v[16:19],a[96:99],v[120:123]\n"
"v_xor_b32 v194,0x10000,v194\n"
M_"v[120:123],v[20:23],a[100:103],v[120:123]\n"
"v_xor_b32 v195,0x10000,v195\n"
M_"v[120:123],v[24:27],a[104:107],v[120:123]\n"
"s_mov_b64 s[48:49],s[24:25]\n"
T_"v[0:1],v192 offset:49152\n"
M_"v[120:123],v[28:31],a[108:111],v[120:123]\n"
T_"v[2:3],v192 offset:49664\n"
M_"v[124:127],v[16:19],a[112:115],v[124:127]\n"
"s_mov_b32 s50,0x1c0000\n"
M_"v[124:127],v[20:23],a[116:119],v[124:127]\n"
T_"v[4:5],v192 offset:53248\n"
M_"v[124:127],v[24:27],a[120:123],v[124:127]\n"
A_"s75,s68,0\n"
T_"v[6:7],v192 offset:53760\n"
M_"v[124:127],v[28:31],a[124:127],v[124:127]\n"
T_"v[8:9],v192 offset:57344\n"
M_"v[112:115],v[32:35],a[0:3],v[112:115]\n"
A_"m0,s56,0\n"
T_"v[10:11],v192 offset:57856\n"
M_"v[112:115],v[36:39],a[4:7],v[112:115]\n"
T_"v[12:13],v192 offset:61440\n"
M_"v[112:115],v[40:43],a[8:11],v[112:115]\n"
B_"v200,s[48:51],s75 offen offset:0  lds\n"
M_"v[112:115],v[44:47],a[12:15],v[112:115]\n"
T_"v[14:15],v192 offset:61952\n"
T_"v[16:17],v193 offset:49152\n"
M_"v[116:119],v[32:35],a[16:19],v[116:119]\n"
T_"v[18:19],v193 offset:49664\n"
T_"v[20:21],v193 offset:53248\n"
M_"v[116:119],v[36:39],a[20:23],v[116:119]\n"
T_"v[22:23],v193 offset:53760\n"
M_"v[116:119],v[40:43],a[24:27],v[116:119]\n"
B_"v201,s[48:51],s75 offen offset:1024  lds\n"
M_"v[116:119],v[44:47],a[28:31],v[116:119]\n"
T_"v[24:25],v193 offset:57344\n"
M_"v[120:123],v[32:35],a[32:35],v[120:123]\n"
T_"v[26:27],v193 offset:57856\n"
M_"v[120:123],v[36:39],a[36:39],v[120:123]\n"
M_"v[120:123],v[40:43],a[40:43],v[120:123]\n"
B_"v202,s[48:51],s75 offen offset:2048  lds\n"
M_"v[120:123],v[44:47],a[44:47],v[120:123]\n"
T_"v[28:29],v193 offset:61440\n"
M_"v[124:127],v[32:35],a[48:51],v[124:127]\n"
T_"v[30:31],v193 offset:61952\n"
M_"v[124:127],v[36:39],a[52:55],v[124:127]\n"
M_"v[124:127],v[40:43],a[56:59],v[124:127]\n"
B_"v203,s[48:51],s75 offen offset:3072  lds\n"
M_"v[124:127],v[44:47],a[60:63],v[124:127]\n"
M_"v[112:115],v[48:51],a[128:131],v[112:115]\n"
T_"v[32:33],v194 offset:49152\n"
M_"v[112:115],v[52:55],a[132:135],v[112:115]\n"
T_"v[34:35],v194 offset:49664\n"
M_"v[112:115],v[56:59],a[136:139],v[112:115]\n"
"s_cmp_lg_u32 s11,0\n"
"s_cbranch_scc1 L_m_ni304\n"
"buffer_inv sc1\n"
"L_m_ni304:\n"
M_"v[112:115],v[60:63],a[140:143],v[112:115]\n"
A_"s75,s68,128\n"
T_"v[36:37],v194 offset:53248\n"
M_"v[116:119],v[48:51],a[144:147],v[116:119]\n"
A_"m0,s56,147456\n"
M_"v[116:119],v[52:55],a[148:151],v[116:119]\n"
T_"v[38:39],v194 offset:53760\n"
M_"v[116:119],v[56:59],a[152:155],v[116:119]\n"
B_"v200,s[48:51],s75 offen offset:0  lds\n"
M_"v[116:119],v[60:63],a[156:159],v[116:119]\n"
T_"v[40:41],v194 offset:57344\n"
C_"v208,v96,v97\n"
M_"v[120:123],v[48:51],a[160:163],v[120:123]\n"
T_"v[42:43],v194 offset:57856\n"
C_"v209,v98,v99\n"
M_"v[120:123],v[52:55],a[164:167],v[120:123]\n"
M_"v[120:123],v[56:59],a[168:171],v[120:123]\n"
B_"v201,s[48:51],s75 offen offset:1024  lds\n"
M_"v[120:123],v[60:63],a[172:175],v[120:123]\n"
T_"v[44:45],v194 offset:61440\n"
C_"v210,v112,v113\n"
M_"v[124:127],v[48:51],a[176:179],v[124:127]\n"
T_"v[46:47],v194 offset:61952\n"
M_"v[124:127],v[52:55],a[180:183],v[124:127]\n"
C_"v211,v114,v115\n"
C_"v212,v100,v101\n"
M_"v[124:127],v[56:59],a[184:187],v[124:127]\n"
B_"v202,s[48:51],s75 offen offset:2048  lds\n"
M_"v[124:127],v[60:63],a[188:191],v[124:127]\n"
W_"lgkmcnt(15)\n"
C_"v213,v102,v103\n"
C_"v214,v116,v117\n"
M_"v[128:131],v[0:3],a[128:131],0\n"
T_"v[48:49],v195 offset:49152\n"
M_"v[128:131],v[4:7],a[132:135],v[128:131]\n"
T_"v[50:51],v195 offset:49664\n"
C_"v215,v118,v119\n"
M_"v[128:131],v[8:11],a[136:139],v[128:131]\n"
B_"v203,s[48:51],s75 offen offset:3072  lds\n"
M_"v[128:131],v[12:15],a[140:143],v[128:131]\n"
T_"v[52:53],v195 offset:53248\n"
C_"v216,v104,v105\n"
M_"v[132:135],v[0:3],a[144:147],0\n"
T_"v[54:55],v195 offset:53760\n"
C_"v217,v106,v107\n"
M_"v[132:135],v[4:7],a[148:151],v[132:135]\n"
C_"v218,v120,v121\n"
C_"v219,v122,v123\n"
M_"v[132:135],v[8:11],a[152:155],v[132:135]\n"
A_"s75,s68,131072\n"
T_"v[56:57],v195 offset:57344\n"
C_"v220,v108,v109\n"
M_"v[132:135],v[12:15],a[156:159],v[132:135]\n"
W_"lgkmcnt(13)\n"
C_"v221,v110,v111\n"
C_"v222,v124,v125\n"
M_"v[144:147],v[16:19],a[128:131],0\n"
A_"m0,s56,16384\n"
T_"v[58:59],v195 offset:57856\n"
M_"v[144:147],v[20:23],a[132:135],v[144:147]\n"
T_"v[60:61],v195 offset:61440\n"
C_"v223,v126,v127\n"
M_"v[144:147],v[24:27],a[136:139],v[144:147]\n"
B_"v200,s[48:51],s75 offen offset:0  lds\n"
M_"v[144:147],v[28:31],a[140:143],v[144:147]\n"
T_"v[62:63],v195 offset:61952\n"
M_"v[148:151],v[16:19],a[144:147],0\n"
M_"v[148:151],v[20:23],a[148:151],v[148:151]\n"
M_"v[148:151],v[24:27],a[152:155],v[148:151]\n"
M_"v[148:151],v[28:31],a[156:159],v[148:151]\n"
W_"lgkmcnt(8)\n"
M_"v[160:163],v[32:35],a[128:131],0\n"
B_"v201,s[48:51],s75 offen offset:1024  lds\n"
M_"v[160:163],v[36:39],a[132:135],v[160:163]\n"
M_"v[160:163],v[40:43],a[136:139],v[160:163]\n"
M_"v[160:163],v[44:47],a[140:143],v[160:163]\n"
M_"v[164:167],v[32:35],a[144:147],0\n"
B_"v202,s[48:51],s75 offen offset:2048  lds\n"
M_"v[164:167],v[36:39],a[148:151],v[164:167]\n"
M_"v[164:167],v[40:43],a[152:155],v[164:167]\n"
M_"v[164:167],v[44:47],a[156:159],v[164:167]\n"
W_"lgkmcnt(0)\n"
M_"v[176:179],v[48:51],a[128:131],0\n"
B_"v203,s[48:51],s75 offen offset:3072  lds\n"
M_"v[176:179],v[52:55],a[132:135],v[176:179]\n"
M_"v[176:179],v[56:59],a[136:139],v[176:179]\n"
M_"v[176:179],v[60:63],a[140:143],v[176:179]\n"
A_"s75,s68,262144\n"
M_"v[180:183],v[48:51],a[144:147],0\n"
M_"v[180:183],v[52:55],a[148:151],v[180:183]\n"
A_"m0,s56,32768\n"
M_"v[180:183],v[56:59],a[152:155],v[180:183]\n"
M_"v[180:183],v[60:63],a[156:159],v[180:183]\n"
B_"v200,s[48:51],s75 offen offset:0  lds\n"
M_"v[136:139],v[0:3],a[160:163],0\n"
"s_cmp_lg_u32 s11,1\n"
"s_cbranch_scc1 L_m_ni305\n"
"buffer_inv sc1\n"
"L_m_ni305:\n"
M_"v[136:139],v[4:7],a[164:167],v[136:139]\n"
M_"v[136:139],v[8:11],a[168:171],v[136:139]\n"
M_"v[136:139],v[12:15],a[172:175],v[136:139]\n"
B_"v201,s[48:51],s75 offen offset:1024  lds\n"
M_"v[140:143],v[0:3],a[176:179],0\n"
M_"v[140:143],v[4:7],a[180:183],v[140:143]\n"
M_"v[140:143],v[8:11],a[184:187],v[140:143]\n"
M_"v[140:143],v[12:15],a[188:191],v[140:143]\n"
B_"v202,s[48:51],s75 offen offset:2048  lds\n"
M_"v[152:155],v[16:19],a[160:163],0\n"
T_"v[0:1],v192 offset:16384\n"
M_"v[152:155],v[20:23],a[164:167],v[152:155]\n"
T_"v[2:3],v192 offset:16896\n"
M_"v[152:155],v[24:27],a[168:171],v[152:155]\n"
M_"v[152:155],v[28:31],a[172:175],v[152:155]\n"
B_"v203,s[48:51],s75 offen offset:3072  lds\n"
M_"v[156:159],v[16:19],a[176:179],0\n"
T_"v[4:5],v192 offset:20480\n"
T_"v[6:7],v192 offset:20992\n"
M_"v[156:159],v[20:23],a[180:183],v[156:159]\n"
M_"v[156:159],v[24:27],a[184:187],v[156:159]\n"
T_"v[8:9],v192 offset:24576\n"
M_"v[156:159],v[28:31],a[188:191],v[156:159]\n"
A_"s75,s16,393216\n"
M_"v[168:171],v[32:35],a[160:163],0\n"
T_"v[10:11],v192 offset:25088\n"
M_"v[168:171],v[36:39],a[164:167],v[168:171]\n"
A_"m0,s56,49152\n"
T_"v[12:13],v192 offset:28672\n"
M_"v[168:171],v[40:43],a[168:171],v[168:171]\n"
M_"v[168:171],v[44:47],a[172:175],v[168:171]\n"
B_"v200,s[48:51],s75 offen offset:0  lds\n"
M_"v[172:175],v[32:35],a[176:179],0\n"
T_"v[14:15],v192 offset:29184\n"
T_"v[16:17],v193 offset:16384\n"
M_"v[172:175],v[36:39],a[180:183],v[172:175]\n"
T_"v[18:19],v193 offset:16896\n"
T_"v[20:21],v193 offset:20480\n"
M_"v[172:175],v[40:43],a[184:187],v[172:175]\n"
T_"v[22:23],v193 offset:20992\n"
T_"v[24:25],v193 offset:24576\n"
M_"v[172:175],v[44:47],a[188:191],v[172:175]\n"
B_"v201,s[48:51],s75 offen offset:1024  lds\n"
M_"v[184:187],v[48:51],a[160:163],0\n"
T_"v[26:27],v193 offset:25088\n"
M_"v[184:187],v[52:55],a[164:167],v[184:187]\n"
T_"v[28:29],v193 offset:28672\n"
M_"v[184:187],v[56:59],a[168:171],v[184:187]\n"
M_"v[184:187],v[60:63],a[172:175],v[184:187]\n"
B_"v202,s[48:51],s75 offen offset:2048  lds\n"
M_"v[188:191],v[48:51],a[176:179],0\n"
"s_lshr_b32 s85,s11,3\n"
T_"v[30:31],v193 offset:29184\n"
T_"v[32:33],v194 offset:16384\n"
M_"v[188:191],v[52:55],a[180:183],v[188:191]\n"
"s_cmp_eq_u32 s85,8\n"
"s_cbranch_scc1 L_slow_PKt\n"
"L_back_PKt:\n"
M_"v[188:191],v[56:59],a[184:187],v[188:191]\n"
T_"v[34:35],v194 offset:16896\n"
T_"v[36:37],v194 offset:20480\n"
M_"v[188:191],v[60:63],a[188:191],v[188:191]\n"
W_"lgkmcnt(11)\n"
T_"v[38:39],v194 offset:20992\n"
T_"v[40:41],v194 offset:24576\n"
M_"v[128:131],v[0:3],a[64:67],v[128:131]\n"
B_"v203,s[48:51],s75 offen offset:3072  lds\n"
M_"v[128:131],v[4:7],a[68:71],v[128:131]\n"
"s_lshl_b32 s85,s31,8\n"
T_"v[42:43],v194 offset:25088\n"
T_"v[44:45],v194 offset:28672\n"
M_"v[128:131],v[8:11],a[72:75],v[128:131]\n"
A_"s85,s85,s30\n"
"v_accvgpr_write_b32 a128,v224\n"
M_"v[128:131],v[12:15],a[76:79],v[128:131]\n"
"s_cmp_eq_u32 s85,0\n"
"s_cselect_b32 s79,0x4f00000,s94\n"
T_"v[46:47],v194 offset:29184\n"
M_"v[132:135],v[0:3],a[80:83],v[132:135]\n"
"buffer_store_dword v254,off,s[52:55],s79 sc1\n"
M_"v[132:135],v[4:7],a[84:87],v[132:135]\n"
"v_mov_b32 v205,v243\n"
T_"v[48:49],v195 offset:16384\n"
M_"v[132:135],v[8:11],a[88:91],v[132:135]\n"
T_"v[50:51],v195 offset:16896\n"
T_"v[52:53],v195 offset:20480\n"
M_"v[132:135],v[12:15],a[92:95],v[132:135]\n"
W_"lgkmcnt(11)\n"
"v_accvgpr_write_b32 a129,v225\n"
T_"v[54:55],v195 offset:20992\n"
M_"v[144:147],v[16:19],a[64:67],v[144:147]\n"
A_"s36,s96,s87\n"
T_"v[56:57],v195 offset:24576\n"
"v_accvgpr_write_b32 a130,v226\n"
M_"v[144:147],v[20:23],a[68:71],v[144:147]\n"
T_"v[58:59],v195 offset:25088\n"
T_"v[60:61],v195 offset:28672\n"
M_"v[144:147],v[24:27],a[72:75],v[144:147]\n"
A_"s37,s98,s87\n"
"v_accvgpr_write_b32 a131,v227\n"
M_"v[144:147],v[28:31],a[76:79],v[144:147]\n"
T_"v[62:63],v195 offset:29184\n"
M_"v[148:151],v[16:19],a[80:83],v[148:151]\n"
"s_xor_b32 s38,s36,0x10000\n"
"v_accvgpr_write_b32 a132,v208\n"
M_"v[148:151],v[20:23],a[84:87],v[148:151]\n"
M_"v[148:151],v[24:27],a[88:91],v[148:151]\n"
"s_cmp_lg_u32 s11,2\n"
"s_cbranch_scc1 L_m_ni306\n"
"buffer_inv sc1\n"
"L_m_ni306:\n"
M_"v[148:151],v[28:31],a[92:95],v[148:151]\n"
W_"lgkmcnt(8)\n"
"v_accvgpr_write_b32 a133,v209\n"
"s_xor_b32 s39,s37,0x10000\n"
M_"v[160:163],v[32:35],a[64:67],v[160:163]\n"
A_"s57,s56,0x0\n"
M_"v[160:163],v[36:39],a[68:71],v[160:163]\n"
"v_accvgpr_write_b32 a134,v210\n"
M_"v[160:163],v[40:43],a[72:75],v[160:163]\n"
"s_sub_u32 s58,s34,1\n"
M_"v[160:163],v[44:47],a[76:79],v[160:163]\n"
M_"v[164:167],v[32:35],a[80:83],v[164:167]\n"
"s_lshl_b32 s59,s34,1\n"
"v_accvgpr_write_b32 a135,v211\n"
M_"v[164:167],v[36:39],a[84:87],v[164:167]\n"
M_"v[164:167],v[40:43],a[88:91],v[164:167]\n"
"s_sub_u32 s59,13,s59\n"
M_"v[164:167],v[44:47],a[92:95],v[164:167]\n"
W_"lgkmcnt(0)\n"
"v_accvgpr_write_b32 a144,v228\n"
M_"v[176:179],v[48:51],a[64:67],v[176:179]\n"
"s_mov_b64 s[62:63],0\n"
M_"v[176:179],v[52:55],a[68:71],v[176:179]\n"
"v_accvgpr_write_b32 a145,v229\n"
M_"v[176:179],v[56:59],a[72:75],v[176:179]\n"
"v_add_u32 v245,0x10000,v244\n"
M_"v[176:179],v[60:63],a[76:79],v[176:179]\n"
M_"v[180:183],v[48:51],a[80:83],v[180:183]\n"
"v_bfe_u32 v251,v244,4,4\n"
"v_accvgpr_write_b32 a146,v230\n"
M_"v[180:183],v[52:55],a[84:87],v[180:183]\n"
M_"v[180:183],v[56:59],a[88:91],v[180:183]\n"
"v_lshrrev_b32 v246,8,v244\n"
"v_accvgpr_write_b32 a147,v231\n"
M_"v[180:183],v[60:63],a[92:95],v[180:183]\n"
M_"v[136:139],v[0:3],a[96:99],v[136:139]\n"
"v_lshlrev_b32 v246,2,v246\n"
M_"v[136:139],v[4:7],a[100:103],v[136:139]\n"
"v_accvgpr_write_b32 a148,v212\n"
M_"v[136:139],v[8:11],a[104:107],v[136:139]\n"
"v_sub_u32 v251,v251,v246\n"
M_"v[136:139],v[12:15],a[108:111],v[136:139]\n"
M_"v[140:143],v[0:3],a[112:115],v[140:143]\n"
"s_lshl_b32 s60,s31,4\n"
"s_barrier\n"
M_"v[140:143],v[4:7],a[116:119],v[140:143]\n"
"v_accvgpr_write_b32 a149,v213\n"
M_"v[140:143],v[8:11],a[120:123],v[140:143]\n"
"v_add_u32 v251,s60,v251\n"
"v_accvgpr_write_b32 a150,v214\n"
M_"v[140:143],v[12:15],a[124:127],v[140:143]\n"
A_"s75,s68,131200\n"
M_"v[152:155],v[16:19],a[96:99],v[152:155]\n"
"s_cmp_eq_u32 s34,0\n"
"s_cselect_b32 s60,0xff800000,0\n"
"v_and_b32 v206,96,v244\n"
M_"v[152:155],v[20:23],a[100:103],v[152:155]\n"
A_"m0,s56,114688\n"
"v_and_b32 v207,16,v244\n"
T_"v[0:1],v192 offset:32768\n"
M_"v[152:155],v[24:27],a[104:107],v[152:155]\n"
"s_and_b32 s10,s30,0xe6\n"
"v_accvgpr_write_b32 a151,v215\n"
T_"v[2:3],v192 offset:33280\n"
M_"v[152:155],v[28:31],a[108:111],v[152:155]\n"
B_"v200,s[48:51],s75 offen offset:0  lds\n"
M_"v[156:159],v[16:19],a[112:115],v[156:159]\n"
"v_lshl_add_u32 v207,v206,2,v207\n"
T_"v[4:5],v192 offset:36864\n"
M_"v[156:159],v[20:23],a[116:119],v[156:159]\n"
"s_lshl4_add_u32 s10,s10,0x9e2000\n"
"v_add_lshl_u32 v207,s10,v207,3\n"
"v_add_u32 v207,20,v207\n"
M_"v[156:159],v[24:27],a[120:123],v[156:159]\n"
"buffer_load_dword v206,v207,s[52:55],0 offen sc1\n"
M_"v[156:159],v[28:31],a[124:127],v[156:159]\n"
B_"v201,s[48:51],s75 offen offset:1024  lds\n"
M_"v[168:171],v[32:35],a[96:99],v[168:171]\n"
"v_accvgpr_write_b32 a160,v232\n"
T_"v[6:7],v192 offset:37376\n"
M_"v[168:171],v[36:39],a[100:103],v[168:171]\n"
"v_mov_b32 v253,s60\n"
"v_accvgpr_write_b32 a161,v233\n"
M_"v[168:171],v[40:43],a[104:107],v[168:171]\n"
T_"v[8:9],v192 offset:40960\n"
"v_bfe_i32 v246,v244,5,1\n"
M_"v[168:171],v[44:47],a[108:111],v[168:171]\n"
B_"v202,s[48:51],s75 offen offset:2048  lds\n"
M_"v[172:175],v[32:35],a[112:115],v[172:175]\n"
T_"v[10:11],v192 offset:41472\n"
T_"v[12:13],v192 offset:45056\n"
M_"v[172:175],v[36:39],a[116:119],v[172:175]\n"
T_"v[14:15],v192 offset:45568\n"
T_"v[16:17],v193 offset:32768\n"
M_"v[172:175],v[40:43],a[120:123],v[172:175]\n"
"v_and_b32 v246,0xaaaaaaaa,v246\n"
"v_accvgpr_write_b32 a162,v234\n"
M_"v[172:175],v[44:47],a[124:127],v[172:175]\n"
T_"v[18:19],v193 offset:33280\n"
"v_or_b32 v246,0x44444444,v246\n"
M_"v[184:187],v[48:51],a[96:99],v[184:187]\n"
B_"v203,s[48:51],s75 offen offset:3072  lds\n"
M_"v[184:187],v[52:55],a[100:103],v[184:187]\n"
"v_accvgpr_write_b32 a163,v235\n"
T_"v[20:21],v193 offset:36864\n"
M_"v[184:187],v[56:59],a[104:107],v[184:187]\n"
T_"v[22:23],v193 offset:37376\n"
"v_add_u32 v240,48,v244\n"
M_"v[184:187],v[60:63],a[108:111],v[184:187]\n"
A_"s75,s68,262272\n"
"v_accvgpr_write_b32 a164,v216\n"
T_"v[24:25],v193 offset:40960\n"
M_"v[188:191],v[48:51],a[112:115],v[188:191]\n"
"s_cmp_lg_u32 s11,3\n"
"s_cbranch_scc1 L_m_ni307\n"
"buffer_inv sc1\n"
"L_m_ni307:\n"
M_"v[188:191],v[52:55],a[116:119],v[188:191]\n"
A_"m0,s56,81920\n"
"v_bfe_i32 v240,v240,5,1\n"
T_"v[26:27],v193 offset:41472\n"
T_"v[28:29],v193 offset:45056\n"
T_"v[30:31],v193 offset:45568\n"
M_"v[188:191],v[56:59],a[120:123],v[188:191]\n"
T_"v[32:33],v194 offset:32768\n"
"v_and_b32 v240,0x3f803f80,v240\n"
M_"v[188:191],v[60:63],a[124:127],v[188:191]\n"
W_"lgkmcnt(9)\n"
"v_accvgpr_write_b32 a165,v217\n"
T_"v[34:35],v194 offset:33280\n"
M_"v[128:131],v[0:3],a[0:3],v[128:131]\n"
B_"v200,s[48:51],s75 offen offset:0  lds\n"
M_"v[128:131],v[4:7],a[4:7],v[128:131]\n"
"v_mov_b32 v241,v240\n"
"v_accvgpr_write_b32 a166,v218\n"
T_"v[36:37],v194 offset:36864\n"
M_"v[128:131],v[8:11],a[8:11],v[128:131]\n"
T_"v[38:39],v194 offset:37376\n"
"v_mov_b32 v242,v240\n"
M_"v[128:131],v[12:15],a[12:15],v[128:131]\n"
"v_accvgpr_write_b32 a167,v219\n"
T_"v[40:41],v194 offset:40960\n"
M_"v[132:135],v[0:3],a[16:19],v[132:135]\n"
B_"v201,s[48:51],s75 offen offset:1024  lds\n"
M_"v[132:135],v[4:7],a[20:23],v[132:135]\n"
"v_mov_b32 v243,v240\n"
T_"v[42:43],v194 offset:41472\n"
T_"v[44:45],v194 offset:45056\n"
T_"v[46:47],v194 offset:45568\n"
M_"v[132:135],v[8:11],a[24:27],v[132:135]\n"
"s_lshl_b32 s60,s33,12\n"
T_"v[48:49],v195 offset:32768\n"
"v_accvgpr_write_b32 a176,v236\n"
M_"v[132:135],v[12:15],a[28:31],v[132:135]\n"
W_"lgkmcnt(9)\n"
T_"v[50:51],v195 offset:33280\n"
"v_accvgpr_write_b32 a177,v237\n"
M_"v[144:147],v[16:19],a[0:3],v[144:147]\n"
A_"s60,s60,0x3a00000\n"
T_"v[52:53],v195 offset:36864\n"
T_"v[54:55],v195 offset:37376\n"
M_"v[144:147],v[20:23],a[4:7],v[144:147]\n"
B_"v202,s[48:51],s75 offen offset:2048  lds\n"
M_"v[144:147],v[24:27],a[8:11],v[144:147]\n"
"s_lshl3_add_u32 s96,s96,s60\n"
"v_accvgpr_write_b32 a178,v238\n"
T_"v[56:57],v195 offset:40960\n"
M_"v[144:147],v[28:31],a[12:15],v[144:147]\n"
"s_lshl3_add_u32 s97,s97,s60\n"
T_"v[58:59],v195 offset:41472\n"
T_"v[60:61],v195 offset:45056\n"
M_"v[148:151],v[16:19],a[16:19],v[148:151]\n"
"s_lshl3_add_u32 s98,s98,s60\n"
T_"v[62:63],v195 offset:45568\n"
"v_accvgpr_write_b32 a179,v239\n"
M_"v[148:151],v[20:23],a[20:23],v[148:151]\n"
B_"v203,s[48:51],s75 offen offset:3072  lds\n"
M_"v[148:151],v[24:27],a[24:27],v[148:151]\n"
"s_lshl3_add_u32 s99,s99,s60\n"
"v_accvgpr_write_b32 a180,v220\n"
"v_accvgpr_write_b32 a181,v221\n"
M_"v[148:151],v[28:31],a[28:31],v[148:151]\n"
W_"lgkmcnt(8)\n"
"s_mov_b32 s60,0x5d800000\n"
M_"v[160:163],v[32:35],a[0:3],v[160:163]\n"
"v_accvgpr_write_b32 a182,v222\n"
M_"v[160:163],v[36:39],a[4:7],v[160:163]\n"
"v_cmp_le_i32 s[64:65],0,v251\n"
M_"v[160:163],v[40:43],a[8:11],v[160:163]\n"
"v_cmp_le_i32 s[66:67],1,v251\n"
M_"v[160:163],v[44:47],a[12:15],v[160:163]\n"
"v_accvgpr_write_b32 a183,v223\n"
"v_cmp_le_i32 s[68:69],2,v251\n"
M_"v[164:167],v[32:35],a[16:19],v[164:167]\n"
M_"v[164:167],v[36:39],a[20:23],v[164:167]\n"
"v_cmp_le_i32 s[70:71],3,v251\n"
M_"v[164:167],v[40:43],a[24:27],v[164:167]\n"
"v_cmp_le_i32 s[72:73],16,v251\n"
M_"v[164:167],v[44:47],a[28:31],v[164:167]\n"
W_"lgkmcnt(0)\n"
M_"v[176:179],v[48:51],a[0:3],v[176:179]\n"
"v_cmp_le_i32 s[74:75],17,v251\n"
M_"v[176:179],v[52:55],a[4:7],v[176:179]\n"
"v_cmp_le_i32 s[76:77],18,v251\n"
M_"v[176:179],v[56:59],a[8:11],v[176:179]\n"
M_"v[176:179],v[60:63],a[12:15],v[176:179]\n"
"v_cmp_le_i32 s[78:79],19,v251\n"
M_"v[180:183],v[48:51],a[16:19],v[180:183]\n"
"v_cmp_le_i32 s[80:81],32,v251\n"
M_"v[180:183],v[52:55],a[20:23],v[180:183]\n"
"v_cmp_le_i32 s[82:83],33,v251\n"
M_"v[180:183],v[56:59],a[24:27],v[180:183]\n"
M_"v[180:183],v[60:63],a[28:31],v[180:183]\n"
"v_cmp_le_i32 s[84:85],34,v251\n"
M_"v[136:139],v[0:3],a[32:35],v[136:139]\n"
"v_cmp_le_i32 s[86:87],35,v251\n"
M_"v[136:139],v[4:7],a[36:39],v[136:139]\n"
M_"v[136:139],v[8:11],a[40:43],v[136:139]\n"
"v_cmp_le_i32 s[88:89],48,v251\n"
M_"v[136:139],v[12:15],a[44:47],v[136:139]\n"
"v_cmp_le_i32 s[90:91],49,v251\n"
M_"v[140:143],v[0:3],a[48:51],v[140:143]\n"
M_"v[140:143],v[4:7],a[52:55],v[140:143]\n"
"v_cmp_le_i32 s[92:93],50,v251\n"
M_"v[140:143],v[8:11],a[56:59],v[140:143]\n"
"v_cmp_le_i32 s[94:95],51,v251\n"
M_"v[140:143],v[12:15],a[60:63],v[140:143]\n"
M_"v[152:155],v[16:19],a[32:35],v[152:155]\n"
T_"v[0:1],v192 offset:0\n"
M_"v[152:155],v[20:23],a[36:39],v[152:155]\n"
T_"v[2:3],v192 offset:512\n"
K_"v224,v253,0,s[64:65]\n"
M_"v[152:155],v[24:27],a[40:43],v[152:155]\n"
W_"vmcnt(7)\n"
K_"v225,v253,0,s[66:67]\n"
M_"v[152:155],v[28:31],a[44:47],v[152:155]\n"
"v_cmp_ne_u32 s[12:13],s32,v206\n"
T_"v[4:5],v192 offset:4096\n"
M_"v[156:159],v[16:19],a[48:51],v[156:159]\n"
"s_cmp_lg_u64 s[12:13],0\n"
"s_cbranch_scc1 L_slow_PHt\n"
"L_back_PHt:\n"
M_"v[156:159],v[20:23],a[52:55],v[156:159]\n"
T_"v[6:7],v192 offset:4608\n"
K_"v226,v253,0,s[68:69]\n"
M_"v[156:159],v[24:27],a[56:59],v[156:159]\n"
T_"v[8:9],v192 offset:8192\n"
K_"v227,v253,0,s[70:71]\n"
M_"v[156:159],v[28:31],a[60:63],v[156:159]\n"
K_"v228,v253,0,s[72:73]\n"
M_"v[168:171],v[32:35],a[32:35],v[168:171]\n"
A_"s10,s14,0\n"
T_"v[10:11],v192 offset:8704\n"
K_"v229,v253,0,s[74:75]\n"
M_"v[168:171],v[36:39],a[36:39],v[168:171]\n"
B_"a[64:67],v244,s[52:55],s10 offen offset:0 sc1\n"
M_"v[168:171],v[40:43],a[40:43],v[168:171]\n"
T_"v[12:13],v192 offset:12288\n"
K_"v230,v253,0,s[76:77]\n"
M_"v[168:171],v[44:47],a[44:47],v[168:171]\n"
T_"v[14:15],v192 offset:12800\n"
T_"v[16:17],v193 offset:0\n"
M_"v[172:175],v[32:35],a[48:51],v[172:175]\n"
T_"v[18:19],v193 offset:512\n"
T_"v[20:21],v193 offset:4096\n"
M_"v[172:175],v[36:39],a[52:55],v[172:175]\n"
B_"a[68:71],v244,s[52:55],s10 offen offset:1024 sc1\n"
M_"v[172:175],v[40:43],a[56:59],v[172:175]\n"
T_"v[22:23],v193 offset:4608\n"
T_"v[24:25],v193 offset:8192\n"
M_"v[172:175],v[44:47],a[60:63],v[172:175]\n"
K_"v231,v253,0,s[78:79]\n"
K_"v232,v253,0,s[80:81]\n"
M_"v[184:187],v[48:51],a[32:35],v[184:187]\n"
T_"v[26:27],v193 offset:8704\n"
K_"v233,v253,0,s[82:83]\n"
M_"v[184:187],v[52:55],a[36:39],v[184:187]\n"
B_"a[72:75],v244,s[52:55],s10 offen offset:2048 sc1\n"
M_"v[184:187],v[56:59],a[40:43],v[184:187]\n"
T_"v[28:29],v193 offset:12288\n"
K_"v234,v253,0,s[84:85]\n"
M_"v[184:187],v[60:63],a[44:47],v[184:187]\n"
T_"v[30:31],v193 offset:12800\n"
T_"v[32:33],v194 offset:0\n"
M_"v[188:191],v[48:51],a[48:51],v[188:191]\n"
T_"v[34:35],v194 offset:512\n"
T_"v[36:37],v194 offset:4096\n"
M_"v[188:191],v[52:55],a[52:55],v[188:191]\n"
B_"a[76:79],v244,s[52:55],s10 offen offset:3072 sc1\n"
M_"v[188:191],v[56:59],a[56:59],v[188:191]\n"
T_"v[38:39],v194 offset:4608\n"
T_"v[40:41],v194 offset:8192\n"
M_"v[188:191],v[60:63],a[60:63],v[188:191]\n"
W_"lgkmcnt(13)\n"
K_"v235,v253,0,s[86:87]\n"
K_"v236,v253,0,s[88:89]\n"
M_"v[128:131],v[0:3],a[192:195],v[128:131]\n"
A_"s10,s15,0\n"
T_"v[42:43],v194 offset:8704\n"
K_"v237,v253,0,s[90:91]\n"
M_"v[128:131],v[4:7],a[196:199],v[128:131]\n"
B_"a[80:83],v244,s[52:55],s10 offen offset:0 sc1\n"
M_"v[128:131],v[8:11],a[200:203],v[128:131]\n"
T_"v[44:45],v194 offset:12288\n"
K_"v238,v253,0,s[92:93]\n"
M_"v[128:131],v[12:15],a[204:207],v[128:131]\n"
T_"v[46:47],v194 offset:12800\n"
T_"v[48:49],v195 offset:0\n"
M_"v[132:135],v[0:3],a[208:211],v[132:135]\n"
T_"v[50:51],v195 offset:512\n"
T_"v[52:53],v195 offset:4096\n"
M_"v[132:135],v[4:7],a[212:215],v[132:135]\n"
B_"a[84:87],v244,s[52:55],s10 offen offset:1024 sc1\n"
M_"v[132:135],v[8:11],a[216:219],v[132:135]\n"
"buffer_load_dword v204,v205,s[44:47],0 offen\n"
M_"v[132:135],v[12:15],a[220:223],v[132:135]\n"
W_"lgkmcnt(11)\n"
T_"v[54:55],v195 offset:4608\n"
T_"v[56:57],v195 offset:8192\n"
M_"v[144:147],v[16:19],a[192:195],v[144:147]\n"
T_"v[58:59],v195 offset:8704\n"
K_"v239,v253,0,s[94:95]\n"
M_"v[144:147],v[20:23],a[196:199],v[144:147]\n"
B_"a[88:91],v244,s[52:55],s10 offen offset:2048 sc1\n"
M_"v[144:147],v[24:27],a[200:203],v[144:147]\n"
T_"v[60:61],v195 offset:12288\n"
"v_mov_b64 v[208:209],0\n"
M_"v[144:147],v[28:31],a[204:207],v[144:147]\n"
T_"v[62:63],v195 offset:12800\n"
"v_mov_b64 v[210:211],0\n"
M_"v[148:151],v[16:19],a[208:211],v[148:151]\n"
"v_mov_b64 v[212:213],0\n"
"v_mov_b64 v[214:215],0\n"
M_"v[148:151],v[20:23],a[212:215],v[148:151]\n"
B_"a[92:95],v244,s[52:55],s10 offen offset:3072 sc1\n"
M_"v[148:151],v[24:27],a[216:219],v[148:151]\n"
"v_mov_b64 v[216:217],0\n"
"v_mov_b64 v[218:219],0\n"
M_"v[148:151],v[28:31],a[220:223],v[148:151]\n"
W_"lgkmcnt(8)\n"
"v_mov_b64 v[220:221],0\n"
"v_mov_b64 v[222:223],0\n"
M_"v[160:163],v[32:35],a[192:195],v[160:163]\n"
A_"s10,s14,0\n"
M_"v[160:163],v[36:39],a[196:199],v[160:163]\n"
A_"s10,s10,s17\n"
M_"v[160:163],v[40:43],a[200:203],v[160:163]\n"
B_"a[96:99],v244,s[52:55],s10 offen offset:0 sc1\n"
M_"v[160:163],v[44:47],a[204:207],v[160:163]\n"
M_"v[164:167],v[32:35],a[208:211],v[164:167]\n"
M_"v[164:167],v[36:39],a[212:215],v[164:167]\n"
M_"v[164:167],v[40:43],a[216:219],v[164:167]\n"
B_"a[100:103],v244,s[52:55],s10 offen offset:1024 sc1\n"
M_"v[164:167],v[44:47],a[220:223],v[164:167]\n"
W_"lgkmcnt(0)\n"
M_"v[176:179],v[48:51],a[192:195],v[176:179]\n"
M_"v[176:179],v[52:55],a[196:199],v[176:179]\n"
M_"v[176:179],v[56:59],a[200:203],v[176:179]\n"
B_"a[104:107],v244,s[52:55],s10 offen offset:2048 sc1\n"
M_"v[176:179],v[60:63],a[204:207],v[176:179]\n"
M_"v[180:183],v[48:51],a[208:211],v[180:183]\n"
M_"v[180:183],v[52:55],a[212:215],v[180:183]\n"
M_"v[180:183],v[56:59],a[216:219],v[180:183]\n"
B_"a[108:111],v244,s[52:55],s10 offen offset:3072 sc1\n"
M_"v[180:183],v[60:63],a[220:223],v[180:183]\n"
C_"v128,v128,v129\n"
C_"v129,v130,v131\n"
M_"v[136:139],v[0:3],a[224:227],v[136:139]\n"
A_"s10,s15,0\n"
C_"v130,v144,v145\n"
M_"v[136:139],v[4:7],a[228:231],v[136:139]\n"
A_"s10,s10,s17\n"
C_"v131,v146,v147\n"
M_"v[136:139],v[8:11],a[232:235],v[136:139]\n"
B_"a[112:115],v244,s[52:55],s10 offen offset:0 sc1\n"
M_"v[136:139],v[12:15],a[236:239],v[136:139]\n"
C_"v160,v160,v161\n"
C_"v161,v162,v163\n"
M_"v[140:143],v[0:3],a[240:243],v[140:143]\n"
W_"vmcnt(14) lgkmcnt(0)\n"
"s_barrier\n"
C_"v162,v176,v177\n"
M_"v[140:143],v[4:7],a[244:247],v[140:143]\n"
C_"v163,v178,v179\n"
C_"v132,v132,v133\n"
M_"v[140:143],v[8:11],a[248:251],v[140:143]\n"
B_"a[116:119],v244,s[52:55],s10 offen offset:1024 sc1\n"
M_"v[140:143],v[12:15],a[252:255],v[140:143]\n"
C_"v133,v134,v135\n"
C_"v134,v148,v149\n"
M_"v[152:155],v[16:19],a[224:227],v[152:155]\n"
"v_xor_b32 v192,0x30000,v192\n"
C_"v135,v150,v151\n"
M_"v[152:155],v[20:23],a[228:231],v[152:155]\n"
"v_xor_b32 v193,0x30000,v193\n"
C_"v164,v164,v165\n"
M_"v[152:155],v[24:27],a[232:235],v[152:155]\n"
B_"a[120:123],v244,s[52:55],s10 offen offset:2048 sc1\n"
M_"v[152:155],v[28:31],a[236:239],v[152:155]\n"
"v_xor_b32 v194,0x30000,v194\n"
"v_xor_b32 v195,0x30000,v195\n"
M_"v[156:159],v[16:19],a[240:243],v[156:159]\n"
T_"v[0:1],v192 offset:16384\n"
C_"v165,v166,v167\n"
M_"v[156:159],v[20:23],a[244:247],v[156:159]\n"
T_"v[2:3],v192 offset:16896\n"
C_"v166,v180,v181\n"
M_"v[156:159],v[24:27],a[248:251],v[156:159]\n"
B_"a[124:127],v244,s[52:55],s10 offen offset:3072 sc1\n"
M_"v[156:159],v[28:31],a[252:255],v[156:159]\n"
T_"v[4:5],v192 offset:20480\n"
T_"v[6:7],v192 offset:20992\n"
M_"v[168:171],v[32:35],a[224:227],v[168:171]\n"
A_"s10,s14,4096\n"
T_"v[8:9],v192 offset:24576\n"
C_"v167,v182,v183\n"
M_"v[168:171],v[36:39],a[228:231],v[168:171]\n"
T_"v[10:11],v192 offset:25088\n"
"v_accvgpr_write_b32 a136,v128\n"
M_"v[168:171],v[40:43],a[232:235],v[168:171]\n"
B_"a[0:3],v244,s[52:55],s10 offen offset:0 sc1\n"
M_"v[168:171],v[44:47],a[236:239],v[168:171]\n"
T_"v[12:13],v192 offset:28672\n"
T_"v[14:15],v192 offset:29184\n"
M_"v[172:175],v[32:35],a[240:243],v[172:175]\n"
T_"v[16:17],v193 offset:16384\n"
T_"v[18:19],v193 offset:16896\n"
M_"v[172:175],v[36:39],a[244:247],v[172:175]\n"
T_"v[20:21],v193 offset:20480\n"
T_"v[22:23],v193 offset:20992\n"
M_"v[172:175],v[40:43],a[248:251],v[172:175]\n"
B_"a[4:7],v244,s[52:55],s10 offen offset:1024 sc1\n"
M_"v[172:175],v[44:47],a[252:255],v[172:175]\n"
T_"v[24:25],v193 offset:24576\n"
"v_accvgpr_write_b32 a137,v129\n"
M_"v[184:187],v[48:51],a[224:227],v[184:187]\n"
T_"v[26:27],v193 offset:25088\n"
"v_accvgpr_write_b32 a138,v130\n"
M_"v[184:187],v[52:55],a[228:231],v[184:187]\n"
T_"v[28:29],v193 offset:28672\n"
"v_accvgpr_write_b32 a139,v131\n"
M_"v[184:187],v[56:59],a[232:235],v[184:187]\n"
B_"a[8:11],v244,s[52:55],s10 offen offset:2048 sc1\n"
M_"v[184:187],v[60:63],a[236:239],v[184:187]\n"
T_"v[30:31],v193 offset:29184\n"
T_"v[32:33],v194 offset:16384\n"
M_"v[188:191],v[48:51],a[240:243],v[188:191]\n"
T_"v[34:35],v194 offset:16896\n"
T_"v[36:37],v194 offset:20480\n"
M_"v[188:191],v[52:55],a[244:247],v[188:191]\n"
T_"v[38:39],v194 offset:20992\n"
"v_accvgpr_write_b32 a140,v160\n"
M_"v[188:191],v[56:59],a[248:251],v[188:191]\n"
B_"a[12:15],v244,s[52:55],s10 offen offset:3072 sc1\n"
M_"v[188:191],v[60:63],a[252:255],v[188:191]\n"
W_"lgkmcnt(12)\n"
T_"v[40:41],v194 offset:24576\n"
W_"vmcnt(17)\n"
M_"v[96:99],v[0:3],a[64:67],0\n"
A_"s44,s40,0x1000000\n"
"s_addc_u32 s45,s41,0\n"
T_"v[42:43],v194 offset:25088\n"
M_"v[96:99],v[4:7],a[68:71],v[96:99]\n"
A_"s10,s15,4096\n"
T_"v[44:45],v194 offset:28672\n"
C_"v136,v136,v137\n"
M_"v[96:99],v[8:11],a[72:75],v[96:99]\n"
B_"a[16:19],v244,s[52:55],s10 offen offset:0 sc1\n"
M_"v[96:99],v[12:15],a[76:79],v[96:99]\n"
W_"vmcnt(13)\n"
T_"v[46:47],v194 offset:29184\n"
T_"v[48:49],v195 offset:16384\n"
M_"v[100:103],v[0:3],a[80:83],0\n"
T_"v[50:51],v195 offset:16896\n"
T_"v[52:53],v195 offset:20480\n"
M_"v[100:103],v[4:7],a[84:87],v[100:103]\n"
T_"v[54:55],v195 offset:20992\n"
C_"v137,v138,v139\n"
M_"v[100:103],v[8:11],a[88:91],v[100:103]\n"
B_"a[20:23],v244,s[52:55],s10 offen offset:1024 sc1\n"
M_"v[100:103],v[12:15],a[92:95],v[100:103]\n"
W_"lgkmcnt(12)\n"
T_"v[56:57],v195 offset:24576\n"
C_"v138,v152,v153\n"
M_"v[104:107],v[16:19],a[64:67],0\n"
T_"v[58:59],v195 offset:25088\n"
C_"v139,v154,v155\n"
M_"v[104:107],v[20:23],a[68:71],v[104:107]\n"
T_"v[60:61],v195 offset:28672\n"
C_"v168,v168,v169\n"
M_"v[104:107],v[24:27],a[72:75],v[104:107]\n"
B_"a[24:27],v244,s[52:55],s10 offen offset:2048 sc1\n"
M_"v[104:107],v[28:31],a[76:79],v[104:107]\n"
T_"v[62:63],v195 offset:29184\n"
"v_xor_b32 v192,0x20000,v192\n"
M_"v[108:111],v[16:19],a[80:83],0\n"
"v_xor_b32 v193,0x20000,v193\n"
"v_xor_b32 v194,0x20000,v194\n"
M_"v[108:111],v[20:23],a[84:87],v[108:111]\n"
"s_mov_b32 s46,0x80000\n"
"v_xor_b32 v195,0x20000,v195\n"
T_"v[0:1],v192 offset:0\n"
M_"v[108:111],v[24:27],a[88:91],v[108:111]\n"
B_"a[28:31],v244,s[52:55],s10 offen offset:3072 sc1\n"
M_"v[108:111],v[28:31],a[92:95],v[108:111]\n"
W_"lgkmcnt(9)\n"
T_"v[2:3],v192 offset:512\n"
T_"v[4:5],v192 offset:4096\n"
M_"v[112:115],v[32:35],a[64:67],0\n"
A_"s10,s14,4096\n"
T_"v[6:7],v192 offset:4608\n"
T_"v[8:9],v192 offset:8192\n"
M_"v[112:115],v[36:39],a[68:71],v[112:115]\n"
A_"s10,s10,s17\n"
T_"v[10:11],v192 offset:8704\n"
C_"v169,v170,v171\n"
M_"v[112:115],v[40:43],a[72:75],v[112:115]\n"
B_"a[32:35],v244,s[52:55],s10 offen offset:0 sc1\n"
M_"v[112:115],v[44:47],a[76:79],v[112:115]\n"
T_"v[12:13],v192 offset:12288\n"
T_"v[14:15],v192 offset:12800\n"
M_"v[116:119],v[32:35],a[80:83],0\n"
T_"v[16:17],v193 offset:0\n"
T_"v[18:19],v193 offset:512\n"
M_"v[116:119],v[36:39],a[84:87],v[116:119]\n"
T_"v[20:21],v193 offset:4096\n"
T_"v[22:23],v193 offset:4608\n"
M_"v[116:119],v[40:43],a[88:91],v[116:119]\n"
B_"a[36:39],v244,s[52:55],s10 offen offset:1024 sc1\n"
M_"v[116:119],v[44:47],a[92:95],v[116:119]\n"
W_"lgkmcnt(12)\n"
T_"v[24:25],v193 offset:8192\n"
C_"v170,v184,v185\n"
M_"v[120:123],v[48:51],a[64:67],0\n"
T_"v[26:27],v193 offset:8704\n"
C_"v171,v186,v187\n"
M_"v[120:123],v[52:55],a[68:71],v[120:123]\n"
C_"v140,v140,v141\n"
C_"v141,v142,v143\n"
M_"v[120:123],v[56:59],a[72:75],v[120:123]\n"
B_"a[40:43],v244,s[52:55],s10 offen offset:2048 sc1\n"
M_"v[120:123],v[60:63],a[76:79],v[120:123]\n"
T_"v[28:29],v193 offset:12288\n"
C_"v142,v156,v157\n"
M_"v[124:127],v[48:51],a[80:83],0\n"
T_"v[30:31],v193 offset:12800\n"
T_"v[32:33],v194 offset:0\n"
M_"v[124:127],v[52:55],a[84:87],v[124:127]\n"
T_"v[34:35],v194 offset:512\n"
T_"v[36:37],v194 offset:4096\n"
M_"v[124:127],v[56:59],a[88:91],v[124:127]\n"
B_"a[44:47],v244,s[52:55],s10 offen offset:3072 sc1\n"
M_"v[124:127],v[60:63],a[92:95],v[124:127]\n"
W_"lgkmcnt(11)\n"
C_"v143,v158,v159\n"
T_"v[38:39],v194 offset:4608\n"
M_"v[64:67],v[0:3],a[64:67],0\n"
A_"s10,s15,4096\n"
C_"v172,v172,v173\n"
C_"v173,v174,v175\n"
M_"v[64:67],v[4:7],a[68:71],v[64:67]\n"
A_"s10,s10,s17\n"
C_"v174,v188,v189\n"
C_"v175,v190,v191\n"
M_"v[64:67],v[8:11],a[72:75],v[64:67]\n"
B_"a[48:51],v244,s[52:55],s10 offen offset:0 sc1\n"
M_"v[64:67],v[12:15],a[76:79],v[64:67]\n"
T_"v[40:41],v194 offset:8192\n"
T_"v[42:43],v194 offset:8704\n"
M_"v[68:71],v[0:3],a[80:83],0\n"
T_"v[44:45],v194 offset:12288\n"
T_"v[46:47],v194 offset:12800\n"
M_"v[68:71],v[4:7],a[84:87],v[68:71]\n"
T_"v[48:49],v195 offset:0\n"
T_"v[50:51],v195 offset:512\n"
M_"v[68:71],v[8:11],a[88:91],v[68:71]\n"
B_"a[52:55],v244,s[52:55],s10 offen offset:1024 sc1\n"
M_"v[68:71],v[12:15],a[92:95],v[68:71]\n"
W_"vmcnt(18)\n"
T_"v[52:53],v195 offset:4096\n"
T_"v[54:55],v195 offset:4608\n"
M_"v[144:147],v[0:3],a[96:99],0\n"
T_"v[56:57],v195 offset:8192\n"
T_"v[58:59],v195 offset:8704\n"
M_"v[144:147],v[4:7],a[100:103],v[144:147]\n"
"v_accvgpr_write_b32 a141,v161\n"
"v_accvgpr_write_b32 a142,v162\n"
M_"v[144:147],v[8:11],a[104:107],v[144:147]\n"
B_"a[56:59],v244,s[52:55],s10 offen offset:2048 sc1\n"
M_"v[144:147],v[12:15],a[108:111],v[144:147]\n"
W_"vmcnt(15)\n"
T_"v[60:61],v195 offset:12288\n"
"v_accvgpr_write_b32 a143,v163\n"
M_"v[176:179],v[0:3],a[112:115],0\n"
T_"v[62:63],v195 offset:12800\n"
"v_accvgpr_write_b32 a152,v132\n"
M_"v[176:179],v[4:7],a[116:119],v[176:179]\n"
"v_accvgpr_write_b32 a153,v133\n"
"v_accvgpr_write_b32 a154,v134\n"
M_"v[176:179],v[8:11],a[120:123],v[176:179]\n"
B_"a[60:63],v244,s[52:55],s10 offen offset:3072 sc1\n"
M_"v[176:179],v[12:15],a[124:127],v[176:179]\n"
W_"lgkmcnt(15)\n"
"v_accvgpr_write_b32 a155,v135\n"
"v_accvgpr_write_b32 a156,v164\n"
M_"v[72:75],v[16:19],a[64:67],0\n"
A_"s10,s14,8192\n"
T_"v[0:1],v192 offset:16384\n"
"v_accvgpr_write_b32 a157,v165\n"
M_"v[72:75],v[20:23],a[68:71],v[72:75]\n"
A_"s10,s10,s17\n"
T_"v[2:3],v192 offset:16896\n"
"v_accvgpr_write_b32 a158,v166\n"
M_"v[72:75],v[24:27],a[72:75],v[72:75]\n"
B_"a[192:195],v244,s[52:55],s10 offen offset:0 sc1\n"
M_"v[72:75],v[28:31],a[76:79],v[72:75]\n"
T_"v[4:5],v192 offset:20480\n"
"v_accvgpr_write_b32 a159,v167\n"
M_"v[76:79],v[16:19],a[80:83],0\n"
"v_accvgpr_write_b32 a168,v136\n"
"v_accvgpr_write_b32 a169,v137\n"
M_"v[76:79],v[20:23],a[84:87],v[76:79]\n"
T_"v[6:7],v192 offset:20992\n"
"v_accvgpr_write_b32 a170,v138\n"
M_"v[76:79],v[24:27],a[88:91],v[76:79]\n"
B_"a[196:199],v244,s[52:55],s10 offen offset:1024 sc1\n"
M_"v[76:79],v[28:31],a[92:95],v[76:79]\n"
T_"v[8:9],v192 offset:24576\n"
"v_accvgpr_write_b32 a171,v139\n"
M_"v[148:151],v[16:19],a[96:99],0\n"
T_"v[10:11],v192 offset:25088\n"
"v_accvgpr_write_b32 a172,v168\n"
M_"v[148:151],v[20:23],a[100:103],v[148:151]\n"
"v_accvgpr_write_b32 a173,v169\n"
"v_accvgpr_write_b32 a174,v170\n"
M_"v[148:151],v[24:27],a[104:107],v[148:151]\n"
B_"a[200:203],v244,s[52:55],s10 offen offset:2048 sc1\n"
M_"v[148:151],v[28:31],a[108:111],v[148:151]\n"
T_"v[12:13],v192 offset:28672\n"
"v_accvgpr_write_b32 a175,v171\n"
M_"v[180:183],v[16:19],a[112:115],0\n"
T_"v[14:15],v192 offset:29184\n"
"v_accvgpr_write_b32 a184,v140\n"
M_"v[180:183],v[20:23],a[116:119],v[180:183]\n"
"v_accvgpr_write_b32 a185,v141\n"
"v_accvgpr_write_b32 a186,v142\n"
M_"v[180:183],v[24:27],a[120:123],v[180:183]\n"
B_"a[204:207],v244,s[52:55],s10 offen offset:3072 sc1\n"
M_"v[180:183],v[28:31],a[124:127],v[180:183]\n"
W_"lgkmcnt(15)\n"
"v_accvgpr_write_b32 a187,v143\n"
"v_accvgpr_write_b32 a188,v172\n"
M_"v[80:83],v[32:35],a[64:67],0\n"
A_"s10,s15,8192\n"
T_"v[16:17],v193 offset:16384\n"
"v_accvgpr_write_b32 a189,v173\n"
M_"v[80:83],v[36:39],a[68:71],v[80:83]\n"
A_"s10,s10,s17\n"
T_"v[18:19],v193 offset:16896\n"
"v_accvgpr_write_b32 a190,v174\n"
M_"v[80:83],v[40:43],a[72:75],v[80:83]\n"
B_"a[208:211],v244,s[52:55],s10 offen offset:0 sc1\n"
M_"v[80:83],v[44:47],a[76:79],v[80:83]\n"
T_"v[20:21],v193 offset:20480\n"
"v_accvgpr_write_b32 a191,v175\n"
M_"v[84:87],v[32:35],a[80:83],0\n"
M_"v[84:87],v[36:39],a[84:87],v[84:87]\n"
T_"v[22:23],v193 offset:20992\n"
M_"v[84:87],v[40:43],a[88:91],v[84:87]\n"
B_"a[212:215],v244,s[52:55],s10 offen offset:1024 sc1\n"
M_"v[84:87],v[44:47],a[92:95],v[84:87]\n"
T_"v[24:25],v193 offset:24576\n"
M_"v[152:155],v[32:35],a[96:99],0\n"
T_"v[26:27],v193 offset:25088\n"
M_"v[152:155],v[36:39],a[100:103],v[152:155]\n"
M_"v[152:155],v[40:43],a[104:107],v[152:155]\n"
B_"a[216:219],v244,s[52:55],s10 offen offset:2048 sc1\n"
M_"v[152:155],v[44:47],a[108:111],v[152:155]\n"
T_"v[28:29],v193 offset:28672\n"
M_"v[184:187],v[32:35],a[112:115],0\n"
T_"v[30:31],v193 offset:29184\n"
M_"v[184:187],v[36:39],a[116:119],v[184:187]\n"
M_"v[184:187],v[40:43],a[120:123],v[184:187]\n"
B_"a[220:223],v244,s[52:55],s10 offen offset:3072 sc1\n"
M_"v[184:187],v[44:47],a[124:127],v[184:187]\n"
W_"lgkmcnt(15)\n"
M_"v[88:91],v[48:51],a[64:67],0\n"
A_"s10,s14,12288\n"
T_"v[32:33],v194 offset:16384\n"
M_"v[88:91],v[52:55],a[68:71],v[88:91]\n"
T_"v[34:35],v194 offset:16896\n"
M_"v[88:91],v[56:59],a[72:75],v[88:91]\n"
B_"a[224:227],v244,s[52:55],s10 offen offset:0 sc1\n"
M_"v[88:91],v[60:63],a[76:79],v[88:91]\n"
T_"v[36:37],v194 offset:20480\n"
M_"v[92:95],v[48:51],a[80:83],0\n"
M_"v[92:95],v[52:55],a[84:87],v[92:95]\n"
T_"v[38:39],v194 offset:20992\n"
M_"v[92:95],v[56:59],a[88:91],v[92:95]\n"
M_"v[92:95],v[60:63],a[92:95],v[92:95]\n"
T_"v[40:41],v194 offset:24576\n"
M_"v[156:159],v[48:51],a[96:99],0\n"
B_"a[228:231],v244,s[52:55],s10 offen offset:1024 sc1\n"
M_"v[156:159],v[52:55],a[100:103],v[156:159]\n"
A_"s10,s15,12288\n"
T_"v[42:43],v194 offset:25088\n"
M_"v[156:159],v[56:59],a[104:107],v[156:159]\n"
T_"v[44:45],v194 offset:28672\n"
M_"v[156:159],v[60:63],a[108:111],v[156:159]\n"
M_"v[188:191],v[48:51],a[112:115],0\n"
B_"a[232:235],v244,s[52:55],s10 offen offset:0 sc1\n"
M_"v[188:191],v[52:55],a[116:119],v[188:191]\n"
T_"v[46:47],v194 offset:29184\n"
M_"v[188:191],v[56:59],a[120:123],v[188:191]\n"
M_"v[188:191],v[60:63],a[124:127],v[188:191]\n"
W_"lgkmcnt(15)\n"
W_"vmcnt(23)\n"
M_"v[64:67],v[0:3],a[0:3],v[64:67]\n"
B_"a[236:239],v244,s[52:55],s10 offen offset:1024 sc1\n"
M_"v[64:67],v[4:7],a[4:7],v[64:67]\n"
A_"s10,s14,8192\n"
T_"v[48:49],v195 offset:16384\n"
T_"v[50:51],v195 offset:16896\n"
M_"v[64:67],v[8:11],a[8:11],v[64:67]\n"
M_"v[64:67],v[12:15],a[12:15],v[64:67]\n"
W_"vmcnt(20)\n"
T_"v[52:53],v195 offset:20480\n"
M_"v[68:71],v[0:3],a[16:19],v[68:71]\n"
B_"a[96:99],v244,s[52:55],s10 offen offset:0 sc1\n"
M_"v[68:71],v[4:7],a[20:23],v[68:71]\n"
T_"v[54:55],v195 offset:20992\n"
M_"v[68:71],v[8:11],a[24:27],v[68:71]\n"
M_"v[68:71],v[12:15],a[28:31],v[68:71]\n"
W_"vmcnt(17)\n"
T_"v[56:57],v195 offset:24576\n"
M_"v[144:147],v[0:3],a[32:35],v[144:147]\n"
B_"a[100:103],v244,s[52:55],s10 offen offset:1024 sc1\n"
M_"v[144:147],v[4:7],a[36:39],v[144:147]\n"
T_"v[58:59],v195 offset:25088\n"
M_"v[144:147],v[8:11],a[40:43],v[144:147]\n"
T_"v[60:61],v195 offset:28672\n"
M_"v[144:147],v[12:15],a[44:47],v[144:147]\n"
W_"vmcnt(14)\n"
M_"v[176:179],v[0:3],a[48:51],v[176:179]\n"
B_"a[104:107],v244,s[52:55],s10 offen offset:2048 sc1\n"
M_"v[176:179],v[4:7],a[52:55],v[176:179]\n"
T_"v[62:63],v195 offset:29184\n"
M_"v[176:179],v[8:11],a[56:59],v[176:179]\n"
M_"v[176:179],v[12:15],a[60:63],v[176:179]\n"
W_"lgkmcnt(15)\n"
M_"v[72:75],v[16:19],a[0:3],v[72:75]\n"
B_"a[108:111],v244,s[52:55],s10 offen offset:3072 sc1\n"
M_"v[72:75],v[20:23],a[4:7],v[72:75]\n"
A_"s10,s15,8192\n"
T_"v[0:1],v192 offset:32768\n"
T_"v[2:3],v192 offset:33280\n"
M_"v[72:75],v[24:27],a[8:11],v[72:75]\n"
M_"v[72:75],v[28:31],a[12:15],v[72:75]\n"
T_"v[4:5],v192 offset:36864\n"
M_"v[76:79],v[16:19],a[16:19],v[76:79]\n"
B_"a[112:115],v244,s[52:55],s10 offen offset:0 sc1\n"
M_"v[76:79],v[20:23],a[20:23],v[76:79]\n"
T_"v[6:7],v192 offset:37376\n"
M_"v[76:79],v[24:27],a[24:27],v[76:79]\n"
M_"v[76:79],v[28:31],a[28:31],v[76:79]\n"
T_"v[8:9],v192 offset:40960\n"
M_"v[148:151],v[16:19],a[32:35],v[148:151]\n"
B_"a[116:119],v244,s[52:55],s10 offen offset:1024 sc1\n"
M_"v[148:151],v[20:23],a[36:39],v[148:151]\n"
T_"v[10:11],v192 offset:41472\n"
M_"v[148:151],v[24:27],a[40:43],v[148:151]\n"
T_"v[12:13],v192 offset:45056\n"
M_"v[148:151],v[28:31],a[44:47],v[148:151]\n"
M_"v[180:183],v[16:19],a[48:51],v[180:183]\n"
B_"a[120:123],v244,s[52:55],s10 offen offset:2048 sc1\n"
M_"v[180:183],v[20:23],a[52:55],v[180:183]\n"
T_"v[14:15],v192 offset:45568\n"
M_"v[180:183],v[24:27],a[56:59],v[180:183]\n"
M_"v[180:183],v[28:31],a[60:63],v[180:183]\n"
W_"lgkmcnt(15)\n"
M_"v[80:83],v[32:35],a[0:3],v[80:83]\n"
B_"a[124:127],v244,s[52:55],s10 offen offset:3072 sc1\n"
M_"v[80:83],v[36:39],a[4:7],v[80:83]\n"
A_"s10,s14,12288\n"
T_"v[16:17],v193 offset:32768\n"
T_"v[18:19],v193 offset:33280\n"
M_"v[80:83],v[40:43],a[8:11],v[80:83]\n"
A_"s10,s10,s17\n"
M_"v[80:83],v[44:47],a[12:15],v[80:83]\n"
T_"v[20:21],v193 offset:36864\n"
M_"v[84:87],v[32:35],a[16:19],v[84:87]\n"
B_"a[240:243],v244,s[52:55],s10 offen offset:0 sc1\n"
M_"v[84:87],v[36:39],a[20:23],v[84:87]\n"
T_"v[22:23],v193 offset:37376\n"
M_"v[84:87],v[40:43],a[24:27],v[84:87]\n"
M_"v[84:87],v[44:47],a[28:31],v[84:87]\n"
T_"v[24:25],v193 offset:40960\n"
M_"v[152:155],v[32:35],a[32:35],v[152:155]\n"
T_"v[26:27],v193 offset:41472\n"
M_"v[152:155],v[36:39],a[36:39],v[152:155]\n"
B_"a[244:247],v244,s[52:55],s10 offen offset:1024 sc1\n"
M_"v[152:155],v[40:43],a[40:43],v[152:155]\n"
A_"s10,s15,12288\n"
T_"v[28:29],v193 offset:45056\n"
A_"s10,s10,s17\n"
M_"v[152:155],v[44:47],a[44:47],v[152:155]\n"
M_"v[184:187],v[32:35],a[48:51],v[184:187]\n"
T_"v[30:31],v193 offset:45568\n"
M_"v[184:187],v[36:39],a[52:55],v[184:187]\n"
B_"a[248:251],v244,s[52:55],s10 offen offset:0 sc1\n"
M_"v[184:187],v[40:43],a[56:59],v[184:187]\n"
M_"v[184:187],v[44:47],a[60:63],v[184:187]\n"
W_"lgkmcnt(15)\n"
M_"v[88:91],v[48:51],a[0:3],v[88:91]\n"
T_"v[32:33],v194 offset:32768\n"
M_"v[88:91],v[52:55],a[4:7],v[88:91]\n"
B_"a[252:255],v244,s[52:55],s10 offen offset:1024 sc1\n"
M_"v[88:91],v[56:59],a[8:11],v[88:91]\n"
T_"v[34:35],v194 offset:33280\n"
M_"v[88:91],v[60:63],a[12:15],v[88:91]\n"
T_"v[36:37],v194 offset:36864\n"
M_"v[92:95],v[48:51],a[16:19],v[92:95]\n"
M_"v[92:95],v[52:55],a[20:23],v[92:95]\n"
T_"v[38:39],v194 offset:37376\n"
M_"v[92:95],v[56:59],a[24:27],v[92:95]\n"
A_"s10,s56,0\n"
M_"v[92:95],v[60:63],a[28:31],v[92:95]\n"
A_"m0,s56,65536\n"
T_"v[40:41],v194 offset:40960\n"
M_"v[156:159],v[48:51],a[32:35],v[156:159]\n"
B_"v244,s[40:43],s10 offen offset:0 lds\n"
M_"v[156:159],v[52:55],a[36:39],v[156:159]\n"
T_"v[42:43],v194 offset:41472\n"
M_"v[156:159],v[56:59],a[40:43],v[156:159]\n"
T_"v[44:45],v194 offset:45056\n"
M_"v[156:159],v[60:63],a[44:47],v[156:159]\n"
M_"v[188:191],v[48:51],a[48:51],v[188:191]\n"
B_"v244,s[40:43],s10 offen offset:1024 lds\n"
M_"v[188:191],v[52:55],a[52:55],v[188:191]\n"
T_"v[46:47],v194 offset:45568\n"
M_"v[188:191],v[56:59],a[56:59],v[188:191]\n"
M_"v[188:191],v[60:63],a[60:63],v[188:191]\n"
W_"lgkmcnt(15)\n"
W_"vmcnt(22)\n"
M_"v[144:147],v[0:3],a[192:195],v[144:147]\n"
B_"v244,s[40:43],s10 offen offset:2048 lds\n"
M_"v[144:147],v[4:7],a[196:199],v[144:147]\n"
"s_mov_b64 s[48:49],s[18:19]\n"
T_"v[48:49],v195 offset:32768\n"
T_"v[50:51],v195 offset:33280\n"
M_"v[144:147],v[8:11],a[200:203],v[144:147]\n"
"s_mov_b32 s50,0x400000\n"
"s_mov_b32 s51,0x20000\n"
"s_and_b32 s13,s30,6\n"
M_"v[144:147],v[12:15],a[204:207],v[144:147]\n"
W_"vmcnt(19)\n"
T_"v[52:53],v195 offset:36864\n"
"s_lshl1_add_u32 s13,s13,s31\n"
M_"v[176:179],v[0:3],a[208:211],v[176:179]\n"
B_"v244,s[40:43],s10 offen offset:3072 lds\n"
M_"v[176:179],v[4:7],a[212:215],v[176:179]\n"
"s_and_b32 s15,s34,1\n"
T_"v[54:55],v195 offset:37376\n"
"s_lshl1_add_u32 s13,s13,s15\n"
M_"v[176:179],v[8:11],a[216:219],v[176:179]\n"
"s_lshl_b32 s14,s13,17\n"
"s_lshl_b32 s21,s13,12\n"
A_"s10,s56,16384\n"
M_"v[176:179],v[12:15],a[220:223],v[176:179]\n"
W_"vmcnt(12)\n"
T_"v[56:57],v195 offset:40960\n"
"s_bfe_u32 s13,s30,0x20003\n"
M_"v[64:67],v[0:3],a[96:99],v[64:67]\n"
"s_lshl2_add_u32 s18,s13,s31\n"
T_"v[58:59],v195 offset:41472\n"
A_"m0,s56,98304\n"
M_"v[64:67],v[4:7],a[100:103],v[64:67]\n"
B_"v244,s[40:43],s10 offen offset:0 lds\n"
M_"v[64:67],v[8:11],a[104:107],v[64:67]\n"
"s_lshr_b32 s13,s18,1\n"
T_"v[60:61],v195 offset:45056\n"
"s_lshl_b32 s13,s13,7\n"
M_"v[64:67],v[12:15],a[108:111],v[64:67]\n"
W_"vmcnt(9)\n"
A_"s14,s14,s13\n"
"s_andn2_b32 s13,1,s34\n"
M_"v[68:71],v[0:3],a[112:115],v[68:71]\n"
"s_lshl4_add_u32 s13,s13,s31\n"
T_"v[62:63],v195 offset:45568\n"
"s_lshl_b32 s16,s13,12\n"
M_"v[68:71],v[4:7],a[116:119],v[68:71]\n"
B_"v244,s[40:43],s10 offen offset:1024 lds\n"
M_"v[68:71],v[8:11],a[120:123],v[68:71]\n"
"s_lshl_b32 s13,s30,7\n"
A_"s17,s13,0x4f10000\n"
A_"s12,s17,4\n"
M_"v[68:71],v[12:15],a[124:127],v[68:71]\n"
W_"lgkmcnt(15)\n"
"s_andn2_b32 s20,s17,0xfff\n"
M_"v[148:151],v[16:19],a[192:195],v[148:151]\n"
"s_cmp_eq_u32 s31,0\n"
"s_cselect_b32 s17,s17,0x7f000000\n"
T_"v[0:1],v192 offset:49152\n"
M_"v[148:151],v[20:23],a[196:199],v[148:151]\n"
B_"v244,s[40:43],s10 offen offset:2048 lds\n"
M_"v[148:151],v[24:27],a[200:203],v[148:151]\n"
"s_mov_b32 s61,s17\n"
"s_barrier\n"
M_"v[148:151],v[28:31],a[204:207],v[148:151]\n"
"s_and_b32 s13,s30,1\n"
T_"v[2:3],v192 offset:49664\n"
T_"v[4:5],v192 offset:53248\n"
M_"v[180:183],v[16:19],a[208:211],v[180:183]\n"
"s_lshl1_add_u32 s13,s34,s13\n"
"s_bfe_u32 s15,s30,0x20001\n"
"s_lshl2_add_u32 s13,s13,s15\n"
M_"v[180:183],v[20:23],a[212:215],v[180:183]\n"
B_"v244,s[40:43],s10 offen offset:3072 lds\n"
M_"v[180:183],v[24:27],a[216:219],v[180:183]\n"
"s_lshl4_add_u32 s18,s13,s18\n"
T_"v[6:7],v192 offset:53760\n"
"s_lshl_b32 s18,s18,10\n"
M_"v[180:183],v[28:31],a[220:223],v[180:183]\n"
A_"s18,s18,0x200000\n"
T_"v[8:9],v193 offset:49152\n"
"s_lshl_b32 s19,s34,18\n"
M_"v[72:75],v[16:19],a[96:99],v[72:75]\n"
"s_sub_u32 s19,0x1e0000,s19\n"
T_"v[10:11],v193 offset:49664\n"
A_"s10,s56,32768\n"
M_"v[72:75],v[20:23],a[100:103],v[72:75]\n"
"s_lshl_b32 s13,s13,17\n"
A_"s21,s21,s13\n"
A_"m0,s56,0\n"
M_"v[72:75],v[24:27],a[104:107],v[72:75]\n"
B_"v244,s[40:43],s10 offen offset:0 lds\n"
M_"v[72:75],v[28:31],a[108:111],v[72:75]\n"
A_"s21,s21,0x3a00000\n"
T_"v[12:13],v193 offset:53248\n"
"s_xor_b32 s22,s21,0x40000\n"
M_"v[76:79],v[16:19],a[112:115],v[76:79]\n"
"s_xor_b32 s23,s21,0x60000\n"
T_"v[14:15],v193 offset:53760\n"
"s_xor_b32 s21,s21,0x20000\n"
M_"v[76:79],v[20:23],a[116:119],v[76:79]\n"
"s_mov_b32 s24,1\n"
"v_lshrrev_b32 v197,6,v244\n"
"v_lshlrev_b32 v255,7,v197\n"
M_"v[76:79],v[24:27],a[120:123],v[76:79]\n"
B_"v244,s[40:43],s10 offen offset:1024 lds\n"
M_"v[76:79],v[28:31],a[124:127],v[76:79]\n"
W_"lgkmcnt(15)\n"
"s_lshl_b32 s13,s31,1\n"
"v_lshrrev_b32 v197,7,v244\n"
M_"v[152:155],v[32:35],a[192:195],v[152:155]\n"
"v_xor_b32 v198,s13,v197\n"
"v_and_b32 v198,3,v198\n"
M_"v[152:155],v[36:39],a[196:199],v[152:155]\n"
"v_lshl_add_u32 v255,v198,5,v255\n"
"v_bfe_u32 v198,v244,4,2\n"
M_"v[152:155],v[40:43],a[200:203],v[152:155]\n"
B_"v244,s[40:43],s10 offen offset:2048 lds\n"
M_"v[152:155],v[44:47],a[204:207],v[152:155]\n"
"v_lshl_add_u32 v255,v198,3,v255\n"
T_"v[16:17],v194 offset:49152\n"
M_"v[184:187],v[32:35],a[208:211],v[184:187]\n"
A_"s13,s31,32\n"
T_"v[18:19],v194 offset:49664\n"
T_"v[20:21],v194 offset:53248\n"
M_"v[184:187],v[36:39],a[212:215],v[184:187]\n"
"s_lshl_b32 s13,s13,12\n"
"v_add_u32 v255,s13,v255\n"
"v_lshlrev_b32 v196,10,v197\n"
M_"v[184:187],v[40:43],a[216:219],v[184:187]\n"
B_"v244,s[40:43],s10 offen offset:3072 lds\n"
M_"v[184:187],v[44:47],a[220:223],v[184:187]\n"
"v_and_b32 v197,6,v197\n"
"v_bfe_u32 v198,v244,4,3\n"
M_"v[80:83],v[32:35],a[96:99],v[80:83]\n"
"v_xor_b32 v197,v197,v198\n"
"v_lshl_add_u32 v196,v197,4,v196\n"
"v_add_u32 v197,7168,v196\n"
"v_add_u32 v198,14336,v196\n"
"v_add_u32 v199,21504,v196\n"
M_"v[80:83],v[36:39],a[100:103],v[80:83]\n"
T_"v[22:23],v194 offset:53760\n"
T_"v[24:25],v195 offset:49152\n"
M_"v[80:83],v[40:43],a[104:107],v[80:83]\n"
"s_xor_b32 s15,s14,0\n"
T_"v[26:27],v195 offset:49664\n"
T_"v[28:29],v195 offset:53248\n"
M_"v[80:83],v[44:47],a[108:111],v[80:83]\n"
"s_and_b32 s13,s16,0xffff\n"
M_"v[84:87],v[32:35],a[112:115],v[84:87]\n"
A_"m0,s13,131072\n"
T_"v[30:31],v195 offset:53760\n"
M_"v[84:87],v[36:39],a[116:119],v[84:87]\n"
B_"v196,s[48:51],s15 offen offset:0  lds\n"
M_"v[84:87],v[40:43],a[120:123],v[84:87]\n"
M_"v[84:87],v[44:47],a[124:127],v[84:87]\n"
W_"lgkmcnt(15)\n"
M_"v[156:159],v[48:51],a[192:195],v[156:159]\n"
T_"v[32:33],v192 offset:57344\n"
M_"v[156:159],v[52:55],a[196:199],v[156:159]\n"
B_"v197,s[48:51],s15 offen offset:1024  lds\n"
M_"v[156:159],v[56:59],a[200:203],v[156:159]\n"
T_"v[34:35],v192 offset:57856\n"
M_"v[156:159],v[60:63],a[204:207],v[156:159]\n"
T_"v[36:37],v192 offset:61440\n"
M_"v[188:191],v[48:51],a[208:211],v[188:191]\n"
M_"v[188:191],v[52:55],a[212:215],v[188:191]\n"
B_"v198,s[48:51],s15 offen offset:2048  lds\n"
M_"v[188:191],v[56:59],a[216:219],v[188:191]\n"
T_"v[38:39],v192 offset:61952\n"
M_"v[188:191],v[60:63],a[220:223],v[188:191]\n"
T_"v[40:41],v193 offset:57344\n"
M_"v[88:91],v[48:51],a[96:99],v[88:91]\n"
T_"v[42:43],v193 offset:57856\n"
M_"v[88:91],v[52:55],a[100:103],v[88:91]\n"
B_"v199,s[48:51],s15 offen offset:3072  lds\n"
M_"v[88:91],v[56:59],a[104:107],v[88:91]\n"
"s_xor_b32 s15,s14,128\n"
T_"v[44:45],v193 offset:61440\n"
M_"v[88:91],v[60:63],a[108:111],v[88:91]\n"
"s_and_b32 s13,s16,0xffff\n"
M_"v[92:95],v[48:51],a[112:115],v[92:95]\n"
A_"m0,s13,147456\n"
T_"v[46:47],v193 offset:61952\n"
M_"v[92:95],v[52:55],a[116:119],v[92:95]\n"
B_"v196,s[48:51],s15 offen offset:0  lds\n"
M_"v[92:95],v[56:59],a[120:123],v[92:95]\n"
M_"v[92:95],v[60:63],a[124:127],v[92:95]\n"
W_"lgkmcnt(15)\n"
M_"v[64:67],v[0:3],a[224:227],v[64:67]\n"
T_"v[48:49],v194 offset:57344\n"
M_"v[64:67],v[4:7],a[228:231],v[64:67]\n"
T_"v[50:51],v194 offset:57856\n"
M_"v[68:71],v[0:3],a[232:235],v[68:71]\n"
B_"v197,s[48:51],s15 offen offset:1024  lds\n"
M_"v[68:71],v[4:7],a[236:239],v[68:71]\n"
W_"vmcnt(20)\n"
T_"v[52:53],v194 offset:61440\n"
M_"v[144:147],v[0:3],a[240:243],v[144:147]\n"
M_"v[144:147],v[4:7],a[244:247],v[144:147]\n"
W_"vmcnt(18)\n"
T_"v[54:55],v194 offset:61952\n"
M_"v[176:179],v[0:3],a[248:251],v[176:179]\n"
B_"v198,s[48:51],s15 offen offset:2048  lds\n"
M_"v[176:179],v[4:7],a[252:255],v[176:179]\n"
T_"v[56:57],v195 offset:57344\n"
M_"v[72:75],v[8:11],a[224:227],v[72:75]\n"
T_"v[58:59],v195 offset:57856\n"
M_"v[72:75],v[12:15],a[228:231],v[72:75]\n"
M_"v[76:79],v[8:11],a[232:235],v[76:79]\n"
B_"v199,s[48:51],s15 offen offset:3072  lds\n"
M_"v[76:79],v[12:15],a[236:239],v[76:79]\n"
T_"v[60:61],v195 offset:61440\n"
M_"v[148:151],v[8:11],a[240:243],v[148:151]\n"
C_"v64,v64,v65\n"
C_"v65,v66,v67\n"
M_"v[148:151],v[12:15],a[244:247],v[148:151]\n"
T_"v[62:63],v195 offset:61952\n"
C_"v66,v72,v73\n"
M_"v[180:183],v[8:11],a[248:251],v[180:183]\n"
C_"v67,v74,v75\n"
M_"v[180:183],v[12:15],a[252:255],v[180:183]\n"
W_"lgkmcnt(15)\n"
M_"v[80:83],v[16:19],a[224:227],v[80:83]\n"
S_"v[64:67],v244,s[52:55],s36 offen offset:0 sc1\n"
M_"v[80:83],v[20:23],a[228:231],v[80:83]\n"
"v_xor_b32 v192,0x10000,v192\n"
"v_xor_b32 v193,0x10000,v193\n"
M_"v[84:87],v[16:19],a[232:235],v[84:87]\n"
"v_xor_b32 v194,0x10000,v194\n"
M_"v[84:87],v[20:23],a[236:239],v[84:87]\n"
"v_xor_b32 v195,0x10000,v195\n"
M_"v[152:155],v[16:19],a[240:243],v[152:155]\n"
T_"v[0:1],v192 offset:49152\n"
M_"v[152:155],v[20:23],a[244:247],v[152:155]\n"
T_"v[2:3],v192 offset:49664\n"
M_"v[184:187],v[16:19],a[248:251],v[184:187]\n"
M_"v[184:187],v[20:23],a[252:255],v[184:187]\n"
C_"v68,v68,v69\n"
C_"v69,v70,v71\n"
M_"v[88:91],v[24:27],a[224:227],v[88:91]\n"
T_"v[4:5],v192 offset:53248\n"
C_"v70,v76,v77\n"
M_"v[88:91],v[28:31],a[228:231],v[88:91]\n"
T_"v[6:7],v192 offset:53760\n"
C_"v71,v78,v79\n"
M_"v[92:95],v[24:27],a[232:235],v[92:95]\n"
S_"v[68:71],v244,s[52:55],s37 offen offset:0 sc1\n"
M_"v[92:95],v[28:31],a[236:239],v[92:95]\n"
T_"v[8:9],v192 offset:57344\n"
T_"v[10:11],v192 offset:57856\n"
M_"v[156:159],v[24:27],a[240:243],v[156:159]\n"
T_"v[12:13],v192 offset:61440\n"
T_"v[14:15],v192 offset:61952\n"
M_"v[156:159],v[28:31],a[244:247],v[156:159]\n"
M_"v[188:191],v[24:27],a[248:251],v[188:191]\n"
M_"v[188:191],v[28:31],a[252:255],v[188:191]\n"
W_"lgkmcnt(15)\n"
M_"v[96:99],v[32:35],a[224:227],v[96:99]\n"
T_"v[16:17],v193 offset:49152\n"
M_"v[96:99],v[36:39],a[228:231],v[96:99]\n"
T_"v[18:19],v193 offset:49664\n"
M_"v[100:103],v[32:35],a[232:235],v[100:103]\n"
C_"v144,v144,v145\n"
C_"v145,v146,v147\n"
M_"v[100:103],v[36:39],a[236:239],v[100:103]\n"
C_"v146,v148,v149\n"
T_"v[20:21],v193 offset:53248\n"
M_"v[104:107],v[40:43],a[224:227],v[104:107]\n"
C_"v147,v150,v151\n"
T_"v[22:23],v193 offset:53760\n"
M_"v[104:107],v[44:47],a[228:231],v[104:107]\n"
S_"v[144:147],v244,s[52:55],s38 offen offset:0 sc1\n"
M_"v[108:111],v[40:43],a[232:235],v[108:111]\n"
T_"v[24:25],v193 offset:57344\n"
M_"v[108:111],v[44:47],a[236:239],v[108:111]\n"
W_"lgkmcnt(13)\n"
M_"v[112:115],v[48:51],a[224:227],v[112:115]\n"
W_"vmcnt(15)\n"
"s_barrier\n"
M_"v[112:115],v[52:55],a[228:231],v[112:115]\n"
T_"v[26:27],v193 offset:57856\n"
T_"v[28:29],v193 offset:61440\n"
M_"v[116:119],v[48:51],a[232:235],v[116:119]\n"
M_"v[116:119],v[52:55],a[236:239],v[116:119]\n"
T_"v[30:31],v193 offset:61952\n"
T_"v[32:33],v194 offset:49152\n"
M_"v[120:123],v[56:59],a[224:227],v[120:123]\n"
T_"v[34:35],v194 offset:49664\n"
T_"v[36:37],v194 offset:53248\n"
M_"v[120:123],v[60:63],a[228:231],v[120:123]\n"
T_"v[38:39],v194 offset:53760\n"
C_"v176,v176,v177\n"
M_"v[124:127],v[56:59],a[232:235],v[124:127]\n"
C_"v177,v178,v179\n"
C_"v178,v180,v181\n"
M_"v[124:127],v[60:63],a[236:239],v[124:127]\n"
W_"lgkmcnt(12)\n"
T_"v[40:41],v194 offset:57344\n"
C_"v179,v182,v183\n"
M_"v[96:99],v[0:3],a[0:3],v[96:99]\n"
S_"v[176:179],v244,s[52:55],s39 offen offset:0 sc1\n"
M_"v[96:99],v[4:7],a[4:7],v[96:99]\n"
A_"s10,s56,0\n"
T_"v[42:43],v194 offset:57856\n"
T_"v[44:45],v194 offset:61440\n"
M_"v[96:99],v[8:11],a[8:11],v[96:99]\n"
A_"m0,s56,49152\n"
D_"a[192:195],v245 offset:0\n"
M_"v[96:99],v[12:15],a[12:15],v[96:99]\n"
T_"v[46:47],v194 offset:61952\n"
T_"v[48:49],v195 offset:49152\n"
M_"v[100:103],v[0:3],a[16:19],v[100:103]\n"
B_"v244,s[44:47],s10 offen offset:0 lds\n"
M_"v[100:103],v[4:7],a[20:23],v[100:103]\n"
T_"v[50:51],v195 offset:49664\n"
T_"v[52:53],v195 offset:53248\n"
M_"v[100:103],v[8:11],a[24:27],v[100:103]\n"
T_"v[54:55],v195 offset:53760\n"
T_"v[56:57],v195 offset:57344\n"
M_"v[100:103],v[12:15],a[28:31],v[100:103]\n"
W_"lgkmcnt(14)\n"
D_"a[196:199],v245 offset:1024\n"
D_"a[200:203],v245 offset:2048\n"
M_"v[104:107],v[16:19],a[0:3],v[104:107]\n"
B_"v244,s[44:47],s10 offen offset:1024 lds\n"
M_"v[104:107],v[20:23],a[4:7],v[104:107]\n"
T_"v[58:59],v195 offset:57856\n"
T_"v[60:61],v195 offset:61440\n"
M_"v[104:107],v[24:27],a[8:11],v[104:107]\n"
D_"a[204:207],v245 offset:3072\n"
M_"v[104:107],v[28:31],a[12:15],v[104:107]\n"
T_"v[62:63],v195 offset:61952\n"
T_"v[0:1],v192 offset:16384\n"
M_"v[108:111],v[16:19],a[16:19],v[108:111]\n"
B_"v244,s[44:47],s10 offen offset:2048 lds\n"
M_"v[108:111],v[20:23],a[20:23],v[108:111]\n"
T_"v[2:3],v192 offset:16896\n"
T_"v[4:5],v192 offset:20480\n"
M_"v[108:111],v[24:27],a[24:27],v[108:111]\n"
T_"v[6:7],v192 offset:20992\n"
T_"v[8:9],v192 offset:24576\n"
M_"v[108:111],v[28:31],a[28:31],v[108:111]\n"
W_"lgkmcnt(15)\n"
D_"a[208:211],v245 offset:4096\n"
D_"a[212:215],v245 offset:5120\n"
M_"v[112:115],v[32:35],a[0:3],v[112:115]\n"
B_"v244,s[44:47],s10 offen offset:3072 lds\n"
M_"v[112:115],v[36:39],a[4:7],v[112:115]\n"
T_"v[10:11],v192 offset:25088\n"
T_"v[12:13],v192 offset:28672\n"
M_"v[112:115],v[40:43],a[8:11],v[112:115]\n"
C_"v80,v80,v81\n"
C_"v81,v82,v83\n"
M_"v[112:115],v[44:47],a[12:15],v[112:115]\n"
T_"v[14:15],v192 offset:29184\n"
T_"v[16:17],v193 offset:16384\n"
M_"v[116:119],v[32:35],a[16:19],v[116:119]\n"
C_"v82,v88,v89\n"
T_"v[18:19],v193 offset:16896\n"
M_"v[116:119],v[36:39],a[20:23],v[116:119]\n"
C_"v83,v90,v91\n"
T_"v[20:21],v193 offset:20480\n"
M_"v[116:119],v[40:43],a[24:27],v[116:119]\n"
S_"v[80:83],v244,s[52:55],s36 offen offset:1024 sc1\n"
M_"v[116:119],v[44:47],a[28:31],v[116:119]\n"
W_"lgkmcnt(13)\n"
T_"v[22:23],v193 offset:20992\n"
D_"a[216:219],v245 offset:6144\n"
M_"v[120:123],v[48:51],a[0:3],v[120:123]\n"
T_"v[24:25],v193 offset:24576\n"
T_"v[26:27],v193 offset:25088\n"
M_"v[120:123],v[52:55],a[4:7],v[120:123]\n"
T_"v[28:29],v193 offset:28672\n"
D_"a[220:223],v245 offset:7168\n"
M_"v[120:123],v[56:59],a[8:11],v[120:123]\n"
D_"a[224:227],v245 offset:8192\n"
M_"v[120:123],v[60:63],a[12:15],v[120:123]\n"
T_"v[30:31],v193 offset:29184\n"
T_"v[32:33],v194 offset:16384\n"
M_"v[124:127],v[48:51],a[16:19],v[124:127]\n"
T_"v[34:35],v194 offset:16896\n"
C_"v84,v84,v85\n"
M_"v[124:127],v[52:55],a[20:23],v[124:127]\n"
C_"v85,v86,v87\n"
T_"v[36:37],v194 offset:20480\n"
M_"v[124:127],v[56:59],a[24:27],v[124:127]\n"
C_"v86,v92,v93\n"
T_"v[38:39],v194 offset:20992\n"
M_"v[124:127],v[60:63],a[28:31],v[124:127]\n"
W_"lgkmcnt(15)\n"
C_"v87,v94,v95\n"
T_"v[40:41],v194 offset:24576\n"
M_"v[96:99],v[0:3],a[96:99],v[96:99]\n"
S_"v[84:87],v244,s[52:55],s37 offen offset:1024 sc1\n"
M_"v[96:99],v[4:7],a[100:103],v[96:99]\n"
A_"s10,s56,49152\n"
T_"v[42:43],v194 offset:25088\n"
T_"v[44:45],v194 offset:28672\n"
M_"v[96:99],v[8:11],a[104:107],v[96:99]\n"
A_"m0,s56,32768\n"
D_"a[228:231],v245 offset:9216\n"
D_"a[232:235],v245 offset:10240\n"
M_"v[96:99],v[12:15],a[108:111],v[96:99]\n"
T_"v[46:47],v194 offset:29184\n"
T_"v[48:49],v195 offset:16384\n"
M_"v[100:103],v[0:3],a[112:115],v[100:103]\n"
B_"v244,s[40:43],s10 offen offset:0 lds\n"
M_"v[100:103],v[4:7],a[116:119],v[100:103]\n"
T_"v[50:51],v195 offset:16896\n"
T_"v[52:53],v195 offset:20480\n"
M_"v[100:103],v[8:11],a[120:123],v[100:103]\n"
T_"v[54:55],v195 offset:20992\n"
D_"a[236:239],v245 offset:11264\n"
M_"v[100:103],v[12:15],a[124:127],v[100:103]\n"
W_"lgkmcnt(15)\n"
C_"v152,v152,v153\n"
C_"v153,v154,v155\n"
M_"v[104:107],v[16:19],a[96:99],v[104:107]\n"
B_"v244,s[40:43],s10 offen offset:1024 lds\n"
M_"v[104:107],v[20:23],a[100:103],v[104:107]\n"
T_"v[56:57],v195 offset:24576\n"
C_"v154,v156,v157\n"
M_"v[104:107],v[24:27],a[104:107],v[104:107]\n"
T_"v[58:59],v195 offset:25088\n"
T_"v[60:61],v195 offset:28672\n"
M_"v[104:107],v[28:31],a[108:111],v[104:107]\n"
T_"v[62:63],v195 offset:29184\n"
C_"v155,v158,v159\n"
M_"v[108:111],v[16:19],a[112:115],v[108:111]\n"
B_"v244,s[40:43],s10 offen offset:2048 lds\n"
M_"v[108:111],v[20:23],a[116:119],v[108:111]\n"
D_"a[240:243],v245 offset:12288\n"
D_"a[244:247],v245 offset:13312\n"
M_"v[108:111],v[24:27],a[120:123],v[108:111]\n"
D_"a[248:251],v245 offset:14336\n"
D_"a[252:255],v245 offset:15360\n"
M_"v[108:111],v[28:31],a[124:127],v[108:111]\n"
W_"lgkmcnt(13)\n"
B_"v244,s[40:43],s10 offen offset:3072 lds\n"
S_"v[152:155],v244,s[52:55],s38 offen offset:1024 sc1\n"
M_"v[112:115],v[32:35],a[96:99],v[112:115]\n"
A_"s40,s40,0x10000\n"
"s_addc_u32 s41,s41,0\n"
"s_mov_b32 s42,0x70000\n"
M_"v[112:115],v[36:39],a[100:103],v[112:115]\n"
A_"s44,s44,0x4000\n"
"s_addc_u32 s45,s45,0\n"
C_"v184,v184,v185\n"
M_"v[112:115],v[40:43],a[104:107],v[112:115]\n"
"s_mov_b32 s46,0x7c000\n"
C_"v185,v186,v187\n"
C_"v186,v188,v189\n"
M_"v[112:115],v[44:47],a[108:111],v[112:115]\n"
C_"v187,v190,v191\n"
M_"v[116:119],v[32:35],a[112:115],v[116:119]\n"
S_"v[184:187],v244,s[52:55],s39 offen offset:1024 sc1\n"
C_"v96,v96,v97\n"
C_"v97,v98,v99\n"
M_"v[116:119],v[36:39],a[116:119],v[116:119]\n"
C_"v98,v104,v105\n"
C_"v99,v106,v107\n"
M_"v[116:119],v[40:43],a[120:123],v[116:119]\n"
"v_mov_b32 v249,v196\n"
M_"v[116:119],v[44:47],a[124:127],v[116:119]\n"
W_"lgkmcnt(4)\n"
S_"v[96:99],v244,s[52:55],s36 offen offset:2048 sc1\n"
M_"v[120:123],v[48:51],a[96:99],v[120:123]\n"
"v_mov_b32 v250,v197\n"
M_"v[120:123],v[52:55],a[100:103],v[120:123]\n"
M_"v[120:123],v[56:59],a[104:107],v[120:123]\n"
M_"v[120:123],v[60:63],a[108:111],v[120:123]\n"
"v_mov_b32 v251,v198\n"
M_"v[124:127],v[48:51],a[112:115],v[124:127]\n"
M_"v[124:127],v[52:55],a[116:119],v[124:127]\n"
M_"v[124:127],v[56:59],a[120:123],v[124:127]\n"
"v_mov_b32 v253,v199\n"
C_"v100,v100,v101\n"
M_"v[124:127],v[60:63],a[124:127],v[124:127]\n"
C_"v101,v102,v103\n"
C_"v102,v108,v109\n"
W_"lgkmcnt(0)\n"
M_"v[32:35],a[192:195],a[160:163],0\n"
C_"v103,v110,v111\n"
M_"v[32:35],a[196:199],a[164:167],v[32:35]\n"
S_"v[100:103],v244,s[52:55],s37 offen offset:2048 sc1\n"
M_"v[32:35],a[200:203],a[168:171],v[32:35]\n"
M_"v[32:35],a[204:207],a[172:175],v[32:35]\n"
M_"v[48:51],a[192:195],a[176:179],0\n"
M_"v[48:51],a[196:199],a[180:183],v[48:51]\n"
M_"v[48:51],a[200:203],a[184:187],v[48:51]\n"
M_"v[48:51],a[204:207],a[188:191],v[48:51]\n"
"s_nop 3\n"
"v_max3_f32 v200,v32,v33,v34\n"
"v_max_f32 v200,v200,v35\n"
M_"v[16:19],a[192:195],a[144:147],0\n"
"v_mov_b32 v201,v200\n"
"s_nop 0\n"
M_"v[16:19],a[196:199],a[148:151],v[16:19]\n"
"v_permlane32_swap_b32 v200,v201\n"
"v_max_f32 v200,v200,v201\n"
M_"v[16:19],a[200:203],a[152:155],v[16:19]\n"
"v_mov_b32 v201,v200\n"
"s_nop 0\n"
M_"v[16:19],a[204:207],a[156:159],v[16:19]\n"
"v_permlane16_swap_b32 v200,v201\n"
"v_min_f32_e64 v202,-v200,-v201\n"
M_"v[0:3],a[192:195],a[128:131],v[224:227]\n"
"v_min_f32_e64 v203,-v200,-v201\n"
"v_mov_b32 v200,v202\n"
M_"v[0:3],a[196:199],a[132:135],v[0:3]\n"
"v_mov_b32 v201,v202\n"
"v_max3_f32 v204,v48,v49,v50\n"
M_"v[0:3],a[200:203],a[136:139],v[0:3]\n"
"v_max_f32 v204,v204,v51\n"
"v_mov_b32 v205,v204\n"
M_"v[0:3],a[204:207],a[140:143],v[0:3]\n"
"s_nop 0\n"
"v_permlane32_swap_b32 v204,v205\n"
"v_max_f32 v204,v204,v205\n"
M_"v[4:7],a[208:211],a[128:131],v[228:231]\n"
"v_mov_b32 v205,v204\n"
D_"a[192:195],v245 offset:32768\n"
M_"v[4:7],a[212:215],a[132:135],v[4:7]\n"
"s_nop 0\n"
"v_permlane16_swap_b32 v204,v205\n"
"v_min_f32_e64 v206,-v204,-v205\n"
M_"v[4:7],a[216:219],a[136:139],v[4:7]\n"
"v_min_f32_e64 v207,-v204,-v205\n"
"v_mov_b32 v204,v206\n"
M_"v[4:7],a[220:223],a[140:143],v[4:7]\n"
"v_mov_b32 v205,v206\n"
"v_max3_f32 v196,v16,v17,v18\n"
M_"v[8:11],a[224:227],a[128:131],v[232:235]\n"
"v_max_f32 v196,v196,v19\n"
"v_mov_b32 v197,v196\n"
M_"v[8:11],a[228:231],a[132:135],v[8:11]\n"
"s_nop 0\n"
D_"a[196:199],v245 offset:33792\n"
D_"a[200:203],v245 offset:34816\n"
M_"v[8:11],a[232:235],a[136:139],v[8:11]\n"
D_"a[204:207],v245 offset:35840\n"
"v_permlane32_swap_b32 v196,v197\n"
M_"v[8:11],a[236:239],a[140:143],v[8:11]\n"
C_"v112,v112,v113\n"
C_"v113,v114,v115\n"
M_"v[36:39],a[208:211],a[160:163],v[200:203]\n"
"v_max_f32 v196,v196,v197\n"
"v_mov_b32 v197,v196\n"
M_"v[36:39],a[212:215],a[164:167],v[36:39]\n"
"s_nop 0\n"
C_"v114,v120,v121\n"
"v_permlane16_swap_b32 v196,v197\n"
M_"v[36:39],a[216:219],a[168:171],v[36:39]\n"
C_"v115,v122,v123\n"
"v_min_f32_e64 v198,-v196,-v197\n"
M_"v[36:39],a[220:223],a[172:175],v[36:39]\n"
S_"v[112:115],v244,s[52:55],s36 offen offset:3072 sc1\n"
M_"v[52:55],a[208:211],a[176:179],v[204:207]\n"
"v_min_f32_e64 v199,-v196,-v197\n"
"v_mov_b32 v196,v198\n"
M_"v[52:55],a[212:215],a[180:183],v[52:55]\n"
"v_mov_b32 v197,v198\n"
"v_max3_f32 v192,v0,v1,v2\n"
M_"v[52:55],a[216:219],a[184:187],v[52:55]\n"
"v_max_f32 v192,v192,v3\n"
"v_mov_b32 v193,v192\n"
M_"v[52:55],a[220:223],a[188:191],v[52:55]\n"
"s_nop 0\n"
"v_permlane32_swap_b32 v192,v193\n"
"v_max_f32 v192,v192,v193\n"
M_"v[40:43],a[224:227],a[160:163],v[200:203]\n"
"v_mov_b32 v193,v192\n"
"v_add_f32 v32,v32,v202\n"
M_"v[40:43],a[228:231],a[164:167],v[40:43]\n"
"s_nop 0\n"
"v_permlane16_swap_b32 v192,v193\n"
"v_min_f32_e64 v194,-v192,-v193\n"
M_"v[40:43],a[232:235],a[168:171],v[40:43]\n"
"v_min_f32_e64 v195,-v192,-v193\n"
"v_mov_b32 v192,v194\n"
M_"v[40:43],a[236:239],a[172:175],v[40:43]\n"
"v_mov_b32 v193,v194\n"
"v_add_f32 v33,v33,v202\n"
M_"v[20:23],a[208:211],a[144:147],v[196:199]\n"
"v_add_f32 v34,v34,v202\n"
"v_add_f32 v35,v35,v202\n"
M_"v[20:23],a[212:215],a[148:151],v[20:23]\n"
"v_add_f32 v48,v48,v206\n"
"v_add_f32 v49,v49,v206\n"
M_"v[20:23],a[216:219],a[152:155],v[20:23]\n"
C_"v116,v116,v117\n"
C_"v117,v118,v119\n"
M_"v[20:23],a[220:223],a[156:159],v[20:23]\n"
C_"v118,v124,v125\n"
"v_add_f32 v50,v50,v206\n"
M_"v[56:59],a[224:227],a[176:179],v[204:207]\n"
C_"v119,v126,v127\n"
"v_add_f32 v51,v51,v206\n"
M_"v[56:59],a[228:231],a[180:183],v[56:59]\n"
S_"v[116:119],v244,s[52:55],s37 offen offset:3072 sc1\n"
M_"v[56:59],a[232:235],a[184:187],v[56:59]\n"
"v_add_f32 v16,v16,v198\n"
"v_add_f32 v17,v17,v198\n"
M_"v[56:59],a[236:239],a[188:191],v[56:59]\n"
"v_add_f32 v18,v18,v198\n"
"v_add_f32 v19,v19,v198\n"
M_"v[24:27],a[224:227],a[144:147],v[196:199]\n"
D_"a[208:211],v245 offset:36864\n"
D_"a[212:215],v245 offset:37888\n"
M_"v[24:27],a[228:231],a[148:151],v[24:27]\n"
D_"a[216:219],v245 offset:38912\n"
D_"a[220:223],v245 offset:39936\n"
M_"v[24:27],a[232:235],a[152:155],v[24:27]\n"
"v_add_f32 v0,v0,v194\n"
"v_add_f32 v1,v1,v194\n"
M_"v[24:27],a[236:239],a[156:159],v[24:27]\n"
"v_add_f32 v2,v2,v194\n"
"v_add_f32 v3,v3,v194\n"
M_"v[12:15],a[240:243],a[128:131],v[236:239]\n"
"v_add_f32 v4,v4,v194\n"
"v_add_f32 v5,v5,v194\n"
M_"v[12:15],a[244:247],a[132:135],v[12:15]\n"
"v_add_f32 v6,v6,v194\n"
"v_add_f32 v7,v7,v194\n"
M_"v[12:15],a[248:251],a[136:139],v[12:15]\n"
D_"a[224:227],v245 offset:40960\n"
D_"a[228:231],v245 offset:41984\n"
M_"v[12:15],a[252:255],a[140:143],v[12:15]\n"
D_"a[232:235],v245 offset:43008\n"
D_"a[236:239],v245 offset:44032\n"
M_"v[28:31],a[240:243],a[144:147],v[196:199]\n"
"v_add_f32 v8,v8,v194\n"
"v_add_f32 v9,v9,v194\n"
M_"v[28:31],a[244:247],a[148:151],v[28:31]\n"
"v_add_f32 v10,v10,v194\n"
"v_add_f32 v11,v11,v194\n"
M_"v[28:31],a[248:251],a[152:155],v[28:31]\n"
E_"v36,v36\n"
M_"v[28:31],a[252:255],a[156:159],v[28:31]\n"
E_"v37,v37\n"
M_"v[44:47],a[240:243],a[160:163],v[200:203]\n"
E_"v38,v38\n"
M_"v[44:47],a[244:247],a[164:167],v[44:47]\n"
E_"v39,v39\n"
M_"v[44:47],a[248:251],a[168:171],v[44:47]\n"
"v_add_f32 v12,v12,v194\n"
"v_add_f32 v13,v13,v194\n"
M_"v[44:47],a[252:255],a[172:175],v[44:47]\n"
"v_add_f32 v14,v14,v194\n"
"v_add_f32 v15,v15,v194\n"
M_"v[60:63],a[240:243],a[176:179],v[204:207]\n"
C_"v146,v36,v37\n"
C_"v147,v38,v39\n"
M_"v[60:63],a[244:247],a[180:183],v[60:63]\n"
M_"v[60:63],a[248:251],a[184:187],v[60:63]\n"
M_"v[60:63],a[252:255],a[188:191],v[60:63]\n"
D_"a[240:243],v245 offset:45056\n"
D_"a[244:247],v245 offset:46080\n"
D_"a[248:251],v245 offset:47104\n"
D_"a[252:255],v245 offset:48128\n"
W_"vmcnt(28) lgkmcnt(8)\n"
"s_branch L_m_att\n"
"L_k2_a0:\n"
M_"v[96:99],a[192:195],a[160:163],v[200:203]\n"
"s_barrier\n"
"v_mov_b32 v248,0xff800000\n"
K_"v224,v248,v196,s[64:65]\n"
K_"v225,v248,v196,s[66:67]\n"
K_"v226,v248,v196,s[68:69]\n"
K_"v227,v248,v196,s[70:71]\n"
M_"v[96:99],a[196:199],a[164:167],v[96:99]\n"
"s_xor_b32 s57,s57,0x10000\n"
"v_xor_b32 v245,0x10000,v245\n"
K_"v228,v248,v196,s[72:73]\n"
K_"v229,v248,v196,s[74:75]\n"
K_"v230,v248,v196,s[76:77]\n"
M_"v[96:99],a[200:203],a[168:171],v[96:99]\n"
A_"m0,s57,16384\n"
K_"v231,v248,v196,s[78:79]\n"
K_"v232,v248,v196,s[80:81]\n"
K_"v233,v248,v196,s[82:83]\n"
K_"v234,v248,v196,s[84:85]\n"
K_"v235,v248,v196,s[86:87]\n"
M_"v[96:99],a[204:207],a[172:175],v[96:99]\n"
B_"v244,s[44:47],s56 offen offset:0 lds\n"
K_"v236,v248,v196,s[88:89]\n"
K_"v237,v248,v196,s[90:91]\n"
K_"v238,v248,v196,s[92:93]\n"
M_"v[112:115],a[192:195],a[176:179],v[204:207]\n"
B_"v244,s[44:47],s56 offen offset:1024 lds\n"
E_"v0,v0\n"
M_"v[112:115],a[196:199],a[180:183],v[112:115]\n"
K_"v239,v248,v196,s[94:95]\n"
E_"v1,v1\n"
E_"v2,v2\n"
M_"v[112:115],a[200:203],a[184:187],v[112:115]\n"
B_"v244,s[44:47],s56 offen offset:2048 lds\n"
C_"v128,v0,v1\n"
E_"v3,v3\n"
M_"v[112:115],a[204:207],a[188:191],v[112:115]\n"
E_"v4,v4\n"
E_"v5,v5\n"
M_"v[80:83],a[192:195],a[144:147],v[224:227]\n"
C_"v129,v2,v3\n"
E_"v6,v6\n"
E_"v7,v7\n"
M_"v[80:83],a[196:199],a[148:151],v[80:83]\n"
B_"v244,s[44:47],s56 offen offset:3072 lds\n"
C_"v130,v4,v5\n"
E_"v8,v8\n"
M_"v[80:83],a[200:203],a[152:155],v[80:83]\n"
C_"v131,v6,v7\n"
E_"v9,v9\n"
E_"v10,v10\n"
M_"v[80:83],a[204:207],a[156:159],v[80:83]\n"
A_"m0,s57,0\n"
D_"a[192:195],v245 offset:0\n"
D_"a[196:199],v245 offset:1024\n"
D_"a[200:203],v245 offset:2048\n"
D_"a[204:207],v245 offset:3072\n"
M_"v[100:103],a[208:211],a[160:163],v[200:203]\n"
B_"v244,s[40:43],s56 offen offset:0 lds\n"
C_"v132,v8,v9\n"
E_"v11,v11\n"
M_"v[100:103],a[212:215],a[164:167],v[100:103]\n"
C_"v133,v10,v11\n"
E_"v12,v12\n"
E_"v13,v13\n"
M_"v[100:103],a[216:219],a[168:171],v[100:103]\n"
B_"v244,s[40:43],s56 offen offset:1024 lds\n"
E_"v14,v14\n"
M_"v[100:103],a[220:223],a[172:175],v[100:103]\n"
C_"v134,v12,v13\n"
E_"v15,v15\n"
E_"v16,v16\n"
M_"v[116:119],a[208:211],a[176:179],v[204:207]\n"
C_"v135,v14,v15\n"
E_"v17,v17\n"
E_"v18,v18\n"
M_"v[116:119],a[212:215],a[180:183],v[116:119]\n"
B_"v244,s[40:43],s56 offen offset:2048 lds\n"
E_"v19,v19\n"
M_"v[116:119],a[216:219],a[184:187],v[116:119]\n"
C_"v136,v16,v17\n"
E_"v20,v20\n"
E_"v21,v21\n"
M_"v[116:119],a[220:223],a[188:191],v[116:119]\n"
B_"v244,s[40:43],s56 offen offset:3072 lds\n"
C_"v137,v18,v19\n"
E_"v22,v22\n"
M_"v[84:87],a[208:211],a[144:147],v[228:231]\n"
E_"v23,v23\n"
E_"v24,v24\n"
M_"v[84:87],a[212:215],a[148:151],v[84:87]\n"
A_"s56,s56,16384\n"
C_"v138,v20,v21\n"
E_"v25,v25\n"
E_"v26,v26\n"
M_"v[84:87],a[216:219],a[152:155],v[84:87]\n"
"v_mov_b32 v64,-1\n"
"v_mov_b32 v65,-1\n"
C_"v139,v22,v23\n"
E_"v27,v27\n"
M_"v[84:87],a[220:223],a[156:159],v[84:87]\n"
W_"lgkmcnt(4)\n"
D_"a[208:211],v245 offset:4096\n"
D_"a[212:215],v245 offset:5120\n"
D_"a[216:219],v245 offset:6144\n"
D_"a[220:223],v245 offset:7168\n"
M_"v[104:107],a[224:227],a[160:163],v[200:203]\n"
"v_mov_b32 v66,-1\n"
"v_mov_b32 v67,-1\n"
C_"v140,v24,v25\n"
E_"v28,v28\n"
M_"v[104:107],a[228:231],a[164:167],v[104:107]\n"
"s_lshl3_add_u32 s13,s30,s31\n"
C_"v141,v26,v27\n"
E_"v29,v29\n"
E_"v30,v30\n"
M_"v[104:107],a[232:235],a[168:171],v[104:107]\n"
"s_lshl_b32 s13,s13,13\n"
C_"v142,v28,v29\n"
E_"v31,v31\n"
E_"v32,v32\n"
M_"v[104:107],a[236:239],a[172:175],v[104:107]\n"
A_"s13,s13,0x5000000\n"
S_"v[64:67],v244,s[52:55],s13 offen offset:0 sc1\n"
C_"v143,v30,v31\n"
M_"v[120:123],a[224:227],a[176:179],v[204:207]\n"
S_"v[64:67],v244,s[52:55],s13 offen offset:1024 sc1\n"
E_"v33,v33\n"
M_"v[120:123],a[228:231],a[180:183],v[120:123]\n"
C_"v144,v32,v33\n"
E_"v34,v34\n"
E_"v35,v35\n"
M_"v[120:123],a[232:235],a[184:187],v[120:123]\n"
S_"v[64:67],v244,s[52:55],s13 offen offset:2048 sc1\n"
E_"v40,v40\n"
M_"v[120:123],a[236:239],a[188:191],v[120:123]\n"
C_"v145,v34,v35\n"
E_"v41,v41\n"
E_"v42,v42\n"
M_"v[88:91],a[224:227],a[144:147],v[232:235]\n"
C_"v148,v40,v41\n"
E_"v43,v43\n"
E_"v44,v44\n"
M_"v[88:91],a[228:231],a[148:151],v[88:91]\n"
A_"s13,s13,4096\n"
S_"v[64:67],v244,s[52:55],s13 offen offset:0 sc1\n"
C_"v149,v42,v43\n"
M_"v[88:91],a[232:235],a[152:155],v[88:91]\n"
E_"v45,v45\n"
E_"v46,v46\n"
M_"v[88:91],a[236:239],a[156:159],v[88:91]\n"
D_"a[224:227],v245 offset:8192\n"
D_"a[228:231],v245 offset:9216\n"
D_"a[232:235],v245 offset:10240\n"
D_"a[236:239],v245 offset:11264\n"
C_"v150,v44,v45\n"
M_"v[108:111],a[240:243],a[160:163],v[200:203]\n"
S_"v[64:67],v244,s[52:55],s13 offen offset:1024 sc1\n"
E_"v47,v47\n"
M_"v[108:111],a[244:247],a[164:167],v[108:111]\n"
C_"v151,v46,v47\n"
E_"v48,v48\n"
E_"v49,v49\n"
M_"v[108:111],a[248:251],a[168:171],v[108:111]\n"
S_"v[64:67],v244,s[52:55],s13 offen offset:2048 sc1\n"
C_"v152,v48,v49\n"
E_"v50,v50\n"
M_"v[108:111],a[252:255],a[172:175],v[108:111]\n"
E_"v51,v51\n"
E_"v52,v52\n"
M_"v[124:127],a[240:243],a[176:179],v[204:207]\n"
C_"v153,v50,v51\n"
E_"v53,v53\n"
E_"v54,v54\n"
M_"v[124:127],a[244:247],a[180:183],v[124:127]\n"
C_"v154,v52,v53\n"
E_"v55,v55\n"
E_"v56,v56\n"
M_"v[124:127],a[248:251],a[184:187],v[124:127]\n"
C_"v155,v54,v55\n"
E_"v57,v57\n"
E_"v58,v58\n"
M_"v[124:127],a[252:255],a[188:191],v[124:127]\n"
E_"v59,v59\n"
E_"v60,v60\n"
M_"v[92:95],a[240:243],a[144:147],v[236:239]\n"
C_"v156,v56,v57\n"
E_"v61,v61\n"
E_"v62,v62\n"
M_"v[92:95],a[244:247],a[148:151],v[92:95]\n"
C_"v157,v58,v59\n"
C_"v158,v60,v61\n"
E_"v63,v63\n"
M_"v[92:95],a[248:251],a[152:155],v[92:95]\n"
C_"v159,v62,v63\n"
M_"v[92:95],a[252:255],a[156:159],v[92:95]\n"
D_"a[240:243],v245 offset:12288\n"
D_"a[244:247],v245 offset:13312\n"
D_"a[248:251],v245 offset:14336\n"
D_"a[252:255],v245 offset:15360\n"
W_"vmcnt(14) lgkmcnt(8)\n"
M_"v[32:35],a[192:195],a[160:163],v[200:203]\n"
"s_barrier\n"
E_"v80,v80\n"
M_"v[32:35],a[196:199],a[164:167],v[32:35]\n"
"buffer_store_dword v254,off,s[52:55],s17 offset:24 sc1\n"
M_"v[32:35],a[200:203],a[168:171],v[32:35]\n"
"v_xor_b32 v192,0,v255\n"
"v_xor_b32 v193,32,v255\n"
M_"v[32:35],a[204:207],a[172:175],v[32:35]\n"
E_"v81,v81\n"
M_"v[48:51],a[192:195],a[176:179],v[204:207]\n"
"v_xor_b32 v194,64,v255\n"
"v_xor_b32 v195,96,v255\n"
"v_bfe_u32 v255,v244,4,5\n"
M_"v[48:51],a[196:199],a[180:183],v[48:51]\n"
"v_lshlrev_b32 v255,7,v255\n"
C_"v168,v80,v81\n"
M_"v[48:51],a[200:203],a[184:187],v[48:51]\n"
E_"v82,v82\n"
M_"v[48:51],a[204:207],a[188:191],v[48:51]\n"
D_"a[192:195],v245 offset:49152\n"
D_"a[196:199],v245 offset:57344\n"
M_"v[36:39],a[208:211],a[160:163],v[200:203]\n"
D_"a[200:203],v245 offset:50176\n"
D_"a[204:207],v245 offset:58368\n"
M_"v[36:39],a[212:215],a[164:167],v[36:39]\n"
T_"a[128:129],v192 offset:0\n"
T_"a[130:131],v192 offset:2048\n"
M_"v[36:39],a[216:219],a[168:171],v[36:39]\n"
T_"a[132:133],v193 offset:0\n"
T_"a[134:135],v193 offset:2048\n"
M_"v[36:39],a[220:223],a[172:175],v[36:39]\n"
T_"a[136:137],v194 offset:0\n"
T_"a[138:139],v194 offset:2048\n"
M_"v[52:55],a[208:211],a[176:179],v[204:207]\n"
T_"a[140:141],v195 offset:0\n"
T_"a[142:143],v195 offset:2048\n"
A_"m0,s57,49152\n"
M_"v[52:55],a[212:215],a[180:183],v[52:55]\n"
B_"v244,s[44:47],s56 offen offset:0 lds\n"
M_"v[52:55],a[216:219],a[184:187],v[52:55]\n"
E_"v83,v83\n"
M_"v[52:55],a[220:223],a[188:191],v[52:55]\n"
W_"lgkmcnt(12)\n"
D_"a[208:211],v245 offset:51200\n"
D_"a[212:215],v245 offset:59392\n"
M_"v[40:43],a[224:227],a[160:163],v[200:203]\n"
D_"a[216:219],v245 offset:52224\n"
D_"a[220:223],v245 offset:60416\n"
M_"v[40:43],a[228:231],a[164:167],v[40:43]\n"
B_"v244,s[44:47],s56 offen offset:1024 lds\n"
M_"v[40:43],a[232:235],a[168:171],v[40:43]\n"
E_"v84,v84\n"
M_"v[40:43],a[236:239],a[172:175],v[40:43]\n"
E_"v85,v85\n"
M_"v[56:59],a[224:227],a[176:179],v[204:207]\n"
C_"v169,v82,v83\n"
E_"v86,v86\n"
M_"v[56:59],a[228:231],a[180:183],v[56:59]\n"
B_"v244,s[44:47],s56 offen offset:2048 lds\n"
M_"v[56:59],a[232:235],a[184:187],v[56:59]\n"
E_"v87,v87\n"
M_"v[56:59],a[236:239],a[188:191],v[56:59]\n"
D_"a[224:227],v245 offset:53248\n"
D_"a[228:231],v245 offset:61440\n"
M_"v[44:47],a[240:243],a[160:163],v[200:203]\n"
D_"a[232:235],v245 offset:54272\n"
D_"a[236:239],v245 offset:62464\n"
M_"v[44:47],a[244:247],a[164:167],v[44:47]\n"
B_"v244,s[44:47],s56 offen offset:3072 lds\n"
M_"v[44:47],a[248:251],a[168:171],v[44:47]\n"
C_"v170,v84,v85\n"
C_"v171,v86,v87\n"
M_"v[44:47],a[252:255],a[172:175],v[44:47]\n"
E_"v88,v88\n"
M_"v[60:63],a[240:243],a[176:179],v[204:207]\n"
E_"v89,v89\n"
M_"v[60:63],a[244:247],a[180:183],v[60:63]\n"
T_"a[144:145],v192 offset:16384\n"
E_"v90,v90\n"
M_"v[60:63],a[248:251],a[184:187],v[60:63]\n"
T_"a[146:147],v192 offset:18432\n"
C_"v172,v88,v89\n"
M_"v[60:63],a[252:255],a[188:191],v[60:63]\n"
W_"lgkmcnt(6)\n"
D_"a[240:243],v245 offset:55296\n"
D_"a[244:247],v245 offset:63488\n"
M_"a[0:3],a[192:195],v[128:131],0\n"
D_"a[248:251],v245 offset:56320\n"
D_"a[252:255],v245 offset:64512\n"
M_"a[0:3],a[196:199],v[132:135],a[0:3]\n"
T_"a[148:149],v193 offset:16384\n"
T_"a[150:151],v193 offset:18432\n"
M_"a[4:7],a[192:195],v[136:139],0\n"
T_"a[152:153],v194 offset:16384\n"
T_"a[154:155],v194 offset:18432\n"
M_"a[4:7],a[196:199],v[140:143],a[4:7]\n"
T_"a[156:157],v195 offset:16384\n"
T_"a[158:159],v195 offset:18432\n"
M_"a[8:11],a[192:195],v[144:147],0\n"
A_"m0,s57,32768\n"
E_"v91,v91\n"
M_"a[8:11],a[196:199],v[148:151],a[8:11]\n"
B_"v244,s[40:43],s56 offen offset:0 lds\n"
C_"v173,v90,v91\n"
M_"a[12:15],a[192:195],v[152:155],0\n"
E_"v92,v92\n"
M_"a[12:15],a[196:199],v[156:159],a[12:15]\n"
D_"a[192:195],v245 offset:32768\n"
D_"a[196:199],v245 offset:33792\n"
M_"a[16:19],a[200:203],v[128:131],0\n"
B_"v244,s[40:43],s56 offen offset:1024 lds\n"
M_"a[16:19],a[204:207],v[132:135],a[16:19]\n"
E_"v93,v93\n"
M_"a[20:23],a[200:203],v[136:139],0\n"
E_"v94,v94\n"
M_"a[20:23],a[204:207],v[140:143],a[20:23]\n"
E_"v95,v95\n"
M_"a[24:27],a[200:203],v[144:147],0\n"
B_"v244,s[40:43],s56 offen offset:2048 lds\n"
M_"a[24:27],a[204:207],v[148:151],a[24:27]\n"
C_"v174,v92,v93\n"
E_"v96,v96\n"
M_"a[28:31],a[200:203],v[152:155],0\n"
E_"v97,v97\n"
M_"a[28:31],a[204:207],v[156:159],a[28:31]\n"
D_"a[200:203],v245 offset:34816\n"
D_"a[204:207],v245 offset:35840\n"
M_"a[32:35],a[208:211],v[128:131],0\n"
B_"v244,s[40:43],s56 offen offset:3072 lds\n"
M_"a[32:35],a[212:215],v[132:135],a[32:35]\n"
C_"v175,v94,v95\n"
C_"v176,v96,v97\n"
M_"a[36:39],a[208:211],v[136:139],0\n"
E_"v98,v98\n"
M_"a[36:39],a[212:215],v[140:143],a[36:39]\n"
E_"v99,v99\n"
M_"a[40:43],a[208:211],v[144:147],0\n"
A_"s56,s56,16384\n"
E_"v100,v100\n"
M_"a[40:43],a[212:215],v[148:151],a[40:43]\n"
W_"lgkmcnt(4)\n"
E_"v101,v101\n"
M_"a[44:47],a[208:211],v[152:155],0\n"
"s_xor_b32 s15,s14,32768\n"
C_"v177,v98,v99\n"
E_"v102,v102\n"
M_"a[44:47],a[212:215],v[156:159],a[44:47]\n"
"s_and_b32 s13,s16,0xffff\n"
D_"a[208:211],v245 offset:36864\n"
D_"a[212:215],v245 offset:37888\n"
M_"a[48:51],a[216:219],v[128:131],0\n"
A_"m0,s13,131072\n"
E_"v103,v103\n"
M_"a[48:51],a[220:223],v[132:135],a[48:51]\n"
B_"v249,s[48:51],s15 offen offset:0  lds\n"
M_"a[52:55],a[216:219],v[136:139],0\n"
C_"v178,v100,v101\n"
C_"v179,v102,v103\n"
M_"a[52:55],a[220:223],v[140:143],a[52:55]\n"
E_"v104,v104\n"
M_"a[56:59],a[216:219],v[144:147],0\n"
B_"v250,s[48:51],s15 offen offset:1024  lds\n"
M_"a[56:59],a[220:223],v[148:151],a[56:59]\n"
E_"v105,v105\n"
M_"a[60:63],a[216:219],v[152:155],0\n"
C_"v180,v104,v105\n"
E_"v106,v106\n"
M_"a[60:63],a[220:223],v[156:159],a[60:63]\n"
D_"a[216:219],v245 offset:38912\n"
D_"a[220:223],v245 offset:39936\n"
M_"a[64:67],a[224:227],v[128:131],0\n"
B_"v251,s[48:51],s15 offen offset:2048  lds\n"
M_"a[64:67],a[228:231],v[132:135],a[64:67]\n"
E_"v107,v107\n"
M_"a[68:71],a[224:227],v[136:139],0\n"
E_"v108,v108\n"
M_"a[68:71],a[228:231],v[140:143],a[68:71]\n"
E_"v109,v109\n"
M_"a[72:75],a[224:227],v[144:147],0\n"
B_"v253,s[48:51],s15 offen offset:3072  lds\n"
M_"a[72:75],a[228:231],v[148:151],a[72:75]\n"
C_"v181,v106,v107\n"
C_"v182,v108,v109\n"
M_"a[76:79],a[224:227],v[152:155],0\n"
E_"v110,v110\n"
M_"a[76:79],a[228:231],v[156:159],a[76:79]\n"
D_"a[224:227],v245 offset:40960\n"
D_"a[228:231],v245 offset:41984\n"
M_"a[80:83],a[232:235],v[128:131],0\n"
"s_xor_b32 s15,s14,32896\n"
E_"v111,v111\n"
M_"a[80:83],a[236:239],v[132:135],a[80:83]\n"
"s_and_b32 s13,s16,0xffff\n"
E_"v112,v112\n"
M_"a[84:87],a[232:235],v[136:139],0\n"
A_"m0,s13,147456\n"
E_"v113,v113\n"
M_"a[84:87],a[236:239],v[140:143],a[84:87]\n"
B_"v249,s[48:51],s15 offen offset:0  lds\n"
M_"a[88:91],a[232:235],v[144:147],0\n"
C_"v183,v110,v111\n"
C_"v184,v112,v113\n"
M_"a[88:91],a[236:239],v[148:151],a[88:91]\n"
E_"v114,v114\n"
M_"a[92:95],a[232:235],v[152:155],0\n"
B_"v250,s[48:51],s15 offen offset:1024  lds\n"
M_"a[92:95],a[236:239],v[156:159],a[92:95]\n"
D_"a[232:235],v245 offset:43008\n"
E_"v115,v115\n"
M_"a[96:99],a[240:243],v[128:131],0\n"
D_"a[236:239],v245 offset:44032\n"
C_"v185,v114,v115\n"
M_"a[96:99],a[244:247],v[132:135],a[96:99]\n"
E_"v116,v116\n"
M_"a[100:103],a[240:243],v[136:139],0\n"
B_"v251,s[48:51],s15 offen offset:2048  lds\n"
M_"a[100:103],a[244:247],v[140:143],a[100:103]\n"
E_"v117,v117\n"
M_"a[104:107],a[240:243],v[144:147],0\n"
E_"v118,v118\n"
M_"a[104:107],a[244:247],v[148:151],a[104:107]\n"
E_"v119,v119\n"
M_"a[108:111],a[240:243],v[152:155],0\n"
B_"v253,s[48:51],s15 offen offset:3072  lds\n"
M_"a[108:111],a[244:247],v[156:159],a[108:111]\n"
D_"a[240:243],v245 offset:45056\n"
D_"a[244:247],v245 offset:46080\n"
C_"v186,v116,v117\n"
M_"a[112:115],a[248:251],v[128:131],0\n"
E_"v120,v120\n"
M_"a[112:115],a[252:255],v[132:135],a[112:115]\n"
E_"v121,v121\n"
M_"a[116:119],a[248:251],v[136:139],0\n"
C_"v187,v118,v119\n"
C_"v188,v120,v121\n"
M_"a[116:119],a[252:255],v[140:143],a[116:119]\n"
E_"v122,v122\n"
M_"a[120:123],a[248:251],v[144:147],0\n"
E_"v123,v123\n"
M_"a[120:123],a[252:255],v[148:151],a[120:123]\n"
E_"v124,v124\n"
M_"a[124:127],a[248:251],v[152:155],0\n"
E_"v125,v125\n"
M_"a[124:127],a[252:255],v[156:159],a[124:127]\n"
D_"a[248:251],v245 offset:47104\n"
D_"a[252:255],v245 offset:48128\n"
C_"v189,v122,v123\n"
"v_smfmac_f32_16x16x64_bf16 v[208:211],v[240:243],v[128:135],v246\n"
E_"v126,v126\n"
"v_smfmac_f32_16x16x64_bf16 v[212:215],v[240:243],v[136:143],v246\n"
E_"v127,v127\n"
"v_smfmac_f32_16x16x64_bf16 v[216:219],v[240:243],v[144:151],v246\n"
C_"v190,v124,v125\n"
C_"v191,v126,v127\n"
"v_smfmac_f32_16x16x64_bf16 v[220:223],v[240:243],v[152:159],v246\n"
W_"vmcnt(17) lgkmcnt(8)\n"
"s_branch L_k2_t4\n"
"L_k2_a1:\n"
M_"v[16:19],a[192:195],a[144:147],v[196:199]\n"
"s_barrier\n"
"v_mov_b32 v248,0xff800000\n"
K_"v224,v248,v192,s[64:65]\n"
M_"v[16:19],a[196:199],a[148:151],v[16:19]\n"
"buffer_store_dword v254,off,s[52:55],s17 offset:24 sc1\n"
M_"v[16:19],a[200:203],a[152:155],v[16:19]\n"
K_"v225,v248,v192,s[66:67]\n"
K_"v226,v248,v192,s[68:69]\n"
M_"v[16:19],a[204:207],a[156:159],v[16:19]\n"
A_"m0,s57,49152\n"
K_"v227,v248,v192,s[70:71]\n"
K_"v228,v248,v192,s[72:73]\n"
M_"v[32:35],a[192:195],a[160:163],v[200:203]\n"
K_"v229,v248,v192,s[74:75]\n"
K_"v230,v248,v192,s[76:77]\n"
M_"v[32:35],a[196:199],a[164:167],v[32:35]\n"
B_"v244,s[44:47],s56 offen offset:0 lds\n"
M_"v[32:35],a[200:203],a[168:171],v[32:35]\n"
K_"v231,v248,v192,s[78:79]\n"
K_"v232,v248,v192,s[80:81]\n"
M_"v[32:35],a[204:207],a[172:175],v[32:35]\n"
K_"v233,v248,v192,s[82:83]\n"
K_"v234,v248,v192,s[84:85]\n"
M_"v[48:51],a[192:195],a[176:179],v[204:207]\n"
K_"v235,v248,v192,s[86:87]\n"
K_"v236,v248,v192,s[88:89]\n"
M_"v[48:51],a[196:199],a[180:183],v[48:51]\n"
K_"v237,v248,v192,s[90:91]\n"
K_"v238,v248,v192,s[92:93]\n"
M_"v[48:51],a[200:203],a[184:187],v[48:51]\n"
E_"v64,v64\n"
M_"v[48:51],a[204:207],a[188:191],v[48:51]\n"
E_"v65,v65\n"
M_"v[0:3],a[192:195],a[128:131],v[224:227]\n"
K_"v239,v248,v192,s[94:95]\n"
C_"v160,v64,v65\n"
M_"v[0:3],a[196:199],a[132:135],v[0:3]\n"
E_"v66,v66\n"
M_"v[0:3],a[200:203],a[136:139],v[0:3]\n"
E_"v67,v67\n"
M_"v[0:3],a[204:207],a[140:143],v[0:3]\n"
D_"a[192:195],v245 offset:49152\n"
D_"a[196:199],v245 offset:57344\n"
M_"v[20:23],a[208:211],a[144:147],v[196:199]\n"
D_"a[200:203],v245 offset:50176\n"
D_"a[204:207],v245 offset:58368\n"
M_"v[20:23],a[212:215],a[148:151],v[20:23]\n"
B_"v244,s[44:47],s56 offen offset:1024 lds\n"
M_"v[20:23],a[216:219],a[152:155],v[20:23]\n"
E_"v68,v68\n"
M_"v[20:23],a[220:223],a[156:159],v[20:23]\n"
E_"v69,v69\n"
M_"v[36:39],a[208:211],a[160:163],v[200:203]\n"
C_"v161,v66,v67\n"
C_"v162,v68,v69\n"
M_"v[36:39],a[212:215],a[164:167],v[36:39]\n"
E_"v70,v70\n"
M_"v[36:39],a[216:219],a[168:171],v[36:39]\n"
E_"v71,v71\n"
M_"v[36:39],a[220:223],a[172:175],v[36:39]\n"
E_"v72,v72\n"
M_"v[52:55],a[208:211],a[176:179],v[204:207]\n"
E_"v73,v73\n"
M_"v[52:55],a[212:215],a[180:183],v[52:55]\n"
C_"v163,v70,v71\n"
C_"v164,v72,v73\n"
M_"v[52:55],a[216:219],a[184:187],v[52:55]\n"
E_"v74,v74\n"
M_"v[52:55],a[220:223],a[188:191],v[52:55]\n"
E_"v75,v75\n"
M_"v[4:7],a[208:211],a[128:131],v[228:231]\n"
B_"v244,s[44:47],s56 offen offset:2048 lds\n"
M_"v[4:7],a[212:215],a[132:135],v[4:7]\n"
E_"v76,v76\n"
M_"v[4:7],a[216:219],a[136:139],v[4:7]\n"
E_"v77,v77\n"
M_"v[4:7],a[220:223],a[140:143],v[4:7]\n"
W_"lgkmcnt(4)\n"
D_"a[208:211],v245 offset:51200\n"
D_"a[212:215],v245 offset:59392\n"
M_"v[24:27],a[224:227],a[144:147],v[196:199]\n"
D_"a[216:219],v245 offset:52224\n"
D_"a[220:223],v245 offset:60416\n"
M_"v[24:27],a[228:231],a[148:151],v[24:27]\n"
C_"v165,v74,v75\n"
C_"v166,v76,v77\n"
M_"v[24:27],a[232:235],a[152:155],v[24:27]\n"
E_"v78,v78\n"
M_"v[24:27],a[236:239],a[156:159],v[24:27]\n"
E_"v79,v79\n"
M_"v[40:43],a[224:227],a[160:163],v[200:203]\n"
E_"v80,v80\n"
M_"v[40:43],a[228:231],a[164:167],v[40:43]\n"
E_"v81,v81\n"
M_"v[40:43],a[232:235],a[168:171],v[40:43]\n"
C_"v167,v78,v79\n"
C_"v168,v80,v81\n"
M_"v[40:43],a[236:239],a[172:175],v[40:43]\n"
B_"v244,s[44:47],s56 offen offset:3072 lds\n"
M_"v[56:59],a[224:227],a[176:179],v[204:207]\n"
E_"v82,v82\n"
M_"v[56:59],a[228:231],a[180:183],v[56:59]\n"
E_"v83,v83\n"
M_"v[56:59],a[232:235],a[184:187],v[56:59]\n"
E_"v84,v84\n"
M_"v[56:59],a[236:239],a[188:191],v[56:59]\n"
E_"v85,v85\n"
M_"v[8:11],a[224:227],a[128:131],v[232:235]\n"
C_"v169,v82,v83\n"
C_"v170,v84,v85\n"
M_"v[8:11],a[228:231],a[132:135],v[8:11]\n"
E_"v86,v86\n"
M_"v[8:11],a[232:235],a[136:139],v[8:11]\n"
E_"v87,v87\n"
M_"v[8:11],a[236:239],a[140:143],v[8:11]\n"
D_"a[224:227],v245 offset:53248\n"
D_"a[228:231],v245 offset:61440\n"
M_"v[28:31],a[240:243],a[144:147],v[196:199]\n"
D_"a[232:235],v245 offset:54272\n"
D_"a[236:239],v245 offset:62464\n"
M_"v[28:31],a[244:247],a[148:151],v[28:31]\n"
E_"v88,v88\n"
M_"v[28:31],a[248:251],a[152:155],v[28:31]\n"
E_"v89,v89\n"
M_"v[28:31],a[252:255],a[156:159],v[28:31]\n"
A_"m0,s57,32768\n"
C_"v171,v86,v87\n"
C_"v172,v88,v89\n"
M_"v[44:47],a[240:243],a[160:163],v[200:203]\n"
E_"v90,v90\n"
M_"v[44:47],a[244:247],a[164:167],v[44:47]\n"
B_"v244,s[40:43],s56 offen offset:0 lds\n"
M_"v[44:47],a[248:251],a[168:171],v[44:47]\n"
M_"v[44:47],a[252:255],a[172:175],v[44:47]\n"
E_"v91,v91\n"
M_"v[60:63],a[240:243],a[176:179],v[204:207]\n"
E_"v92,v92\n"
M_"v[60:63],a[244:247],a[180:183],v[60:63]\n"
E_"v93,v93\n"
M_"v[60:63],a[248:251],a[184:187],v[60:63]\n"
C_"v173,v90,v91\n"
C_"v174,v92,v93\n"
M_"v[60:63],a[252:255],a[188:191],v[60:63]\n"
E_"v94,v94\n"
M_"v[12:15],a[240:243],a[128:131],v[236:239]\n"
E_"v95,v95\n"
M_"v[12:15],a[244:247],a[132:135],v[12:15]\n"
E_"v96,v96\n"
M_"v[12:15],a[248:251],a[136:139],v[12:15]\n"
E_"v97,v97\n"
M_"v[12:15],a[252:255],a[140:143],v[12:15]\n"
W_"lgkmcnt(4)\n"
D_"a[240:243],v245 offset:55296\n"
D_"a[244:247],v245 offset:63488\n"
M_"a[0:3],a[192:195],v[128:131],0\n"
D_"a[248:251],v245 offset:56320\n"
D_"a[252:255],v245 offset:64512\n"
M_"a[0:3],a[196:199],v[132:135],a[0:3]\n"
B_"v244,s[40:43],s56 offen offset:1024 lds\n"
M_"a[4:7],a[192:195],v[136:139],0\n"
K_"v224,v248,v196,s[64:65]\n"
K_"v225,v248,v196,s[66:67]\n"
M_"a[4:7],a[196:199],v[140:143],a[4:7]\n"
K_"v226,v248,v196,s[68:69]\n"
K_"v227,v248,v196,s[70:71]\n"
M_"a[8:11],a[192:195],v[144:147],0\n"
K_"v228,v248,v196,s[72:73]\n"
K_"v229,v248,v196,s[74:75]\n"
M_"a[8:11],a[196:199],v[148:151],a[8:11]\n"
K_"v230,v248,v196,s[76:77]\n"
K_"v231,v248,v196,s[78:79]\n"
M_"a[12:15],a[192:195],v[152:155],0\n"
K_"v232,v248,v196,s[80:81]\n"
K_"v233,v248,v196,s[82:83]\n"
M_"a[12:15],a[196:199],v[156:159],a[12:15]\n"
D_"a[192:195],v245 offset:32768\n"
D_"a[196:199],v245 offset:33792\n"
M_"a[16:19],a[200:203],v[128:131],0\n"
K_"v234,v248,v196,s[84:85]\n"
K_"v235,v248,v196,s[86:87]\n"
M_"a[16:19],a[204:207],v[132:135],a[16:19]\n"
K_"v236,v248,v196,s[88:89]\n"
K_"v237,v248,v196,s[90:91]\n"
M_"a[20:23],a[200:203],v[136:139],0\n"
K_"v238,v248,v196,s[92:93]\n"
K_"v239,v248,v196,s[94:95]\n"
M_"a[20:23],a[204:207],v[140:143],a[20:23]\n"
M_"a[24:27],a[200:203],v[144:147],0\n"
B_"v244,s[40:43],s56 offen offset:2048 lds\n"
M_"a[24:27],a[204:207],v[148:151],a[24:27]\n"
C_"v175,v94,v95\n"
C_"v176,v96,v97\n"
M_"a[28:31],a[200:203],v[152:155],0\n"
E_"v98,v98\n"
M_"a[28:31],a[204:207],v[156:159],a[28:31]\n"
D_"a[200:203],v245 offset:34816\n"
D_"a[204:207],v245 offset:35840\n"
M_"a[32:35],a[208:211],v[128:131],0\n"
E_"v99,v99\n"
M_"a[32:35],a[212:215],v[132:135],a[32:35]\n"
E_"v100,v100\n"
M_"a[36:39],a[208:211],v[136:139],0\n"
E_"v101,v101\n"
M_"a[36:39],a[212:215],v[140:143],a[36:39]\n"
M_"a[40:43],a[208:211],v[144:147],0\n"
C_"v177,v98,v99\n"
C_"v178,v100,v101\n"
M_"a[40:43],a[212:215],v[148:151],a[40:43]\n"
E_"v102,v102\n"
M_"a[44:47],a[208:211],v[152:155],0\n"
E_"v103,v103\n"
M_"a[44:47],a[212:215],v[156:159],a[44:47]\n"
D_"a[208:211],v245 offset:36864\n"
D_"a[212:215],v245 offset:37888\n"
M_"a[48:51],a[216:219],v[128:131],0\n"
B_"v244,s[40:43],s56 offen offset:3072 lds\n"
M_"a[48:51],a[220:223],v[132:135],a[48:51]\n"
E_"v104,v104\n"
M_"a[52:55],a[216:219],v[136:139],0\n"
E_"v105,v105\n"
M_"a[52:55],a[220:223],v[140:143],a[52:55]\n"
C_"v179,v102,v103\n"
C_"v180,v104,v105\n"
M_"a[56:59],a[216:219],v[144:147],0\n"
E_"v106,v106\n"
M_"a[56:59],a[220:223],v[148:151],a[56:59]\n"
E_"v107,v107\n"
M_"a[60:63],a[216:219],v[152:155],0\n"
E_"v108,v108\n"
M_"a[60:63],a[220:223],v[156:159],a[60:63]\n"
W_"lgkmcnt(6)\n"
D_"a[216:219],v245 offset:38912\n"
D_"a[220:223],v245 offset:39936\n"
M_"a[64:67],a[224:227],v[128:131],0\n"
E_"v109,v109\n"
M_"a[64:67],a[228:231],v[132:135],a[64:67]\n"
C_"v181,v106,v107\n"
C_"v182,v108,v109\n"
M_"a[68:71],a[224:227],v[136:139],0\n"
M_"a[68:71],a[228:231],v[140:143],a[68:71]\n"
A_"s56,s56,16384\n"
E_"v110,v110\n"
M_"a[72:75],a[224:227],v[144:147],0\n"
E_"v111,v111\n"
M_"a[72:75],a[228:231],v[148:151],a[72:75]\n"
E_"v112,v112\n"
M_"a[76:79],a[224:227],v[152:155],0\n"
E_"v113,v113\n"
M_"a[76:79],a[228:231],v[156:159],a[76:79]\n"
D_"a[224:227],v245 offset:40960\n"
D_"a[228:231],v245 offset:41984\n"
M_"a[80:83],a[232:235],v[128:131],0\n"
C_"v183,v110,v111\n"
C_"v184,v112,v113\n"
M_"a[80:83],a[236:239],v[132:135],a[80:83]\n"
M_"a[84:87],a[232:235],v[136:139],0\n"
E_"v114,v114\n"
M_"a[84:87],a[236:239],v[140:143],a[84:87]\n"
E_"v115,v115\n"
M_"a[88:91],a[232:235],v[144:147],0\n"
E_"v116,v116\n"
M_"a[88:91],a[236:239],v[148:151],a[88:91]\n"
E_"v117,v117\n"
M_"a[92:95],a[232:235],v[152:155],0\n"
C_"v185,v114,v115\n"
C_"v186,v116,v117\n"
M_"a[92:95],a[236:239],v[156:159],a[92:95]\n"
D_"a[232:235],v245 offset:43008\n"
D_"a[236:239],v245 offset:44032\n"
M_"a[96:99],a[240:243],v[128:131],0\n"
E_"v118,v118\n"
M_"a[96:99],a[244:247],v[132:135],a[96:99]\n"
E_"v119,v119\n"
M_"a[100:103],a[240:243],v[136:139],0\n"
E_"v120,v120\n"
M_"a[100:103],a[244:247],v[140:143],a[100:103]\n"
E_"v121,v121\n"
M_"a[104:107],a[240:243],v[144:147],0\n"
C_"v187,v118,v119\n"
C_"v188,v120,v121\n"
M_"a[104:107],a[244:247],v[148:151],a[104:107]\n"
E_"v122,v122\n"
M_"a[108:111],a[240:243],v[152:155],0\n"
E_"v123,v123\n"
M_"a[108:111],a[244:247],v[156:159],a[108:111]\n"
D_"a[240:243],v245 offset:45056\n"
D_"a[244:247],v245 offset:46080\n"
M_"a[112:115],a[248:251],v[128:131],0\n"
E_"v124,v124\n"
M_"a[112:115],a[252:255],v[132:135],a[112:115]\n"
E_"v125,v125\n"
M_"a[116:119],a[248:251],v[136:139],0\n"
C_"v189,v122,v123\n"
C_"v190,v124,v125\n"
M_"a[116:119],a[252:255],v[140:143],a[116:119]\n"
E_"v126,v126\n"
M_"a[120:123],a[248:251],v[144:147],0\n"
E_"v127,v127\n"
M_"a[120:123],a[252:255],v[148:151],a[120:123]\n"
C_"v191,v126,v127\n"
M_"a[124:127],a[248:251],v[152:155],0\n"
M_"a[124:127],a[252:255],v[156:159],a[124:127]\n"
D_"a[248:251],v245 offset:47104\n"
D_"a[252:255],v245 offset:48128\n"
"v_smfmac_f32_16x16x64_bf16 v[208:211],v[240:243],v[128:135],v246\n"
"v_smfmac_f32_16x16x64_bf16 v[212:215],v[240:243],v[136:143],v246\n"
"v_smfmac_f32_16x16x64_bf16 v[216:219],v[240:243],v[144:151],v246\n"
"v_smfmac_f32_16x16x64_bf16 v[220:223],v[240:243],v[152:159],v246\n"
W_"vmcnt(9) lgkmcnt(8)\n"
"s_branch L_k2_t2\n"
"L_m_end:\n"
"L_m_exit:\n"
W_"vmcnt(0) lgkmcnt(0)\n"
"s_and_b32 s33,s16,0x10000\n"
"s_or_b32 s25,s25,s33\n"
"s_mov_b32 s33,s32\n"
"s_mov_b64 s[34:35],%5\n"
  : "=&{s30}"(b), "=&{s31}"(wid), "=&{s33}"(tag), "=&{s[34:35]}"(ka), "=&{s25}"(bad) : "s"(ka0), "s"(b0), "s"(wid0), "n"(K2_FORCE_SAFE) : "v0", "v1", "v2", "v3", "v4", "v5", "v6", "v7", "v8", "v9", "v10", "v11", "v12", "v13", "v14", "v15", "v16", "v17", "v18", "v19", "v20", "v21", "v22", "v23", "v24", "v25", "v26", "v27", "v28", "v29", "v30", "v31", "v32", "v33", "v34", "v35", "v36", "v37", "v38", "v39", "v40", "v41", "v42", "v43", "v44", "v45", "v46", "v47", "v48", "v49", "v50", "v51", "v52", "v53", "v54", "v55", "v56", "v57", "v58", "v59", "v60", "v61", "v62", "v63", "v64", "v65", "v66", "v67", "v68", "v69", "v70", "v71", "v72", "v73", "v74", "v75", "v76", "v77", "v78", "v79", "v80", "v81", "v82", "v83", "v84", "v85", "v86", "v87", "v88", "v89", "v90", "v91", "v92", "v93", "v94", "v95", "v96", "v97", "v98", "v99", "v100", "v101", "v102", "v103", "v104", "v105", "v106", "v107", "v108", "v109", "v110", "v111", "v112", "v113", "v114", "v115", "v116", "v117", "v118", "v119", "v120", "v121", "v122", "v123", "v124", "v125", "v126", "v127", "v128", "v129", "v130", "v131", "v132", "v133", "v134", "v135", "v136", "v137", "v138", "v139", "v140", "v141", "v142", "v143", "v144", "v145", "v146", "v147", "v148", "v149", "v150", "v151", "v152", "v153", "v154", "v155", "v156", "v157", "v158", "v159", "v160", "v161", "v162", "v163", "v164", "v165", "v166", "v167", "v168", "v169", "v170", "v171", "v172", "v173", "v174", "v175", "v176", "v177", "v178", "v179", "v180", "v181", "v182", "v183", "v184", "v185", "v186", "v187", "v188", "v189", "v190", "v191", "v192", "v193", "v194", "v195", "v196", "v197", "v198", "v199", "v200", "v201", "v202", "v203", "v204", "v205", "v206", "v207", "v208", "v209", "v210", "v211", "v212", "v213", "v214", "v215", "v216", "v217", "v218", "v219", "v220", "v221", "v222", "v223", "v224", "v225", "v226", "v227", "v228", "v229", "v230", "v231", "v232", "v233", "v234", "v235", "v236", "v237", "v238", "v239", "v240", "v241", "v242", "v243", "v244", "v245", "v246", "v247", "v248", "v249", "v250", "v251", "v252", "v253", "v254", "v255", "a0", "a1", "a2", "a3", "a4", "a5", "a6", "a7", "a8", "a9", "a10", "a11", "a12", "a13", "a14", "a15", "a16", "a17", "a18", "a19", "a20", "a21", "a22", "a23", "a24", "a25", "a26", "a27", "a28", "a29", "a30", "a31", "a32", "a33", "a34", "a35", "a36", "a37", "a38", "a39", "a40", "a41", "a42", "a43", "a44", "a45", "a46", "a47", "a48", "a49", "a50", "a51", "a52", "a53", "a54", "a55", "a56", "a57", "a58", "a59", "a60", "a61", "a62", "a63", "a64", "a65", "a66", "a67", "a68", "a69", "a70", "a71", "a72", "a73", "a74", "a75", "a76", "a77", "a78", "a79", "a80", "a81", "a82", "a83", "a84", "a85", "a86", "a87", "a88", "a89", "a90", "a91", "a92", "a93", "a94", "a95", "a96", "a97", "a98", "a99", "a100", "a101", "a102", "a103", "a104", "a105", "a106", "a107", "a108", "a109", "a110", "a111", "a112", "a113", "a114", "a115", "a116", "a117", "a118", "a119", "a120", "a121", "a122", "a123", "a124", "a125", "a126", "a127", "a128", "a129", "a130", "a131", "a132", "a133", "a134", "a135", "a136", "a137", "a138", "a139", "a140", "a141", "a142", "a143", "a144", "a145", "a146", "a147", "a148", "a149", "a150", "a151", "a152", "a153", "a154", "a155", "a156", "a157", "a158", "a159", "a160", "a161", "a162", "a163", "a164", "a165", "a166", "a167", "a168", "a169", "a170", "a171", "a172", "a173", "a174", "a175", "a176", "a177", "a178", "a179", "a180", "a181", "a182", "a183", "a184", "a185", "a186", "a187", "a188", "a189", "a190", "a191", "a192", "a193", "a194", "a195", "a196", "a197", "a198", "a199", "a200", "a201", "a202", "a203", "a204", "a205", "a206", "a207", "a208", "a209", "a210", "a211", "a212", "a213", "a214", "a215", "a216", "a217", "a218", "a219", "a220", "a221", "a222", "a223", "a224", "a225", "a226", "a227", "a228", "a229", "a230", "a231", "a232", "a233", "a234", "a235", "a236", "a237", "a238", "a239", "a240", "a241", "a242", "a243", "a244", "a245", "a246", "a247", "a248", "a249", "a250", "a251", "a252", "a253", "a254", "a255", "s8", "s9", "s10", "s11", "s12", "s13", "s14", "s15", "s16", "s17", "s18", "s19", "s20", "s21", "s22", "s23", "s24", "s26", "s27", "s28", "s29", "s32", "s36", "s37", "s38", "s39", "s40", "s41", "s42", "s43", "s44", "s45", "s46", "s47", "s48", "s49", "s50", "s51", "s52", "s53", "s54", "s55", "s56", "s57", "s58", "s59", "s60", "s61", "s62", "s63", "s64", "s65", "s66", "s67", "s68", "s69", "s70", "s71", "s72", "s73", "s74", "s75", "s76", "s77", "s78", "s79", "s80", "s81", "s82", "s83", "s84", "s85", "s86", "s87", "s88", "s89", "s90", "s91", "s92", "s93", "s94", "s95", "s96", "s97", "s98", "s99", "s100", "s101", "memory", "vcc", "scc");
  {
    const __attribute__((address_space(4))) KArgs* A = (const __attribute__((address_space(4))) KArgs*)ka;
    char* w = A->ws;
    const unsigned h = (b & 7) * 4 + ((b >> 3) & 3), a = b >> 5;
    const int l = __builtin_amdgcn_mbcnt_hi(-1, __builtin_amdgcn_mbcnt_lo(-1, 0));
    for (int i = (bad & 1) ? 0 : 2; i < ((bad & 2) ? 4 : 2); ++i)
      attn_safe_qblock((const u16*)(w + WS_XF), A->wq, (const u16*)(w + WS_KF), (const u16*)(w + WS_VF), (u16*)(w + WS_CF), h, (i < 2 ? 8 * a : 120 - 8 * a) + 4 * (i & 1) + wid, l);
  }
  asm volatile(
W_"vmcnt(0)\n"
"s_mov_b32 s36,s25\n"
"s_load_dwordx16 s[8:23],s[34:35],0x0\n"
"s_load_dwordx4 s[24:27],s[34:35],0x40\n"
"s_load_dwordx2 s[28:29],s[34:35],0x50\n"
"s_mov_b32 s32,s33\n"
W_"lgkmcnt(0)\n"
"s_and_b32 s33,s30,7\n"
"s_lshl_b32 s33,s33,2\n"
"s_bfe_u32 s34,s30,0x20003\n"
A_"s33,s33,s34\n"
"s_lshr_b32 s34,s30,5\n"
"s_mov_b64 s[52:53],s[28:29]\n"
"s_mov_b32 s54,0x6000000\n"
"s_mov_b32 s55,0x20000\n"
"s_mov_b32 s35,s36\n"
"v_mbcnt_lo_u32_b32 v244,-1,0\n"
"v_mbcnt_hi_u32_b32 v244,-1,v244\n"
"v_lshlrev_b32 v244,4,v244\n"
"v_mov_b32 v254,s32\n"
"s_mov_b64 s[48:49],s[18:19]\n"
"s_mov_b32 s50,0x400000\n"
"s_mov_b32 s51,0x20000\n"
"s_and_b32 s13,s30,6\n"
"s_lshl1_add_u32 s13,s13,s31\n"
"s_and_b32 s15,s34,1\n"
"s_lshl1_add_u32 s13,s13,s15\n"
"s_lshl_b32 s14,s13,17\n"
"s_lshl_b32 s21,s13,12\n"
"s_bfe_u32 s13,s30,0x20003\n"
"s_lshl2_add_u32 s18,s13,s31\n"
"s_lshr_b32 s13,s18,1\n"
"s_lshl_b32 s13,s13,7\n"
A_"s14,s14,s13\n"
"s_andn2_b32 s13,1,s34\n"
"s_lshl4_add_u32 s13,s13,s31\n"
"s_lshl_b32 s16,s13,12\n"
"s_lshl_b32 s13,s30,7\n"
A_"s17,s13,0x4f10000\n"
A_"s12,s17,4\n"
"s_andn2_b32 s20,s17,0xfff\n"
"s_cmp_eq_u32 s31,0\n"
"s_cselect_b32 s17,s17,0x7f000000\n"
"s_mov_b32 s61,s17\n"
"s_and_b32 s13,s30,1\n"
"s_lshl1_add_u32 s13,s34,s13\n"
"s_bfe_u32 s15,s30,0x20001\n"
"s_lshl2_add_u32 s13,s13,s15\n"
"s_lshl4_add_u32 s18,s13,s18\n"
"s_lshl_b32 s18,s18,10\n"
A_"s18,s18,0x200000\n"
"s_lshl_b32 s19,s34,18\n"
"s_sub_u32 s19,0x1e0000,s19\n"
"s_lshl_b32 s13,s13,17\n"
A_"s21,s21,s13\n"
A_"s21,s21,0x3a00000\n"
"s_xor_b32 s22,s21,0x40000\n"
"s_xor_b32 s23,s21,0x60000\n"
"s_xor_b32 s21,s21,0x20000\n"
"s_mov_b32 s24,1\n"
"v_lshrrev_b32 v250,6,v244\n"
"v_lshlrev_b32 v255,7,v250\n"
"s_lshl_b32 s13,s31,1\n"
"v_lshrrev_b32 v250,7,v244\n"
"v_xor_b32 v251,s13,v250\n"
"v_and_b32 v251,3,v251\n"
"v_lshl_add_u32 v255,v251,5,v255\n"
"v_bfe_u32 v251,v244,4,2\n"
"v_lshl_add_u32 v255,v251,3,v255\n"
A_"s13,s31,32\n"
"s_lshl_b32 s13,s13,12\n"
"v_add_u32 v255,s13,v255\n"
"v_lshlrev_b32 v249,10,v250\n"
"v_and_b32 v250,6,v250\n"
"v_bfe_u32 v251,v244,4,3\n"
"v_xor_b32 v250,v250,v251\n"
"v_lshl_add_u32 v249,v250,4,v249\n"
"v_add_u32 v250,7168,v249\n"
"v_add_u32 v251,14336,v249\n"
"v_add_u32 v253,21504,v249\n"
"s_and_b32 s16,s16,0xffff\n"
"s_and_b32 s13,s35,0x10000\n"
"s_or_b32 s16,s16,s13\n"
"v_xor_b32 v192,0,v255\n"
"v_xor_b32 v193,32,v255\n"
"v_xor_b32 v194,64,v255\n"
"v_xor_b32 v195,96,v255\n"
"v_bfe_u32 v255,v244,4,5\n"
"v_lshlrev_b32 v255,7,v255\n"
"v_bfe_u32 v212,v244,6,2\n"
"v_lshrrev_b32 v213,8,v244\n"
"v_cmp_eq_u32 s[10:11],v212,v213\n"
"v_mov_b32 v208,0x3f80\n"
"v_mov_b32 v209,0\n"
"v_and_b32 v212,48,v244\n"
K_"v208,0,v208,s[10:11]\n"
"v_lshlrev_b64 v[210:211],v212,v[208:209]\n"
"v_mov_b32 v208,0\n"
"v_mov_b32 v212,0\n"
"v_mov_b32 v213,0\n"
"v_bfe_i32 v246,v244,5,1\n"
"v_and_b32 v246,0xaaaaaaaa,v246\n"
"v_or_b32 v246,0x44444444,v246\n"
"s_xor_b32 s21,s21,4096\n"
"s_xor_b32 s22,s22,4096\n"
"s_xor_b32 s23,s23,4096\n"
"s_mov_b32 s13,s18\n"
B_"v[128:131],v244,s[52:55],s13 offen sc1\n"
A_"s13,s13,0x200000\n"
B_"v[132:135],v244,s[52:55],s13 offen sc1\n"
A_"s13,s13,0x200000\n"
B_"v[136:139],v244,s[52:55],s13 offen sc1\n"
A_"s13,s13,0x200000\n"
B_"v[140:143],v244,s[52:55],s13 offen sc1\n"
A_"s13,s18,s19\n"
B_"v[160:163],v244,s[52:55],s13 offen sc1\n"
A_"s13,s13,0x200000\n"
B_"v[164:167],v244,s[52:55],s13 offen sc1\n"
A_"s13,s13,0x200000\n"
B_"v[168:171],v244,s[52:55],s13 offen sc1\n"
A_"s13,s13,0x200000\n"
B_"v[172:175],v244,s[52:55],s13 offen sc1\n"
"s_xor_b32 s15,s14,0\n"
"s_and_b32 s13,s16,0xffff\n"
A_"m0,s13,131072\n"
"s_nop 0\n"
B_"v249,s[48:51],s15 offen offset:0  lds\n"
B_"v250,s[48:51],s15 offen offset:1024  lds\n"
B_"v251,s[48:51],s15 offen offset:2048  lds\n"
B_"v253,s[48:51],s15 offen offset:3072  lds\n"
"s_xor_b32 s15,s14,128\n"
"s_and_b32 s13,s16,0xffff\n"
A_"m0,s13,147456\n"
"s_nop 0\n"
B_"v249,s[48:51],s15 offen offset:0  lds\n"
B_"v250,s[48:51],s15 offen offset:1024  lds\n"
B_"v251,s[48:51],s15 offen offset:2048  lds\n"
B_"v253,s[48:51],s15 offen offset:3072  lds\n"
W_"vmcnt(0)\n"
T_"a[128:129],v192 offset:0\n"
T_"a[130:131],v192 offset:2048\n"
T_"a[132:133],v193 offset:0\n"
T_"a[134:135],v193 offset:2048\n"
T_"a[136:137],v194 offset:0\n"
T_"a[138:139],v194 offset:2048\n"
T_"a[140:141],v195 offset:0\n"
T_"a[142:143],v195 offset:2048\n"
T_"a[144:145],v192 offset:16384\n"
T_"a[146:147],v192 offset:18432\n"
T_"a[148:149],v193 offset:16384\n"
T_"a[150:151],v193 offset:18432\n"
T_"a[152:153],v194 offset:16384\n"
T_"a[154:155],v194 offset:18432\n"
T_"a[156:157],v195 offset:16384\n"
T_"a[158:159],v195 offset:18432\n"
W_"lgkmcnt(0)\n"
"s_xor_b32 s15,s14,32768\n"
"s_and_b32 s13,s16,0xffff\n"
A_"m0,s13,131072\n"
"s_nop 0\n"
B_"v249,s[48:51],s15 offen offset:0  lds\n"
B_"v250,s[48:51],s15 offen offset:1024  lds\n"
B_"v251,s[48:51],s15 offen offset:2048  lds\n"
B_"v253,s[48:51],s15 offen offset:3072  lds\n"
"s_xor_b32 s15,s14,32896\n"
"s_and_b32 s13,s16,0xffff\n"
A_"m0,s13,147456\n"
"s_nop 0\n"
B_"v249,s[48:51],s15 offen offset:0  lds\n"
B_"v250,s[48:51],s15 offen offset:1024  lds\n"
B_"v251,s[48:51],s15 offen offset:2048  lds\n"
B_"v253,s[48:51],s15 offen offset:3072  lds\n"
W_"vmcnt(0)\n"
T_"a[0:1],v192 offset:0\n"
T_"a[2:3],v192 offset:2048\n"
T_"a[16:17],v193 offset:0\n"
T_"a[18:19],v193 offset:2048\n"
T_"a[32:33],v194 offset:0\n"
T_"a[34:35],v194 offset:2048\n"
T_"a[48:49],v195 offset:0\n"
T_"a[50:51],v195 offset:2048\n"
T_"a[64:65],v192 offset:16384\n"
T_"a[66:67],v192 offset:18432\n"
T_"a[80:81],v193 offset:16384\n"
T_"a[82:83],v193 offset:18432\n"
T_"a[96:97],v194 offset:16384\n"
T_"a[98:99],v194 offset:18432\n"
T_"a[112:113],v195 offset:16384\n"
T_"a[114:115],v195 offset:18432\n"
W_"lgkmcnt(0)\n"
"s_xor_b32 s15,s14,65536\n"
"s_and_b32 s13,s16,0xffff\n"
A_"m0,s13,131072\n"
"s_nop 0\n"
B_"v249,s[48:51],s15 offen offset:0  lds\n"
B_"v250,s[48:51],s15 offen offset:1024  lds\n"
B_"v251,s[48:51],s15 offen offset:2048  lds\n"
B_"v253,s[48:51],s15 offen offset:3072  lds\n"
"s_xor_b32 s15,s14,65664\n"
"s_and_b32 s13,s16,0xffff\n"
A_"m0,s13,147456\n"
"s_nop 0\n"
B_"v249,s[48:51],s15 offen offset:0  lds\n"
B_"v250,s[48:51],s15 offen offset:1024  lds\n"
B_"v251,s[48:51],s15 offen offset:2048  lds\n"
B_"v253,s[48:51],s15 offen offset:3072  lds\n"
W_"vmcnt(0)\n"
T_"a[4:5],v192 offset:0\n"
T_"a[6:7],v192 offset:2048\n"
T_"a[20:21],v193 offset:0\n"
T_"a[22:23],v193 offset:2048\n"
T_"a[36:37],v194 offset:0\n"
T_"a[38:39],v194 offset:2048\n"
T_"a[52:53],v195 offset:0\n"
T_"a[54:55],v195 offset:2048\n"
T_"a[68:69],v192 offset:16384\n"
T_"a[70:71],v192 offset:18432\n"
T_"a[84:85],v193 offset:16384\n"
T_"a[86:87],v193 offset:18432\n"
T_"a[100:101],v194 offset:16384\n"
T_"a[102:103],v194 offset:18432\n"
T_"a[116:117],v195 offset:16384\n"
T_"a[118:119],v195 offset:18432\n"
W_"lgkmcnt(0)\n"
M_"v[200:203],v[210:213],v[128:131],0\n"
M_"v[200:203],v[210:213],v[132:135],v[200:203]\n"
M_"v[200:203],v[210:213],v[136:139],v[200:203]\n"
M_"v[200:203],v[210:213],v[140:143],v[200:203]\n"
M_"v[204:207],v[208:211],v[128:131],0\n"
M_"v[204:207],v[208:211],v[132:135],v[204:207]\n"
M_"v[204:207],v[208:211],v[136:139],v[204:207]\n"
M_"v[204:207],v[208:211],v[140:143],v[204:207]\n"
M_"v[128:131],v[210:213],v[160:163],0\n"
M_"v[128:131],v[210:213],v[164:167],v[128:131]\n"
M_"v[128:131],v[210:213],v[168:171],v[128:131]\n"
M_"v[128:131],v[210:213],v[172:175],v[128:131]\n"
M_"v[132:135],v[208:211],v[160:163],0\n"
M_"v[132:135],v[208:211],v[164:167],v[132:135]\n"
M_"v[132:135],v[208:211],v[168:171],v[132:135]\n"
M_"v[132:135],v[208:211],v[172:175],v[132:135]\n"
"s_xor_b32 s15,s14,98304\n"
"s_and_b32 s13,s16,0xffff\n"
A_"m0,s13,131072\n"
"s_nop 0\n"
B_"v249,s[48:51],s15 offen offset:0  lds\n"
B_"v250,s[48:51],s15 offen offset:1024  lds\n"
B_"v251,s[48:51],s15 offen offset:2048  lds\n"
B_"v253,s[48:51],s15 offen offset:3072  lds\n"
"s_xor_b32 s15,s14,98432\n"
"s_and_b32 s13,s16,0xffff\n"
A_"m0,s13,147456\n"
"s_nop 0\n"
B_"v249,s[48:51],s15 offen offset:0  lds\n"
B_"v250,s[48:51],s15 offen offset:1024  lds\n"
B_"v251,s[48:51],s15 offen offset:2048  lds\n"
B_"v253,s[48:51],s15 offen offset:3072  lds\n"
"s_nop 7\n"
"s_branch L_p3_slow2\n"
  :: "{s[34:35]}"(ka), "{s30}"(b), "{s31}"(wid), "{s33}"(tag), "{s25}"(bad) : "v0", "v1", "v2", "v3", "v4", "v5", "v6", "v7", "v8", "v9", "v10", "v11", "v12", "v13", "v14", "v15", "v16", "v17", "v18", "v19", "v20", "v21", "v22", "v23", "v24", "v25", "v26", "v27", "v28", "v29", "v30", "v31", "v32", "v33", "v34", "v35", "v36", "v37", "v38", "v39", "v40", "v41", "v42", "v43", "v44", "v45", "v46", "v47", "v48", "v49", "v50", "v51", "v52", "v53", "v54", "v55", "v56", "v57", "v58", "v59", "v60", "v61", "v62", "v63", "v64", "v65", "v66", "v67", "v68", "v69", "v70", "v71", "v72", "v73", "v74", "v75", "v76", "v77", "v78", "v79", "v80", "v81", "v82", "v83", "v84", "v85", "v86", "v87", "v88", "v89", "v90", "v91", "v92", "v93", "v94", "v95", "v96", "v97", "v98", "v99", "v100", "v101", "v102", "v103", "v104", "v105", "v106", "v107", "v108", "v109", "v110", "v111", "v112", "v113", "v114", "v115", "v116", "v117", "v118", "v119", "v120", "v121", "v122", "v123", "v124", "v125", "v126", "v127", "v128", "v129", "v130", "v131", "v132", "v133", "v134", "v135", "v136", "v137", "v138", "v139", "v140", "v141", "v142", "v143", "v144", "v145", "v146", "v147", "v148", "v149", "v150", "v151", "v152", "v153", "v154", "v155", "v156", "v157", "v158", "v159", "v160", "v161", "v162", "v163", "v164", "v165", "v166", "v167", "v168", "v169", "v170", "v171", "v172", "v173", "v174", "v175", "v176", "v177", "v178", "v179", "v180", "v181", "v182", "v183", "v184", "v185", "v186", "v187", "v188", "v189", "v190", "v191", "v192", "v193", "v194", "v195", "v196", "v197", "v198", "v199", "v200", "v201", "v202", "v203", "v204", "v205", "v206", "v207", "v208", "v209", "v210", "v211", "v212", "v213", "v214", "v215", "v216", "v217", "v218", "v219", "v220", "v221", "v222", "v223", "v224", "v225", "v226", "v227", "v228", "v229", "v230", "v231", "v232", "v233", "v234", "v235", "v236", "v237", "v238", "v239", "v240", "v241", "v242", "v243", "v244", "v245", "v246", "v247", "v248", "v249", "v250", "v251", "v252", "v253", "v254", "v255", "a0", "a1", "a2", "a3", "a4", "a5", "a6", "a7", "a8", "a9", "a10", "a11", "a12", "a13", "a14", "a15", "a16", "a17", "a18", "a19", "a20", "a21", "a22", "a23", "a24", "a25", "a26", "a27", "a28", "a29", "a30", "a31", "a32", "a33", "a34", "a35", "a36", "a37", "a38", "a39", "a40", "a41", "a42", "a43", "a44", "a45", "a46", "a47", "a48", "a49", "a50", "a51", "a52", "a53", "a54", "a55", "a56", "a57", "a58", "a59", "a60", "a61", "a62", "a63", "a64", "a65", "a66", "a67", "a68", "a69", "a70", "a71", "a72", "a73", "a74", "a75", "a76", "a77", "a78", "a79", "a80", "a81", "a82", "a83", "a84", "a85", "a86", "a87", "a88", "a89", "a90", "a91", "a92", "a93", "a94", "a95", "a96", "a97", "a98", "a99", "a100", "a101", "a102", "a103", "a104", "a105", "a106", "a107", "a108", "a109", "a110", "a111", "a112", "a113", "a114", "a115", "a116", "a117", "a118", "a119", "a120", "a121", "a122", "a123", "a124", "a125", "a126", "a127", "a128", "a129", "a130", "a131", "a132", "a133", "a134", "a135", "a136", "a137", "a138", "a139", "a140", "a141", "a142", "a143", "a144", "a145", "a146", "a147", "a148", "a149", "a150", "a151", "a152", "a153", "a154", "a155", "a156", "a157", "a158", "a159", "a160", "a161", "a162", "a163", "a164", "a165", "a166", "a167", "a168", "a169", "a170", "a171", "a172", "a173", "a174", "a175", "a176", "a177", "a178", "a179", "a180", "a181", "a182", "a183", "a184", "a185", "a186", "a187", "a188", "a189", "a190", "a191", "a192", "a193", "a194", "a195", "a196", "a197", "a198", "a199", "a200", "a201", "a202", "a203", "a204", "a205", "a206", "a207", "a208", "a209", "a210", "a211", "a212", "a213", "a214", "a215", "a216", "a217", "a218", "a219", "a220", "a221", "a222", "a223", "a224", "a225", "a226", "a227", "a228", "a229", "a230", "a231", "a232", "a233", "a234", "a235", "a236", "a237", "a238", "a239", "a240", "a241", "a242", "a243", "a244", "a245", "a246", "a247", "a248", "a249", "a250", "a251", "a252", "a253", "a254", "a255", "s8", "s9", "s10", "s11", "s12", "s13", "s14", "s15", "s16", "s17", "s18", "s19", "s20", "s21", "s22", "s23", "s24", "s26", "s27", "s28", "s29", "s32", "s36", "s37", "s38", "s39", "s40", "s41", "s42", "s43", "s44", "s45", "s46", "s47", "s48", "s49", "s50", "s51", "s52", "s53", "s54", "s55", "s56", "s57", "s58", "s59", "s60", "s61", "s62", "s63", "s64", "s65", "s66", "s67", "s68", "s69", "s70", "s71", "s72", "s73", "s74", "s75", "s76", "s77", "s78", "s79", "s80", "s81", "s82", "s83", "s84", "s85", "s86", "s87", "s88", "s89", "s90", "s91", "s92", "s93", "s94", "s95", "s96", "s97", "s98", "s99", "s100", "s101", "memory", "vcc", "scc");
}
extern "C" void kernel_launch(void* const* d_in, const int* in_sizes, int n_in, void* d_out, int out_size, void* d_ws, size_t ws_size, hipStream_t stream) {
  fused<<<256, 256, 0, stream>>>((const u16*)d_in[0], (const u16*)d_in[1], (const u16*)d_in[2], (const u16*)d_in[3], (const u16*)d_in[4], (const u16*)d_in[5],
                                 (const u16*)d_in[7], (const u16*)d_in[8], (const u16*)d_in[9], (u16*)d_out, (char*)d_ws);
}
```
